```python
import torch

def _softmax(x, axis=-1):
    return torch.softmax(x, dim=axis)

def _gelu(x):
    return torch.nn.functional.gelu(x, approximate='tanh')

def _silu(x):
    return torch.nn.functional.silu(x)

def _sigmoid(x):
    return torch.sigmoid(x)

def _layernorm(x, axis=-1, eps=1e-05):
    m = x.mean(dim=axis, keepdim=True)
    v = x.var(dim=axis, keepdim=True, unbiased=False)
    return (x - m) / torch.sqrt(v + eps)

def _scatter_add(tgt, idx, src, axis=0):
    return tgt.clone().index_add_(axis, idx, src)

def _segment_sum(src, seg, n):
    out = torch.zeros((n,) + src.shape[1:], dtype=src.dtype)
    return out.index_add_(0, seg, src)

def _pad(x, pw):
    flat = tuple((v for pair in reversed(pw) for v in pair))
    return torch.nn.functional.pad(x, flat)
kernel_name = 'adaLN_cond__compressed_kv_cla__glu_gated_bilinear__prompt'

def setup_inputs():

    _w = lambda a: (lambda t: t.to(torch.bfloat16) if t.is_floating_point() else t)(torch.as_tensor(a))

    def _A_setup():
        B, H, Sq, Sk, D, M = (1, 32, 2048, 2048, 128, 512)
        Dc = 4 * D
        return {'x': _w((lambda *s: torch.randn(*s, dtype=torch.bfloat16) * 0.05)(B, Sq, M)), 'c_kv': _w((lambda *s: torch.randn(*s, dtype=torch.bfloat16) * 0.8)(B, Sk, Dc)), 'w_q': _w((lambda *s: torch.randn(*s, dtype=torch.bfloat16) * 1.6)(M, H, D) * M ** (-0.5)), 'w_uk': _w((lambda *s: torch.randn(*s, dtype=torch.bfloat16) * 1.6)(Dc, H, D) * Dc ** (-0.5)), 'w_uv': _w((lambda *s: torch.randn(*s, dtype=torch.bfloat16) * 0.8)(Dc, H, D) * Dc ** (-0.5)), 'w_o': _w((lambda *s: torch.randn(*s, dtype=torch.bfloat16) * 0.8)(H, D, M) * (H * D) ** (-0.5))}
    _A_in = _A_setup()

    def _B_setup():
        B, S, D, F = (1, 2048, 512, 1792)
        return {'x': _w((lambda *s: torch.randn(*s, dtype=torch.bfloat16) * 0.05)(B, S, D)), 'w_gate': _w((lambda *s: torch.randn(*s, dtype=torch.bfloat16) * 0.4)(D, F) * D ** (-0.5)), 'w_up': _w((lambda *s: torch.randn(*s, dtype=torch.bfloat16) * 0.4)(D, F) * D ** (-0.5)), 'w_down': _w((lambda *s: torch.randn(*s, dtype=torch.bfloat16) * 0.4)(F, D) * F ** (-0.5))}
    _B_in = _B_setup()
    return {'A_x': _A_in['x'], 'A_c_kv': _A_in['c_kv'], 'A_w_q': _A_in['w_q'], 'A_w_uk': _A_in['w_uk'], 'A_w_uv': _A_in['w_uv'], 'A_w_o': _A_in['w_o'], 'B_x': _B_in['x'], 'B_w_gate': _B_in['w_gate'], 'B_w_up': _B_in['w_up'], 'B_w_down': _B_in['w_down'], 'c': _w((lambda *s: torch.randn(*s, dtype=torch.bfloat16) * 0.2)(1, 512) * 8.0), 'w_mod': _w((lambda *s: torch.randn(*s, dtype=torch.bfloat16) * 0.2)(512, 6 * 512) * 512 ** (-0.5) * 8.0)}

def reference(A_x, A_c_kv, A_w_q, A_w_uk, A_w_uv, A_w_o, B_x, B_w_gate, B_w_up, B_w_down, c, w_mod):

    def _A_ref(x, c_kv, w_q, w_uk, w_uv, w_o):
        B, Sq, M = (x.shape[0], x.shape[1], x.shape[2])
        Sk = c_kv.shape[1]
        H, D = (w_q.shape[1], w_q.shape[2])
        q = torch.einsum('bqm,mhd->bhqd', x, w_q)
        k = torch.einsum('bkc,chd->bhkd', c_kv, w_uk)
        v = torch.einsum('bkc,chd->bhkd', c_kv, w_uv)
        scores = torch.einsum('bhqd,bhkd->bhqk', q, k) * D ** (-0.5)
        off = Sk - Sq
        qi = torch.arange(Sq)[:, None]
        kj = torch.arange(Sk)[None, :]
        cmask = kj <= qi + off
        scores = torch.where(cmask[None, None], scores, -1000000000.0)
        p = _softmax(scores, axis=-1)
        ctx = torch.einsum('bhqk,bhkd->bhqd', p, v)
        return torch.einsum('bhqd,hdm->bqm', ctx, w_o)

    def _B_ref(x, w_gate, w_up, w_down):
        g = torch.einsum('bsd,df->bsf', x, w_gate)
        u = torch.einsum('bsd,df->bsf', x, w_up)
        h = g * u
        return torch.einsum('bsf,fd->bsd', h, w_down)
    x = A_x
    mod = (_silu(c) @ w_mod).reshape(c.shape[0], 6, 1, -1)
    sa, ca, ga, sb, cb, gb = (mod[:, 0].clone(), mod[:, 1].clone(), mod[:, 2].clone(), mod[:, 3].clone(), mod[:, 4].clone(), mod[:, 5].clone())
    h = _layernorm(x, axis=-1) * (1 + ca) + sa
    x = x + ga * _A_ref(h, A_c_kv, A_w_q, A_w_uk, A_w_uv, A_w_o)
    h = _layernorm(x, axis=-1) * (1 + cb) + sb
    x = x + gb * _B_ref(h, B_w_gate, B_w_up, B_w_down)
    return x
```

```cpp
#include <hip/hip_runtime.h>
#include <hip/hip_bf16.h>
typedef unsigned short u16;
__device__ __forceinline__ float bf2f(u16 v){ return __uint_as_float(((unsigned)v)<<16); }
__device__ __forceinline__ u16 f2bf(float f){ unsigned u=__float_as_uint(f); u += 0x7fff + ((u>>16)&1); return (u16)(u>>16); }
__device__ __forceinline__ float rbf(float f){ return bf2f(f2bf(f)); }

__global__ void k_mod(const u16* c, const u16* wmod, float* mod){
  int n = blockIdx.x*blockDim.x+threadIdx.x; if(n>=3072) return;
  float acc=0;
  for(int k=0;k<512;k++){ float x=bf2f(c[k]); float s=rbf(x/(1.f+__expf(-x))); acc += s*bf2f(wmod[k*3072+n]); }
  mod[n]=rbf(acc);
}
__global__ void k_ln(const u16* x, const float* shift, const float* scale, u16* h){
  int row=blockIdx.x; int t=threadIdx.x;
  float v[8]; float s=0;
  for(int i=0;i<8;i++){ v[i]=bf2f(x[row*512+t*8+i]); s+=v[i]; }
  for(int o=32;o>0;o>>=1) s+=__shfl_xor(s,o);
  float m=s/512.f; float q=0;
  for(int i=0;i<8;i++){ float d=v[i]-m; q+=d*d; }
  for(int o=32;o>0;o>>=1) q+=__shfl_xor(q,o);
  float r=rsqrtf(q/512.f+1e-5f);
  for(int i=0;i<8;i++){ int cidx=t*8+i; float y=rbf((v[i]-m)*r); y=rbf(y*rbf(1.f+scale[cidx])); y=y+shift[cidx]; h[row*512+cidx]=f2bf(y);}
}
template<int MODE>
__global__ void k_gemm(const u16* A, const u16* B, u16* C, int M,int N,int K, const u16* res, const float* gate, const u16* B2){
  __shared__ float As[16][17], Bs[16][17], Bs2[16][17];
  int tx=threadIdx.x&15, ty=threadIdx.x>>4; int row=blockIdx.y*16+ty, col=blockIdx.x*16+tx;
  float acc=0, acc2=0;
  for(int k0=0;k0<K;k0+=16){
    As[ty][tx]=bf2f(A[(size_t)row*K+k0+tx]); Bs[ty][tx]=bf2f(B[(size_t)(k0+ty)*N+col]);
    if(MODE==2) Bs2[ty][tx]=bf2f(B2[(size_t)(k0+ty)*N+col]);
    __syncthreads();
    for(int k=0;k<16;k++){ acc+=As[ty][k]*Bs[k][tx]; if(MODE==2) acc2+=As[ty][k]*Bs2[k][tx]; }
    __syncthreads();
  }
  if(MODE==0) C[(size_t)row*N+col]=f2bf(acc);
  if(MODE==1) C[(size_t)row*N+col]=f2bf(bf2f(res[(size_t)row*N+col])+rbf(gate[col]*rbf(acc)));
  if(MODE==2) C[(size_t)row*N+col]=f2bf(rbf(acc)*rbf(acc2));
}
__global__ void k_attn(const u16* Q,const u16* Kk,const u16* V,u16* O){
  int row=blockIdx.x, h=blockIdx.y, t=threadIdx.x;
  __shared__ float qs[128]; __shared__ float p[2048];
  qs[t]=bf2f(Q[(size_t)row*4096+h*128+t]); qs[t+64]=bf2f(Q[(size_t)row*4096+h*128+t+64]);
  __syncthreads();
  float mx=-1e30f;
  for(int j=t;j<=row;j+=64){ float s=0; const u16* kp=Kk+(size_t)j*4096+h*128; for(int d=0;d<128;d++) s+=qs[d]*bf2f(kp[d]); s*=0.08838834764831845f; p[j]=s; mx=fmaxf(mx,s);}
  for(int o=32;o>0;o>>=1) mx=fmaxf(mx,__shfl_xor(mx,o));
  float l=0; for(int j=t;j<=row;j+=64){ float e=__expf(p[j]-mx); p[j]=e; l+=e; }
  for(int o=32;o>0;o>>=1) l+=__shfl_xor(l,o);
  __syncthreads();
  float a0=0,a1=0; for(int j=0;j<=row;j++){ float e=p[j]; a0+=e*bf2f(V[(size_t)j*4096+h*128+t]); a1+=e*bf2f(V[(size_t)j*4096+h*128+t+64]); }
  O[(size_t)row*4096+h*128+t]=f2bf(a0/l); O[(size_t)row*4096+h*128+t+64]=f2bf(a1/l);
}
extern "C" void kernel_launch(void* const* d_in, const int* in_sizes, int n_in, void* d_out, int out_size, void* d_ws, size_t ws_size, hipStream_t stream){
  const u16* x=(const u16*)d_in[0]; const u16* ckv=(const u16*)d_in[1]; const u16* wq=(const u16*)d_in[2]; const u16* wuk=(const u16*)d_in[3];
  const u16* wuv=(const u16*)d_in[4]; const u16* wo=(const u16*)d_in[5]; const u16* wg=(const u16*)d_in[7]; const u16* wu=(const u16*)d_in[8];
  const u16* wd=(const u16*)d_in[9]; const u16* c=(const u16*)d_in[10]; const u16* wmod=(const u16*)d_in[11];
  char* ws=(char*)d_ws; float* mod=(float*)ws; u16* h=(u16*)(ws+(1<<20)); u16* q=(u16*)(ws+(4<<20)); u16* k=(u16*)(ws+(20<<20)); u16* v=(u16*)(ws+(36<<20));
  u16* ctx=(u16*)(ws+(52<<20)); u16* x1=(u16*)(ws+(68<<20)); u16* h2=(u16*)(ws+(72<<20)); u16* hh=(u16*)(ws+(76<<20));
  k_mod<<<12,256,0,stream>>>(c,wmod,mod);
  k_ln<<<2048,64,0,stream>>>(x,mod,mod+512,h);
  k_gemm<0><<<dim3(256,128),256,0,stream>>>(h,wq,q,2048,4096,512,0,0,0);
  k_gemm<0><<<dim3(256,128),256,0,stream>>>(ckv,wuk,k,2048,4096,512,0,0,0);
  k_gemm<0><<<dim3(256,128),256,0,stream>>>(ckv,wuv,v,2048,4096,512,0,0,0);
  k_attn<<<dim3(2048,32),64,0,stream>>>(q,k,v,ctx);
  k_gemm<1><<<dim3(32,128),256,0,stream>>>(ctx,wo,x1,2048,512,4096,x,mod+1024,0);
  k_ln<<<2048,64,0,stream>>>(x1,mod+1536,mod+2048,h2);
  k_gemm<2><<<dim3(112,128),256,0,stream>>>(h2,wg,hh,2048,1792,512,0,0,wu);
  k_gemm<1><<<dim3(32,128),256,0,stream>>>(hh,wd,(u16*)d_out,2048,512,1792,x1,mod+2560,0);
}
```

```cpp
#include <hip/hip_runtime.h>
#include <hip/hip_bf16.h>
#include <stdint.h>

typedef unsigned short u16;
typedef unsigned u32x4 __attribute__((ext_vector_type(4)));
typedef unsigned u32x2 __attribute__((ext_vector_type(2)));
typedef float f32x4 __attribute__((ext_vector_type(4)));
typedef float f32x2 __attribute__((ext_vector_type(2)));
typedef __bf16 bf16x2 __attribute__((ext_vector_type(2)));
typedef __bf16 bf16x8 __attribute__((ext_vector_type(8)));
#define DI __device__ __forceinline__
#define LDSP __attribute__((address_space(3)))

constexpr size_t WS_MP  = 393216;
constexpr size_t WS_LN1 = 16384;
constexpr size_t WS_P3  = 32768;
constexpr size_t WS_UV  = 65536;
constexpr size_t WS_ST  = 131072;
constexpr size_t WS_FLAG = 458752;
constexpr int FL_A = 0, FL_G = 1024, FL_H = 2048, FL_KV = 4096, FL_C4 = 6144, FL_P = 14336, FL_T = 15360, FL_M = 16384;
constexpr size_t WS_EPOCH = 0;
constexpr size_t WS_PART = 64u << 20;
constexpr size_t WS_H   = 1u << 20;
constexpr size_t WS_X1  = 3u << 20;
constexpr size_t WS_A2  = 5u << 20;
constexpr size_t WS_HH  = 7u << 20;
constexpr size_t WS_CF  = 14u << 20;
constexpr size_t WS_K   = 16u << 20;
constexpr size_t WS_V   = 32u << 20;
constexpr size_t WS_CTX = 48u << 20;

DI float bflo(unsigned w) { return __uint_as_float(w << 16); }
DI float bfhi(unsigned w) { return __uint_as_float(w & 0xffff0000u); }
DI unsigned pk2(float lo, float hi) { f32x2 v = {lo, hi}; return __builtin_bit_cast(unsigned, __builtin_convertvector(v, bf16x2)); }
DI float rbf(float x) { return bfhi(pk2(0.f, x)); }
DI float dot2(unsigned a, unsigned b, float c) { return __builtin_amdgcn_fdot2_f32_bf16(__builtin_bit_cast(bf16x2, a), __builtin_bit_cast(bf16x2, b), c, false); }
DI void wg_barrier() { asm volatile("s_waitcnt lgkmcnt(0)\n\ts_barrier" ::: "memory"); }
DI float xsum(float v) {
  auto a = __builtin_amdgcn_permlane16_swap(__float_as_uint(v), __float_as_uint(v), false, false); v = __uint_as_float(a[0]) + __uint_as_float(a[1]);
  auto b = __builtin_amdgcn_permlane32_swap(__float_as_uint(v), __float_as_uint(v), false, false); return __uint_as_float(b[0]) + __uint_as_float(b[1]); }
template <int C> DI float dpp(float v) { return __uint_as_float(__builtin_amdgcn_update_dpp(0u, __float_as_uint(v), C, 0xf, 0xf, false)); }
DI void touch1_last(const void* p) { unsigned d; asm volatile("global_load_dword %0, %1, off\n\ts_endpgm" : "=&v"(d) : "v"(p) : "memory"); }
DI void st_wt(void* p, u32x4 v) { asm volatile("global_store_dwordx4 %0, %1, off sc1\n\ts_nop 1" :: "v"(p), "v"(v) : "memory"); }
DI void touch2_last(const void* p, const void* q) { unsigned d; asm volatile("global_load_dword %0, %1, off\n\tglobal_load_dword %0, %2, off\n\ts_endpgm" : "=&v"(d) : "v"(p), "v"(q) : "memory"); }
DI void touch5_last(const void* p, const void* q, const void* r, const void* s, const void* t) { unsigned d;
  asm volatile("global_load_dword %0, %1, off\n\tglobal_load_dword %0, %2, off\n\tglobal_load_dword %0, %3, off\n\tglobal_load_dword %0, %4, off\n\tglobal_load_dword %0, %5, off\n\ts_endpgm"
               : "=&v"(d) : "v"(p), "v"(q), "v"(r), "v"(s), "v"(t) : "memory"); }
DI float shx(float v, int m) { return __shfl_xor(v, m); }
#define DWS ws
#define STAMP(i)
#define STAMPS(i, sr)
#define STAMP3()
#define MIMIC 0
#define K4_RT ((xcd >> 1) * 2 + (j / 14))
#define K4_CT ((xcd & 1) * 14 + (j % 14))
#define K5_RT ((xcd >> 1) * 8 + (j >> 2))
#define K5_CT ((xcd & 1) * 4 + (j & 3))
#define K5_PFBASE ((((j - 28) * 448 + (t >> 5)) * 64) + (xcd & 1) * 32 + (t & 31))
#define K5_PFN 4
#define K5_PFSTEP 512
#define R3_RB (xcd * 16 + (j >> 1))
#define WARM 2
#define WARMS "2"
#define ACC16_OUT "=&v"(ac[0]), "=&v"(ac[1]), "=&v"(ac[2]), "=&v"(ac[3]), "=&v"(ac[4]), "=&v"(ac[5]), "=&v"(ac[6]), "=&v"(ac[7])
#define AC(q) ac[(q) >> 1][(q) & 1]
DI int lane_now() { int l; asm volatile("v_mbcnt_lo_u32_b32 %0, -1, 0\n\tv_mbcnt_hi_u32_b32 %0, -1, %0" : "=v"(l)); return l; }
DI void st_wt8(void* p, f32x2 v) { asm volatile("global_store_dwordx2 %0, %1, off sc1" :: "v"(p), "v"(v) : "memory"); }
struct TailArgs { const u16* wuk; const u16* wuv; const u16* x; const u16* wq; const u16* wo; const u16* wg; const u16* wu; const u16* wd; char* ws; u16* out; const u16* ckv; const u16* cvec; const u16* wmod; };
typedef const __attribute__((address_space(4))) TailArgs* TailArgsP;
DI TailArgsP tail_args() { TailArgsP p = (TailArgsP)__builtin_amdgcn_kernarg_segment_ptr(); asm volatile("" : "+s"(p)); return p; }
DI u16 ld_dev(const u16* p) { return *p; }
DI f32x2 ld_dev8(const f32x2* p) { return __builtin_bit_cast(f32x2, __hip_atomic_load((const unsigned long long*)p, __ATOMIC_RELAXED, __HIP_MEMORY_SCOPE_AGENT)); }
DI int kmap_pos(int k) { return ((k & 15) >> 2) * 128 + ((k >> 4) & 1) * 4 + (k & 3); }
DI void attn_rows_exact(const u16* wq, char* ws, LDSP float* sc, int head, int row0, int lane) {
  const u16* hf = (const u16*)(ws + WS_H); const u16* kf = (const u16*)(ws + WS_K) + (size_t)head * 262144; const u16* vf = (const u16*)(ws + WS_V) + (size_t)head * 262144;
  u16* cx = (u16*)(ws + WS_CTX);
  LDSP float* qs = sc; LDSP float* p = sc + 128;
  for (int row = row0; row < row0 + 32; ++row) {
    float q0 = 0.f, q1 = 0.f;
    for (int m = 0; m < 512; ++m) {
      const float hv = bflo(ld_dev(hf + (size_t)(((row >> 7) * 16 + (m >> 5)) * 8 + ((row >> 4) & 7)) * 512 + (row & 15) * 8 + kmap_pos(m)));
      const unsigned ww = *(const unsigned*)(wq + (size_t)m * 4096 + head * 128 + lane * 2);
      q0 = fmaf(hv, bflo(ww), q0); q1 = fmaf(hv, bfhi(ww), q1); }
    qs[lane * 2] = rbf(q0); qs[lane * 2 + 1] = rbf(q1);
    asm volatile("s_waitcnt lgkmcnt(0)" ::: "memory");
    float mx = -__builtin_inff();
    for (int key = lane; key <= row; key += 64) {
      const u16* kp = kf + (size_t)(key >> 6) * 8192 + (((key >> 5) & 1) * 8 + ((key >> 4) & 1)) * 512 + (key & 15) * 8;
      float s = 0.f;
      for (int d = 0; d < 128; ++d) s = fmaf(qs[d], bflo(ld_dev(kp + (d >> 5) * 1024 + kmap_pos(d))), s);
      s = (MIMIC >= 1 ? rbf(s) : s) * 0.08838834764831845f;
      if (MIMIC >= 2) s = rbf(s);
      p[key] = s; mx = fmaxf(mx, s); }
    for (int m = 1; m < 64; m <<= 1) mx = fmaxf(mx, shx(mx, m));
    float l = 0.f;
    for (int key = lane; key <= row; key += 64) { const float e = rbf(__expf(p[key] - mx)); p[key] = e; l += e; }
    for (int m = 1; m < 64; m <<= 1) l += shx(l, m);
    asm volatile("s_waitcnt lgkmcnt(0)" ::: "memory");
    for (int dd = 0; dd < 2; ++dd) { const int d = lane + 64 * dd; float o = 0.f;
      for (int key = 0; key <= row; ++key)
        o = fmaf(p[key], bflo(ld_dev(vf + (size_t)(key >> 6) * 8192 + (((key >> 5) & 1) * 8 + (d >> 4)) * 512 + (d & 15) * 8 + kmap_pos(key))), o);
      __hip_atomic_store(cx + (size_t)((row >> 4) * 128 + head * 4 + (d >> 5)) * 512 + (row & 15) * 8 + kmap_pos(d), (u16)(pk2(o / l, 0.f) & 0xffffu), __ATOMIC_RELAXED, __HIP_MEMORY_SCOPE_AGENT); }
    asm volatile("s_waitcnt lgkmcnt(0)" ::: "memory");
  }
}
DI float wsum(float v) { v += dpp<0x128>(v); v += dpp<0x124>(v); v += dpp<0x122>(v); v += dpp<0x121>(v); return xsum(v); }
DI void flag_set(char* ws, int off, unsigned tag) { (void)__hip_atomic_exchange((unsigned*)(ws + WS_FLAG + off), tag, __ATOMIC_RELAXED, __HIP_MEMORY_SCOPE_AGENT); }
#undef DWS
#define DWS (tail_args()->ws)
__global__ __launch_bounds__(256, 1) void k_block(const u16* wuk_, const u16* wuv_, const u16* x_, const u16* wq_, const u16* wo_, const u16* wg_, const u16* wu_, const u16* wd_, char* ws_, u16* out_,
                                                  const u16* ckv_, const u16* cvec_, const u16* wmod_) {
  __shared__ __attribute__((aligned(16))) char lds[163840];
  (void)wuk_; (void)wuv_; (void)x_; (void)wq_; (void)wo_; (void)wg_; (void)wu_; (void)wd_; (void)ws_; (void)out_; (void)ckv_; (void)cvec_; (void)wmod_;
  static_assert(WS_P3 == 32768 && WS_MP == 393216 && WS_FLAG == 458752 && WS_H == (1u << 20) && WS_CF == (14u << 20) && WS_K == (16u << 20) && WS_V == (32u << 20) &&
                WS_CTX == (48u << 20) && FL_H == 2048 && FL_KV == 4096 && FL_T == 15360 && FL_M == 16384, "offsets are literal in the asm");
  const int b0 = blockIdx.x, w0 = __builtin_amdgcn_readfirstlane(threadIdx.x >> 6);
#define IDS int b = b0, w = w0; asm volatile("" : "+s"(b), "+s"(w)); const int xcd = b & 7, j = b >> 3; (void)xcd; (void)j
  const unsigned ldsb = (unsigned)(uintptr_t)(LDSP char*)lds;
  unsigned tag;
  { IDS; TailArgsP A = tail_args(); char* ws = A->ws;
#define DIAGK 4
    STAMP(0);
    tag = *(const __attribute__((address_space(4))) unsigned*)(uintptr_t)(ws + WS_EPOCH);
    const int lane = lane_now(), t = w * 64 + lane, rb = xcd * 16 + (j >> 1), ks = (j & 1) * 8 + w * 2, bm = b < 192 ? b : 191, cb = bm >> 2, kq = bm & 3;
    unsigned len; asm("s_mov_b32 %0, code_k5_end-code_begin" : "=s"(len));
    const unsigned co = ((j * 4 + w) * 8 + (lane >> 3)) * 128;
    u32x4 ck[2], wm[4], xr[4]; unsigned cv[4], dummy;
    asm volatile(
    "s_getpc_b64 s[40:41]\n"
    "code_begin:\n"
    "global_load_dword %14, %25, s[40:41]\n"
    "global_load_dwordx4 %0, %21, %15\n"
    "v_add_u32 v16, 0x2000, %21\n"
    "global_load_dwordx4 %1, v16, %15\n"
    "global_load_dwordx4 %2, %22, %16\n"
    "global_load_ushort %6, %23, %17 offset:0\n"
    "v_add_u32 v16, 196608, %22\n"
    "global_load_dwordx4 %3, v16, %16\n"
    "global_load_ushort %7, %23, %17 offset:64\n"
    "v_add_u32 v16, 393216, %22\n"
    "global_load_dwordx4 %4, v16, %16\n"
    "global_load_ushort %8, %23, %17 offset:128\n"
    "v_add_u32 v16, 589824, %22\n"
    "global_load_dwordx4 %5, v16, %16\n"
    "global_load_ushort %9, %23, %17 offset:192\n"
    "global_load_dwordx4 %10, %24, %18 offset:0\n"
    "global_load_dwordx4 %11, %24, %18 offset:1024\n"
    "global_load_dwordx4 %12, %24, %18 offset:2048\n"
    "global_load_dwordx4 %13, %24, %18 offset:3072\n"
    "s_mov_b64 s[76:77], %19\n"
    "s_and_b32 s77, s77, 0xffff\n"
    "s_mov_b32 s78, 4186624\n"
    "s_mov_b32 s79, 0x20000\n"
    "s_mov_b32 s20, %20\n"
    "v_mbcnt_lo_u32_b32 v16, -1, 0\n"
    "v_mbcnt_hi_u32_b32 v16, -1, v16\n"
    "v_lshrrev_b32 v17, 3, v16\n"
    "s_lshl_b32 s30, s20, 5\n"
    "v_add_u32 v17, s30, v17\n"
    "s_mov_b32 s30, 8192\n"
    "v_mul_lo_u32 v17, v17, s30\n"
    "v_lshrrev_b32 v18, 4, v16\n"
    "v_lshlrev_b32 v18, 1, v18\n"
    "v_and_b32 v19, 7, v16\n"
    "v_xor_b32 v18, v18, v19\n"
    "v_lshl_add_u32 v4, v18, 4, v17\n"
    "v_add_u32 v5, 64512, v4\n"
    "v_add_u32 v6, 129024, v4\n"
    "v_add_u32 v7, 193536, v4\n"
    "s_lshl_b32 s28, s20, 12\n"
    "s_mov_b32 s29, 0\n"
    "s_add_u32 m0, s28, 0\n"
    "s_nop 0\n"
    "buffer_load_dwordx4 v4, s[76:79], s29 offen offset:0 lds\n"
    "buffer_load_dwordx4 v5, s[76:79], s29 offen offset:1024 lds\n"
    "buffer_load_dwordx4 v6, s[76:79], s29 offen offset:2048 lds\n"
    "buffer_load_dwordx4 v7, s[76:79], s29 offen offset:3072 lds\n"
    "s_mov_b32 s29, 128\n"
    "s_add_u32 m0, s28, 16384\n"
    "s_nop 0\n"
    "buffer_load_dwordx4 v4, s[76:79], s29 offen offset:0 lds\n"
    "buffer_load_dwordx4 v5, s[76:79], s29 offen offset:1024 lds\n"
    "buffer_load_dwordx4 v6, s[76:79], s29 offen offset:2048 lds\n"
    "buffer_load_dwordx4 v7, s[76:79], s29 offen offset:3072 lds\n"
    "s_waitcnt vmcnt(8)\n"
      : "=&v"(ck[0]), "=&v"(ck[1]), "=&v"(wm[0]), "=&v"(wm[1]), "=&v"(wm[2]), "=&v"(wm[3]), "=&v"(cv[0]), "=&v"(cv[1]), "=&v"(cv[2]), "=&v"(cv[3]),
        "=&v"(xr[0]), "=&v"(xr[1]), "=&v"(xr[2]), "=&v"(xr[3]), "=&v"(dummy)
      : "s"(A->ckv), "s"(A->wmod), "s"(A->cvec), "s"(A->x), "s"((const char*)(j >= 16 ? A->wuv : A->wuk) + (4 * xcd + 2 * ((j >> 3) & 1)) * 256), "s"(w),
        "v"((rb * 16 + (lane >> 3)) * 1024 + ks * 64 + (lane & 7) * 16), "v"((kq * 128 + (t >> 3)) * 6144 + cb * 128 + (t & 7) * 16), "v"((kq * 128 + (t >> 3)) * 2),
        "v"((rb * 16 + w * 4) * 1024 + lane * 16), "v"(co < len ? co : len), "s"(ldsb)
      : "v4","v5","v6","v7","v16","v17","v18","v19","s20","s28","s29","s30","s40","s41","s76","s77","s78","s79","scc","vcc","memory");
    STAMP(1);
    tag = tag * 0x9E3779B1u + 0x7F4A7C15u;
    LDSP u32x2 (*tl)[17] = (LDSP u32x2 (*)[17])(lds + 131072 + 8192 + w * 2304);
    { const int r = lane >> 3, ch = (lane & 7) * 2;
      tl[r][ch] = u32x2{ck[0][0], ck[0][1]}; tl[r][ch + 1] = u32x2{ck[0][2], ck[0][3]}; tl[r + 8][ch] = u32x2{ck[1][0], ck[1][1]}; tl[r + 8][ch + 1] = u32x2{ck[1][2], ck[1][3]}; }
    asm volatile("s_waitcnt lgkmcnt(0)" ::: "memory");
    { const int c = lane & 15, g = lane >> 4;
      const u32x2 f0 = tl[c][g], f1 = tl[c][4 + g], f2 = tl[c][8 + g], f3 = tl[c][12 + g];
      char* op = ws + WS_CF + ((size_t)(((rb >> 3) * 16 + ks) * 8 + (rb & 7)) * 64 + lane) * 16;
      st_wt(op, u32x4{f0[0], f0[1], f1[0], f1[1]}); st_wt(op + 8192, u32x4{f2[0], f2[1], f3[0], f3[1]}); }
    {
      const int l = j * 64 + lane;
      const u16* pk = A->wuk; const u16* pv = A->wuv; const u16* pq = A->wq; asm volatile("" : "+s"(pk), "+s"(pv), "+s"(pq));
      const u16* p = w == 3 ? pq + (size_t)(l >> 3) * 4096 + xcd * 512 + (l & 7) * 64
                            : ((l & 2) ? pv : pk) + (size_t)(w == 0 ? 256 + ((l >> 3) & 255) : (l >> 2)) * 4096 + xcd * 512 + (l & 1) * 256 + (w == 0 ? ((l >> 2) & 1) * 64 : (w + 1) * 64);
      asm volatile("global_load_dword v255, %0, off" :: "v"(p) : "v255", "memory");
      if (w == 3) asm volatile("global_load_dword v255, %0, off" :: "v"(p + 256 * 4096) : "v255", "memory");
      else asm volatile("global_load_dword v255, %0, off" :: "v"(p) : "v255", "memory");
    }
    __builtin_amdgcn_sched_barrier(0);
    float v[8];
#pragma unroll
    for (int e = 0; e < 8; ++e) v[e] = 0.f;
#pragma unroll
    for (int i = 0; i < 4; ++i) { const float c0 = bflo(cv[i]), s = rbf(c0 * __builtin_amdgcn_rcpf(1.f + __expf(-c0)));
#pragma unroll
      for (int e = 0; e < 4; ++e) { v[2 * e] = fmaf(s, bflo(wm[i][e]), v[2 * e]); v[2 * e + 1] = fmaf(s, bfhi(wm[i][e]), v[2 * e + 1]); } }
#pragma unroll
    for (int e = 0; e < 4; ++e) { auto q = __builtin_amdgcn_permlane32_swap(__float_as_uint(v[e]), __float_as_uint(v[e + 4]), false, false); v[e] = __uint_as_float(q[0]) + __uint_as_float(q[1]); }
#pragma unroll
    for (int e = 0; e < 2; ++e) { auto q = __builtin_amdgcn_permlane16_swap(__float_as_uint(v[e]), __float_as_uint(v[e + 2]), false, false); v[e] = __uint_as_float(q[0]) + __uint_as_float(q[1]); }
    v[0] += dpp<0x128>(v[0]); v[1] += dpp<0x128>(v[1]);
    LDSP float* part = (LDSP float*)(lds + 131072 + 4096);
    if ((lane & 8) == 0) *(LDSP f32x2*)&part[w * 64 + (lane & 7) * 8 + (lane >> 4) * 2] = f32x2{v[0], v[1]};
    asm volatile("s_waitcnt vmcnt(" WARMS ") lgkmcnt(0)\n\ts_barrier" ::: "memory");
    if (w == 0 && lane == 0) flag_set(ws, FL_T + xcd * 128 + j * 4, tag);
    if (w == 1 && b < 192) __hip_atomic_store((float*)(ws + WS_MP) + (cb * 64 + lane) * 4 + kq, (part[lane] + part[64 + lane]) + (part[128 + lane] + part[192 + lane]), __ATOMIC_RELAXED, __HIP_MEMORY_SCOPE_AGENT);
    float sm = 0.f, sq = 0.f;
#pragma unroll
    for (int i = 0; i < 4; ++i) { float s0 = 0.f, q0 = 0.f;
#pragma unroll
      for (int e = 0; e < 4; ++e) { s0 = dot2(xr[i][e], 0x3f803f80u, s0); q0 = dot2(xr[i][e], xr[i][e], q0); }
      s0 = wsum(s0); q0 = wsum(q0);
      if (lane == i) { sm = s0; sq = q0; } }
    const float mean = sm * (1.f / 512.f), rs = rsqrtf(fmaxf(sq * (1.f / 512.f) - mean * mean, 0.f) + 1e-5f);
    if (lane < 4) ((LDSP f32x2*)(lds + 131072 + 3072))[w * 4 + lane] = f32x2{rs, -mean * rs};
    STAMP(2);
#undef DIAGK
  }
  { int b = b0, w = w0;
#define DIAGK 0
    STAMP(0);
    unsigned bad;
    asm volatile(
    "s_load_dwordx8 s[40:47], %1, 0\n"
    "s_load_dwordx8 s[48:55], %1, 32\n"
    "s_load_dwordx2 s[60:61], %1, 64\n"
    "s_mov_b32 s20, %2\n"
    "s_mov_b32 s64, %3\n"
    "s_mov_b32 s94, %5\n"
    "v_mbcnt_lo_u32_b32 v16, -1, 0\n"
    "v_mbcnt_hi_u32_b32 v16, -1, v16\n"
    "s_bfe_u32 s22, s64, 0x30003\n"
    "s_and_b32 s87, s64, 7\n"
    "s_lshl_b32 s87, s87, 2\n"
    "s_lshr_b32 s30, s64, 6\n"
    "s_add_u32 s87, s87, s30\n"
    "s_and_b32 s65, s87, 0x1c\n"
    "s_and_b32 s31, s30, 1\n"
    "s_lshl_b32 s31, s31, 1\n"
    "s_add_u32 s65, s65, s31\n"
    "s_lshr_b32 s66, s64, 7\n"
    "s_mov_b32 s85, s22\n"
    "v_lshrrev_b32 v17, 3, v16\n"
    "s_lshl_b32 s30, s20, 5\n"
    "v_add_u32 v17, s30, v17\n"
    "s_mov_b32 s30, 8192\n"
    "v_mul_lo_u32 v17, v17, s30\n"
    "v_lshrrev_b32 v18, 4, v16\n"
    "v_lshlrev_b32 v18, 1, v18\n"
    "v_and_b32 v19, 7, v16\n"
    "v_xor_b32 v18, v18, v19\n"
    "v_lshl_add_u32 v4, v18, 4, v17\n"
    "v_add_u32 v5, 64512, v4\n"
    "v_add_u32 v6, 129024, v4\n"
    "v_add_u32 v7, 193536, v4\n"
    "s_lshl_b32 s28, s20, 12\n"
    "s_lshr_b32 s30, s20, 1\n"
    "s_lshl_b32 s30, s30, 17\n"
    "s_and_b32 s31, s20, 1\n"
    "s_lshl_b32 s31, s31, 12\n"
    "s_add_u32 s30, s30, s31\n"
    "v_lshlrev_b32 v17, 4, v16\n"
    "v_add_u32 v0, s30, v17\n"
    "v_add_u32 v1, 2048, v0\n"
    "v_mov_b32 v2, v17\n"
    "s_waitcnt lgkmcnt(0)\n"
    "s_lshl_b32 s30, s85, 18\n"
    "s_add_u32 s72, s60, 14680064\n"
    "s_addc_u32 s73, s61, 0\n"
    "s_add_u32 s72, s72, s30\n"
    "s_addc_u32 s73, s73, 0\n"
    "s_and_b32 s73, s73, 0xffff\n"
    "s_mov_b32 s74, 262144\n"
    "s_mov_b32 s75, 0x20000\n"
    "s_cmp_lg_u32 s66, 0\n"
    "s_cselect_b64 s[76:77], s[42:43], s[40:41]\n"
    "s_add_u32 s67, s66, 1\n"
    "s_lshl_b32 s67, s67, 24\n"
    "s_lshl_b32 s30, s65, 8\n"
    "s_add_u32 s76, s76, 0\n"
    "s_addc_u32 s77, s77, 0\n"
    "s_add_u32 s76, s76, s30\n"
    "s_addc_u32 s77, s77, 0\n"
    "s_and_b32 s77, s77, 0xffff\n"
    "s_mov_b32 s78, 4186624\n"
    "s_mov_b32 s79, 0x20000\n"
    "s_mov_b64 s[62:63], s[44:45]\n"
    "s_mov_b32 s29, 1048576\n"
    "s_add_u32 m0, s28, 32768\n"
    "s_nop 0\n"
    "buffer_load_dwordx4 v4, s[76:79], s29 offen offset:0 lds\n"
    "buffer_load_dwordx4 v5, s[76:79], s29 offen offset:1024 lds\n"
    "buffer_load_dwordx4 v6, s[76:79], s29 offen offset:2048 lds\n"
    "buffer_load_dwordx4 v7, s[76:79], s29 offen offset:3072 lds\n"
    "s_mov_b32 s29, 1048704\n"
    "s_add_u32 m0, s28, 49152\n"
    "s_nop 0\n"
    "buffer_load_dwordx4 v4, s[76:79], s29 offen offset:0 lds\n"
    "buffer_load_dwordx4 v5, s[76:79], s29 offen offset:1024 lds\n"
    "buffer_load_dwordx4 v6, s[76:79], s29 offen offset:2048 lds\n"
    "buffer_load_dwordx4 v7, s[76:79], s29 offen offset:3072 lds\n"
    "v_lshrrev_b32 v17, 2, v16\n"
    "v_lshlrev_b32 v17, 7, v17\n"
    "v_and_b32 v18, 3, v16\n"
    "v_lshl_add_u32 v17, v18, 3, v17\n"
    "v_bfe_u32 v18, v16, 3, 2\n"
    "v_lshlrev_b32 v18, 5, v18\n"
    "v_xor_b32 v19, 0, v18\n"
    "v_add_u32 v8, v17, v19\n"
    "v_add_u32 v12, 0x10000, v8\n"
    "v_xor_b32 v19, 32, v18\n"
    "v_add_u32 v9, v17, v19\n"
    "v_add_u32 v13, 0x10000, v9\n"
    "v_xor_b32 v19, 64, v18\n"
    "v_add_u32 v10, v17, v19\n"
    "v_add_u32 v14, 0x10000, v10\n"
    "v_xor_b32 v19, 96, v18\n"
    "v_add_u32 v11, v17, v19\n"
    "v_add_u32 v15, 0x10000, v11\n"
    "s_lshl_b32 s30, s65, 19\n"
    "s_add_u32 s30, s30, s67\n"
    "s_lshl_b32 s31, s85, 16\n"
    "s_add_u32 s30, s30, s31\n"
    "s_add_u32 s80, s60, 0\n"
    "s_addc_u32 s81, s61, 0\n"
    "s_add_u32 s80, s80, s30\n"
    "s_addc_u32 s81, s81, 0\n"
    "s_and_b32 s81, s81, 0xffff\n"
    "s_mov_b32 s82, 589824\n"
    "s_mov_b32 s83, 0x20000\n"
    "s_lshl_b32 s30, s20, 14\n"
    "v_add_u32 v21, s30, v2\n"
    "s_mov_b64 s[16:17], s[48:49]\n"
    "s_mov_b64 s[10:11], s[50:51]\n"
    "s_mov_b64 s[12:13], s[52:53]\n"
    "s_mov_b64 s[14:15], s[54:55]\n"
    "s_lshr_b32 s30, s64, 3\n"
    "s_lshl_b32 s30, s30, 2\n"
    "s_add_u32 s30, s30, s20\n"
    "s_lshl_b32 s30, s30, 15\n"
    "s_and_b32 s31, s64, 7\n"
    "s_lshl_b32 s31, s31, 10\n"
    "s_add_u32 s30, s30, s31\n"
    "s_add_u32 s92, s46, s30\n"
    "s_addc_u32 s93, s47, 0\n"
    "s_lshl_b32 s30, s87, 8\n"
    "s_add_u32 s44, s46, 0\n"
    "s_addc_u32 s45, s47, 0\n"
    "s_add_u32 s44, s44, s30\n"
    "s_addc_u32 s45, s45, 0\n"
    "s_and_b32 s45, s45, 0xffff\n"
    "s_mov_b32 s46, 4186368\n"
    "s_mov_b32 s47, 0x20000\n"
    "s_add_u32 s40, s60, 1048576\n"
    "s_addc_u32 s41, s61, 0\n"
    "s_and_b32 s41, s41, 0xffff\n"
    "s_mov_b32 s42, 2097152\n"
    "s_mov_b32 s43, 0x20000\n"
    "s_lshl_b32 s30, s87, 19\n"
    "s_add_u32 s48, s60, 16777216\n"
    "s_addc_u32 s49, s61, 0\n"
    "s_add_u32 s48, s48, s30\n"
    "s_addc_u32 s49, s49, 0\n"
    "s_and_b32 s49, s49, 0xffff\n"
    "s_mov_b32 s50, 0\n"
    "s_mov_b32 s51, 0x20000\n"
    "s_add_u32 s52, s60, 33554432\n"
    "s_addc_u32 s53, s61, 0\n"
    "s_add_u32 s52, s52, s30\n"
    "s_addc_u32 s53, s53, 0\n"
    "s_and_b32 s53, s53, 0xffff\n"
    "s_mov_b32 s54, 0\n"
    "s_mov_b32 s55, 0x20000\n"
    "s_lshl_b32 s30, s87, 12\n"
    "s_add_u32 s56, s60, 50331648\n"
    "s_addc_u32 s57, s61, 0\n"
    "s_add_u32 s56, s56, s30\n"
    "s_addc_u32 s57, s57, 0\n"
    "s_and_b32 s57, s57, 0xffff\n"
    "s_mov_b32 s58, 16650240\n"
    "s_mov_b32 s59, 0x20000\n"
    "s_sub_u32 s30, 16, s22\n"
    "s_lshl_b32 s50, s30, 15\n"
    "s_mov_b32 s54, s50\n"
    "v_lshrrev_b32 v18, 4, v16\n"
    "v_lshlrev_b32 v18, 13, v18\n"
    "v_bfe_u32 v19, v16, 1, 3\n"
    "v_lshl_add_u32 v3, v19, 7, v18\n"
    "s_and_b32 s30, s64, 7\n"
    "s_lshl_b32 s30, s30, 4\n"
    "s_lshr_b32 s31, s64, 4\n"
    "s_add_u32 s84, s30, s31\n"
    "s_bfe_u32 s33, s64, 0x10003\n"
    "s_lshl_b32 s30, s84, 14\n"
    "s_add_u32 s62, s62, s30\n"
    "s_addc_u32 s63, s63, 0\n"
    "s_lshl_b32 s30, s33, 2\n"
    "s_add_u32 s30, s30, s20\n"
    "s_lshl_b32 s36, s30, 1\n"
    "v_and_b32 v17, 15, v16\n"
    "v_lshrrev_b32 v18, 4, v16\n"
    "s_lshl_b32 s30, s36, 6\n"
    "v_lshlrev_b32 v22, 10, v17\n"
    "v_lshl_add_u32 v22, v18, 3, v22\n"
    "v_add_u32 v22, s30, v22\n"
    "s_lshl_b32 s30, s33, 4\n"
    "v_add_u32 v230, s30, v17\n"
    "v_lshl_add_u32 v230, v18, 5, v230\n"
    "s_lshr_b32 s30, s64, 3\n"
    "s_lshl_b32 s30, s30, 2\n"
    "s_add_u32 s30, s30, 64\n"
    "v_and_b32 v231, 3, v16\n"
    "v_add_u32 v231, s30, v231\n"
    "v_cmp_gt_u32 vcc, 32, v16\n"
    "s_nop 1\n"
    "v_cndmask_b32 v230, v231, v230, vcc\n"
    "v_lshlrev_b32 v230, 2, v230\n"
    "v_add_u32 v230, 475136, v230\n"
    "s_lshl_b32 s30, s33, 12\n"
    "s_lshl_b32 s31, s20, 10\n"
    "s_add_u32 s30, s30, s31\n"
    "s_add_u32 s30, s30, 393216\n"
    "v_add_u32 v23, s30, v2\n"
    "v_and_b32 v17, 7, v16\n"
    "s_lshl_b32 s30, s64, 7\n"
    "s_add_u32 s30, s30, 409600\n"
    "v_lshlrev_b32 v228, 4, v17\n"
    "v_add_u32 v228, s30, v228\n"
    "s_lshr_b32 s30, s84, 3\n"
    "s_lshl_b32 s34, s30, 7\n"
    "s_add_u32 s34, s34, 460800\n"
    "s_bfe_u32 s31, s64, 0x40003\n"
    "s_lshl_b32 s31, s31, 2\n"
    "s_add_u32 s34, s34, s31\n"
    "s_lshl_b32 s30, s30, 4\n"
    "s_add_u32 s30, s30, s36\n"
    "s_lshl_b32 s30, s30, 3\n"
    "s_and_b32 s31, s84, 7\n"
    "s_add_u32 s30, s30, s31\n"
    "s_lshl_b32 s30, s30, 10\n"
    "s_add_u32 s30, s30, 1048576\n"
    "s_add_u32 s68, s60, s30\n"
    "s_addc_u32 s69, s61, 0\n"
    "s_and_b32 s30, s64, 63\n"
    "s_lshl_b32 s30, s30, 3\n"
    "v_add_u32 v17, s30, v17\n"
    "s_lshr_b32 s30, s64, 6\n"
    "s_lshl_b32 s31, s30, 2\n"
    "s_lshr_b32 s38, 0x3120, s31\n"
    "s_and_b32 s38, s38, 3\n"
    "s_lshl_b32 s38, s38, 11\n"
    "s_add_u32 s38, s38, 32768\n"
    "s_cmp_eq_u32 s30, 2\n"
    "s_cselect_b32 s67, 1.0, 0\n"
    "v_lshlrev_b32 v229, 2, v17\n"
    "v_add_u32 v229, s38, v229\n"
    "s_lshl_b32 s70, s22, 7\n"
    "s_add_u32 s70, s70, 460800\n"
    "s_lshl_b32 s71, s22, 8\n"
    "s_sub_u32 s71, 1920, s71\n"
    "s_lshl_b32 s35, s65, 6\n"
    "s_add_u32 s35, s35, 462848\n"
    "s_lshl_b32 s30, s66, 3\n"
    "s_add_u32 s30, s30, s22\n"
    "s_lshl_b32 s30, s30, 2\n"
    "s_add_u32 s35, s35, s30\n"
    "s_lshr_b32 s85, s87, 1\n"
    "s_lshl_b32 s85, s85, 7\n"
    "s_add_u32 s85, s85, 462848\n"
    "s_lshl_b32 s99, s22, 7\n"
    "s_add_u32 s99, s99, 474112\n"
    "s_cmp_lg_u32 s20, 0\n"
    "s_cbranch_scc1 L_pt_%=_x\n"
    "v_min_u32 v18, 31, v16\n"
    "v_lshlrev_b32 v18, 2, v18\n"
    "v_add_u32 v18, s99, v18\n"
    "global_load_dword v19, v18, s[60:61] sc1\n"
    "s_waitcnt vmcnt(0)\n"
    "v_cmp_ne_u32 vcc, s94, v19\n"
    "s_mov_b32 s30, 0x400000\n"
    "s_nop 1\n"
    "s_cbranch_vccz L_pt_%=_x\n"
    "L_pt_%=:\n"
    "s_sleep 2\n"
    "global_load_dword v19, v18, s[60:61] sc1\n"
    "s_waitcnt vmcnt(0)\n"
    "v_cmp_ne_u32 vcc, s94, v19\n"
    "s_sub_u32 s30, s30, 1\n"
    "s_cbranch_scc1 L_pt_%=_x\n"
    "s_nop 0\n"
    "s_cbranch_vccnz L_pt_%=\n"
    "L_pt_%=_x:\n"
    "s_waitcnt vmcnt(0)\n"
    "s_barrier\n"
    "s_lshl_b32 s99, s64, 2\n"
    "s_add_u32 s99, s99, 475136\n"
    "s_cmp_lg_u32 s20, 0\n"
    "s_cbranch_scc1 L_s0_%=\n"
    "v_mov_b32 v18, s99\n"
    "v_mov_b32 v19, s94\n"
    "s_mov_b64 exec, 1\n"
    "global_atomic_swap v18, v19, s[60:61]\n"
    "s_mov_b64 exec, -1\n"
    "L_s0_%=:\n"
    "s_mov_b32 s88, 0\n"
    "buffer_load_dwordx4 a[0:3], v0, s[72:75], s88 offen offset:0 sc1\n"
    "buffer_load_dwordx4 a[64:67], v0, s[72:75], s88 offen offset:1024 sc1\n"
    "buffer_load_dwordx4 a[128:131], v1, s[72:75], s88 offen offset:0 sc1\n"
    "buffer_load_dwordx4 a[192:195], v1, s[72:75], s88 offen offset:1024 sc1\n"
    "s_mov_b32 s88, 8192\n"
    "buffer_load_dwordx4 a[4:7], v0, s[72:75], s88 offen offset:0 sc1\n"
    "buffer_load_dwordx4 a[68:71], v0, s[72:75], s88 offen offset:1024 sc1\n"
    "buffer_load_dwordx4 a[132:135], v1, s[72:75], s88 offen offset:0 sc1\n"
    "buffer_load_dwordx4 a[196:199], v1, s[72:75], s88 offen offset:1024 sc1\n"
    "s_cmp_lg_u32 s66, 0\n"
    "s_cbranch_scc1 L_k1_v_%=\n"
    "ds_read_b64_tr_b16 v[24:25], v8 offset:0\n"
    "ds_read_b64_tr_b16 v[26:27], v8 offset:2048\n"
    "ds_read_b64_tr_b16 v[28:29], v9 offset:0\n"
    "ds_read_b64_tr_b16 v[30:31], v9 offset:2048\n"
    "ds_read_b64_tr_b16 v[32:33], v10 offset:0\n"
    "ds_read_b64_tr_b16 v[34:35], v10 offset:2048\n"
    "ds_read_b64_tr_b16 v[36:37], v11 offset:0\n"
    "ds_read_b64_tr_b16 v[38:39], v11 offset:2048\n"
    "ds_read_b64_tr_b16 v[40:41], v8 offset:16384\n"
    "ds_read_b64_tr_b16 v[42:43], v8 offset:18432\n"
    "ds_read_b64_tr_b16 v[44:45], v9 offset:16384\n"
    "ds_read_b64_tr_b16 v[46:47], v9 offset:18432\n"
    "s_waitcnt vmcnt(4)\n"
    "s_waitcnt lgkmcnt(8)\n"
    "v_mfma_f32_16x16x32_bf16 v[56:59], v[24:27], a[0:3], 0\n"
    "ds_read_b64_tr_b16 v[48:49], v10 offset:16384\n"
    "s_mov_b32 s88, 16384\n"
    "v_mfma_f32_16x16x32_bf16 v[64:67], v[24:27], a[64:67], 0\n"
    "ds_read_b64_tr_b16 v[50:51], v10 offset:18432\n"
    "buffer_load_dwordx4 a[8:11], v0, s[72:75], s88 offen offset:0 sc1\n"
    "v_mfma_f32_16x16x32_bf16 v[72:75], v[24:27], a[128:131], 0\n"
    "v_mfma_f32_16x16x32_bf16 v[80:83], v[24:27], a[192:195], 0\n"
    "v_mfma_f32_16x16x32_bf16 v[60:63], v[28:31], a[0:3], 0\n"
    "ds_read_b64_tr_b16 v[52:53], v11 offset:16384\n"
    "v_mfma_f32_16x16x32_bf16 v[68:71], v[28:31], a[64:67], 0\n"
    "ds_read_b64_tr_b16 v[54:55], v11 offset:18432\n"
    "buffer_load_dwordx4 a[72:75], v0, s[72:75], s88 offen offset:1024 sc1\n"
    "v_mfma_f32_16x16x32_bf16 v[76:79], v[28:31], a[128:131], 0\n"
    "v_mfma_f32_16x16x32_bf16 v[84:87], v[28:31], a[192:195], 0\n"
    "s_waitcnt lgkmcnt(8)\n"
    "v_mfma_f32_16x16x32_bf16 v[88:91], v[32:35], a[0:3], 0\n"
    "ds_read_b64_tr_b16 v[24:25], v8 offset:4096\n"
    "v_mfma_f32_16x16x32_bf16 v[96:99], v[32:35], a[64:67], 0\n"
    "ds_read_b64_tr_b16 v[26:27], v8 offset:6144\n"
    "v_mfma_f32_16x16x32_bf16 v[104:107], v[32:35], a[128:131], 0\n"
    "buffer_load_dwordx4 a[136:139], v1, s[72:75], s88 offen offset:0 sc1\n"
    "v_mfma_f32_16x16x32_bf16 v[112:115], v[32:35], a[192:195], 0\n"
    "v_mfma_f32_16x16x32_bf16 v[92:95], v[36:39], a[0:3], 0\n"
    "ds_read_b64_tr_b16 v[28:29], v9 offset:4096\n"
    "v_mfma_f32_16x16x32_bf16 v[100:103], v[36:39], a[64:67], 0\n"
    "ds_read_b64_tr_b16 v[30:31], v9 offset:6144\n"
    "v_mfma_f32_16x16x32_bf16 v[108:111], v[36:39], a[128:131], 0\n"
    "buffer_load_dwordx4 a[200:203], v1, s[72:75], s88 offen offset:1024 sc1\n"
    "v_mfma_f32_16x16x32_bf16 v[116:119], v[36:39], a[192:195], 0\n"
    "s_mov_b32 s88, 24576\n"
    "s_waitcnt lgkmcnt(8)\n"
    "v_mfma_f32_16x16x32_bf16 v[120:123], v[40:43], a[0:3], 0\n"
    "ds_read_b64_tr_b16 v[32:33], v10 offset:4096\n"
    "v_mfma_f32_16x16x32_bf16 v[128:131], v[40:43], a[64:67], 0\n"
    "ds_read_b64_tr_b16 v[34:35], v10 offset:6144\n"
    "v_mfma_f32_16x16x32_bf16 v[136:139], v[40:43], a[128:131], 0\n"
    "v_mfma_f32_16x16x32_bf16 v[144:147], v[40:43], a[192:195], 0\n"
    "buffer_load_dwordx4 a[12:15], v0, s[72:75], s88 offen offset:0 sc1\n"
    "v_mfma_f32_16x16x32_bf16 v[124:127], v[44:47], a[0:3], 0\n"
    "ds_read_b64_tr_b16 v[36:37], v11 offset:4096\n"
    "v_mfma_f32_16x16x32_bf16 v[132:135], v[44:47], a[64:67], 0\n"
    "ds_read_b64_tr_b16 v[38:39], v11 offset:6144\n"
    "v_mfma_f32_16x16x32_bf16 v[140:143], v[44:47], a[128:131], 0\n"
    "v_mfma_f32_16x16x32_bf16 v[148:151], v[44:47], a[192:195], 0\n"
    "buffer_load_dwordx4 a[76:79], v0, s[72:75], s88 offen offset:1024 sc1\n"
    "s_waitcnt lgkmcnt(8)\n"
    "v_mfma_f32_16x16x32_bf16 v[152:155], v[48:51], a[0:3], 0\n"
    "ds_read_b64_tr_b16 v[40:41], v8 offset:20480\n"
    "v_mfma_f32_16x16x32_bf16 v[160:163], v[48:51], a[64:67], 0\n"
    "ds_read_b64_tr_b16 v[42:43], v8 offset:22528\n"
    "v_mfma_f32_16x16x32_bf16 v[168:171], v[48:51], a[128:131], 0\n"
    "v_mfma_f32_16x16x32_bf16 v[176:179], v[48:51], a[192:195], 0\n"
    "v_mfma_f32_16x16x32_bf16 v[156:159], v[52:55], a[0:3], 0\n"
    "ds_read_b64_tr_b16 v[44:45], v9 offset:20480\n"
    "buffer_load_dwordx4 a[140:143], v1, s[72:75], s88 offen offset:0 sc1\n"
    "v_mfma_f32_16x16x32_bf16 v[164:167], v[52:55], a[64:67], 0\n"
    "ds_read_b64_tr_b16 v[46:47], v9 offset:22528\n"
    "v_mfma_f32_16x16x32_bf16 v[172:175], v[52:55], a[128:131], 0\n"
    "v_mfma_f32_16x16x32_bf16 v[180:183], v[52:55], a[192:195], 0\n"
    "s_waitcnt vmcnt(7)\n"
    "s_waitcnt lgkmcnt(8)\n"
    "v_mfma_f32_16x16x32_bf16 v[56:59], v[24:27], a[4:7], v[56:59]\n"
    "ds_read_b64_tr_b16 v[48:49], v10 offset:20480\n"
    "buffer_load_dwordx4 a[204:207], v1, s[72:75], s88 offen offset:1024 sc1\n"
    "v_mfma_f32_16x16x32_bf16 v[64:67], v[24:27], a[68:71], v[64:67]\n"
    "ds_read_b64_tr_b16 v[50:51], v10 offset:22528\n"
    "s_mov_b32 s88, 32768\n"
    "v_mfma_f32_16x16x32_bf16 v[72:75], v[24:27], a[132:135], v[72:75]\n"
    "v_mfma_f32_16x16x32_bf16 v[80:83], v[24:27], a[196:199], v[80:83]\n"
    "v_mfma_f32_16x16x32_bf16 v[60:63], v[28:31], a[4:7], v[60:63]\n"
    "ds_read_b64_tr_b16 v[52:53], v11 offset:20480\n"
    "v_mfma_f32_16x16x32_bf16 v[68:71], v[28:31], a[68:71], v[68:71]\n"
    "ds_read_b64_tr_b16 v[54:55], v11 offset:22528\n"
    "buffer_load_dwordx4 a[16:19], v0, s[72:75], s88 offen offset:0 sc1\n"
    "v_mfma_f32_16x16x32_bf16 v[76:79], v[28:31], a[132:135], v[76:79]\n"
    "v_mfma_f32_16x16x32_bf16 v[84:87], v[28:31], a[196:199], v[84:87]\n"
    "s_waitcnt lgkmcnt(8)\n"
    "v_mfma_f32_16x16x32_bf16 v[88:91], v[32:35], a[4:7], v[88:91]\n"
    "ds_read_b64_tr_b16 v[24:25], v8 offset:8192\n"
    "v_mfma_f32_16x16x32_bf16 v[96:99], v[32:35], a[68:71], v[96:99]\n"
    "ds_read_b64_tr_b16 v[26:27], v8 offset:10240\n"
    "buffer_load_dwordx4 a[80:83], v0, s[72:75], s88 offen offset:1024 sc1\n"
    "v_mfma_f32_16x16x32_bf16 v[104:107], v[32:35], a[132:135], v[104:107]\n"
    "v_mfma_f32_16x16x32_bf16 v[112:115], v[32:35], a[196:199], v[112:115]\n"
    "v_mfma_f32_16x16x32_bf16 v[92:95], v[36:39], a[4:7], v[92:95]\n"
    "ds_read_b64_tr_b16 v[28:29], v9 offset:8192\n"
    "v_mfma_f32_16x16x32_bf16 v[100:103], v[36:39], a[68:71], v[100:103]\n"
    "ds_read_b64_tr_b16 v[30:31], v9 offset:10240\n"
    "v_mfma_f32_16x16x32_bf16 v[108:111], v[36:39], a[132:135], v[108:111]\n"
    "buffer_load_dwordx4 a[144:147], v1, s[72:75], s88 offen offset:0 sc1\n"
    "v_mfma_f32_16x16x32_bf16 v[116:119], v[36:39], a[196:199], v[116:119]\n"
    "s_waitcnt lgkmcnt(8)\n"
    "v_mfma_f32_16x16x32_bf16 v[120:123], v[40:43], a[4:7], v[120:123]\n"
    "ds_read_b64_tr_b16 v[32:33], v10 offset:8192\n"
    "v_mfma_f32_16x16x32_bf16 v[128:131], v[40:43], a[68:71], v[128:131]\n"
    "ds_read_b64_tr_b16 v[34:35], v10 offset:10240\n"
    "v_mfma_f32_16x16x32_bf16 v[136:139], v[40:43], a[132:135], v[136:139]\n"
    "buffer_load_dwordx4 a[208:211], v1, s[72:75], s88 offen offset:1024 sc1\n"
    "v_mfma_f32_16x16x32_bf16 v[144:147], v[40:43], a[196:199], v[144:147]\n"
    "s_mov_b32 s88, 40960\n"
    "v_mfma_f32_16x16x32_bf16 v[124:127], v[44:47], a[4:7], v[124:127]\n"
    "ds_read_b64_tr_b16 v[36:37], v11 offset:8192\n"
    "v_mfma_f32_16x16x32_bf16 v[132:135], v[44:47], a[68:71], v[132:135]\n"
    "ds_read_b64_tr_b16 v[38:39], v11 offset:10240\n"
    "v_mfma_f32_16x16x32_bf16 v[140:143], v[44:47], a[132:135], v[140:143]\n"
    "v_mfma_f32_16x16x32_bf16 v[148:151], v[44:47], a[196:199], v[148:151]\n"
    "buffer_load_dwordx4 a[20:23], v0, s[72:75], s88 offen offset:0 sc1\n"
    "s_waitcnt lgkmcnt(8)\n"
    "v_mfma_f32_16x16x32_bf16 v[152:155], v[48:51], a[4:7], v[152:155]\n"
    "ds_read_b64_tr_b16 v[40:41], v8 offset:24576\n"
    "v_mfma_f32_16x16x32_bf16 v[160:163], v[48:51], a[68:71], v[160:163]\n"
    "ds_read_b64_tr_b16 v[42:43], v8 offset:26624\n"
    "v_mfma_f32_16x16x32_bf16 v[168:171], v[48:51], a[132:135], v[168:171]\n"
    "v_mfma_f32_16x16x32_bf16 v[176:179], v[48:51], a[196:199], v[176:179]\n"
    "buffer_load_dwordx4 a[84:87], v0, s[72:75], s88 offen offset:1024 sc1\n"
    "v_mfma_f32_16x16x32_bf16 v[156:159], v[52:55], a[4:7], v[156:159]\n"
    "ds_read_b64_tr_b16 v[44:45], v9 offset:24576\n"
    "v_mfma_f32_16x16x32_bf16 v[164:167], v[52:55], a[68:71], v[164:167]\n"
    "ds_read_b64_tr_b16 v[46:47], v9 offset:26624\n"
    "v_mfma_f32_16x16x32_bf16 v[172:175], v[52:55], a[132:135], v[172:175]\n"
    "v_mfma_f32_16x16x32_bf16 v[180:183], v[52:55], a[196:199], v[180:183]\n"
    "s_waitcnt vmcnt(10)\n"
    "s_waitcnt lgkmcnt(8)\n"
    "v_mfma_f32_16x16x32_bf16 v[56:59], v[24:27], a[8:11], v[56:59]\n"
    "ds_read_b64_tr_b16 v[48:49], v10 offset:24576\n"
    "buffer_load_dwordx4 a[148:151], v1, s[72:75], s88 offen offset:0 sc1\n"
    "v_mfma_f32_16x16x32_bf16 v[64:67], v[24:27], a[72:75], v[64:67]\n"
    "ds_read_b64_tr_b16 v[50:51], v10 offset:26624\n"
    "v_mfma_f32_16x16x32_bf16 v[72:75], v[24:27], a[136:139], v[72:75]\n"
    "v_mfma_f32_16x16x32_bf16 v[80:83], v[24:27], a[200:203], v[80:83]\n"
    "v_mfma_f32_16x16x32_bf16 v[60:63], v[28:31], a[8:11], v[60:63]\n"
    "ds_read_b64_tr_b16 v[52:53], v11 offset:24576\n"
    "buffer_load_dwordx4 a[212:215], v1, s[72:75], s88 offen offset:1024 sc1\n"
    "v_mfma_f32_16x16x32_bf16 v[68:71], v[28:31], a[72:75], v[68:71]\n"
    "ds_read_b64_tr_b16 v[54:55], v11 offset:26624\n"
    "s_mov_b32 s88, 49152\n"
    "v_mfma_f32_16x16x32_bf16 v[76:79], v[28:31], a[136:139], v[76:79]\n"
    "v_mfma_f32_16x16x32_bf16 v[84:87], v[28:31], a[200:203], v[84:87]\n"
    "s_waitcnt lgkmcnt(8)\n"
    "v_mfma_f32_16x16x32_bf16 v[88:91], v[32:35], a[8:11], v[88:91]\n"
    "ds_read_b64_tr_b16 v[24:25], v8 offset:12288\n"
    "v_mfma_f32_16x16x32_bf16 v[96:99], v[32:35], a[72:75], v[96:99]\n"
    "ds_read_b64_tr_b16 v[26:27], v8 offset:14336\n"
    "buffer_load_dwordx4 a[24:27], v0, s[72:75], s88 offen offset:0 sc1\n"
    "v_mfma_f32_16x16x32_bf16 v[104:107], v[32:35], a[136:139], v[104:107]\n"
    "v_mfma_f32_16x16x32_bf16 v[112:115], v[32:35], a[200:203], v[112:115]\n"
    "v_mfma_f32_16x16x32_bf16 v[92:95], v[36:39], a[8:11], v[92:95]\n"
    "ds_read_b64_tr_b16 v[28:29], v9 offset:12288\n"
    "v_mfma_f32_16x16x32_bf16 v[100:103], v[36:39], a[72:75], v[100:103]\n"
    "ds_read_b64_tr_b16 v[30:31], v9 offset:14336\n"
    "buffer_load_dwordx4 a[88:91], v0, s[72:75], s88 offen offset:1024 sc1\n"
    "v_mfma_f32_16x16x32_bf16 v[108:111], v[36:39], a[136:139], v[108:111]\n"
    "v_mfma_f32_16x16x32_bf16 v[116:119], v[36:39], a[200:203], v[116:119]\n"
    "s_waitcnt lgkmcnt(8)\n"
    "v_mfma_f32_16x16x32_bf16 v[120:123], v[40:43], a[8:11], v[120:123]\n"
    "ds_read_b64_tr_b16 v[32:33], v10 offset:12288\n"
    "v_mfma_f32_16x16x32_bf16 v[128:131], v[40:43], a[72:75], v[128:131]\n"
    "ds_read_b64_tr_b16 v[34:35], v10 offset:14336\n"
    "v_mfma_f32_16x16x32_bf16 v[136:139], v[40:43], a[136:139], v[136:139]\n"
    "buffer_load_dwordx4 a[152:155], v1, s[72:75], s88 offen offset:0 sc1\n"
    "v_mfma_f32_16x16x32_bf16 v[144:147], v[40:43], a[200:203], v[144:147]\n"
    "v_mfma_f32_16x16x32_bf16 v[124:127], v[44:47], a[8:11], v[124:127]\n"
    "ds_read_b64_tr_b16 v[36:37], v11 offset:12288\n"
    "v_mfma_f32_16x16x32_bf16 v[132:135], v[44:47], a[72:75], v[132:135]\n"
    "ds_read_b64_tr_b16 v[38:39], v11 offset:14336\n"
    "v_mfma_f32_16x16x32_bf16 v[140:143], v[44:47], a[136:139], v[140:143]\n"
    "buffer_load_dwordx4 a[216:219], v1, s[72:75], s88 offen offset:1024 sc1\n"
    "v_mfma_f32_16x16x32_bf16 v[148:151], v[44:47], a[200:203], v[148:151]\n"
    "s_mov_b32 s29, 2097152\n"
    "s_waitcnt lgkmcnt(8)\n"
    "v_mfma_f32_16x16x32_bf16 v[152:155], v[48:51], a[8:11], v[152:155]\n"
    "ds_read_b64_tr_b16 v[40:41], v8 offset:28672\n"
    "s_add_u32 m0, s28, 65536\n"
    "v_mfma_f32_16x16x32_bf16 v[160:163], v[48:51], a[72:75], v[160:163]\n"
    "ds_read_b64_tr_b16 v[42:43], v8 offset:30720\n"
    "v_mfma_f32_16x16x32_bf16 v[168:171], v[48:51], a[136:139], v[168:171]\n"
    "v_mfma_f32_16x16x32_bf16 v[176:179], v[48:51], a[200:203], v[176:179]\n"
    "buffer_load_dwordx4 v4, s[76:79], s29 offen offset:0 lds\n"
    "v_mfma_f32_16x16x32_bf16 v[156:159], v[52:55], a[8:11], v[156:159]\n"
    "ds_read_b64_tr_b16 v[44:45], v9 offset:28672\n"
    "v_mfma_f32_16x16x32_bf16 v[164:167], v[52:55], a[72:75], v[164:167]\n"
    "ds_read_b64_tr_b16 v[46:47], v9 offset:30720\n"
    "v_mfma_f32_16x16x32_bf16 v[172:175], v[52:55], a[136:139], v[172:175]\n"
    "v_mfma_f32_16x16x32_bf16 v[180:183], v[52:55], a[200:203], v[180:183]\n"
    "buffer_load_dwordx4 v5, s[76:79], s29 offen offset:1024 lds\n"
    "s_waitcnt vmcnt(14)\n"
    "s_waitcnt lgkmcnt(8)\n"
    "v_mfma_f32_16x16x32_bf16 v[56:59], v[24:27], a[12:15], v[56:59]\n"
    "ds_read_b64_tr_b16 v[48:49], v10 offset:28672\n"
    "v_mfma_f32_16x16x32_bf16 v[64:67], v[24:27], a[76:79], v[64:67]\n"
    "ds_read_b64_tr_b16 v[50:51], v10 offset:30720\n"
    "v_mfma_f32_16x16x32_bf16 v[72:75], v[24:27], a[140:143], v[72:75]\n"
    "v_mfma_f32_16x16x32_bf16 v[80:83], v[24:27], a[204:207], v[80:83]\n"
    "v_mfma_f32_16x16x32_bf16 v[60:63], v[28:31], a[12:15], v[60:63]\n"
    "ds_read_b64_tr_b16 v[52:53], v11 offset:28672\n"
    "buffer_load_dwordx4 v6, s[76:79], s29 offen offset:2048 lds\n"
    "v_mfma_f32_16x16x32_bf16 v[68:71], v[28:31], a[76:79], v[68:71]\n"
    "ds_read_b64_tr_b16 v[54:55], v11 offset:30720\n"
    "v_mfma_f32_16x16x32_bf16 v[76:79], v[28:31], a[140:143], v[76:79]\n"
    "v_mfma_f32_16x16x32_bf16 v[84:87], v[28:31], a[204:207], v[84:87]\n"
    "s_waitcnt lgkmcnt(8)\n"
    "v_mfma_f32_16x16x32_bf16 v[88:91], v[32:35], a[12:15], v[88:91]\n"
    "buffer_load_dwordx4 v7, s[76:79], s29 offen offset:3072 lds\n"
    "v_mfma_f32_16x16x32_bf16 v[96:99], v[32:35], a[76:79], v[96:99]\n"
    "s_mov_b32 s29, 2097280\n"
    "v_mfma_f32_16x16x32_bf16 v[104:107], v[32:35], a[140:143], v[104:107]\n"
    "s_add_u32 m0, s28, 81920\n"
    "v_mfma_f32_16x16x32_bf16 v[112:115], v[32:35], a[204:207], v[112:115]\n"
    "v_mfma_f32_16x16x32_bf16 v[92:95], v[36:39], a[12:15], v[92:95]\n"
    "v_mfma_f32_16x16x32_bf16 v[100:103], v[36:39], a[76:79], v[100:103]\n"
    "buffer_load_dwordx4 v4, s[76:79], s29 offen offset:0 lds\n"
    "v_mfma_f32_16x16x32_bf16 v[108:111], v[36:39], a[140:143], v[108:111]\n"
    "v_mfma_f32_16x16x32_bf16 v[116:119], v[36:39], a[204:207], v[116:119]\n"
    "s_waitcnt vmcnt(17) lgkmcnt(0)\n"
    "s_barrier\n"
    "v_mfma_f32_16x16x32_bf16 v[120:123], v[40:43], a[12:15], v[120:123]\n"
    "ds_read_b64_tr_b16 v[24:25], v8 offset:32768\n"
    "v_mfma_f32_16x16x32_bf16 v[128:131], v[40:43], a[76:79], v[128:131]\n"
    "ds_read_b64_tr_b16 v[26:27], v8 offset:34816\n"
    "buffer_load_dwordx4 v5, s[76:79], s29 offen offset:1024 lds\n"
    "v_mfma_f32_16x16x32_bf16 v[136:139], v[40:43], a[140:143], v[136:139]\n"
    "ds_read_b64_tr_b16 v[28:29], v9 offset:32768\n"
    "v_mfma_f32_16x16x32_bf16 v[144:147], v[40:43], a[204:207], v[144:147]\n"
    "ds_read_b64_tr_b16 v[30:31], v9 offset:34816\n"
    "v_mfma_f32_16x16x32_bf16 v[124:127], v[44:47], a[12:15], v[124:127]\n"
    "ds_read_b64_tr_b16 v[32:33], v10 offset:32768\n"
    "v_mfma_f32_16x16x32_bf16 v[132:135], v[44:47], a[76:79], v[132:135]\n"
    "ds_read_b64_tr_b16 v[34:35], v10 offset:34816\n"
    "v_mfma_f32_16x16x32_bf16 v[140:143], v[44:47], a[140:143], v[140:143]\n"
    "ds_read_b64_tr_b16 v[36:37], v11 offset:32768\n"
    "buffer_load_dwordx4 v6, s[76:79], s29 offen offset:2048 lds\n"
    "v_mfma_f32_16x16x32_bf16 v[148:151], v[44:47], a[204:207], v[148:151]\n"
    "ds_read_b64_tr_b16 v[38:39], v11 offset:34816\n"
    "v_mfma_f32_16x16x32_bf16 v[152:155], v[48:51], a[12:15], v[152:155]\n"
    "ds_read_b64_tr_b16 v[40:41], v8 offset:49152\n"
    "v_mfma_f32_16x16x32_bf16 v[160:163], v[48:51], a[76:79], v[160:163]\n"
    "ds_read_b64_tr_b16 v[42:43], v8 offset:51200\n"
    "v_mfma_f32_16x16x32_bf16 v[168:171], v[48:51], a[140:143], v[168:171]\n"
    "buffer_load_dwordx4 v7, s[76:79], s29 offen offset:3072 lds\n"
    "v_mfma_f32_16x16x32_bf16 v[176:179], v[48:51], a[204:207], v[176:179]\n"
    "s_mov_b32 s88, 57344\n"
    "v_mfma_f32_16x16x32_bf16 v[156:159], v[52:55], a[12:15], v[156:159]\n"
    "ds_read_b64_tr_b16 v[44:45], v9 offset:49152\n"
    "v_mfma_f32_16x16x32_bf16 v[164:167], v[52:55], a[76:79], v[164:167]\n"
    "ds_read_b64_tr_b16 v[46:47], v9 offset:51200\n"
    "v_mfma_f32_16x16x32_bf16 v[172:175], v[52:55], a[140:143], v[172:175]\n"
    "v_mfma_f32_16x16x32_bf16 v[180:183], v[52:55], a[204:207], v[180:183]\n"
    "buffer_load_dwordx4 a[28:31], v0, s[72:75], s88 offen offset:0 sc1\n"
    "s_waitcnt vmcnt(17)\n"
    "s_waitcnt lgkmcnt(8)\n"
    "v_mfma_f32_16x16x32_bf16 v[56:59], v[24:27], a[16:19], v[56:59]\n"
    "ds_read_b64_tr_b16 v[48:49], v10 offset:49152\n"
    "v_mfma_f32_16x16x32_bf16 v[64:67], v[24:27], a[80:83], v[64:67]\n"
    "ds_read_b64_tr_b16 v[50:51], v10 offset:51200\n"
    "v_mfma_f32_16x16x32_bf16 v[72:75], v[24:27], a[144:147], v[72:75]\n"
    "v_mfma_f32_16x16x32_bf16 v[80:83], v[24:27], a[208:211], v[80:83]\n"
    "buffer_load_dwordx4 a[92:95], v0, s[72:75], s88 offen offset:1024 sc1\n"
    "v_mfma_f32_16x16x32_bf16 v[60:63], v[28:31], a[16:19], v[60:63]\n"
    "ds_read_b64_tr_b16 v[52:53], v11 offset:49152\n"
    "v_mfma_f32_16x16x32_bf16 v[68:71], v[28:31], a[80:83], v[68:71]\n"
    "ds_read_b64_tr_b16 v[54:55], v11 offset:51200\n"
    "v_mfma_f32_16x16x32_bf16 v[76:79], v[28:31], a[144:147], v[76:79]\n"
    "v_mfma_f32_16x16x32_bf16 v[84:87], v[28:31], a[208:211], v[84:87]\n"
    "s_waitcnt lgkmcnt(8)\n"
    "v_mfma_f32_16x16x32_bf16 v[88:91], v[32:35], a[16:19], v[88:91]\n"
    "ds_read_b64_tr_b16 v[24:25], v8 offset:36864\n"
    "buffer_load_dwordx4 a[156:159], v1, s[72:75], s88 offen offset:0 sc1\n"
    "v_mfma_f32_16x16x32_bf16 v[96:99], v[32:35], a[80:83], v[96:99]\n"
    "ds_read_b64_tr_b16 v[26:27], v8 offset:38912\n"
    "v_mfma_f32_16x16x32_bf16 v[104:107], v[32:35], a[144:147], v[104:107]\n"
    "v_mfma_f32_16x16x32_bf16 v[112:115], v[32:35], a[208:211], v[112:115]\n"
    "v_mfma_f32_16x16x32_bf16 v[92:95], v[36:39], a[16:19], v[92:95]\n"
    "ds_read_b64_tr_b16 v[28:29], v9 offset:36864\n"
    "buffer_load_dwordx4 a[220:223], v1, s[72:75], s88 offen offset:1024 sc1\n"
    "v_mfma_f32_16x16x32_bf16 v[100:103], v[36:39], a[80:83], v[100:103]\n"
    "ds_read_b64_tr_b16 v[30:31], v9 offset:38912\n"
    "v_mfma_f32_16x16x32_bf16 v[108:111], v[36:39], a[144:147], v[108:111]\n"
    "v_mfma_f32_16x16x32_bf16 v[116:119], v[36:39], a[208:211], v[116:119]\n"
    "s_waitcnt lgkmcnt(8)\n"
    "v_mfma_f32_16x16x32_bf16 v[120:123], v[40:43], a[16:19], v[120:123]\n"
    "ds_read_b64_tr_b16 v[32:33], v10 offset:36864\n"
    "v_mfma_f32_16x16x32_bf16 v[128:131], v[40:43], a[80:83], v[128:131]\n"
    "ds_read_b64_tr_b16 v[34:35], v10 offset:38912\n"
    "global_load_dword v231, v230, s[60:61] sc1\n"
    "v_mfma_f32_16x16x32_bf16 v[136:139], v[40:43], a[144:147], v[136:139]\n"
    "s_mov_b32 s88, 65536\n"
    "v_mfma_f32_16x16x32_bf16 v[144:147], v[40:43], a[208:211], v[144:147]\n"
    "v_mfma_f32_16x16x32_bf16 v[124:127], v[44:47], a[16:19], v[124:127]\n"
    "ds_read_b64_tr_b16 v[36:37], v11 offset:36864\n"
    "v_mfma_f32_16x16x32_bf16 v[132:135], v[44:47], a[80:83], v[132:135]\n"
    "ds_read_b64_tr_b16 v[38:39], v11 offset:38912\n"
    "buffer_load_dwordx4 a[32:35], v0, s[72:75], s88 offen offset:0 sc1\n"
    "v_mfma_f32_16x16x32_bf16 v[140:143], v[44:47], a[144:147], v[140:143]\n"
    "v_mfma_f32_16x16x32_bf16 v[148:151], v[44:47], a[208:211], v[148:151]\n"
    "s_waitcnt lgkmcnt(8)\n"
    "v_mfma_f32_16x16x32_bf16 v[152:155], v[48:51], a[16:19], v[152:155]\n"
    "ds_read_b64_tr_b16 v[40:41], v8 offset:53248\n"
    "v_mfma_f32_16x16x32_bf16 v[160:163], v[48:51], a[80:83], v[160:163]\n"
    "ds_read_b64_tr_b16 v[42:43], v8 offset:55296\n"
    "v_mfma_f32_16x16x32_bf16 v[168:171], v[48:51], a[144:147], v[168:171]\n"
    "buffer_load_dwordx4 a[96:99], v0, s[72:75], s88 offen offset:1024 sc1\n"
    "v_mfma_f32_16x16x32_bf16 v[176:179], v[48:51], a[208:211], v[176:179]\n"
    "v_mfma_f32_16x16x32_bf16 v[156:159], v[52:55], a[16:19], v[156:159]\n"
    "ds_read_b64_tr_b16 v[44:45], v9 offset:53248\n"
    "v_mfma_f32_16x16x32_bf16 v[164:167], v[52:55], a[80:83], v[164:167]\n"
    "ds_read_b64_tr_b16 v[46:47], v9 offset:55296\n"
    "v_mfma_f32_16x16x32_bf16 v[172:175], v[52:55], a[144:147], v[172:175]\n"
    "buffer_load_dwordx4 a[160:163], v1, s[72:75], s88 offen offset:0 sc1\n"
    "v_mfma_f32_16x16x32_bf16 v[180:183], v[52:55], a[208:211], v[180:183]\n"
    "s_waitcnt vmcnt(20)\n"
    "s_waitcnt lgkmcnt(8)\n"
    "v_mfma_f32_16x16x32_bf16 v[56:59], v[24:27], a[20:23], v[56:59]\n"
    "ds_read_b64_tr_b16 v[48:49], v10 offset:53248\n"
    "v_mfma_f32_16x16x32_bf16 v[64:67], v[24:27], a[84:87], v[64:67]\n"
    "ds_read_b64_tr_b16 v[50:51], v10 offset:55296\n"
    "v_mfma_f32_16x16x32_bf16 v[72:75], v[24:27], a[148:151], v[72:75]\n"
    "v_mfma_f32_16x16x32_bf16 v[80:83], v[24:27], a[212:215], v[80:83]\n"
    "buffer_load_dwordx4 a[224:227], v1, s[72:75], s88 offen offset:1024 sc1\n"
    "v_mfma_f32_16x16x32_bf16 v[60:63], v[28:31], a[20:23], v[60:63]\n"
    "ds_read_b64_tr_b16 v[52:53], v11 offset:53248\n"
    "s_mov_b32 s88, 73728\n"
    "v_mfma_f32_16x16x32_bf16 v[68:71], v[28:31], a[84:87], v[68:71]\n"
    "ds_read_b64_tr_b16 v[54:55], v11 offset:55296\n"
    "v_mfma_f32_16x16x32_bf16 v[76:79], v[28:31], a[148:151], v[76:79]\n"
    "v_mfma_f32_16x16x32_bf16 v[84:87], v[28:31], a[212:215], v[84:87]\n"
    "buffer_load_dwordx4 a[36:39], v0, s[72:75], s88 offen offset:0 sc1\n"
    "s_waitcnt lgkmcnt(8)\n"
    "v_mfma_f32_16x16x32_bf16 v[88:91], v[32:35], a[20:23], v[88:91]\n"
    "ds_read_b64_tr_b16 v[24:25], v8 offset:40960\n"
    "v_mfma_f32_16x16x32_bf16 v[96:99], v[32:35], a[84:87], v[96:99]\n"
    "ds_read_b64_tr_b16 v[26:27], v8 offset:43008\n"
    "v_mfma_f32_16x16x32_bf16 v[104:107], v[32:35], a[148:151], v[104:107]\n"
    "v_mfma_f32_16x16x32_bf16 v[112:115], v[32:35], a[212:215], v[112:115]\n"
    "v_mfma_f32_16x16x32_bf16 v[92:95], v[36:39], a[20:23], v[92:95]\n"
    "ds_read_b64_tr_b16 v[28:29], v9 offset:40960\n"
    "buffer_load_dwordx4 a[100:103], v0, s[72:75], s88 offen offset:1024 sc1\n"
    "v_mfma_f32_16x16x32_bf16 v[100:103], v[36:39], a[84:87], v[100:103]\n"
    "ds_read_b64_tr_b16 v[30:31], v9 offset:43008\n"
    "v_mfma_f32_16x16x32_bf16 v[108:111], v[36:39], a[148:151], v[108:111]\n"
    "v_mfma_f32_16x16x32_bf16 v[116:119], v[36:39], a[212:215], v[116:119]\n"
    "s_waitcnt lgkmcnt(8)\n"
    "v_mfma_f32_16x16x32_bf16 v[120:123], v[40:43], a[20:23], v[120:123]\n"
    "ds_read_b64_tr_b16 v[32:33], v10 offset:40960\n"
    "buffer_load_dwordx4 a[164:167], v1, s[72:75], s88 offen offset:0 sc1\n"
    "v_mfma_f32_16x16x32_bf16 v[128:131], v[40:43], a[84:87], v[128:131]\n"
    "ds_read_b64_tr_b16 v[34:35], v10 offset:43008\n"
    "v_mfma_f32_16x16x32_bf16 v[136:139], v[40:43], a[148:151], v[136:139]\n"
    "v_mfma_f32_16x16x32_bf16 v[144:147], v[40:43], a[212:215], v[144:147]\n"
    "v_mfma_f32_16x16x32_bf16 v[124:127], v[44:47], a[20:23], v[124:127]\n"
    "ds_read_b64_tr_b16 v[36:37], v11 offset:40960\n"
    "v_mfma_f32_16x16x32_bf16 v[132:135], v[44:47], a[84:87], v[132:135]\n"
    "ds_read_b64_tr_b16 v[38:39], v11 offset:43008\n"
    "buffer_load_dwordx4 a[228:231], v1, s[72:75], s88 offen offset:1024 sc1\n"
    "v_mfma_f32_16x16x32_bf16 v[140:143], v[44:47], a[148:151], v[140:143]\n"
    "s_mov_b32 s88, 81920\n"
    "v_mfma_f32_16x16x32_bf16 v[148:151], v[44:47], a[212:215], v[148:151]\n"
    "s_waitcnt lgkmcnt(8)\n"
    "v_mfma_f32_16x16x32_bf16 v[152:155], v[48:51], a[20:23], v[152:155]\n"
    "ds_read_b64_tr_b16 v[40:41], v8 offset:57344\n"
    "v_mfma_f32_16x16x32_bf16 v[160:163], v[48:51], a[84:87], v[160:163]\n"
    "ds_read_b64_tr_b16 v[42:43], v8 offset:59392\n"
    "buffer_load_dwordx4 a[40:43], v0, s[72:75], s88 offen offset:0 sc1\n"
    "v_mfma_f32_16x16x32_bf16 v[168:171], v[48:51], a[148:151], v[168:171]\n"
    "v_mfma_f32_16x16x32_bf16 v[176:179], v[48:51], a[212:215], v[176:179]\n"
    "v_mfma_f32_16x16x32_bf16 v[156:159], v[52:55], a[20:23], v[156:159]\n"
    "ds_read_b64_tr_b16 v[44:45], v9 offset:57344\n"
    "v_mfma_f32_16x16x32_bf16 v[164:167], v[52:55], a[84:87], v[164:167]\n"
    "ds_read_b64_tr_b16 v[46:47], v9 offset:59392\n"
    "v_mfma_f32_16x16x32_bf16 v[172:175], v[52:55], a[148:151], v[172:175]\n"
    "buffer_load_dwordx4 a[104:107], v0, s[72:75], s88 offen offset:1024 sc1\n"
    "v_mfma_f32_16x16x32_bf16 v[180:183], v[52:55], a[212:215], v[180:183]\n"
    "s_waitcnt vmcnt(23)\n"
    "s_waitcnt lgkmcnt(8)\n"
    "v_mfma_f32_16x16x32_bf16 v[56:59], v[24:27], a[24:27], v[56:59]\n"
    "ds_read_b64_tr_b16 v[48:49], v10 offset:57344\n"
    "v_mfma_f32_16x16x32_bf16 v[64:67], v[24:27], a[88:91], v[64:67]\n"
    "ds_read_b64_tr_b16 v[50:51], v10 offset:59392\n"
    "v_mfma_f32_16x16x32_bf16 v[72:75], v[24:27], a[152:155], v[72:75]\n"
    "buffer_load_dwordx4 a[168:171], v1, s[72:75], s88 offen offset:0 sc1\n"
    "v_mfma_f32_16x16x32_bf16 v[80:83], v[24:27], a[216:219], v[80:83]\n"
    "v_mfma_f32_16x16x32_bf16 v[60:63], v[28:31], a[24:27], v[60:63]\n"
    "ds_read_b64_tr_b16 v[52:53], v11 offset:57344\n"
    "v_mfma_f32_16x16x32_bf16 v[68:71], v[28:31], a[88:91], v[68:71]\n"
    "ds_read_b64_tr_b16 v[54:55], v11 offset:59392\n"
    "v_mfma_f32_16x16x32_bf16 v[76:79], v[28:31], a[152:155], v[76:79]\n"
    "v_mfma_f32_16x16x32_bf16 v[84:87], v[28:31], a[216:219], v[84:87]\n"
    "buffer_load_dwordx4 a[232:235], v1, s[72:75], s88 offen offset:1024 sc1\n"
    "s_waitcnt lgkmcnt(8)\n"
    "v_mfma_f32_16x16x32_bf16 v[88:91], v[32:35], a[24:27], v[88:91]\n"
    "ds_read_b64_tr_b16 v[24:25], v8 offset:45056\n"
    "s_mov_b32 s29, 3145728\n"
    "v_mfma_f32_16x16x32_bf16 v[96:99], v[32:35], a[88:91], v[96:99]\n"
    "ds_read_b64_tr_b16 v[26:27], v8 offset:47104\n"
    "s_add_u32 m0, s28, 98304\n"
    "v_mfma_f32_16x16x32_bf16 v[104:107], v[32:35], a[152:155], v[104:107]\n"
    "v_mfma_f32_16x16x32_bf16 v[112:115], v[32:35], a[216:219], v[112:115]\n"
    "buffer_load_dwordx4 v4, s[76:79], s29 offen offset:0 lds\n"
    "v_mfma_f32_16x16x32_bf16 v[92:95], v[36:39], a[24:27], v[92:95]\n"
    "ds_read_b64_tr_b16 v[28:29], v9 offset:45056\n"
    "v_mfma_f32_16x16x32_bf16 v[100:103], v[36:39], a[88:91], v[100:103]\n"
    "ds_read_b64_tr_b16 v[30:31], v9 offset:47104\n"
    "v_mfma_f32_16x16x32_bf16 v[108:111], v[36:39], a[152:155], v[108:111]\n"
    "v_mfma_f32_16x16x32_bf16 v[116:119], v[36:39], a[216:219], v[116:119]\n"
    "s_waitcnt lgkmcnt(8)\n"
    "v_mfma_f32_16x16x32_bf16 v[120:123], v[40:43], a[24:27], v[120:123]\n"
    "ds_read_b64_tr_b16 v[32:33], v10 offset:45056\n"
    "buffer_load_dwordx4 v5, s[76:79], s29 offen offset:1024 lds\n"
    "v_mfma_f32_16x16x32_bf16 v[128:131], v[40:43], a[88:91], v[128:131]\n"
    "ds_read_b64_tr_b16 v[34:35], v10 offset:47104\n"
    "v_mfma_f32_16x16x32_bf16 v[136:139], v[40:43], a[152:155], v[136:139]\n"
    "v_mfma_f32_16x16x32_bf16 v[144:147], v[40:43], a[216:219], v[144:147]\n"
    "v_mfma_f32_16x16x32_bf16 v[124:127], v[44:47], a[24:27], v[124:127]\n"
    "ds_read_b64_tr_b16 v[36:37], v11 offset:45056\n"
    "buffer_load_dwordx4 v6, s[76:79], s29 offen offset:2048 lds\n"
    "v_mfma_f32_16x16x32_bf16 v[132:135], v[44:47], a[88:91], v[132:135]\n"
    "ds_read_b64_tr_b16 v[38:39], v11 offset:47104\n"
    "v_mfma_f32_16x16x32_bf16 v[140:143], v[44:47], a[152:155], v[140:143]\n"
    "v_mfma_f32_16x16x32_bf16 v[148:151], v[44:47], a[216:219], v[148:151]\n"
    "s_waitcnt lgkmcnt(8)\n"
    "v_mfma_f32_16x16x32_bf16 v[152:155], v[48:51], a[24:27], v[152:155]\n"
    "ds_read_b64_tr_b16 v[40:41], v8 offset:61440\n"
    "v_mfma_f32_16x16x32_bf16 v[160:163], v[48:51], a[88:91], v[160:163]\n"
    "ds_read_b64_tr_b16 v[42:43], v8 offset:63488\n"
    "buffer_load_dwordx4 v7, s[76:79], s29 offen offset:3072 lds\n"
    "v_mfma_f32_16x16x32_bf16 v[168:171], v[48:51], a[152:155], v[168:171]\n"
    "s_mov_b32 s29, 3145856\n"
    "v_mfma_f32_16x16x32_bf16 v[176:179], v[48:51], a[216:219], v[176:179]\n"
    "s_add_u32 m0, s28, 114688\n"
    "v_mfma_f32_16x16x32_bf16 v[156:159], v[52:55], a[24:27], v[156:159]\n"
    "ds_read_b64_tr_b16 v[44:45], v9 offset:61440\n"
    "v_mfma_f32_16x16x32_bf16 v[164:167], v[52:55], a[88:91], v[164:167]\n"
    "ds_read_b64_tr_b16 v[46:47], v9 offset:63488\n"
    "buffer_load_dwordx4 v4, s[76:79], s29 offen offset:0 lds\n"
    "v_mfma_f32_16x16x32_bf16 v[172:175], v[52:55], a[152:155], v[172:175]\n"
    "v_mfma_f32_16x16x32_bf16 v[180:183], v[52:55], a[216:219], v[180:183]\n"
    "s_waitcnt vmcnt(18)\n"
    "s_waitcnt lgkmcnt(8)\n"
    "v_mfma_f32_16x16x32_bf16 v[56:59], v[24:27], a[28:31], v[56:59]\n"
    "ds_read_b64_tr_b16 v[48:49], v10 offset:61440\n"
    "v_mfma_f32_16x16x32_bf16 v[64:67], v[24:27], a[92:95], v[64:67]\n"
    "ds_read_b64_tr_b16 v[50:51], v10 offset:63488\n"
    "v_mfma_f32_16x16x32_bf16 v[72:75], v[24:27], a[156:159], v[72:75]\n"
    "buffer_load_dwordx4 v5, s[76:79], s29 offen offset:1024 lds\n"
    "v_mfma_f32_16x16x32_bf16 v[80:83], v[24:27], a[220:223], v[80:83]\n"
    "v_mfma_f32_16x16x32_bf16 v[60:63], v[28:31], a[28:31], v[60:63]\n"
    "ds_read_b64_tr_b16 v[52:53], v11 offset:61440\n"
    "v_mfma_f32_16x16x32_bf16 v[68:71], v[28:31], a[92:95], v[68:71]\n"
    "ds_read_b64_tr_b16 v[54:55], v11 offset:63488\n"
    "v_mfma_f32_16x16x32_bf16 v[76:79], v[28:31], a[156:159], v[76:79]\n"
    "buffer_load_dwordx4 v6, s[76:79], s29 offen offset:2048 lds\n"
    "v_mfma_f32_16x16x32_bf16 v[84:87], v[28:31], a[220:223], v[84:87]\n"
    "s_waitcnt lgkmcnt(8)\n"
    "v_mfma_f32_16x16x32_bf16 v[88:91], v[32:35], a[28:31], v[88:91]\n"
    "v_mfma_f32_16x16x32_bf16 v[96:99], v[32:35], a[92:95], v[96:99]\n"
    "v_mfma_f32_16x16x32_bf16 v[104:107], v[32:35], a[156:159], v[104:107]\n"
    "v_mfma_f32_16x16x32_bf16 v[112:115], v[32:35], a[220:223], v[112:115]\n"
    "buffer_load_dwordx4 v7, s[76:79], s29 offen offset:3072 lds\n"
    "v_mfma_f32_16x16x32_bf16 v[92:95], v[36:39], a[28:31], v[92:95]\n"
    "s_mov_b32 s88, 90112\n"
    "v_mfma_f32_16x16x32_bf16 v[100:103], v[36:39], a[92:95], v[100:103]\n"
    "v_mfma_f32_16x16x32_bf16 v[108:111], v[36:39], a[156:159], v[108:111]\n"
    "v_mfma_f32_16x16x32_bf16 v[116:119], v[36:39], a[220:223], v[116:119]\n"
    "buffer_load_dwordx4 a[44:47], v0, s[72:75], s88 offen offset:0 sc1\n"
    "s_waitcnt vmcnt(22) lgkmcnt(0)\n"
    "s_barrier\n"
    "v_mfma_f32_16x16x32_bf16 v[120:123], v[40:43], a[28:31], v[120:123]\n"
    "ds_read_b64_tr_b16 v[24:25], v12 offset:0\n"
    "v_mfma_f32_16x16x32_bf16 v[128:131], v[40:43], a[92:95], v[128:131]\n"
    "ds_read_b64_tr_b16 v[26:27], v12 offset:2048\n"
    "v_mfma_f32_16x16x32_bf16 v[136:139], v[40:43], a[156:159], v[136:139]\n"
    "ds_read_b64_tr_b16 v[28:29], v13 offset:0\n"
    "v_mfma_f32_16x16x32_bf16 v[144:147], v[40:43], a[220:223], v[144:147]\n"
    "ds_read_b64_tr_b16 v[30:31], v13 offset:2048\n"
    "v_mfma_f32_16x16x32_bf16 v[124:127], v[44:47], a[28:31], v[124:127]\n"
    "ds_read_b64_tr_b16 v[32:33], v14 offset:0\n"
    "buffer_load_dwordx4 a[108:111], v0, s[72:75], s88 offen offset:1024 sc1\n"
    "v_mfma_f32_16x16x32_bf16 v[132:135], v[44:47], a[92:95], v[132:135]\n"
    "ds_read_b64_tr_b16 v[34:35], v14 offset:2048\n"
    "v_mfma_f32_16x16x32_bf16 v[140:143], v[44:47], a[156:159], v[140:143]\n"
    "ds_read_b64_tr_b16 v[36:37], v15 offset:0\n"
    "v_mfma_f32_16x16x32_bf16 v[148:151], v[44:47], a[220:223], v[148:151]\n"
    "ds_read_b64_tr_b16 v[38:39], v15 offset:2048\n"
    "v_mfma_f32_16x16x32_bf16 v[152:155], v[48:51], a[28:31], v[152:155]\n"
    "ds_read_b64_tr_b16 v[40:41], v12 offset:16384\n"
    "buffer_load_dwordx4 a[172:175], v1, s[72:75], s88 offen offset:0 sc1\n"
    "v_mfma_f32_16x16x32_bf16 v[160:163], v[48:51], a[92:95], v[160:163]\n"
    "ds_read_b64_tr_b16 v[42:43], v12 offset:18432\n"
    "v_mfma_f32_16x16x32_bf16 v[168:171], v[48:51], a[156:159], v[168:171]\n"
    "v_mfma_f32_16x16x32_bf16 v[176:179], v[48:51], a[220:223], v[176:179]\n"
    "v_mfma_f32_16x16x32_bf16 v[156:159], v[52:55], a[28:31], v[156:159]\n"
    "ds_read_b64_tr_b16 v[44:45], v13 offset:16384\n"
    "v_mfma_f32_16x16x32_bf16 v[164:167], v[52:55], a[92:95], v[164:167]\n"
    "ds_read_b64_tr_b16 v[46:47], v13 offset:18432\n"
    "buffer_load_dwordx4 a[236:239], v1, s[72:75], s88 offen offset:1024 sc1\n"
    "v_mfma_f32_16x16x32_bf16 v[172:175], v[52:55], a[156:159], v[172:175]\n"
    "s_waitcnt vmcnt(24)\n"
    "v_cmp_ne_u32 vcc, s94, v231\n"
    "s_mov_b32 s30, 0x400000\n"
    "s_nop 1\n"
    "s_cbranch_vccz L_pmk_%=_x\n"
    "L_pmk_%=:\n"
    "s_sleep 2\n"
    "global_load_dword v231, v230, s[60:61] sc1\n"
    "s_waitcnt vmcnt(0)\n"
    "v_cmp_ne_u32 vcc, s94, v231\n"
    "s_sub_u32 s30, s30, 1\n"
    "s_cbranch_scc1 L_pmk_%=_x\n"
    "s_nop 0\n"
    "s_cbranch_vccnz L_pmk_%=\n"
    "L_pmk_%=_x:\n"
    "v_mfma_f32_16x16x32_bf16 v[180:183], v[52:55], a[220:223], v[180:183]\n"
    "s_waitcnt vmcnt(20)\n"
    "s_waitcnt lgkmcnt(8)\n"
    "v_mfma_f32_16x16x32_bf16 v[56:59], v[24:27], a[32:35], v[56:59]\n"
    "ds_read_b64_tr_b16 v[48:49], v14 offset:16384\n"
    "v_mfma_f32_16x16x32_bf16 v[64:67], v[24:27], a[96:99], v[64:67]\n"
    "ds_read_b64_tr_b16 v[50:51], v14 offset:18432\n"
    "global_load_dwordx4 v[216:219], v23, s[60:61] sc1\n"
    "v_mfma_f32_16x16x32_bf16 v[72:75], v[24:27], a[160:163], v[72:75]\n"
    "v_add_u32 v23, 0x2000, v23\n"
    "v_mfma_f32_16x16x32_bf16 v[80:83], v[24:27], a[224:227], v[80:83]\n"
    "v_mfma_f32_16x16x32_bf16 v[60:63], v[28:31], a[32:35], v[60:63]\n"
    "ds_read_b64_tr_b16 v[52:53], v15 offset:16384\n"
    "v_mfma_f32_16x16x32_bf16 v[68:71], v[28:31], a[96:99], v[68:71]\n"
    "ds_read_b64_tr_b16 v[54:55], v15 offset:18432\n"
    "v_mfma_f32_16x16x32_bf16 v[76:79], v[28:31], a[160:163], v[76:79]\n"
    "global_load_dwordx4 v[220:223], v23, s[60:61] sc1\n"
    "v_mfma_f32_16x16x32_bf16 v[84:87], v[28:31], a[224:227], v[84:87]\n"
    "s_waitcnt lgkmcnt(8)\n"
    "v_mfma_f32_16x16x32_bf16 v[88:91], v[32:35], a[32:35], v[88:91]\n"
    "ds_read_b64_tr_b16 v[24:25], v12 offset:4096\n"
    "v_mfma_f32_16x16x32_bf16 v[96:99], v[32:35], a[96:99], v[96:99]\n"
    "ds_read_b64_tr_b16 v[26:27], v12 offset:6144\n"
    "v_mfma_f32_16x16x32_bf16 v[104:107], v[32:35], a[160:163], v[104:107]\n"
    "global_load_dwordx4 v[224:227], v228, s[60:61] sc1\n"
    "v_mfma_f32_16x16x32_bf16 v[112:115], v[32:35], a[224:227], v[112:115]\n"
    "v_mfma_f32_16x16x32_bf16 v[92:95], v[36:39], a[32:35], v[92:95]\n"
    "ds_read_b64_tr_b16 v[28:29], v13 offset:4096\n"
    "v_mfma_f32_16x16x32_bf16 v[100:103], v[36:39], a[96:99], v[100:103]\n"
    "ds_read_b64_tr_b16 v[30:31], v13 offset:6144\n"
    "v_mfma_f32_16x16x32_bf16 v[108:111], v[36:39], a[160:163], v[108:111]\n"
    "v_mfma_f32_16x16x32_bf16 v[116:119], v[36:39], a[224:227], v[116:119]\n"
    "global_load_dwordx2 v[232:233], v22, s[62:63] offset:0\n"
    "s_waitcnt lgkmcnt(8)\n"
    "v_mfma_f32_16x16x32_bf16 v[120:123], v[40:43], a[32:35], v[120:123]\n"
    "ds_read_b64_tr_b16 v[32:33], v14 offset:4096\n"
    "v_mfma_f32_16x16x32_bf16 v[128:131], v[40:43], a[96:99], v[128:131]\n"
    "ds_read_b64_tr_b16 v[34:35], v14 offset:6144\n"
    "v_mfma_f32_16x16x32_bf16 v[136:139], v[40:43], a[160:163], v[136:139]\n"
    "v_mfma_f32_16x16x32_bf16 v[144:147], v[40:43], a[224:227], v[144:147]\n"
    "global_load_dwordx2 v[234:235], v22, s[62:63] offset:32\n"
    "v_mfma_f32_16x16x32_bf16 v[124:127], v[44:47], a[32:35], v[124:127]\n"
    "ds_read_b64_tr_b16 v[36:37], v15 offset:4096\n"
    "v_mfma_f32_16x16x32_bf16 v[132:135], v[44:47], a[96:99], v[132:135]\n"
    "ds_read_b64_tr_b16 v[38:39], v15 offset:6144\n"
    "v_mfma_f32_16x16x32_bf16 v[140:143], v[44:47], a[160:163], v[140:143]\n"
    "v_mfma_f32_16x16x32_bf16 v[148:151], v[44:47], a[224:227], v[148:151]\n"
    "s_waitcnt lgkmcnt(8)\n"
    "v_mfma_f32_16x16x32_bf16 v[152:155], v[48:51], a[32:35], v[152:155]\n"
    "ds_read_b64_tr_b16 v[40:41], v12 offset:20480\n"
    "global_load_dwordx2 v[236:237], v22, s[62:63] offset:64\n"
    "v_mfma_f32_16x16x32_bf16 v[160:163], v[48:51], a[96:99], v[160:163]\n"
    "ds_read_b64_tr_b16 v[42:43], v12 offset:22528\n"
    "v_mfma_f32_16x16x32_bf16 v[168:171], v[48:51], a[160:163], v[168:171]\n"
    "v_mfma_f32_16x16x32_bf16 v[176:179], v[48:51], a[224:227], v[176:179]\n"
    "v_mfma_f32_16x16x32_bf16 v[156:159], v[52:55], a[32:35], v[156:159]\n"
    "ds_read_b64_tr_b16 v[44:45], v13 offset:20480\n"
    "global_load_dwordx2 v[238:239], v22, s[62:63] offset:96\n"
    "v_mfma_f32_16x16x32_bf16 v[164:167], v[52:55], a[96:99], v[164:167]\n"
    "ds_read_b64_tr_b16 v[46:47], v13 offset:22528\n"
    "s_mov_b32 s88, 98304\n"
    "v_mfma_f32_16x16x32_bf16 v[172:175], v[52:55], a[160:163], v[172:175]\n"
    "v_mfma_f32_16x16x32_bf16 v[180:183], v[52:55], a[224:227], v[180:183]\n"
    "s_waitcnt vmcnt(23)\n"
    "s_waitcnt lgkmcnt(8)\n"
    "v_mfma_f32_16x16x32_bf16 v[56:59], v[24:27], a[36:39], v[56:59]\n"
    "ds_read_b64_tr_b16 v[48:49], v14 offset:20480\n"
    "v_mfma_f32_16x16x32_bf16 v[64:67], v[24:27], a[100:103], v[64:67]\n"
    "ds_read_b64_tr_b16 v[50:51], v14 offset:22528\n"
    "buffer_load_dwordx4 a[48:51], v0, s[72:75], s88 offen offset:0 sc1\n"
    "v_mfma_f32_16x16x32_bf16 v[72:75], v[24:27], a[164:167], v[72:75]\n"
    "v_mfma_f32_16x16x32_bf16 v[80:83], v[24:27], a[228:231], v[80:83]\n"
    "v_mfma_f32_16x16x32_bf16 v[60:63], v[28:31], a[36:39], v[60:63]\n"
    "ds_read_b64_tr_b16 v[52:53], v15 offset:20480\n"
    "v_mfma_f32_16x16x32_bf16 v[68:71], v[28:31], a[100:103], v[68:71]\n"
    "ds_read_b64_tr_b16 v[54:55], v15 offset:22528\n"
    "buffer_load_dwordx4 a[112:115], v0, s[72:75], s88 offen offset:1024 sc1\n"
    "v_mfma_f32_16x16x32_bf16 v[76:79], v[28:31], a[164:167], v[76:79]\n"
    "v_mfma_f32_16x16x32_bf16 v[84:87], v[28:31], a[228:231], v[84:87]\n"
    "s_waitcnt lgkmcnt(8)\n"
    "v_mfma_f32_16x16x32_bf16 v[88:91], v[32:35], a[36:39], v[88:91]\n"
    "ds_read_b64_tr_b16 v[24:25], v12 offset:8192\n"
    "v_mfma_f32_16x16x32_bf16 v[96:99], v[32:35], a[100:103], v[96:99]\n"
    "ds_read_b64_tr_b16 v[26:27], v12 offset:10240\n"
    "v_mfma_f32_16x16x32_bf16 v[104:107], v[32:35], a[164:167], v[104:107]\n"
    "buffer_load_dwordx4 a[176:179], v1, s[72:75], s88 offen offset:0 sc1\n"
    "v_mfma_f32_16x16x32_bf16 v[112:115], v[32:35], a[228:231], v[112:115]\n"
    "v_mfma_f32_16x16x32_bf16 v[92:95], v[36:39], a[36:39], v[92:95]\n"
    "ds_read_b64_tr_b16 v[28:29], v13 offset:8192\n"
    "v_mfma_f32_16x16x32_bf16 v[100:103], v[36:39], a[100:103], v[100:103]\n"
    "ds_read_b64_tr_b16 v[30:31], v13 offset:10240\n"
    "v_mfma_f32_16x16x32_bf16 v[108:111], v[36:39], a[164:167], v[108:111]\n"
    "buffer_load_dwordx4 a[240:243], v1, s[72:75], s88 offen offset:1024 sc1\n"
    "v_mfma_f32_16x16x32_bf16 v[116:119], v[36:39], a[228:231], v[116:119]\n"
    "s_mov_b32 s88, 106496\n"
    "s_waitcnt lgkmcnt(8)\n"
    "v_mfma_f32_16x16x32_bf16 v[120:123], v[40:43], a[36:39], v[120:123]\n"
    "ds_read_b64_tr_b16 v[32:33], v14 offset:8192\n"
    "v_mfma_f32_16x16x32_bf16 v[128:131], v[40:43], a[100:103], v[128:131]\n"
    "ds_read_b64_tr_b16 v[34:35], v14 offset:10240\n"
    "v_mfma_f32_16x16x32_bf16 v[136:139], v[40:43], a[164:167], v[136:139]\n"
    "v_mfma_f32_16x16x32_bf16 v[144:147], v[40:43], a[228:231], v[144:147]\n"
    "buffer_load_dwordx4 a[52:55], v0, s[72:75], s88 offen offset:0 sc1\n"
    "v_mfma_f32_16x16x32_bf16 v[124:127], v[44:47], a[36:39], v[124:127]\n"
    "ds_read_b64_tr_b16 v[36:37], v15 offset:8192\n"
    "v_mfma_f32_16x16x32_bf16 v[132:135], v[44:47], a[100:103], v[132:135]\n"
    "ds_read_b64_tr_b16 v[38:39], v15 offset:10240\n"
    "v_mfma_f32_16x16x32_bf16 v[140:143], v[44:47], a[164:167], v[140:143]\n"
    "v_mfma_f32_16x16x32_bf16 v[148:151], v[44:47], a[228:231], v[148:151]\n"
    "buffer_load_dwordx4 a[116:119], v0, s[72:75], s88 offen offset:1024 sc1\n"
    "s_waitcnt lgkmcnt(8)\n"
    "v_mfma_f32_16x16x32_bf16 v[152:155], v[48:51], a[36:39], v[152:155]\n"
    "ds_read_b64_tr_b16 v[40:41], v12 offset:24576\n"
    "v_mfma_f32_16x16x32_bf16 v[160:163], v[48:51], a[100:103], v[160:163]\n"
    "ds_read_b64_tr_b16 v[42:43], v12 offset:26624\n"
    "v_mfma_f32_16x16x32_bf16 v[168:171], v[48:51], a[164:167], v[168:171]\n"
    "v_mfma_f32_16x16x32_bf16 v[176:179], v[48:51], a[228:231], v[176:179]\n"
    "v_mfma_f32_16x16x32_bf16 v[156:159], v[52:55], a[36:39], v[156:159]\n"
    "ds_read_b64_tr_b16 v[44:45], v13 offset:24576\n"
    "buffer_load_dwordx4 a[180:183], v1, s[72:75], s88 offen offset:0 sc1\n"
    "v_mfma_f32_16x16x32_bf16 v[164:167], v[52:55], a[100:103], v[164:167]\n"
    "ds_read_b64_tr_b16 v[46:47], v13 offset:26624\n"
    "v_mfma_f32_16x16x32_bf16 v[172:175], v[52:55], a[164:167], v[172:175]\n"
    "v_mfma_f32_16x16x32_bf16 v[180:183], v[52:55], a[228:231], v[180:183]\n"
    "s_waitcnt vmcnt(26)\n"
    "s_waitcnt lgkmcnt(8)\n"
    "v_mfma_f32_16x16x32_bf16 v[56:59], v[24:27], a[40:43], v[56:59]\n"
    "ds_read_b64_tr_b16 v[48:49], v14 offset:24576\n"
    "buffer_load_dwordx4 a[244:247], v1, s[72:75], s88 offen offset:1024 sc1\n"
    "v_mfma_f32_16x16x32_bf16 v[64:67], v[24:27], a[104:107], v[64:67]\n"
    "ds_read_b64_tr_b16 v[50:51], v14 offset:26624\n"
    "s_mov_b32 s88, 114688\n"
    "v_mfma_f32_16x16x32_bf16 v[72:75], v[24:27], a[168:171], v[72:75]\n"
    "v_mfma_f32_16x16x32_bf16 v[80:83], v[24:27], a[232:235], v[80:83]\n"
    "v_mfma_f32_16x16x32_bf16 v[60:63], v[28:31], a[40:43], v[60:63]\n"
    "ds_read_b64_tr_b16 v[52:53], v15 offset:24576\n"
    "s_waitcnt vmcnt(8)\n"
    "v_add_f32 v216, v216, v217\n"
    "v_mfma_f32_16x16x32_bf16 v[68:71], v[28:31], a[104:107], v[68:71]\n"
    "ds_read_b64_tr_b16 v[54:55], v15 offset:26624\n"
    "buffer_load_dwordx4 a[56:59], v0, s[72:75], s88 offen offset:0 sc1\n"
    "v_add_f32 v218, v218, v219\n"
    "v_add_f32 v216, v216, v218\n"
    "v_mfma_f32_16x16x32_bf16 v[76:79], v[28:31], a[168:171], v[76:79]\n"
    "v_cvt_pk_bf16_f32 v216, 0, v216\n"
    "v_add_f32 v220, v220, v221\n"
    "v_mfma_f32_16x16x32_bf16 v[84:87], v[28:31], a[232:235], v[84:87]\n"
    "v_add_f32 v222, v222, v223\n"
    "v_add_f32 v220, v220, v222\n"
    "s_waitcnt lgkmcnt(8)\n"
    "v_mfma_f32_16x16x32_bf16 v[88:91], v[32:35], a[40:43], v[88:91]\n"
    "ds_read_b64_tr_b16 v[24:25], v12 offset:12288\n"
    "v_cvt_pk_bf16_f32 v220, 0, v220\n"
    "v_add_f32 v224, v224, v225\n"
    "v_mfma_f32_16x16x32_bf16 v[96:99], v[32:35], a[104:107], v[96:99]\n"
    "ds_read_b64_tr_b16 v[26:27], v12 offset:14336\n"
    "buffer_load_dwordx4 a[120:123], v0, s[72:75], s88 offen offset:1024 sc1\n"
    "v_add_f32 v226, v226, v227\n"
    "v_add_f32 v224, v224, v226\n"
    "v_mfma_f32_16x16x32_bf16 v[104:107], v[32:35], a[168:171], v[104:107]\n"
    "v_cvt_pk_bf16_f32 v224, 0, v224\n"
    "v_add_f32 v220, 1.0, v220\n"
    "v_mfma_f32_16x16x32_bf16 v[112:115], v[32:35], a[232:235], v[112:115]\n"
    "v_cvt_pk_bf16_f32 v220, 0, v220\n"
    "v_add_f32 v224, s67, v224\n"
    "v_mfma_f32_16x16x32_bf16 v[92:95], v[36:39], a[40:43], v[92:95]\n"
    "ds_read_b64_tr_b16 v[28:29], v13 offset:12288\n"
    "v_cvt_pk_bf16_f32 v224, 0, v224\n"
    "s_lshl_b32 s30, s20, 8\n"
    "v_mfma_f32_16x16x32_bf16 v[100:103], v[36:39], a[104:107], v[100:103]\n"
    "ds_read_b64_tr_b16 v[30:31], v13 offset:14336\n"
    "v_lshlrev_b32 v17, 2, v16\n"
    "v_add_u32 v17, s30, v17\n"
    "v_mfma_f32_16x16x32_bf16 v[108:111], v[36:39], a[168:171], v[108:111]\n"
    "buffer_load_dwordx4 a[184:187], v1, s[72:75], s88 offen offset:0 sc1\n"
    "v_add_u32 v17, 0x20000, v17\n"
    "v_mfma_f32_16x16x32_bf16 v[116:119], v[36:39], a[232:235], v[116:119]\n"
    "s_waitcnt lgkmcnt(8)\n"
    "v_mfma_f32_16x16x32_bf16 v[120:123], v[40:43], a[40:43], v[120:123]\n"
    "ds_read_b64_tr_b16 v[32:33], v14 offset:12288\n"
    "v_mfma_f32_16x16x32_bf16 v[128:131], v[40:43], a[104:107], v[128:131]\n"
    "ds_read_b64_tr_b16 v[34:35], v14 offset:14336\n"
    "v_mfma_f32_16x16x32_bf16 v[136:139], v[40:43], a[168:171], v[136:139]\n"
    "buffer_load_dwordx4 a[248:251], v1, s[72:75], s88 offen offset:1024 sc1\n"
    "ds_write_b32 v17, v216 offset:0\n"
    "v_mfma_f32_16x16x32_bf16 v[144:147], v[40:43], a[232:235], v[144:147]\n"
    "s_mov_b32 s88, 122880\n"
    "v_mfma_f32_16x16x32_bf16 v[124:127], v[44:47], a[40:43], v[124:127]\n"
    "ds_read_b64_tr_b16 v[36:37], v15 offset:12288\n"
    "v_mfma_f32_16x16x32_bf16 v[132:135], v[44:47], a[104:107], v[132:135]\n"
    "ds_read_b64_tr_b16 v[38:39], v15 offset:14336\n"
    "ds_write_b32 v17, v220 offset:1024\n"
    "v_mfma_f32_16x16x32_bf16 v[140:143], v[44:47], a[168:171], v[140:143]\n"
    "v_mfma_f32_16x16x32_bf16 v[148:151], v[44:47], a[232:235], v[148:151]\n"
    "buffer_load_dwordx4 a[60:63], v0, s[72:75], s88 offen offset:0 sc1\n"
    "s_waitcnt lgkmcnt(10)\n"
    "v_mfma_f32_16x16x32_bf16 v[152:155], v[48:51], a[40:43], v[152:155]\n"
    "ds_read_b64_tr_b16 v[40:41], v12 offset:28672\n"
    "v_mfma_f32_16x16x32_bf16 v[160:163], v[48:51], a[104:107], v[160:163]\n"
    "ds_read_b64_tr_b16 v[42:43], v12 offset:30720\n"
    "s_mov_b64 exec, 0xff\n\tglobal_store_dword v229, v224, s[60:61] sc1\n\ts_mov_b64 exec, -1\n"
    "v_mfma_f32_16x16x32_bf16 v[168:171], v[48:51], a[168:171], v[168:171]\n"
    "v_mfma_f32_16x16x32_bf16 v[176:179], v[48:51], a[232:235], v[176:179]\n"
    "buffer_load_dwordx4 a[124:127], v0, s[72:75], s88 offen offset:1024 sc1\n"
    "v_mfma_f32_16x16x32_bf16 v[156:159], v[52:55], a[40:43], v[156:159]\n"
    "ds_read_b64_tr_b16 v[44:45], v13 offset:28672\n"
    "v_mfma_f32_16x16x32_bf16 v[164:167], v[52:55], a[104:107], v[164:167]\n"
    "ds_read_b64_tr_b16 v[46:47], v13 offset:30720\n"
    "v_mfma_f32_16x16x32_bf16 v[172:175], v[52:55], a[168:171], v[172:175]\n"
    "v_mfma_f32_16x16x32_bf16 v[180:183], v[52:55], a[232:235], v[180:183]\n"
    "s_waitcnt lgkmcnt(10)\n"
    "v_mfma_f32_16x16x32_bf16 v[56:59], v[24:27], a[44:47], v[56:59]\n"
    "ds_read_b64_tr_b16 v[48:49], v14 offset:28672\n"
    "buffer_load_dwordx4 a[188:191], v1, s[72:75], s88 offen offset:0 sc1\n"
    "v_mfma_f32_16x16x32_bf16 v[64:67], v[24:27], a[108:111], v[64:67]\n"
    "ds_read_b64_tr_b16 v[50:51], v14 offset:30720\n"
    "v_mfma_f32_16x16x32_bf16 v[72:75], v[24:27], a[172:175], v[72:75]\n"
    "v_mfma_f32_16x16x32_bf16 v[80:83], v[24:27], a[236:239], v[80:83]\n"
    "v_mfma_f32_16x16x32_bf16 v[60:63], v[28:31], a[44:47], v[60:63]\n"
    "ds_read_b64_tr_b16 v[52:53], v15 offset:28672\n"
    "buffer_load_dwordx4 a[252:255], v1, s[72:75], s88 offen offset:1024 sc1\n"
    "v_mfma_f32_16x16x32_bf16 v[68:71], v[28:31], a[108:111], v[68:71]\n"
    "ds_read_b64_tr_b16 v[54:55], v15 offset:30720\n"
    "s_mov_b32 s29, 256\n"
    "v_mfma_f32_16x16x32_bf16 v[76:79], v[28:31], a[172:175], v[76:79]\n"
    "s_add_u32 m0, s28, 0\n"
    "v_mfma_f32_16x16x32_bf16 v[84:87], v[28:31], a[236:239], v[84:87]\n"
    "s_waitcnt lgkmcnt(9)\n"
    "v_mfma_f32_16x16x32_bf16 v[88:91], v[32:35], a[44:47], v[88:91]\n"
    "v_mfma_f32_16x16x32_bf16 v[96:99], v[32:35], a[108:111], v[96:99]\n"
    "buffer_load_dwordx4 v4, s[76:79], s29 offen offset:0 lds\n"
    "v_mfma_f32_16x16x32_bf16 v[104:107], v[32:35], a[172:175], v[104:107]\n"
    "v_mfma_f32_16x16x32_bf16 v[112:115], v[32:35], a[236:239], v[112:115]\n"
    "v_mfma_f32_16x16x32_bf16 v[92:95], v[36:39], a[44:47], v[92:95]\n"
    "v_mfma_f32_16x16x32_bf16 v[100:103], v[36:39], a[108:111], v[100:103]\n"
    "buffer_load_dwordx4 v5, s[76:79], s29 offen offset:1024 lds\n"
    "v_mfma_f32_16x16x32_bf16 v[108:111], v[36:39], a[172:175], v[108:111]\n"
    "v_mfma_f32_16x16x32_bf16 v[116:119], v[36:39], a[236:239], v[116:119]\n"
    "s_waitcnt vmcnt(19) lgkmcnt(0)\n"
    "s_barrier\n"
    "v_mfma_f32_16x16x32_bf16 v[120:123], v[40:43], a[44:47], v[120:123]\n"
    "ds_read_b64_tr_b16 v[24:25], v12 offset:32768\n"
    "v_mfma_f32_16x16x32_bf16 v[128:131], v[40:43], a[108:111], v[128:131]\n"
    "ds_read_b64_tr_b16 v[26:27], v12 offset:34816\n"
    "v_mfma_f32_16x16x32_bf16 v[136:139], v[40:43], a[172:175], v[136:139]\n"
    "ds_read_b64_tr_b16 v[28:29], v13 offset:32768\n"
    "buffer_load_dwordx4 v6, s[76:79], s29 offen offset:2048 lds\n"
    "v_mfma_f32_16x16x32_bf16 v[144:147], v[40:43], a[236:239], v[144:147]\n"
    "ds_read_b64_tr_b16 v[30:31], v13 offset:34816\n"
    "v_mfma_f32_16x16x32_bf16 v[124:127], v[44:47], a[44:47], v[124:127]\n"
    "ds_read_b64_tr_b16 v[32:33], v14 offset:32768\n"
    "v_mfma_f32_16x16x32_bf16 v[132:135], v[44:47], a[108:111], v[132:135]\n"
    "ds_read_b64_tr_b16 v[34:35], v14 offset:34816\n"
    "v_mfma_f32_16x16x32_bf16 v[140:143], v[44:47], a[172:175], v[140:143]\n"
    "ds_read_b64_tr_b16 v[36:37], v15 offset:32768\n"
    "buffer_load_dwordx4 v7, s[76:79], s29 offen offset:3072 lds\n"
    "v_mfma_f32_16x16x32_bf16 v[148:151], v[44:47], a[236:239], v[148:151]\n"
    "ds_read_b64_tr_b16 v[38:39], v15 offset:34816\n"
    "s_mov_b32 s29, 1048832\n"
    "v_mfma_f32_16x16x32_bf16 v[152:155], v[48:51], a[44:47], v[152:155]\n"
    "ds_read_b64_tr_b16 v[40:41], v12 offset:49152\n"
    "s_add_u32 m0, s28, 16384\n"
    "v_mfma_f32_16x16x32_bf16 v[160:163], v[48:51], a[108:111], v[160:163]\n"
    "ds_read_b64_tr_b16 v[42:43], v12 offset:51200\n"
    "v_mfma_f32_16x16x32_bf16 v[168:171], v[48:51], a[172:175], v[168:171]\n"
    "v_mfma_f32_16x16x32_bf16 v[176:179], v[48:51], a[236:239], v[176:179]\n"
    "buffer_load_dwordx4 v4, s[76:79], s29 offen offset:0 lds\n"
    "v_mfma_f32_16x16x32_bf16 v[156:159], v[52:55], a[44:47], v[156:159]\n"
    "ds_read_b64_tr_b16 v[44:45], v13 offset:49152\n"
    "v_mfma_f32_16x16x32_bf16 v[164:167], v[52:55], a[108:111], v[164:167]\n"
    "ds_read_b64_tr_b16 v[46:47], v13 offset:51200\n"
    "v_mfma_f32_16x16x32_bf16 v[172:175], v[52:55], a[172:175], v[172:175]\n"
    "v_mfma_f32_16x16x32_bf16 v[180:183], v[52:55], a[236:239], v[180:183]\n"
    "buffer_load_dwordx4 v5, s[76:79], s29 offen offset:1024 lds\n"
    "s_waitcnt vmcnt(19)\n"
    "s_waitcnt lgkmcnt(8)\n"
    "v_mfma_f32_16x16x32_bf16 v[56:59], v[24:27], a[48:51], v[56:59]\n"
    "ds_read_b64_tr_b16 v[48:49], v14 offset:49152\n"
    "v_mfma_f32_16x16x32_bf16 v[64:67], v[24:27], a[112:115], v[64:67]\n"
    "ds_read_b64_tr_b16 v[50:51], v14 offset:51200\n"
    "v_lshlrev_b32 v22, 4, v16\n"
    "v_mfma_f32_16x16x32_bf16 v[72:75], v[24:27], a[176:179], v[72:75]\n"
    "v_and_b32 v240, 15, v16\n"
    "v_mfma_f32_16x16x32_bf16 v[80:83], v[24:27], a[240:243], v[80:83]\n"
    "v_lshlrev_b32 v240, 3, v240\n"
    "v_mfma_f32_16x16x32_bf16 v[60:63], v[28:31], a[48:51], v[60:63]\n"
    "ds_read_b64_tr_b16 v[52:53], v15 offset:49152\n"
    "buffer_load_dwordx4 v6, s[76:79], s29 offen offset:2048 lds\n"
    "v_add_u32 v240, 0x20c00, v240\n"
    "v_mfma_f32_16x16x32_bf16 v[68:71], v[28:31], a[112:115], v[68:71]\n"
    "ds_read_b64_tr_b16 v[54:55], v15 offset:51200\n"
    "ds_read_b64 v[240:241], v240\n"
    "v_mfma_f32_16x16x32_bf16 v[76:79], v[28:31], a[176:179], v[76:79]\n"
    "s_waitcnt lgkmcnt(0)\n"
    "v_mfma_f32_16x16x32_bf16 v[84:87], v[28:31], a[240:243], v[84:87]\n"
    "s_lshl_b32 s30, s20, 8\n"
    "v_mfma_f32_16x16x32_bf16 v[88:91], v[32:35], a[48:51], v[88:91]\n"
    "ds_read_b64_tr_b16 v[24:25], v12 offset:36864\n"
    "buffer_load_dwordx4 v7, s[76:79], s29 offen offset:3072 lds\n"
    "v_lshrrev_b32 v17, 4, v16\n"
    "v_mfma_f32_16x16x32_bf16 v[96:99], v[32:35], a[112:115], v[96:99]\n"
    "ds_read_b64_tr_b16 v[26:27], v12 offset:38912\n"
    "s_mov_b32 s29, 2097408\n"
    "v_lshlrev_b32 v17, 4, v17\n"
    "v_mfma_f32_16x16x32_bf16 v[104:107], v[32:35], a[176:179], v[104:107]\n"
    "s_add_u32 m0, s28, 32768\n"
    "v_add_u32 v17, s30, v17\n"
    "v_mfma_f32_16x16x32_bf16 v[112:115], v[32:35], a[240:243], v[112:115]\n"
    "v_mfma_f32_16x16x32_bf16 v[92:95], v[36:39], a[48:51], v[92:95]\n"
    "ds_read_b64_tr_b16 v[28:29], v13 offset:36864\n"
    "v_add_u32 v17, 0x20000, v17\n"
    "v_mfma_f32_16x16x32_bf16 v[100:103], v[36:39], a[112:115], v[100:103]\n"
    "ds_read_b64_tr_b16 v[30:31], v13 offset:38912\n"
    "buffer_load_dwordx4 v4, s[76:79], s29 offen offset:0 lds\n"
    "ds_read_b128 v[242:245], v17 offset:0\n"
    "v_mfma_f32_16x16x32_bf16 v[108:111], v[36:39], a[176:179], v[108:111]\n"
    "ds_read_b128 v[246:249], v17 offset:1024\n"
    "v_mfma_f32_16x16x32_bf16 v[116:119], v[36:39], a[240:243], v[116:119]\n"
    "v_lshlrev_b32 v250, 16, v232\n"
    "v_mfma_f32_16x16x32_bf16 v[120:123], v[40:43], a[48:51], v[120:123]\n"
    "ds_read_b64_tr_b16 v[32:33], v14 offset:36864\n"
    "v_and_b32 v232, 0xffff0000, v232\n"
    "v_mfma_f32_16x16x32_bf16 v[128:131], v[40:43], a[112:115], v[128:131]\n"
    "ds_read_b64_tr_b16 v[34:35], v14 offset:38912\n"
    "buffer_load_dwordx4 v5, s[76:79], s29 offen offset:1024 lds\n"
    "v_lshlrev_b32 v251, 16, v233\n"
    "v_mfma_f32_16x16x32_bf16 v[136:139], v[40:43], a[176:179], v[136:139]\n"
    "v_and_b32 v233, 0xffff0000, v233\n"
    "v_mfma_f32_16x16x32_bf16 v[144:147], v[40:43], a[240:243], v[144:147]\n"
    "v_fma_f32 v250, v250, v240, v241\n"
    "v_mfma_f32_16x16x32_bf16 v[124:127], v[44:47], a[48:51], v[124:127]\n"
    "ds_read_b64_tr_b16 v[36:37], v15 offset:36864\n"
    "v_fma_f32 v232, v232, v240, v241\n"
    "v_mfma_f32_16x16x32_bf16 v[132:135], v[44:47], a[112:115], v[132:135]\n"
    "ds_read_b64_tr_b16 v[38:39], v15 offset:38912\n"
    "v_fma_f32 v251, v251, v240, v241\n"
    "v_mfma_f32_16x16x32_bf16 v[140:143], v[44:47], a[176:179], v[140:143]\n"
    "buffer_load_dwordx4 v6, s[76:79], s29 offen offset:2048 lds\n"
    "v_fma_f32 v233, v233, v240, v241\n"
    "v_mfma_f32_16x16x32_bf16 v[148:151], v[44:47], a[240:243], v[148:151]\n"
    "v_mfma_f32_16x16x32_bf16 v[152:155], v[48:51], a[48:51], v[152:155]\n"
    "ds_read_b64_tr_b16 v[40:41], v12 offset:53248\n"
    "s_waitcnt lgkmcnt(5)\n"
    "v_mfma_f32_16x16x32_bf16 v[160:163], v[48:51], a[112:115], v[160:163]\n"
    "ds_read_b64_tr_b16 v[42:43], v12 offset:55296\n"
    "v_fma_f32 v250, v250, v246, v242\n"
    "v_mfma_f32_16x16x32_bf16 v[168:171], v[48:51], a[176:179], v[168:171]\n"
    "buffer_load_dwordx4 v7, s[76:79], s29 offen offset:3072 lds\n"
    "v_fma_f32 v232, v232, v247, v243\n"
    "v_mfma_f32_16x16x32_bf16 v[176:179], v[48:51], a[240:243], v[176:179]\n"
    "s_mov_b32 s29, 3145984\n"
    "v_fma_f32 v251, v251, v248, v244\n"
    "v_mfma_f32_16x16x32_bf16 v[156:159], v[52:55], a[48:51], v[156:159]\n"
    "ds_read_b64_tr_b16 v[44:45], v13 offset:53248\n"
    "s_add_u32 m0, s28, 49152\n"
    "v_fma_f32 v233, v233, v249, v245\n"
    "v_mfma_f32_16x16x32_bf16 v[164:167], v[52:55], a[112:115], v[164:167]\n"
    "ds_read_b64_tr_b16 v[46:47], v13 offset:55296\n"
    "v_cvt_pk_bf16_f32 v252, v250, v232\n"
    "v_mfma_f32_16x16x32_bf16 v[172:175], v[52:55], a[176:179], v[172:175]\n"
    "v_cvt_pk_bf16_f32 v253, v251, v233\n"
    "v_mfma_f32_16x16x32_bf16 v[180:183], v[52:55], a[240:243], v[180:183]\n"
    "buffer_load_dwordx4 v4, s[76:79], s29 offen offset:0 lds\n"
    "ds_read_b128 v[242:245], v17 offset:64\n"
    "s_waitcnt vmcnt(22)\n"
    "v_mfma_f32_16x16x32_bf16 v[56:59], v[24:27], a[52:55], v[56:59]\n"
    "ds_read_b64_tr_b16 v[48:49], v14 offset:53248\n"
    "ds_read_b128 v[246:249], v17 offset:1088\n"
    "v_mfma_f32_16x16x32_bf16 v[64:67], v[24:27], a[116:119], v[64:67]\n"
    "ds_read_b64_tr_b16 v[50:51], v14 offset:55296\n"
    "v_lshlrev_b32 v250, 16, v234\n"
    "v_mfma_f32_16x16x32_bf16 v[72:75], v[24:27], a[180:183], v[72:75]\n"
    "v_and_b32 v234, 0xffff0000, v234\n"
    "v_mfma_f32_16x16x32_bf16 v[80:83], v[24:27], a[244:247], v[80:83]\n"
    "buffer_load_dwordx4 v5, s[76:79], s29 offen offset:1024 lds\n"
    "v_mfma_f32_16x16x32_bf16 v[60:63], v[28:31], a[52:55], v[60:63]\n"
    "ds_read_b64_tr_b16 v[52:53], v15 offset:53248\n"
    "v_lshlrev_b32 v251, 16, v235\n"
    "v_mfma_f32_16x16x32_bf16 v[68:71], v[28:31], a[116:119], v[68:71]\n"
    "ds_read_b64_tr_b16 v[54:55], v15 offset:55296\n"
    "v_and_b32 v235, 0xffff0000, v235\n"
    "v_mfma_f32_16x16x32_bf16 v[76:79], v[28:31], a[180:183], v[76:79]\n"
    "v_fma_f32 v250, v250, v240, v241\n"
    "v_mfma_f32_16x16x32_bf16 v[84:87], v[28:31], a[244:247], v[84:87]\n"
    "v_fma_f32 v234, v234, v240, v241\n"
    "s_waitcnt lgkmcnt(10)\n"
    "v_mfma_f32_16x16x32_bf16 v[88:91], v[32:35], a[52:55], v[88:91]\n"
    "ds_read_b64_tr_b16 v[24:25], v12 offset:40960\n"
    "buffer_load_dwordx4 v6, s[76:79], s29 offen offset:2048 lds\n"
    "v_fma_f32 v251, v251, v240, v241\n"
    "v_mfma_f32_16x16x32_bf16 v[96:99], v[32:35], a[116:119], v[96:99]\n"
    "ds_read_b64_tr_b16 v[26:27], v12 offset:43008\n"
    "v_fma_f32 v235, v235, v240, v241\n"
    "v_mfma_f32_16x16x32_bf16 v[104:107], v[32:35], a[180:183], v[104:107]\n"
    "s_waitcnt lgkmcnt(5)\n"
    "v_mfma_f32_16x16x32_bf16 v[112:115], v[32:35], a[244:247], v[112:115]\n"
    "v_fma_f32 v250, v250, v246, v242\n"
    "v_mfma_f32_16x16x32_bf16 v[92:95], v[36:39], a[52:55], v[92:95]\n"
    "ds_read_b64_tr_b16 v[28:29], v13 offset:40960\n"
    "buffer_load_dwordx4 v7, s[76:79], s29 offen offset:3072 lds\n"
    "v_fma_f32 v234, v234, v247, v243\n"
    "v_mfma_f32_16x16x32_bf16 v[100:103], v[36:39], a[116:119], v[100:103]\n"
    "ds_read_b64_tr_b16 v[30:31], v13 offset:43008\n"
    "s_mov_b32 s29, 384\n"
    "v_fma_f32 v251, v251, v248, v244\n"
    "v_mfma_f32_16x16x32_bf16 v[108:111], v[36:39], a[180:183], v[108:111]\n"
    "s_add_u32 m0, s28, 65536\n"
    "v_fma_f32 v235, v235, v249, v245\n"
    "v_mfma_f32_16x16x32_bf16 v[116:119], v[36:39], a[244:247], v[116:119]\n"
    "v_mfma_f32_16x16x32_bf16 v[120:123], v[40:43], a[52:55], v[120:123]\n"
    "ds_read_b64_tr_b16 v[32:33], v14 offset:40960\n"
    "v_cvt_pk_bf16_f32 v254, v250, v234\n"
    "v_mfma_f32_16x16x32_bf16 v[128:131], v[40:43], a[116:119], v[128:131]\n"
    "ds_read_b64_tr_b16 v[34:35], v14 offset:43008\n"
    "buffer_load_dwordx4 v4, s[76:79], s29 offen offset:0 lds\n"
    "v_cvt_pk_bf16_f32 v255, v251, v235\n"
    "v_mfma_f32_16x16x32_bf16 v[136:139], v[40:43], a[180:183], v[136:139]\n"
    "global_store_dwordx4 v22, v[252:255], s[68:69] sc1\n\ts_nop 1\n"
    "v_mfma_f32_16x16x32_bf16 v[144:147], v[40:43], a[244:247], v[144:147]\n"
    "v_add_u32 v22, 0x2000, v22\n"
    "v_mfma_f32_16x16x32_bf16 v[124:127], v[44:47], a[52:55], v[124:127]\n"
    "ds_read_b64_tr_b16 v[36:37], v15 offset:40960\n"
    "ds_read_b128 v[242:245], v17 offset:128\n"
    "v_mfma_f32_16x16x32_bf16 v[132:135], v[44:47], a[116:119], v[132:135]\n"
    "ds_read_b64_tr_b16 v[38:39], v15 offset:43008\n"
    "buffer_load_dwordx4 v5, s[76:79], s29 offen offset:1024 lds\n"
    "ds_read_b128 v[246:249], v17 offset:1152\n"
    "v_mfma_f32_16x16x32_bf16 v[140:143], v[44:47], a[180:183], v[140:143]\n"
    "v_lshlrev_b32 v250, 16, v236\n"
    "v_mfma_f32_16x16x32_bf16 v[148:151], v[44:47], a[244:247], v[148:151]\n"
    "v_and_b32 v236, 0xffff0000, v236\n"
    "s_waitcnt lgkmcnt(10)\n"
    "v_mfma_f32_16x16x32_bf16 v[152:155], v[48:51], a[52:55], v[152:155]\n"
    "ds_read_b64_tr_b16 v[40:41], v12 offset:57344\n"
    "v_lshlrev_b32 v251, 16, v237\n"
    "v_mfma_f32_16x16x32_bf16 v[160:163], v[48:51], a[116:119], v[160:163]\n"
    "ds_read_b64_tr_b16 v[42:43], v12 offset:59392\n"
    "v_and_b32 v237, 0xffff0000, v237\n"
    "v_mfma_f32_16x16x32_bf16 v[168:171], v[48:51], a[180:183], v[168:171]\n"
    "buffer_load_dwordx4 v6, s[76:79], s29 offen offset:2048 lds\n"
    "v_mfma_f32_16x16x32_bf16 v[176:179], v[48:51], a[244:247], v[176:179]\n"
    "v_fma_f32 v250, v250, v240, v241\n"
    "v_mfma_f32_16x16x32_bf16 v[156:159], v[52:55], a[52:55], v[156:159]\n"
    "ds_read_b64_tr_b16 v[44:45], v13 offset:57344\n"
    "v_fma_f32 v236, v236, v240, v241\n"
    "v_mfma_f32_16x16x32_bf16 v[164:167], v[52:55], a[116:119], v[164:167]\n"
    "ds_read_b64_tr_b16 v[46:47], v13 offset:59392\n"
    "v_fma_f32 v251, v251, v240, v241\n"
    "v_mfma_f32_16x16x32_bf16 v[172:175], v[52:55], a[180:183], v[172:175]\n"
    "buffer_load_dwordx4 v7, s[76:79], s29 offen offset:3072 lds\n"
    "v_fma_f32 v237, v237, v240, v241\n"
    "v_mfma_f32_16x16x32_bf16 v[180:183], v[52:55], a[244:247], v[180:183]\n"
    "s_mov_b32 s29, 1048960\n"
    "s_waitcnt lgkmcnt(4)\n"
    "s_waitcnt vmcnt(26)\n"
    "v_mfma_f32_16x16x32_bf16 v[56:59], v[24:27], a[56:59], v[56:59]\n"
    "ds_read_b64_tr_b16 v[48:49], v14 offset:57344\n"
    "s_add_u32 m0, s28, 81920\n"
    "v_fma_f32 v250, v250, v246, v242\n"
    "v_mfma_f32_16x16x32_bf16 v[64:67], v[24:27], a[120:123], v[64:67]\n"
    "ds_read_b64_tr_b16 v[50:51], v14 offset:59392\n"
    "v_fma_f32 v236, v236, v247, v243\n"
    "v_mfma_f32_16x16x32_bf16 v[72:75], v[24:27], a[184:187], v[72:75]\n"
    "v_fma_f32 v251, v251, v248, v244\n"
    "v_mfma_f32_16x16x32_bf16 v[80:83], v[24:27], a[248:251], v[80:83]\n"
    "buffer_load_dwordx4 v4, s[76:79], s29 offen offset:0 lds\n"
    "v_fma_f32 v237, v237, v249, v245\n"
    "v_mfma_f32_16x16x32_bf16 v[60:63], v[28:31], a[56:59], v[60:63]\n"
    "ds_read_b64_tr_b16 v[52:53], v15 offset:57344\n"
    "v_cvt_pk_bf16_f32 v252, v250, v236\n"
    "v_mfma_f32_16x16x32_bf16 v[68:71], v[28:31], a[120:123], v[68:71]\n"
    "ds_read_b64_tr_b16 v[54:55], v15 offset:59392\n"
    "v_cvt_pk_bf16_f32 v253, v251, v237\n"
    "v_mfma_f32_16x16x32_bf16 v[76:79], v[28:31], a[184:187], v[76:79]\n"
    "v_mfma_f32_16x16x32_bf16 v[84:87], v[28:31], a[248:251], v[84:87]\n"
    "buffer_load_dwordx4 v5, s[76:79], s29 offen offset:1024 lds\n"
    "ds_read_b128 v[242:245], v17 offset:192\n"
    "v_mfma_f32_16x16x32_bf16 v[88:91], v[32:35], a[56:59], v[88:91]\n"
    "ds_read_b64_tr_b16 v[24:25], v12 offset:45056\n"
    "ds_read_b128 v[246:249], v17 offset:1216\n"
    "v_mfma_f32_16x16x32_bf16 v[96:99], v[32:35], a[120:123], v[96:99]\n"
    "ds_read_b64_tr_b16 v[26:27], v12 offset:47104\n"
    "v_lshlrev_b32 v250, 16, v238\n"
    "v_mfma_f32_16x16x32_bf16 v[104:107], v[32:35], a[184:187], v[104:107]\n"
    "v_and_b32 v238, 0xffff0000, v238\n"
    "v_mfma_f32_16x16x32_bf16 v[112:115], v[32:35], a[248:251], v[112:115]\n"
    "v_lshlrev_b32 v251, 16, v239\n"
    "v_mfma_f32_16x16x32_bf16 v[92:95], v[36:39], a[56:59], v[92:95]\n"
    "ds_read_b64_tr_b16 v[28:29], v13 offset:45056\n"
    "buffer_load_dwordx4 v6, s[76:79], s29 offen offset:2048 lds\n"
    "v_and_b32 v239, 0xffff0000, v239\n"
    "v_mfma_f32_16x16x32_bf16 v[100:103], v[36:39], a[120:123], v[100:103]\n"
    "ds_read_b64_tr_b16 v[30:31], v13 offset:47104\n"
    "v_fma_f32 v250, v250, v240, v241\n"
    "v_mfma_f32_16x16x32_bf16 v[108:111], v[36:39], a[184:187], v[108:111]\n"
    "v_fma_f32 v238, v238, v240, v241\n"
    "v_mfma_f32_16x16x32_bf16 v[116:119], v[36:39], a[248:251], v[116:119]\n"
    "v_fma_f32 v251, v251, v240, v241\n"
    "s_waitcnt lgkmcnt(10)\n"
    "v_mfma_f32_16x16x32_bf16 v[120:123], v[40:43], a[56:59], v[120:123]\n"
    "ds_read_b64_tr_b16 v[32:33], v14 offset:45056\n"
    "buffer_load_dwordx4 v7, s[76:79], s29 offen offset:3072 lds\n"
    "v_fma_f32 v239, v239, v240, v241\n"
    "v_mfma_f32_16x16x32_bf16 v[128:131], v[40:43], a[120:123], v[128:131]\n"
    "ds_read_b64_tr_b16 v[34:35], v14 offset:47104\n"
    "s_waitcnt lgkmcnt(5)\n"
    "v_mfma_f32_16x16x32_bf16 v[136:139], v[40:43], a[184:187], v[136:139]\n"
    "v_mfma_f32_16x16x32_bf16 v[144:147], v[40:43], a[248:251], v[144:147]\n"
    "v_fma_f32 v250, v250, v246, v242\n"
    "v_mfma_f32_16x16x32_bf16 v[124:127], v[44:47], a[56:59], v[124:127]\n"
    "ds_read_b64_tr_b16 v[36:37], v15 offset:45056\n"
    "v_fma_f32 v238, v238, v247, v243\n"
    "v_mfma_f32_16x16x32_bf16 v[132:135], v[44:47], a[120:123], v[132:135]\n"
    "ds_read_b64_tr_b16 v[38:39], v15 offset:47104\n"
    "v_fma_f32 v251, v251, v248, v244\n"
    "v_mfma_f32_16x16x32_bf16 v[140:143], v[44:47], a[184:187], v[140:143]\n"
    "v_fma_f32 v239, v239, v249, v245\n"
    "v_mfma_f32_16x16x32_bf16 v[148:151], v[44:47], a[248:251], v[148:151]\n"
    "v_cvt_pk_bf16_f32 v254, v250, v238\n"
    "v_mfma_f32_16x16x32_bf16 v[152:155], v[48:51], a[56:59], v[152:155]\n"
    "ds_read_b64_tr_b16 v[40:41], v12 offset:61440\n"
    "v_cvt_pk_bf16_f32 v255, v251, v239\n"
    "v_mfma_f32_16x16x32_bf16 v[160:163], v[48:51], a[120:123], v[160:163]\n"
    "ds_read_b64_tr_b16 v[42:43], v12 offset:63488\n"
    "global_store_dwordx4 v22, v[252:255], s[68:69] sc1\n\ts_nop 1\n"
    "v_mfma_f32_16x16x32_bf16 v[168:171], v[48:51], a[184:187], v[168:171]\n"
    "v_mfma_f32_16x16x32_bf16 v[176:179], v[48:51], a[248:251], v[176:179]\n"
    "v_mfma_f32_16x16x32_bf16 v[156:159], v[52:55], a[56:59], v[156:159]\n"
    "ds_read_b64_tr_b16 v[44:45], v13 offset:61440\n"
    "v_mfma_f32_16x16x32_bf16 v[164:167], v[52:55], a[120:123], v[164:167]\n"
    "ds_read_b64_tr_b16 v[46:47], v13 offset:63488\n"
    "v_mfma_f32_16x16x32_bf16 v[172:175], v[52:55], a[184:187], v[172:175]\n"
    "v_mfma_f32_16x16x32_bf16 v[180:183], v[52:55], a[248:251], v[180:183]\n"
    "s_waitcnt vmcnt(26)\n"
    "s_waitcnt lgkmcnt(8)\n"
    "v_mfma_f32_16x16x32_bf16 v[56:59], v[24:27], a[60:63], v[56:59]\n"
    "ds_read_b64_tr_b16 v[48:49], v14 offset:61440\n"
    "v_mfma_f32_16x16x32_bf16 v[64:67], v[24:27], a[124:127], v[64:67]\n"
    "ds_read_b64_tr_b16 v[50:51], v14 offset:63488\n"
    "v_mfma_f32_16x16x32_bf16 v[72:75], v[24:27], a[188:191], v[72:75]\n"
    "v_mfma_f32_16x16x32_bf16 v[80:83], v[24:27], a[252:255], v[80:83]\n"
    "v_mfma_f32_16x16x32_bf16 v[60:63], v[28:31], a[60:63], v[60:63]\n"
    "ds_read_b64_tr_b16 v[52:53], v15 offset:61440\n"
    "v_mfma_f32_16x16x32_bf16 v[68:71], v[28:31], a[124:127], v[68:71]\n"
    "ds_read_b64_tr_b16 v[54:55], v15 offset:63488\n"
    "v_mfma_f32_16x16x32_bf16 v[76:79], v[28:31], a[188:191], v[76:79]\n"
    "v_mfma_f32_16x16x32_bf16 v[84:87], v[28:31], a[252:255], v[84:87]\n"
    "s_waitcnt lgkmcnt(8)\n"
    "v_mfma_f32_16x16x32_bf16 v[88:91], v[32:35], a[60:63], v[88:91]\n"
    "v_mfma_f32_16x16x32_bf16 v[96:99], v[32:35], a[124:127], v[96:99]\n"
    "v_mfma_f32_16x16x32_bf16 v[104:107], v[32:35], a[188:191], v[104:107]\n"
    "v_mfma_f32_16x16x32_bf16 v[112:115], v[32:35], a[252:255], v[112:115]\n"
    "v_mfma_f32_16x16x32_bf16 v[92:95], v[36:39], a[60:63], v[92:95]\n"
    "v_mfma_f32_16x16x32_bf16 v[100:103], v[36:39], a[124:127], v[100:103]\n"
    "v_mfma_f32_16x16x32_bf16 v[108:111], v[36:39], a[188:191], v[108:111]\n"
    "v_mfma_f32_16x16x32_bf16 v[116:119], v[36:39], a[252:255], v[116:119]\n"
    "s_waitcnt vmcnt(22) lgkmcnt(0)\n"
    "s_barrier\n"
    "v_mfma_f32_16x16x32_bf16 v[120:123], v[40:43], a[60:63], v[120:123]\n"
    "ds_read_b64_tr_b16 v[24:25], v8 offset:0\n"
    "s_mov_b32 s29, 2097536\n"
    "v_mfma_f32_16x16x32_bf16 v[128:131], v[40:43], a[124:127], v[128:131]\n"
    "ds_read_b64_tr_b16 v[26:27], v8 offset:2048\n"
    "s_add_u32 m0, s28, 98304\n"
    "v_mfma_f32_16x16x32_bf16 v[136:139], v[40:43], a[188:191], v[136:139]\n"
    "ds_read_b64_tr_b16 v[28:29], v9 offset:0\n"
    "buffer_load_dwordx4 v4, s[76:79], s29 offen offset:0 lds\n"
    "v_mfma_f32_16x16x32_bf16 v[144:147], v[40:43], a[252:255], v[144:147]\n"
    "ds_read_b64_tr_b16 v[30:31], v9 offset:2048\n"
    "v_mfma_f32_16x16x32_bf16 v[124:127], v[44:47], a[60:63], v[124:127]\n"
    "ds_read_b64_tr_b16 v[32:33], v8 offset:4096\n"
    "v_mfma_f32_16x16x32_bf16 v[132:135], v[44:47], a[124:127], v[132:135]\n"
    "ds_read_b64_tr_b16 v[34:35], v8 offset:6144\n"
    "v_mfma_f32_16x16x32_bf16 v[140:143], v[44:47], a[188:191], v[140:143]\n"
    "ds_read_b64_tr_b16 v[36:37], v9 offset:4096\n"
    "buffer_load_dwordx4 v5, s[76:79], s29 offen offset:1024 lds\n"
    "v_mfma_f32_16x16x32_bf16 v[148:151], v[44:47], a[252:255], v[148:151]\n"
    "ds_read_b64_tr_b16 v[38:39], v9 offset:6144\n"
    "v_mfma_f32_16x16x32_bf16 v[152:155], v[48:51], a[60:63], v[152:155]\n"
    "ds_read_b64_tr_b16 v[40:41], v8 offset:8192\n"
    "v_mfma_f32_16x16x32_bf16 v[160:163], v[48:51], a[124:127], v[160:163]\n"
    "ds_read_b64_tr_b16 v[42:43], v8 offset:10240\n"
    "v_mfma_f32_16x16x32_bf16 v[168:171], v[48:51], a[188:191], v[168:171]\n"
    "v_mfma_f32_16x16x32_bf16 v[176:179], v[48:51], a[252:255], v[176:179]\n"
    "buffer_load_dwordx4 v6, s[76:79], s29 offen offset:2048 lds\n"
    "v_mfma_f32_16x16x32_bf16 v[156:159], v[52:55], a[60:63], v[156:159]\n"
    "ds_read_b64_tr_b16 v[44:45], v9 offset:8192\n"
    "v_mfma_f32_16x16x32_bf16 v[164:167], v[52:55], a[124:127], v[164:167]\n"
    "ds_read_b64_tr_b16 v[46:47], v9 offset:10240\n"
    "v_mfma_f32_16x16x32_bf16 v[172:175], v[52:55], a[188:191], v[172:175]\n"
    "v_mfma_f32_16x16x32_bf16 v[180:183], v[52:55], a[252:255], v[180:183]\n"
    "buffer_load_dwordx4 v7, s[76:79], s29 offen offset:3072 lds\n"
    "s_waitcnt lgkmcnt(8)\n"
    "v_mfma_f32_16x16x32_bf16 v[184:187], v[24:27], a[0:3], 0\n"
    "ds_read_b64_tr_b16 v[48:49], v8 offset:12288\n"
    "s_mov_b32 s29, 3146112\n"
    "v_mfma_f32_16x16x32_bf16 v[192:195], v[24:27], a[64:67], 0\n"
    "ds_read_b64_tr_b16 v[50:51], v8 offset:14336\n"
    "s_add_u32 m0, s28, 114688\n"
    "v_mfma_f32_16x16x32_bf16 v[200:203], v[24:27], a[128:131], 0\n"
    "v_mfma_f32_16x16x32_bf16 v[208:211], v[24:27], a[192:195], 0\n"
    "v_mfma_f32_16x16x32_bf16 v[188:191], v[28:31], a[0:3], 0\n"
    "ds_read_b64_tr_b16 v[52:53], v9 offset:12288\n"
    "buffer_load_dwordx4 v4, s[76:79], s29 offen offset:0 lds\n"
    "v_mfma_f32_16x16x32_bf16 v[196:199], v[28:31], a[64:67], 0\n"
    "ds_read_b64_tr_b16 v[54:55], v9 offset:14336\n"
    "v_mfma_f32_16x16x32_bf16 v[204:207], v[28:31], a[128:131], 0\n"
    "v_mfma_f32_16x16x32_bf16 v[212:215], v[28:31], a[192:195], 0\n"
    "s_waitcnt lgkmcnt(8)\n"
    "v_mfma_f32_16x16x32_bf16 v[184:187], v[32:35], a[4:7], v[184:187]\n"
    "buffer_load_dwordx4 v5, s[76:79], s29 offen offset:1024 lds\n"
    "v_mfma_f32_16x16x32_bf16 v[192:195], v[32:35], a[68:71], v[192:195]\n"
    "v_mfma_f32_16x16x32_bf16 v[200:203], v[32:35], a[132:135], v[200:203]\n"
    "v_cvt_pk_bf16_f32 v216, v56, v57\n"
    "v_mfma_f32_16x16x32_bf16 v[208:211], v[32:35], a[196:199], v[208:211]\n"
    "v_mfma_f32_16x16x32_bf16 v[188:191], v[36:39], a[4:7], v[188:191]\n"
    "v_cvt_pk_bf16_f32 v217, v58, v59\n"
    "v_mfma_f32_16x16x32_bf16 v[196:199], v[36:39], a[68:71], v[196:199]\n"
    "buffer_load_dwordx4 v6, s[76:79], s29 offen offset:2048 lds\n"
    "v_mfma_f32_16x16x32_bf16 v[204:207], v[36:39], a[132:135], v[204:207]\n"
    "v_mfma_f32_16x16x32_bf16 v[212:215], v[36:39], a[196:199], v[212:215]\n"
    "v_cvt_pk_bf16_f32 v218, v60, v61\n"
    "s_waitcnt vmcnt(7) lgkmcnt(0)\n"
    "s_barrier\n"
    "s_cmp_lg_u32 s20, 0\n"
    "s_cbranch_scc1 L_shk_%=\n"
    "v_mov_b32 v18, s34\n"
    "v_mov_b32 v19, s94\n"
    "s_mov_b64 exec, 1\n"
    "global_atomic_swap v18, v19, s[60:61]\n"
    "s_mov_b64 exec, -1\n"
    "L_shk_%=:\n"
    "v_mfma_f32_16x16x32_bf16 v[184:187], v[40:43], a[8:11], v[184:187]\n"
    "ds_read_b64_tr_b16 v[24:25], v8 offset:16384\n"
    "v_mfma_f32_16x16x32_bf16 v[192:195], v[40:43], a[72:75], v[192:195]\n"
    "ds_read_b64_tr_b16 v[26:27], v8 offset:18432\n"
    "buffer_load_dwordx4 v7, s[76:79], s29 offen offset:3072 lds\n"
    "v_cvt_pk_bf16_f32 v219, v62, v63\n"
    "v_mfma_f32_16x16x32_bf16 v[200:203], v[40:43], a[136:139], v[200:203]\n"
    "ds_read_b64_tr_b16 v[28:29], v9 offset:16384\n"
    "v_mfma_f32_16x16x32_bf16 v[208:211], v[40:43], a[200:203], v[208:211]\n"
    "ds_read_b64_tr_b16 v[30:31], v9 offset:18432\n"
    "s_mov_b32 s99, 0\n"
    "v_mfma_f32_16x16x32_bf16 v[188:191], v[44:47], a[8:11], v[188:191]\n"
    "ds_read_b64_tr_b16 v[32:33], v8 offset:20480\n"
    "v_mfma_f32_16x16x32_bf16 v[196:199], v[44:47], a[72:75], v[196:199]\n"
    "ds_read_b64_tr_b16 v[34:35], v8 offset:22528\n"
    "v_mfma_f32_16x16x32_bf16 v[204:207], v[44:47], a[136:139], v[204:207]\n"
    "ds_read_b64_tr_b16 v[36:37], v9 offset:20480\n"
    "buffer_store_dwordx4 v[216:219], v21, s[80:83], s99 offen offset:0 sc1\n"
    "v_mfma_f32_16x16x32_bf16 v[212:215], v[44:47], a[200:203], v[212:215]\n"
    "ds_read_b64_tr_b16 v[38:39], v9 offset:22528\n"
    "v_mfma_f32_16x16x32_bf16 v[184:187], v[48:51], a[12:15], v[184:187]\n"
    "ds_read_b64_tr_b16 v[40:41], v8 offset:24576\n"
    "v_cvt_pk_bf16_f32 v220, v64, v65\n"
    "v_mfma_f32_16x16x32_bf16 v[192:195], v[48:51], a[76:79], v[192:195]\n"
    "ds_read_b64_tr_b16 v[42:43], v8 offset:26624\n"
    "v_mfma_f32_16x16x32_bf16 v[200:203], v[48:51], a[140:143], v[200:203]\n"
    "v_cvt_pk_bf16_f32 v221, v66, v67\n"
    "v_mfma_f32_16x16x32_bf16 v[208:211], v[48:51], a[204:207], v[208:211]\n"
    "v_mfma_f32_16x16x32_bf16 v[188:191], v[52:55], a[12:15], v[188:191]\n"
    "ds_read_b64_tr_b16 v[44:45], v9 offset:24576\n"
    "v_mfma_f32_16x16x32_bf16 v[196:199], v[52:55], a[76:79], v[196:199]\n"
    "ds_read_b64_tr_b16 v[46:47], v9 offset:26624\n"
    "v_cvt_pk_bf16_f32 v222, v68, v69\n"
    "v_mfma_f32_16x16x32_bf16 v[204:207], v[52:55], a[140:143], v[204:207]\n"
    "v_mfma_f32_16x16x32_bf16 v[212:215], v[52:55], a[204:207], v[212:215]\n"
    "v_cvt_pk_bf16_f32 v223, v70, v71\n"
    "s_waitcnt lgkmcnt(8)\n"
    "v_mfma_f32_16x16x32_bf16 v[184:187], v[24:27], a[16:19], v[184:187]\n"
    "ds_read_b64_tr_b16 v[48:49], v8 offset:28672\n"
    "v_mfma_f32_16x16x32_bf16 v[192:195], v[24:27], a[80:83], v[192:195]\n"
    "ds_read_b64_tr_b16 v[50:51], v8 offset:30720\n"
    "v_mfma_f32_16x16x32_bf16 v[200:203], v[24:27], a[144:147], v[200:203]\n"
    "s_mov_b32 s99, 0\n"
    "v_mfma_f32_16x16x32_bf16 v[208:211], v[24:27], a[208:211], v[208:211]\n"
    "v_mfma_f32_16x16x32_bf16 v[188:191], v[28:31], a[16:19], v[188:191]\n"
    "ds_read_b64_tr_b16 v[52:53], v9 offset:28672\n"
    "buffer_store_dwordx4 v[220:223], v21, s[80:83], s99 offen offset:1024 sc1\n"
    "v_mfma_f32_16x16x32_bf16 v[196:199], v[28:31], a[80:83], v[196:199]\n"
    "ds_read_b64_tr_b16 v[54:55], v9 offset:30720\n"
    "v_mfma_f32_16x16x32_bf16 v[204:207], v[28:31], a[144:147], v[204:207]\n"
    "v_cvt_pk_bf16_f32 v224, v72, v73\n"
    "v_mfma_f32_16x16x32_bf16 v[212:215], v[28:31], a[208:211], v[212:215]\n"
    "s_waitcnt lgkmcnt(8)\n"
    "v_mfma_f32_16x16x32_bf16 v[184:187], v[32:35], a[20:23], v[184:187]\n"
    "v_mfma_f32_16x16x32_bf16 v[192:195], v[32:35], a[84:87], v[192:195]\n"
    "v_cvt_pk_bf16_f32 v225, v74, v75\n"
    "v_mfma_f32_16x16x32_bf16 v[200:203], v[32:35], a[148:151], v[200:203]\n"
    "v_mfma_f32_16x16x32_bf16 v[208:211], v[32:35], a[212:215], v[208:211]\n"
    "v_cvt_pk_bf16_f32 v226, v76, v77\n"
    "v_mfma_f32_16x16x32_bf16 v[188:191], v[36:39], a[20:23], v[188:191]\n"
    "v_mfma_f32_16x16x32_bf16 v[196:199], v[36:39], a[84:87], v[196:199]\n"
    "v_cvt_pk_bf16_f32 v227, v78, v79\n"
    "v_mfma_f32_16x16x32_bf16 v[204:207], v[36:39], a[148:151], v[204:207]\n"
    "v_mfma_f32_16x16x32_bf16 v[212:215], v[36:39], a[212:215], v[212:215]\n"
    "s_waitcnt lgkmcnt(4)\n"
    "v_mfma_f32_16x16x32_bf16 v[184:187], v[40:43], a[24:27], v[184:187]\n"
    "ds_read_b64_tr_b16 v[24:25], v8 offset:32768\n"
    "s_mov_b32 s99, 8192\n"
    "v_mfma_f32_16x16x32_bf16 v[192:195], v[40:43], a[88:91], v[192:195]\n"
    "ds_read_b64_tr_b16 v[26:27], v8 offset:34816\n"
    "v_mfma_f32_16x16x32_bf16 v[200:203], v[40:43], a[152:155], v[200:203]\n"
    "ds_read_b64_tr_b16 v[28:29], v9 offset:32768\n"
    "buffer_store_dwordx4 v[224:227], v21, s[80:83], s99 offen offset:0 sc1\n"
    "v_mfma_f32_16x16x32_bf16 v[208:211], v[40:43], a[216:219], v[208:211]\n"
    "ds_read_b64_tr_b16 v[30:31], v9 offset:34816\n"
    "v_mfma_f32_16x16x32_bf16 v[188:191], v[44:47], a[24:27], v[188:191]\n"
    "ds_read_b64_tr_b16 v[32:33], v8 offset:36864\n"
    "v_cvt_pk_bf16_f32 v228, v80, v81\n"
    "v_mfma_f32_16x16x32_bf16 v[196:199], v[44:47], a[88:91], v[196:199]\n"
    "ds_read_b64_tr_b16 v[34:35], v8 offset:38912\n"
    "v_mfma_f32_16x16x32_bf16 v[204:207], v[44:47], a[152:155], v[204:207]\n"
    "ds_read_b64_tr_b16 v[36:37], v9 offset:36864\n"
    "v_mfma_f32_16x16x32_bf16 v[212:215], v[44:47], a[216:219], v[212:215]\n"
    "ds_read_b64_tr_b16 v[38:39], v9 offset:38912\n"
    "v_cvt_pk_bf16_f32 v229, v82, v83\n"
    "s_waitcnt lgkmcnt(8)\n"
    "v_mfma_f32_16x16x32_bf16 v[184:187], v[48:51], a[28:31], v[184:187]\n"
    "ds_read_b64_tr_b16 v[40:41], v8 offset:40960\n"
    "v_mfma_f32_16x16x32_bf16 v[192:195], v[48:51], a[92:95], v[192:195]\n"
    "ds_read_b64_tr_b16 v[42:43], v8 offset:43008\n"
    "v_cvt_pk_bf16_f32 v230, v84, v85\n"
    "v_mfma_f32_16x16x32_bf16 v[200:203], v[48:51], a[156:159], v[200:203]\n"
    "v_mfma_f32_16x16x32_bf16 v[208:211], v[48:51], a[220:223], v[208:211]\n"
    "v_mfma_f32_16x16x32_bf16 v[188:191], v[52:55], a[28:31], v[188:191]\n"
    "ds_read_b64_tr_b16 v[44:45], v9 offset:40960\n"
    "v_cvt_pk_bf16_f32 v231, v86, v87\n"
    "v_mfma_f32_16x16x32_bf16 v[196:199], v[52:55], a[92:95], v[196:199]\n"
    "ds_read_b64_tr_b16 v[46:47], v9 offset:43008\n"
    "v_mfma_f32_16x16x32_bf16 v[204:207], v[52:55], a[156:159], v[204:207]\n"
    "s_mov_b32 s99, 8192\n"
    "v_mfma_f32_16x16x32_bf16 v[212:215], v[52:55], a[220:223], v[212:215]\n"
    "s_waitcnt lgkmcnt(8)\n"
    "v_mfma_f32_16x16x32_bf16 v[184:187], v[24:27], a[32:35], v[184:187]\n"
    "ds_read_b64_tr_b16 v[48:49], v8 offset:45056\n"
    "buffer_store_dwordx4 v[228:231], v21, s[80:83], s99 offen offset:1024 sc1\n"
    "v_mfma_f32_16x16x32_bf16 v[192:195], v[24:27], a[96:99], v[192:195]\n"
    "ds_read_b64_tr_b16 v[50:51], v8 offset:47104\n"
    "v_mfma_f32_16x16x32_bf16 v[200:203], v[24:27], a[160:163], v[200:203]\n"
    "v_mfma_f32_16x16x32_bf16 v[208:211], v[24:27], a[224:227], v[208:211]\n"
    "v_cvt_pk_bf16_f32 v216, v88, v89\n"
    "v_mfma_f32_16x16x32_bf16 v[188:191], v[28:31], a[32:35], v[188:191]\n"
    "ds_read_b64_tr_b16 v[52:53], v9 offset:45056\n"
    "v_mfma_f32_16x16x32_bf16 v[196:199], v[28:31], a[96:99], v[196:199]\n"
    "ds_read_b64_tr_b16 v[54:55], v9 offset:47104\n"
    "v_cvt_pk_bf16_f32 v217, v90, v91\n"
    "v_mfma_f32_16x16x32_bf16 v[204:207], v[28:31], a[160:163], v[204:207]\n"
    "v_mfma_f32_16x16x32_bf16 v[212:215], v[28:31], a[224:227], v[212:215]\n"
    "v_cvt_pk_bf16_f32 v218, v92, v93\n"
    "s_waitcnt lgkmcnt(8)\n"
    "v_mfma_f32_16x16x32_bf16 v[184:187], v[32:35], a[36:39], v[184:187]\n"
    "v_mfma_f32_16x16x32_bf16 v[192:195], v[32:35], a[100:103], v[192:195]\n"
    "v_mfma_f32_16x16x32_bf16 v[200:203], v[32:35], a[164:167], v[200:203]\n"
    "v_cvt_pk_bf16_f32 v219, v94, v95\n"
    "v_mfma_f32_16x16x32_bf16 v[208:211], v[32:35], a[228:231], v[208:211]\n"
    "v_mfma_f32_16x16x32_bf16 v[188:191], v[36:39], a[36:39], v[188:191]\n"
    "s_mov_b32 s99, 0\n"
    "v_mfma_f32_16x16x32_bf16 v[196:199], v[36:39], a[100:103], v[196:199]\n"
    "v_mfma_f32_16x16x32_bf16 v[204:207], v[36:39], a[164:167], v[204:207]\n"
    "buffer_store_dwordx4 v[216:219], v21, s[80:83], s99 offen offset:2048 sc1\n"
    "v_mfma_f32_16x16x32_bf16 v[212:215], v[36:39], a[228:231], v[212:215]\n"
    "s_waitcnt lgkmcnt(4)\n"
    "v_mfma_f32_16x16x32_bf16 v[184:187], v[40:43], a[40:43], v[184:187]\n"
    "ds_read_b64_tr_b16 v[24:25], v8 offset:49152\n"
    "v_mfma_f32_16x16x32_bf16 v[192:195], v[40:43], a[104:107], v[192:195]\n"
    "ds_read_b64_tr_b16 v[26:27], v8 offset:51200\n"
    "v_cvt_pk_bf16_f32 v220, v96, v97\n"
    "v_mfma_f32_16x16x32_bf16 v[200:203], v[40:43], a[168:171], v[200:203]\n"
    "ds_read_b64_tr_b16 v[28:29], v9 offset:49152\n"
    "v_mfma_f32_16x16x32_bf16 v[208:211], v[40:43], a[232:235], v[208:211]\n"
    "ds_read_b64_tr_b16 v[30:31], v9 offset:51200\n"
    "v_cvt_pk_bf16_f32 v221, v98, v99\n"
    "v_mfma_f32_16x16x32_bf16 v[188:191], v[44:47], a[40:43], v[188:191]\n"
    "ds_read_b64_tr_b16 v[32:33], v8 offset:53248\n"
    "v_mfma_f32_16x16x32_bf16 v[196:199], v[44:47], a[104:107], v[196:199]\n"
    "ds_read_b64_tr_b16 v[34:35], v8 offset:55296\n"
    "v_mfma_f32_16x16x32_bf16 v[204:207], v[44:47], a[168:171], v[204:207]\n"
    "ds_read_b64_tr_b16 v[36:37], v9 offset:53248\n"
    "v_cvt_pk_bf16_f32 v222, v100, v101\n"
    "v_mfma_f32_16x16x32_bf16 v[212:215], v[44:47], a[232:235], v[212:215]\n"
    "ds_read_b64_tr_b16 v[38:39], v9 offset:55296\n"
    "s_waitcnt lgkmcnt(8)\n"
    "v_mfma_f32_16x16x32_bf16 v[184:187], v[48:51], a[44:47], v[184:187]\n"
    "ds_read_b64_tr_b16 v[40:41], v8 offset:57344\n"
    "v_cvt_pk_bf16_f32 v223, v102, v103\n"
    "v_mfma_f32_16x16x32_bf16 v[192:195], v[48:51], a[108:111], v[192:195]\n"
    "ds_read_b64_tr_b16 v[42:43], v8 offset:59392\n"
    "v_mfma_f32_16x16x32_bf16 v[200:203], v[48:51], a[172:175], v[200:203]\n"
    "s_mov_b32 s99, 0\n"
    "v_mfma_f32_16x16x32_bf16 v[208:211], v[48:51], a[236:239], v[208:211]\n"
    "v_mfma_f32_16x16x32_bf16 v[188:191], v[52:55], a[44:47], v[188:191]\n"
    "ds_read_b64_tr_b16 v[44:45], v9 offset:57344\n"
    "v_mfma_f32_16x16x32_bf16 v[196:199], v[52:55], a[108:111], v[196:199]\n"
    "ds_read_b64_tr_b16 v[46:47], v9 offset:59392\n"
    "buffer_store_dwordx4 v[220:223], v21, s[80:83], s99 offen offset:3072 sc1\n"
    "v_mfma_f32_16x16x32_bf16 v[204:207], v[52:55], a[172:175], v[204:207]\n"
    "v_mfma_f32_16x16x32_bf16 v[212:215], v[52:55], a[236:239], v[212:215]\n"
    "v_cvt_pk_bf16_f32 v224, v104, v105\n"
    "s_waitcnt lgkmcnt(8)\n"
    "v_mfma_f32_16x16x32_bf16 v[184:187], v[24:27], a[48:51], v[184:187]\n"
    "ds_read_b64_tr_b16 v[48:49], v8 offset:61440\n"
    "v_mfma_f32_16x16x32_bf16 v[192:195], v[24:27], a[112:115], v[192:195]\n"
    "ds_read_b64_tr_b16 v[50:51], v8 offset:63488\n"
    "v_cvt_pk_bf16_f32 v225, v106, v107\n"
    "v_mfma_f32_16x16x32_bf16 v[200:203], v[24:27], a[176:179], v[200:203]\n"
    "v_mfma_f32_16x16x32_bf16 v[208:211], v[24:27], a[240:243], v[208:211]\n"
    "v_mfma_f32_16x16x32_bf16 v[188:191], v[28:31], a[48:51], v[188:191]\n"
    "ds_read_b64_tr_b16 v[52:53], v9 offset:61440\n"
    "v_cvt_pk_bf16_f32 v226, v108, v109\n"
    "v_mfma_f32_16x16x32_bf16 v[196:199], v[28:31], a[112:115], v[196:199]\n"
    "ds_read_b64_tr_b16 v[54:55], v9 offset:63488\n"
    "v_mfma_f32_16x16x32_bf16 v[204:207], v[28:31], a[176:179], v[204:207]\n"
    "v_cvt_pk_bf16_f32 v227, v110, v111\n"
    "v_mfma_f32_16x16x32_bf16 v[212:215], v[28:31], a[240:243], v[212:215]\n"
    "s_waitcnt lgkmcnt(8)\n"
    "v_mfma_f32_16x16x32_bf16 v[184:187], v[32:35], a[52:55], v[184:187]\n"
    "v_mfma_f32_16x16x32_bf16 v[192:195], v[32:35], a[116:119], v[192:195]\n"
    "s_mov_b32 s99, 8192\n"
    "v_mfma_f32_16x16x32_bf16 v[200:203], v[32:35], a[180:183], v[200:203]\n"
    "v_mfma_f32_16x16x32_bf16 v[208:211], v[32:35], a[244:247], v[208:211]\n"
    "buffer_store_dwordx4 v[224:227], v21, s[80:83], s99 offen offset:2048 sc1\n"
    "v_mfma_f32_16x16x32_bf16 v[188:191], v[36:39], a[52:55], v[188:191]\n"
    "v_mfma_f32_16x16x32_bf16 v[196:199], v[36:39], a[116:119], v[196:199]\n"
    "v_cvt_pk_bf16_f32 v228, v112, v113\n"
    "v_mfma_f32_16x16x32_bf16 v[204:207], v[36:39], a[180:183], v[204:207]\n"
    "v_mfma_f32_16x16x32_bf16 v[212:215], v[36:39], a[244:247], v[212:215]\n"
    "s_waitcnt lgkmcnt(4)\n"
    "v_mfma_f32_16x16x32_bf16 v[184:187], v[40:43], a[56:59], v[184:187]\n"
    "ds_read_b64_tr_b16 v[24:25], v10 offset:0\n"
    "v_cvt_pk_bf16_f32 v229, v114, v115\n"
    "v_mfma_f32_16x16x32_bf16 v[192:195], v[40:43], a[120:123], v[192:195]\n"
    "ds_read_b64_tr_b16 v[26:27], v10 offset:2048\n"
    "v_mfma_f32_16x16x32_bf16 v[200:203], v[40:43], a[184:187], v[200:203]\n"
    "ds_read_b64_tr_b16 v[28:29], v11 offset:0\n"
    "v_cvt_pk_bf16_f32 v230, v116, v117\n"
    "v_mfma_f32_16x16x32_bf16 v[208:211], v[40:43], a[248:251], v[208:211]\n"
    "ds_read_b64_tr_b16 v[30:31], v11 offset:2048\n"
    "v_mfma_f32_16x16x32_bf16 v[188:191], v[44:47], a[56:59], v[188:191]\n"
    "ds_read_b64_tr_b16 v[32:33], v10 offset:4096\n"
    "v_cvt_pk_bf16_f32 v231, v118, v119\n"
    "v_mfma_f32_16x16x32_bf16 v[196:199], v[44:47], a[120:123], v[196:199]\n"
    "ds_read_b64_tr_b16 v[34:35], v10 offset:6144\n"
    "v_mfma_f32_16x16x32_bf16 v[204:207], v[44:47], a[184:187], v[204:207]\n"
    "ds_read_b64_tr_b16 v[36:37], v11 offset:4096\n"
    "v_mfma_f32_16x16x32_bf16 v[212:215], v[44:47], a[248:251], v[212:215]\n"
    "ds_read_b64_tr_b16 v[38:39], v11 offset:6144\n"
    "s_mov_b32 s99, 8192\n"
    "s_waitcnt lgkmcnt(8)\n"
    "v_mfma_f32_16x16x32_bf16 v[184:187], v[48:51], a[60:63], v[184:187]\n"
    "ds_read_b64_tr_b16 v[40:41], v10 offset:8192\n"
    "v_mfma_f32_16x16x32_bf16 v[192:195], v[48:51], a[124:127], v[192:195]\n"
    "ds_read_b64_tr_b16 v[42:43], v10 offset:10240\n"
    "buffer_store_dwordx4 v[228:231], v21, s[80:83], s99 offen offset:3072 sc1\n"
    "v_mfma_f32_16x16x32_bf16 v[200:203], v[48:51], a[188:191], v[200:203]\n"
    "v_mfma_f32_16x16x32_bf16 v[208:211], v[48:51], a[252:255], v[208:211]\n"
    "v_mfma_f32_16x16x32_bf16 v[188:191], v[52:55], a[60:63], v[188:191]\n"
    "ds_read_b64_tr_b16 v[44:45], v11 offset:8192\n"
    "v_mfma_f32_16x16x32_bf16 v[196:199], v[52:55], a[124:127], v[196:199]\n"
    "ds_read_b64_tr_b16 v[46:47], v11 offset:10240\n"
    "v_mfma_f32_16x16x32_bf16 v[204:207], v[52:55], a[188:191], v[204:207]\n"
    "v_mfma_f32_16x16x32_bf16 v[212:215], v[52:55], a[252:255], v[212:215]\n"
    "s_waitcnt lgkmcnt(8)\n"
    "v_mfma_f32_16x16x32_bf16 v[56:59], v[24:27], a[0:3], 0\n"
    "ds_read_b64_tr_b16 v[48:49], v10 offset:12288\n"
    "v_mfma_f32_16x16x32_bf16 v[64:67], v[24:27], a[64:67], 0\n"
    "ds_read_b64_tr_b16 v[50:51], v10 offset:14336\n"
    "v_mfma_f32_16x16x32_bf16 v[72:75], v[24:27], a[128:131], 0\n"
    "v_mfma_f32_16x16x32_bf16 v[80:83], v[24:27], a[192:195], 0\n"
    "s_lshl_b32 s31, s20, 11\n"
    "v_mfma_f32_16x16x32_bf16 v[60:63], v[28:31], a[0:3], 0\n"
    "ds_read_b64_tr_b16 v[52:53], v11 offset:12288\n"
    "v_mfma_f32_16x16x32_bf16 v[68:71], v[28:31], a[64:67], 0\n"
    "ds_read_b64_tr_b16 v[54:55], v11 offset:14336\n"
    "v_mfma_f32_16x16x32_bf16 v[76:79], v[28:31], a[128:131], 0\n"
    "v_add_u32 v235, s31, v2\n"
    "v_mfma_f32_16x16x32_bf16 v[84:87], v[28:31], a[192:195], 0\n"
    "s_waitcnt lgkmcnt(8)\n"
    "v_mfma_f32_16x16x32_bf16 v[56:59], v[32:35], a[4:7], v[56:59]\n"
    "v_mfma_f32_16x16x32_bf16 v[64:67], v[32:35], a[68:71], v[64:67]\n"
    "s_sub_u32 s30, 15, s22\n"
    "v_mfma_f32_16x16x32_bf16 v[72:75], v[32:35], a[132:135], v[72:75]\n"
    "v_mfma_f32_16x16x32_bf16 v[80:83], v[32:35], a[196:199], v[80:83]\n"
    "v_cvt_pk_bf16_f32 v216, v120, v121\n"
    "v_mfma_f32_16x16x32_bf16 v[60:63], v[36:39], a[4:7], v[60:63]\n"
    "s_lshl_b32 s30, s30, 17\n"
    "v_mfma_f32_16x16x32_bf16 v[68:71], v[36:39], a[68:71], v[68:71]\n"
    "v_mfma_f32_16x16x32_bf16 v[76:79], v[36:39], a[132:135], v[76:79]\n"
    "v_cvt_pk_bf16_f32 v217, v122, v123\n"
    "v_mfma_f32_16x16x32_bf16 v[84:87], v[36:39], a[196:199], v[84:87]\n"
    "v_add_u32 v234, s30, v235\n"
    "s_waitcnt vmcnt(16) lgkmcnt(0)\n"
    "s_barrier\n"
    "v_mfma_f32_16x16x32_bf16 v[56:59], v[40:43], a[8:11], v[56:59]\n"
    "ds_read_b64_tr_b16 v[24:25], v10 offset:16384\n"
    "s_mov_b32 s29, 0\n"
    "v_mfma_f32_16x16x32_bf16 v[64:67], v[40:43], a[72:75], v[64:67]\n"
    "ds_read_b64_tr_b16 v[26:27], v10 offset:18432\n"
    "s_add_u32 m0, s28, 0\n"
    "v_cvt_pk_bf16_f32 v218, v124, v125\n"
    "v_mfma_f32_16x16x32_bf16 v[72:75], v[40:43], a[136:139], v[72:75]\n"
    "ds_read_b64_tr_b16 v[28:29], v11 offset:16384\n"
    "buffer_load_dwordx4 v4, s[44:47], s29 offen offset:0 lds\n"
    "v_mfma_f32_16x16x32_bf16 v[80:83], v[40:43], a[200:203], v[80:83]\n"
    "ds_read_b64_tr_b16 v[30:31], v11 offset:18432\n"
    "s_lshl_b32 s30, s22, 17\n"
    "v_mfma_f32_16x16x32_bf16 v[60:63], v[44:47], a[8:11], v[60:63]\n"
    "ds_read_b64_tr_b16 v[32:33], v10 offset:20480\n"
    "v_cvt_pk_bf16_f32 v219, v126, v127\n"
    "v_mfma_f32_16x16x32_bf16 v[68:71], v[44:47], a[72:75], v[68:71]\n"
    "ds_read_b64_tr_b16 v[34:35], v10 offset:22528\n"
    "v_mfma_f32_16x16x32_bf16 v[76:79], v[44:47], a[136:139], v[76:79]\n"
    "ds_read_b64_tr_b16 v[36:37], v11 offset:20480\n"
    "buffer_load_dwordx4 v5, s[44:47], s29 offen offset:1024 lds\n"
    "v_add_u32 v235, s30, v235\n"
    "v_mfma_f32_16x16x32_bf16 v[84:87], v[44:47], a[200:203], v[84:87]\n"
    "ds_read_b64_tr_b16 v[38:39], v11 offset:22528\n"
    "s_mov_b32 s99, 4096\n"
    "v_mfma_f32_16x16x32_bf16 v[56:59], v[48:51], a[12:15], v[56:59]\n"
    "ds_read_b64_tr_b16 v[40:41], v10 offset:24576\n"
    "v_mfma_f32_16x16x32_bf16 v[64:67], v[48:51], a[76:79], v[64:67]\n"
    "ds_read_b64_tr_b16 v[42:43], v10 offset:26624\n"
    "v_mfma_f32_16x16x32_bf16 v[72:75], v[48:51], a[140:143], v[72:75]\n"
    "buffer_store_dwordx4 v[216:219], v21, s[80:83], s99 offen offset:0 sc1\n"
    "v_mfma_f32_16x16x32_bf16 v[80:83], v[48:51], a[204:207], v[80:83]\n"
    "v_mfma_f32_16x16x32_bf16 v[60:63], v[52:55], a[12:15], v[60:63]\n"
    "ds_read_b64_tr_b16 v[44:45], v11 offset:24576\n"
    "v_mfma_f32_16x16x32_bf16 v[68:71], v[52:55], a[76:79], v[68:71]\n"
    "ds_read_b64_tr_b16 v[46:47], v11 offset:26624\n"
    "v_mfma_f32_16x16x32_bf16 v[76:79], v[52:55], a[140:143], v[76:79]\n"
    "v_cvt_pk_bf16_f32 v220, v128, v129\n"
    "v_mfma_f32_16x16x32_bf16 v[84:87], v[52:55], a[204:207], v[84:87]\n"
    "buffer_load_dwordx4 v6, s[44:47], s29 offen offset:2048 lds\n"
    "s_waitcnt lgkmcnt(8)\n"
    "v_mfma_f32_16x16x32_bf16 v[56:59], v[24:27], a[16:19], v[56:59]\n"
    "ds_read_b64_tr_b16 v[48:49], v10 offset:28672\n"
    "v_mfma_f32_16x16x32_bf16 v[64:67], v[24:27], a[80:83], v[64:67]\n"
    "ds_read_b64_tr_b16 v[50:51], v10 offset:30720\n"
    "v_cvt_pk_bf16_f32 v221, v130, v131\n"
    "v_mfma_f32_16x16x32_bf16 v[72:75], v[24:27], a[144:147], v[72:75]\n"
    "v_mfma_f32_16x16x32_bf16 v[80:83], v[24:27], a[208:211], v[80:83]\n"
    "buffer_load_dwordx4 v7, s[44:47], s29 offen offset:3072 lds\n"
    "v_mfma_f32_16x16x32_bf16 v[60:63], v[28:31], a[16:19], v[60:63]\n"
    "ds_read_b64_tr_b16 v[52:53], v11 offset:28672\n"
    "v_bfe_u32 v232, v16, 4, 1\n"
    "v_cvt_pk_bf16_f32 v222, v132, v133\n"
    "v_mfma_f32_16x16x32_bf16 v[68:71], v[28:31], a[80:83], v[68:71]\n"
    "ds_read_b64_tr_b16 v[54:55], v11 offset:30720\n"
    "v_mul_u32_u24 v232, s71, v232\n"
    "v_mfma_f32_16x16x32_bf16 v[76:79], v[28:31], a[144:147], v[76:79]\n"
    "v_and_b32 v233, 15, v16\n"
    "v_mfma_f32_16x16x32_bf16 v[84:87], v[28:31], a[208:211], v[84:87]\n"
    "v_lshl_add_u32 v232, v233, 2, v232\n"
    "v_cvt_pk_bf16_f32 v223, v134, v135\n"
    "s_waitcnt lgkmcnt(8)\n"
    "v_mfma_f32_16x16x32_bf16 v[56:59], v[32:35], a[20:23], v[56:59]\n"
    "v_add_u32 v232, s70, v232\n"
    "v_mfma_f32_16x16x32_bf16 v[64:67], v[32:35], a[84:87], v[64:67]\n"
    "global_load_dword v233, v232, s[60:61] sc1\n"
    "v_mfma_f32_16x16x32_bf16 v[72:75], v[32:35], a[148:151], v[72:75]\n"
    "s_mov_b32 s99, 4096\n"
    "v_mfma_f32_16x16x32_bf16 v[80:83], v[32:35], a[212:215], v[80:83]\n"
    "v_mfma_f32_16x16x32_bf16 v[60:63], v[36:39], a[20:23], v[60:63]\n"
    "v_mfma_f32_16x16x32_bf16 v[68:71], v[36:39], a[84:87], v[68:71]\n"
    "buffer_store_dwordx4 v[220:223], v21, s[80:83], s99 offen offset:1024 sc1\n"
    "v_mfma_f32_16x16x32_bf16 v[76:79], v[36:39], a[148:151], v[76:79]\n"
    "v_mfma_f32_16x16x32_bf16 v[84:87], v[36:39], a[212:215], v[84:87]\n"
    "s_waitcnt vmcnt(23) lgkmcnt(0)\n"
    "s_barrier\n"
    "v_mfma_f32_16x16x32_bf16 v[56:59], v[40:43], a[24:27], v[56:59]\n"
    "ds_read_b64_tr_b16 v[24:25], v10 offset:32768\n"
    "s_mov_b32 s29, 128\n"
    "v_mfma_f32_16x16x32_bf16 v[64:67], v[40:43], a[88:91], v[64:67]\n"
    "ds_read_b64_tr_b16 v[26:27], v10 offset:34816\n"
    "s_add_u32 m0, s28, 16384\n"
    "v_cvt_pk_bf16_f32 v224, v136, v137\n"
    "v_mfma_f32_16x16x32_bf16 v[72:75], v[40:43], a[152:155], v[72:75]\n"
    "ds_read_b64_tr_b16 v[28:29], v11 offset:32768\n"
    "buffer_load_dwordx4 v4, s[44:47], s29 offen offset:0 lds\n"
    "v_mfma_f32_16x16x32_bf16 v[80:83], v[40:43], a[216:219], v[80:83]\n"
    "ds_read_b64_tr_b16 v[30:31], v11 offset:34816\n"
    "v_mfma_f32_16x16x32_bf16 v[60:63], v[44:47], a[24:27], v[60:63]\n"
    "ds_read_b64_tr_b16 v[32:33], v10 offset:36864\n"
    "v_cvt_pk_bf16_f32 v225, v138, v139\n"
    "v_mfma_f32_16x16x32_bf16 v[68:71], v[44:47], a[88:91], v[68:71]\n"
    "ds_read_b64_tr_b16 v[34:35], v10 offset:38912\n"
    "v_mfma_f32_16x16x32_bf16 v[76:79], v[44:47], a[152:155], v[76:79]\n"
    "ds_read_b64_tr_b16 v[36:37], v11 offset:36864\n"
    "buffer_load_dwordx4 v5, s[44:47], s29 offen offset:1024 lds\n"
    "v_mfma_f32_16x16x32_bf16 v[84:87], v[44:47], a[216:219], v[84:87]\n"
    "ds_read_b64_tr_b16 v[38:39], v11 offset:38912\n"
    "v_cvt_pk_bf16_f32 v226, v140, v141\n"
    "v_mfma_f32_16x16x32_bf16 v[56:59], v[48:51], a[28:31], v[56:59]\n"
    "ds_read_b64_tr_b16 v[40:41], v10 offset:40960\n"
    "v_mfma_f32_16x16x32_bf16 v[64:67], v[48:51], a[92:95], v[64:67]\n"
    "ds_read_b64_tr_b16 v[42:43], v10 offset:43008\n"
    "v_mfma_f32_16x16x32_bf16 v[72:75], v[48:51], a[156:159], v[72:75]\n"
    "v_cvt_pk_bf16_f32 v227, v142, v143\n"
    "v_mfma_f32_16x16x32_bf16 v[80:83], v[48:51], a[220:223], v[80:83]\n"
    "buffer_load_dwordx4 v6, s[44:47], s29 offen offset:2048 lds\n"
    "v_mfma_f32_16x16x32_bf16 v[60:63], v[52:55], a[28:31], v[60:63]\n"
    "ds_read_b64_tr_b16 v[44:45], v11 offset:40960\n"
    "v_mfma_f32_16x16x32_bf16 v[68:71], v[52:55], a[92:95], v[68:71]\n"
    "ds_read_b64_tr_b16 v[46:47], v11 offset:43008\n"
    "s_mov_b32 s99, 12288\n"
    "v_mfma_f32_16x16x32_bf16 v[76:79], v[52:55], a[156:159], v[76:79]\n"
    "v_mfma_f32_16x16x32_bf16 v[84:87], v[52:55], a[220:223], v[84:87]\n"
    "buffer_load_dwordx4 v7, s[44:47], s29 offen offset:3072 lds\n"
    "s_waitcnt lgkmcnt(8)\n"
    "v_mfma_f32_16x16x32_bf16 v[56:59], v[24:27], a[32:35], v[56:59]\n"
    "ds_read_b64_tr_b16 v[48:49], v10 offset:45056\n"
    "v_mfma_f32_16x16x32_bf16 v[64:67], v[24:27], a[96:99], v[64:67]\n"
    "ds_read_b64_tr_b16 v[50:51], v10 offset:47104\n"
    "v_mfma_f32_16x16x32_bf16 v[72:75], v[24:27], a[160:163], v[72:75]\n"
    "v_mfma_f32_16x16x32_bf16 v[80:83], v[24:27], a[224:227], v[80:83]\n"
    "buffer_store_dwordx4 v[224:227], v21, s[80:83], s99 offen offset:0 sc1\n"
    "v_cvt_pk_bf16_f32 v228, v144, v145\n"
    "v_mfma_f32_16x16x32_bf16 v[60:63], v[28:31], a[32:35], v[60:63]\n"
    "ds_read_b64_tr_b16 v[52:53], v11 offset:45056\n"
    "v_mfma_f32_16x16x32_bf16 v[68:71], v[28:31], a[96:99], v[68:71]\n"
    "ds_read_b64_tr_b16 v[54:55], v11 offset:47104\n"
    "v_mfma_f32_16x16x32_bf16 v[76:79], v[28:31], a[160:163], v[76:79]\n"
    "v_mfma_f32_16x16x32_bf16 v[84:87], v[28:31], a[224:227], v[84:87]\n"
    "v_cvt_pk_bf16_f32 v229, v146, v147\n"
    "s_waitcnt lgkmcnt(8)\n"
    "v_mfma_f32_16x16x32_bf16 v[56:59], v[32:35], a[36:39], v[56:59]\n"
    "v_mfma_f32_16x16x32_bf16 v[64:67], v[32:35], a[100:103], v[64:67]\n"
    "v_mfma_f32_16x16x32_bf16 v[72:75], v[32:35], a[164:167], v[72:75]\n"
    "v_cvt_pk_bf16_f32 v230, v148, v149\n"
    "v_mfma_f32_16x16x32_bf16 v[80:83], v[32:35], a[228:231], v[80:83]\n"
    "v_mfma_f32_16x16x32_bf16 v[60:63], v[36:39], a[36:39], v[60:63]\n"
    "v_mfma_f32_16x16x32_bf16 v[68:71], v[36:39], a[100:103], v[68:71]\n"
    "v_cvt_pk_bf16_f32 v231, v150, v151\n"
    "v_mfma_f32_16x16x32_bf16 v[76:79], v[36:39], a[164:167], v[76:79]\n"
    "v_mfma_f32_16x16x32_bf16 v[84:87], v[36:39], a[228:231], v[84:87]\n"
    "s_waitcnt vmcnt(28) lgkmcnt(0)\n"
    "s_barrier\n"
    "v_mfma_f32_16x16x32_bf16 v[56:59], v[40:43], a[40:43], v[56:59]\n"
    "ds_read_b64_tr_b16 v[24:25], v10 offset:49152\n"
    "s_mov_b32 s29, 1048576\n"
    "s_mov_b32 s99, 12288\n"
    "v_mfma_f32_16x16x32_bf16 v[64:67], v[40:43], a[104:107], v[64:67]\n"
    "ds_read_b64_tr_b16 v[26:27], v10 offset:51200\n"
    "s_add_u32 m0, s28, 32768\n"
    "v_mfma_f32_16x16x32_bf16 v[72:75], v[40:43], a[168:171], v[72:75]\n"
    "ds_read_b64_tr_b16 v[28:29], v11 offset:49152\n"
    "buffer_load_dwordx4 v4, s[44:47], s29 offen offset:0 lds\n"
    "v_mfma_f32_16x16x32_bf16 v[80:83], v[40:43], a[232:235], v[80:83]\n"
    "ds_read_b64_tr_b16 v[30:31], v11 offset:51200\n"
    "v_mfma_f32_16x16x32_bf16 v[60:63], v[44:47], a[40:43], v[60:63]\n"
    "ds_read_b64_tr_b16 v[32:33], v10 offset:53248\n"
    "v_mfma_f32_16x16x32_bf16 v[68:71], v[44:47], a[104:107], v[68:71]\n"
    "ds_read_b64_tr_b16 v[34:35], v10 offset:55296\n"
    "buffer_store_dwordx4 v[228:231], v21, s[80:83], s99 offen offset:1024 sc1\n"
    "v_mfma_f32_16x16x32_bf16 v[76:79], v[44:47], a[168:171], v[76:79]\n"
    "ds_read_b64_tr_b16 v[36:37], v11 offset:53248\n"
    "v_cvt_pk_bf16_f32 v216, v152, v153\n"
    "v_mfma_f32_16x16x32_bf16 v[84:87], v[44:47], a[232:235], v[84:87]\n"
    "ds_read_b64_tr_b16 v[38:39], v11 offset:55296\n"
    "v_mfma_f32_16x16x32_bf16 v[56:59], v[48:51], a[44:47], v[56:59]\n"
    "ds_read_b64_tr_b16 v[40:41], v10 offset:57344\n"
    "v_mfma_f32_16x16x32_bf16 v[64:67], v[48:51], a[108:111], v[64:67]\n"
    "ds_read_b64_tr_b16 v[42:43], v10 offset:59392\n"
    "v_mfma_f32_16x16x32_bf16 v[72:75], v[48:51], a[172:175], v[72:75]\n"
    "buffer_load_dwordx4 v5, s[44:47], s29 offen offset:1024 lds\n"
    "v_cvt_pk_bf16_f32 v217, v154, v155\n"
    "v_mfma_f32_16x16x32_bf16 v[80:83], v[48:51], a[236:239], v[80:83]\n"
    "v_mfma_f32_16x16x32_bf16 v[60:63], v[52:55], a[44:47], v[60:63]\n"
    "ds_read_b64_tr_b16 v[44:45], v11 offset:57344\n"
    "v_mfma_f32_16x16x32_bf16 v[68:71], v[52:55], a[108:111], v[68:71]\n"
    "ds_read_b64_tr_b16 v[46:47], v11 offset:59392\n"
    "v_cvt_pk_bf16_f32 v218, v156, v157\n"
    "v_mfma_f32_16x16x32_bf16 v[76:79], v[52:55], a[172:175], v[76:79]\n"
    "buffer_load_dwordx4 v6, s[44:47], s29 offen offset:2048 lds\n"
    "v_mfma_f32_16x16x32_bf16 v[84:87], v[52:55], a[236:239], v[84:87]\n"
    "s_waitcnt lgkmcnt(8)\n"
    "v_mfma_f32_16x16x32_bf16 v[56:59], v[24:27], a[48:51], v[56:59]\n"
    "ds_read_b64_tr_b16 v[48:49], v10 offset:61440\n"
    "v_cvt_pk_bf16_f32 v219, v158, v159\n"
    "v_mfma_f32_16x16x32_bf16 v[64:67], v[24:27], a[112:115], v[64:67]\n"
    "ds_read_b64_tr_b16 v[50:51], v10 offset:63488\n"
    "v_mfma_f32_16x16x32_bf16 v[72:75], v[24:27], a[176:179], v[72:75]\n"
    "v_mfma_f32_16x16x32_bf16 v[80:83], v[24:27], a[240:243], v[80:83]\n"
    "buffer_load_dwordx4 v7, s[44:47], s29 offen offset:3072 lds\n"
    "s_mov_b32 s99, 4096\n"
    "v_mfma_f32_16x16x32_bf16 v[60:63], v[28:31], a[48:51], v[60:63]\n"
    "ds_read_b64_tr_b16 v[52:53], v11 offset:61440\n"
    "v_mfma_f32_16x16x32_bf16 v[68:71], v[28:31], a[112:115], v[68:71]\n"
    "ds_read_b64_tr_b16 v[54:55], v11 offset:63488\n"
    "v_mfma_f32_16x16x32_bf16 v[76:79], v[28:31], a[176:179], v[76:79]\n"
    "buffer_store_dwordx4 v[216:219], v21, s[80:83], s99 offen offset:2048 sc1\n"
    "v_mfma_f32_16x16x32_bf16 v[84:87], v[28:31], a[240:243], v[84:87]\n"
    "s_waitcnt lgkmcnt(8)\n"
    "v_mfma_f32_16x16x32_bf16 v[56:59], v[32:35], a[52:55], v[56:59]\n"
    "v_mfma_f32_16x16x32_bf16 v[64:67], v[32:35], a[116:119], v[64:67]\n"
    "v_cvt_pk_bf16_f32 v220, v160, v161\n"
    "v_mfma_f32_16x16x32_bf16 v[72:75], v[32:35], a[180:183], v[72:75]\n"
    "v_mfma_f32_16x16x32_bf16 v[80:83], v[32:35], a[244:247], v[80:83]\n"
    "v_mfma_f32_16x16x32_bf16 v[60:63], v[36:39], a[52:55], v[60:63]\n"
    "v_cvt_pk_bf16_f32 v221, v162, v163\n"
    "v_mfma_f32_16x16x32_bf16 v[68:71], v[36:39], a[116:119], v[68:71]\n"
    "v_mfma_f32_16x16x32_bf16 v[76:79], v[36:39], a[180:183], v[76:79]\n"
    "v_mfma_f32_16x16x32_bf16 v[84:87], v[36:39], a[244:247], v[84:87]\n"
    "s_waitcnt vmcnt(34) lgkmcnt(0)\n"
    "s_barrier\n"
    "v_mfma_f32_16x16x32_bf16 v[56:59], v[40:43], a[56:59], v[56:59]\n"
    "ds_read_b64_tr_b16 v[24:25], v12 offset:0\n"
    "s_mov_b32 s29, 1048704\n"
    "v_cvt_pk_bf16_f32 v222, v164, v165\n"
    "v_mfma_f32_16x16x32_bf16 v[64:67], v[40:43], a[120:123], v[64:67]\n"
    "ds_read_b64_tr_b16 v[26:27], v12 offset:2048\n"
    "s_add_u32 m0, s28, 49152\n"
    "v_mfma_f32_16x16x32_bf16 v[72:75], v[40:43], a[184:187], v[72:75]\n"
    "ds_read_b64_tr_b16 v[28:29], v13 offset:0\n"
    "buffer_load_dwordx4 v4, s[44:47], s29 offen offset:0 lds\n"
    "v_mfma_f32_16x16x32_bf16 v[80:83], v[40:43], a[248:251], v[80:83]\n"
    "ds_read_b64_tr_b16 v[30:31], v13 offset:2048\n"
    "v_cvt_pk_bf16_f32 v223, v166, v167\n"
    "v_mfma_f32_16x16x32_bf16 v[60:63], v[44:47], a[56:59], v[60:63]\n"
    "ds_read_b64_tr_b16 v[32:33], v12 offset:4096\n"
    "v_mfma_f32_16x16x32_bf16 v[68:71], v[44:47], a[120:123], v[68:71]\n"
    "ds_read_b64_tr_b16 v[34:35], v12 offset:6144\n"
    "v_mfma_f32_16x16x32_bf16 v[76:79], v[44:47], a[184:187], v[76:79]\n"
    "ds_read_b64_tr_b16 v[36:37], v13 offset:4096\n"
    "buffer_load_dwordx4 v5, s[44:47], s29 offen offset:1024 lds\n"
    "s_mov_b32 s99, 4096\n"
    "v_mfma_f32_16x16x32_bf16 v[84:87], v[44:47], a[248:251], v[84:87]\n"
    "ds_read_b64_tr_b16 v[38:39], v13 offset:6144\n"
    "v_mfma_f32_16x16x32_bf16 v[56:59], v[48:51], a[60:63], v[56:59]\n"
    "ds_read_b64_tr_b16 v[40:41], v12 offset:8192\n"
    "v_mfma_f32_16x16x32_bf16 v[64:67], v[48:51], a[124:127], v[64:67]\n"
    "ds_read_b64_tr_b16 v[42:43], v12 offset:10240\n"
    "v_mfma_f32_16x16x32_bf16 v[72:75], v[48:51], a[188:191], v[72:75]\n"
    "buffer_store_dwordx4 v[220:223], v21, s[80:83], s99 offen offset:3072 sc1\n"
    "v_mfma_f32_16x16x32_bf16 v[80:83], v[48:51], a[252:255], v[80:83]\n"
    "v_mfma_f32_16x16x32_bf16 v[60:63], v[52:55], a[60:63], v[60:63]\n"
    "ds_read_b64_tr_b16 v[44:45], v13 offset:8192\n"
    "v_mfma_f32_16x16x32_bf16 v[68:71], v[52:55], a[124:127], v[68:71]\n"
    "ds_read_b64_tr_b16 v[46:47], v13 offset:10240\n"
    "v_mfma_f32_16x16x32_bf16 v[76:79], v[52:55], a[188:191], v[76:79]\n"
    "v_mfma_f32_16x16x32_bf16 v[84:87], v[52:55], a[252:255], v[84:87]\n"
    "buffer_load_dwordx4 v6, s[44:47], s29 offen offset:2048 lds\n"
    "s_waitcnt lgkmcnt(8)\n"
    "v_mfma_f32_16x16x32_bf16 v[88:91], v[24:27], a[0:3], 0\n"
    "ds_read_b64_tr_b16 v[48:49], v12 offset:12288\n"
    "v_mfma_f32_16x16x32_bf16 v[96:99], v[24:27], a[64:67], 0\n"
    "ds_read_b64_tr_b16 v[50:51], v12 offset:14336\n"
    "v_mfma_f32_16x16x32_bf16 v[104:107], v[24:27], a[128:131], 0\n"
    "v_mfma_f32_16x16x32_bf16 v[112:115], v[24:27], a[192:195], 0\n"
    "buffer_load_dwordx4 v7, s[44:47], s29 offen offset:3072 lds\n"
    "v_mfma_f32_16x16x32_bf16 v[92:95], v[28:31], a[0:3], 0\n"
    "ds_read_b64_tr_b16 v[52:53], v13 offset:12288\n"
    "v_mfma_f32_16x16x32_bf16 v[100:103], v[28:31], a[64:67], 0\n"
    "ds_read_b64_tr_b16 v[54:55], v13 offset:14336\n"
    "v_mfma_f32_16x16x32_bf16 v[108:111], v[28:31], a[128:131], 0\n"
    "v_mfma_f32_16x16x32_bf16 v[116:119], v[28:31], a[192:195], 0\n"
    "s_waitcnt lgkmcnt(8)\n"
    "v_mfma_f32_16x16x32_bf16 v[88:91], v[32:35], a[4:7], v[88:91]\n"
    "v_mfma_f32_16x16x32_bf16 v[96:99], v[32:35], a[68:71], v[96:99]\n"
    "v_mfma_f32_16x16x32_bf16 v[104:107], v[32:35], a[132:135], v[104:107]\n"
    "v_mfma_f32_16x16x32_bf16 v[112:115], v[32:35], a[196:199], v[112:115]\n"
    "v_cvt_pk_bf16_f32 v224, v168, v169\n"
    "v_mfma_f32_16x16x32_bf16 v[92:95], v[36:39], a[4:7], v[92:95]\n"
    "v_mfma_f32_16x16x32_bf16 v[100:103], v[36:39], a[68:71], v[100:103]\n"
    "v_mfma_f32_16x16x32_bf16 v[108:111], v[36:39], a[132:135], v[108:111]\n"
    "v_cvt_pk_bf16_f32 v225, v170, v171\n"
    "v_mfma_f32_16x16x32_bf16 v[116:119], v[36:39], a[196:199], v[116:119]\n"
    "s_waitcnt lgkmcnt(4)\n"
    "v_mfma_f32_16x16x32_bf16 v[88:91], v[40:43], a[8:11], v[88:91]\n"
    "ds_read_b64_tr_b16 v[24:25], v12 offset:16384\n"
    "v_mfma_f32_16x16x32_bf16 v[96:99], v[40:43], a[72:75], v[96:99]\n"
    "ds_read_b64_tr_b16 v[26:27], v12 offset:18432\n"
    "v_cvt_pk_bf16_f32 v226, v172, v173\n"
    "v_mfma_f32_16x16x32_bf16 v[104:107], v[40:43], a[136:139], v[104:107]\n"
    "ds_read_b64_tr_b16 v[28:29], v13 offset:16384\n"
    "v_mfma_f32_16x16x32_bf16 v[112:115], v[40:43], a[200:203], v[112:115]\n"
    "ds_read_b64_tr_b16 v[30:31], v13 offset:18432\n"
    "v_mfma_f32_16x16x32_bf16 v[92:95], v[44:47], a[8:11], v[92:95]\n"
    "ds_read_b64_tr_b16 v[32:33], v12 offset:20480\n"
    "v_cvt_pk_bf16_f32 v227, v174, v175\n"
    "v_mfma_f32_16x16x32_bf16 v[100:103], v[44:47], a[72:75], v[100:103]\n"
    "ds_read_b64_tr_b16 v[34:35], v12 offset:22528\n"
    "v_mfma_f32_16x16x32_bf16 v[108:111], v[44:47], a[136:139], v[108:111]\n"
    "ds_read_b64_tr_b16 v[36:37], v13 offset:20480\n"
    "v_mfma_f32_16x16x32_bf16 v[116:119], v[44:47], a[200:203], v[116:119]\n"
    "ds_read_b64_tr_b16 v[38:39], v13 offset:22528\n"
    "s_mov_b32 s99, 12288\n"
    "s_waitcnt lgkmcnt(8)\n"
    "v_mfma_f32_16x16x32_bf16 v[88:91], v[48:51], a[12:15], v[88:91]\n"
    "ds_read_b64_tr_b16 v[40:41], v12 offset:24576\n"
    "v_mfma_f32_16x16x32_bf16 v[96:99], v[48:51], a[76:79], v[96:99]\n"
    "ds_read_b64_tr_b16 v[42:43], v12 offset:26624\n"
    "v_mfma_f32_16x16x32_bf16 v[104:107], v[48:51], a[140:143], v[104:107]\n"
    "buffer_store_dwordx4 v[224:227], v21, s[80:83], s99 offen offset:2048 sc1\n"
    "v_mfma_f32_16x16x32_bf16 v[112:115], v[48:51], a[204:207], v[112:115]\n"
    "v_mfma_f32_16x16x32_bf16 v[92:95], v[52:55], a[12:15], v[92:95]\n"
    "ds_read_b64_tr_b16 v[44:45], v13 offset:24576\n"
    "v_mfma_f32_16x16x32_bf16 v[100:103], v[52:55], a[76:79], v[100:103]\n"
    "ds_read_b64_tr_b16 v[46:47], v13 offset:26624\n"
    "v_mfma_f32_16x16x32_bf16 v[108:111], v[52:55], a[140:143], v[108:111]\n"
    "v_cvt_pk_bf16_f32 v228, v176, v177\n"
    "v_mfma_f32_16x16x32_bf16 v[116:119], v[52:55], a[204:207], v[116:119]\n"
    "s_waitcnt lgkmcnt(8)\n"
    "v_mfma_f32_16x16x32_bf16 v[88:91], v[24:27], a[16:19], v[88:91]\n"
    "ds_read_b64_tr_b16 v[48:49], v12 offset:28672\n"
    "v_mfma_f32_16x16x32_bf16 v[96:99], v[24:27], a[80:83], v[96:99]\n"
    "ds_read_b64_tr_b16 v[50:51], v12 offset:30720\n"
    "v_cvt_pk_bf16_f32 v229, v178, v179\n"
    "v_mfma_f32_16x16x32_bf16 v[104:107], v[24:27], a[144:147], v[104:107]\n"
    "v_mfma_f32_16x16x32_bf16 v[112:115], v[24:27], a[208:211], v[112:115]\n"
    "v_mfma_f32_16x16x32_bf16 v[92:95], v[28:31], a[16:19], v[92:95]\n"
    "ds_read_b64_tr_b16 v[52:53], v13 offset:28672\n"
    "v_cvt_pk_bf16_f32 v230, v180, v181\n"
    "v_mfma_f32_16x16x32_bf16 v[100:103], v[28:31], a[80:83], v[100:103]\n"
    "ds_read_b64_tr_b16 v[54:55], v13 offset:30720\n"
    "v_mfma_f32_16x16x32_bf16 v[108:111], v[28:31], a[144:147], v[108:111]\n"
    "v_mfma_f32_16x16x32_bf16 v[116:119], v[28:31], a[208:211], v[116:119]\n"
    "v_cvt_pk_bf16_f32 v231, v182, v183\n"
    "s_waitcnt lgkmcnt(8)\n"
    "v_mfma_f32_16x16x32_bf16 v[88:91], v[32:35], a[20:23], v[88:91]\n"
    "v_mfma_f32_16x16x32_bf16 v[96:99], v[32:35], a[84:87], v[96:99]\n"
    "v_mfma_f32_16x16x32_bf16 v[104:107], v[32:35], a[148:151], v[104:107]\n"
    "s_mov_b32 s99, 12288\n"
    "v_mfma_f32_16x16x32_bf16 v[112:115], v[32:35], a[212:215], v[112:115]\n"
    "v_mfma_f32_16x16x32_bf16 v[92:95], v[36:39], a[20:23], v[92:95]\n"
    "v_mfma_f32_16x16x32_bf16 v[100:103], v[36:39], a[84:87], v[100:103]\n"
    "buffer_store_dwordx4 v[228:231], v21, s[80:83], s99 offen offset:3072 sc1\n"
    "v_mfma_f32_16x16x32_bf16 v[108:111], v[36:39], a[148:151], v[108:111]\n"
    "v_mfma_f32_16x16x32_bf16 v[116:119], v[36:39], a[212:215], v[116:119]\n"
    "s_waitcnt vmcnt(14) lgkmcnt(0)\n"
    "s_barrier\n"
    "v_mfma_f32_16x16x32_bf16 v[88:91], v[40:43], a[24:27], v[88:91]\n"
    "ds_read_b64_tr_b16 v[24:25], v12 offset:32768\n"
    "v_mfma_f32_16x16x32_bf16 v[96:99], v[40:43], a[88:91], v[96:99]\n"
    "ds_read_b64_tr_b16 v[26:27], v12 offset:34816\n"
    "v_cvt_pk_bf16_f32 v216, v184, v185\n"
    "v_mfma_f32_16x16x32_bf16 v[104:107], v[40:43], a[152:155], v[104:107]\n"
    "ds_read_b64_tr_b16 v[28:29], v13 offset:32768\n"
    "v_mfma_f32_16x16x32_bf16 v[112:115], v[40:43], a[216:219], v[112:115]\n"
    "ds_read_b64_tr_b16 v[30:31], v13 offset:34816\n"
    "v_mfma_f32_16x16x32_bf16 v[92:95], v[44:47], a[24:27], v[92:95]\n"
    "ds_read_b64_tr_b16 v[32:33], v12 offset:36864\n"
    "v_cvt_pk_bf16_f32 v217, v186, v187\n"
    "v_mfma_f32_16x16x32_bf16 v[100:103], v[44:47], a[88:91], v[100:103]\n"
    "ds_read_b64_tr_b16 v[34:35], v12 offset:38912\n"
    "v_mfma_f32_16x16x32_bf16 v[108:111], v[44:47], a[152:155], v[108:111]\n"
    "ds_read_b64_tr_b16 v[36:37], v13 offset:36864\n"
    "v_mfma_f32_16x16x32_bf16 v[116:119], v[44:47], a[216:219], v[116:119]\n"
    "ds_read_b64_tr_b16 v[38:39], v13 offset:38912\n"
    "v_cvt_pk_bf16_f32 v218, v188, v189\n"
    "v_mfma_f32_16x16x32_bf16 v[88:91], v[48:51], a[28:31], v[88:91]\n"
    "ds_read_b64_tr_b16 v[40:41], v12 offset:40960\n"
    "v_mfma_f32_16x16x32_bf16 v[96:99], v[48:51], a[92:95], v[96:99]\n"
    "ds_read_b64_tr_b16 v[42:43], v12 offset:43008\n"
    "v_mfma_f32_16x16x32_bf16 v[104:107], v[48:51], a[156:159], v[104:107]\n"
    "v_cvt_pk_bf16_f32 v219, v190, v191\n"
    "v_mfma_f32_16x16x32_bf16 v[112:115], v[48:51], a[220:223], v[112:115]\n"
    "v_mfma_f32_16x16x32_bf16 v[92:95], v[52:55], a[28:31], v[92:95]\n"
    "ds_read_b64_tr_b16 v[44:45], v13 offset:40960\n"
    "v_mfma_f32_16x16x32_bf16 v[100:103], v[52:55], a[92:95], v[100:103]\n"
    "ds_read_b64_tr_b16 v[46:47], v13 offset:43008\n"
    "s_mov_b32 s99, 524288\n"
    "v_mfma_f32_16x16x32_bf16 v[108:111], v[52:55], a[156:159], v[108:111]\n"
    "v_mfma_f32_16x16x32_bf16 v[116:119], v[52:55], a[220:223], v[116:119]\n"
    "s_waitcnt lgkmcnt(8)\n"
    "v_mfma_f32_16x16x32_bf16 v[88:91], v[24:27], a[32:35], v[88:91]\n"
    "ds_read_b64_tr_b16 v[48:49], v12 offset:45056\n"
    "buffer_store_dwordx4 v[216:219], v21, s[80:83], s99 offen offset:0 sc1\n"
    "v_mfma_f32_16x16x32_bf16 v[96:99], v[24:27], a[96:99], v[96:99]\n"
    "ds_read_b64_tr_b16 v[50:51], v12 offset:47104\n"
    "v_mfma_f32_16x16x32_bf16 v[104:107], v[24:27], a[160:163], v[104:107]\n"
    "v_mfma_f32_16x16x32_bf16 v[112:115], v[24:27], a[224:227], v[112:115]\n"
    "v_cvt_pk_bf16_f32 v220, v192, v193\n"
    "v_mfma_f32_16x16x32_bf16 v[92:95], v[28:31], a[32:35], v[92:95]\n"
    "ds_read_b64_tr_b16 v[52:53], v13 offset:45056\n"
    "v_mfma_f32_16x16x32_bf16 v[100:103], v[28:31], a[96:99], v[100:103]\n"
    "ds_read_b64_tr_b16 v[54:55], v13 offset:47104\n"
    "v_mfma_f32_16x16x32_bf16 v[108:111], v[28:31], a[160:163], v[108:111]\n"
    "v_mfma_f32_16x16x32_bf16 v[116:119], v[28:31], a[224:227], v[116:119]\n"
    "v_cvt_pk_bf16_f32 v221, v194, v195\n"
    "s_waitcnt lgkmcnt(8)\n"
    "v_mfma_f32_16x16x32_bf16 v[88:91], v[32:35], a[36:39], v[88:91]\n"
    "v_mfma_f32_16x16x32_bf16 v[96:99], v[32:35], a[100:103], v[96:99]\n"
    "v_mfma_f32_16x16x32_bf16 v[104:107], v[32:35], a[164:167], v[104:107]\n"
    "v_cvt_pk_bf16_f32 v222, v196, v197\n"
    "v_mfma_f32_16x16x32_bf16 v[112:115], v[32:35], a[228:231], v[112:115]\n"
    "v_mfma_f32_16x16x32_bf16 v[92:95], v[36:39], a[36:39], v[92:95]\n"
    "v_mfma_f32_16x16x32_bf16 v[100:103], v[36:39], a[100:103], v[100:103]\n"
    "v_cvt_pk_bf16_f32 v223, v198, v199\n"
    "v_mfma_f32_16x16x32_bf16 v[108:111], v[36:39], a[164:167], v[108:111]\n"
    "v_mfma_f32_16x16x32_bf16 v[116:119], v[36:39], a[228:231], v[116:119]\n"
    "s_waitcnt lgkmcnt(4)\n"
    "v_mfma_f32_16x16x32_bf16 v[88:91], v[40:43], a[40:43], v[88:91]\n"
    "ds_read_b64_tr_b16 v[24:25], v12 offset:49152\n"
    "s_waitcnt vmcnt(15)\n"
    "v_cmp_ne_u32 vcc, s94, v233\n"
    "s_mov_b32 s30, 0x400000\n"
    "s_nop 1\n"
    "s_cbranch_vccz L_phk_%=_x\n"
    "L_phk_%=:\n"
    "s_sleep 2\n"
    "global_load_dword v233, v232, s[60:61] sc1\n"
    "s_waitcnt vmcnt(0)\n"
    "v_cmp_ne_u32 vcc, s94, v233\n"
    "s_sub_u32 s30, s30, 1\n"
    "s_cbranch_scc1 L_phk_%=_x\n"
    "s_nop 0\n"
    "s_cbranch_vccnz L_phk_%=\n"
    "L_phk_%=_x:\n"
    "s_mov_b32 s99, 524288\n"
    "v_mfma_f32_16x16x32_bf16 v[96:99], v[40:43], a[104:107], v[96:99]\n"
    "ds_read_b64_tr_b16 v[26:27], v12 offset:51200\n"
    "v_mfma_f32_16x16x32_bf16 v[104:107], v[40:43], a[168:171], v[104:107]\n"
    "ds_read_b64_tr_b16 v[28:29], v13 offset:49152\n"
    "v_mfma_f32_16x16x32_bf16 v[112:115], v[40:43], a[232:235], v[112:115]\n"
    "ds_read_b64_tr_b16 v[30:31], v13 offset:51200\n"
    "buffer_store_dwordx4 v[220:223], v21, s[80:83], s99 offen offset:1024 sc1\n"
    "v_mfma_f32_16x16x32_bf16 v[92:95], v[44:47], a[40:43], v[92:95]\n"
    "ds_read_b64_tr_b16 v[32:33], v12 offset:53248\n"
    "v_mfma_f32_16x16x32_bf16 v[100:103], v[44:47], a[104:107], v[100:103]\n"
    "ds_read_b64_tr_b16 v[34:35], v12 offset:55296\n"
    "v_mfma_f32_16x16x32_bf16 v[108:111], v[44:47], a[168:171], v[108:111]\n"
    "ds_read_b64_tr_b16 v[36:37], v13 offset:53248\n"
    "v_cvt_pk_bf16_f32 v224, v200, v201\n"
    "v_mfma_f32_16x16x32_bf16 v[116:119], v[44:47], a[232:235], v[116:119]\n"
    "ds_read_b64_tr_b16 v[38:39], v13 offset:55296\n"
    "s_waitcnt lgkmcnt(8)\n"
    "v_mfma_f32_16x16x32_bf16 v[88:91], v[48:51], a[44:47], v[88:91]\n"
    "ds_read_b64_tr_b16 v[40:41], v12 offset:57344\n"
    "v_mfma_f32_16x16x32_bf16 v[96:99], v[48:51], a[108:111], v[96:99]\n"
    "ds_read_b64_tr_b16 v[42:43], v12 offset:59392\n"
    "v_mfma_f32_16x16x32_bf16 v[104:107], v[48:51], a[172:175], v[104:107]\n"
    "v_cvt_pk_bf16_f32 v225, v202, v203\n"
    "v_mfma_f32_16x16x32_bf16 v[112:115], v[48:51], a[236:239], v[112:115]\n"
    "v_mfma_f32_16x16x32_bf16 v[92:95], v[52:55], a[44:47], v[92:95]\n"
    "ds_read_b64_tr_b16 v[44:45], v13 offset:57344\n"
    "v_mfma_f32_16x16x32_bf16 v[100:103], v[52:55], a[108:111], v[100:103]\n"
    "ds_read_b64_tr_b16 v[46:47], v13 offset:59392\n"
    "v_cvt_pk_bf16_f32 v226, v204, v205\n"
    "v_mfma_f32_16x16x32_bf16 v[108:111], v[52:55], a[172:175], v[108:111]\n"
    "v_mfma_f32_16x16x32_bf16 v[116:119], v[52:55], a[236:239], v[116:119]\n"
    "s_waitcnt lgkmcnt(8)\n"
    "v_mfma_f32_16x16x32_bf16 v[88:91], v[24:27], a[48:51], v[88:91]\n"
    "ds_read_b64_tr_b16 v[48:49], v12 offset:61440\n"
    "v_cvt_pk_bf16_f32 v227, v206, v207\n"
    "v_mfma_f32_16x16x32_bf16 v[96:99], v[24:27], a[112:115], v[96:99]\n"
    "ds_read_b64_tr_b16 v[50:51], v12 offset:63488\n"
    "v_mfma_f32_16x16x32_bf16 v[104:107], v[24:27], a[176:179], v[104:107]\n"
    "v_mfma_f32_16x16x32_bf16 v[112:115], v[24:27], a[240:243], v[112:115]\n"
    "s_mov_b32 s99, 532480\n"
    "v_mfma_f32_16x16x32_bf16 v[92:95], v[28:31], a[48:51], v[92:95]\n"
    "ds_read_b64_tr_b16 v[52:53], v13 offset:61440\n"
    "v_mfma_f32_16x16x32_bf16 v[100:103], v[28:31], a[112:115], v[100:103]\n"
    "ds_read_b64_tr_b16 v[54:55], v13 offset:63488\n"
    "v_mfma_f32_16x16x32_bf16 v[108:111], v[28:31], a[176:179], v[108:111]\n"
    "buffer_store_dwordx4 v[224:227], v21, s[80:83], s99 offen offset:0 sc1\n"
    "v_mfma_f32_16x16x32_bf16 v[116:119], v[28:31], a[240:243], v[116:119]\n"
    "s_waitcnt lgkmcnt(8)\n"
    "v_mfma_f32_16x16x32_bf16 v[88:91], v[32:35], a[52:55], v[88:91]\n"
    "v_mfma_f32_16x16x32_bf16 v[96:99], v[32:35], a[116:119], v[96:99]\n"
    "v_cvt_pk_bf16_f32 v228, v208, v209\n"
    "v_mfma_f32_16x16x32_bf16 v[104:107], v[32:35], a[180:183], v[104:107]\n"
    "v_mfma_f32_16x16x32_bf16 v[112:115], v[32:35], a[244:247], v[112:115]\n"
    "v_mfma_f32_16x16x32_bf16 v[92:95], v[36:39], a[52:55], v[92:95]\n"
    "v_cvt_pk_bf16_f32 v229, v210, v211\n"
    "v_mfma_f32_16x16x32_bf16 v[100:103], v[36:39], a[116:119], v[100:103]\n"
    "v_mfma_f32_16x16x32_bf16 v[108:111], v[36:39], a[180:183], v[108:111]\n"
    "v_mfma_f32_16x16x32_bf16 v[116:119], v[36:39], a[244:247], v[116:119]\n"
    "s_waitcnt lgkmcnt(4)\n"
    "v_mfma_f32_16x16x32_bf16 v[88:91], v[40:43], a[56:59], v[88:91]\n"
    "ds_read_b64_tr_b16 v[24:25], v14 offset:0\n"
    "v_cvt_pk_bf16_f32 v230, v212, v213\n"
    "v_mfma_f32_16x16x32_bf16 v[96:99], v[40:43], a[120:123], v[96:99]\n"
    "ds_read_b64_tr_b16 v[26:27], v14 offset:2048\n"
    "v_mfma_f32_16x16x32_bf16 v[104:107], v[40:43], a[184:187], v[104:107]\n"
    "ds_read_b64_tr_b16 v[28:29], v15 offset:0\n"
    "v_mfma_f32_16x16x32_bf16 v[112:115], v[40:43], a[248:251], v[112:115]\n"
    "ds_read_b64_tr_b16 v[30:31], v15 offset:2048\n"
    "v_cvt_pk_bf16_f32 v231, v214, v215\n"
    "v_mfma_f32_16x16x32_bf16 v[92:95], v[44:47], a[56:59], v[92:95]\n"
    "ds_read_b64_tr_b16 v[32:33], v14 offset:4096\n"
    "v_mfma_f32_16x16x32_bf16 v[100:103], v[44:47], a[120:123], v[100:103]\n"
    "ds_read_b64_tr_b16 v[34:35], v14 offset:6144\n"
    "v_mfma_f32_16x16x32_bf16 v[108:111], v[44:47], a[184:187], v[108:111]\n"
    "ds_read_b64_tr_b16 v[36:37], v15 offset:4096\n"
    "s_mov_b32 s99, 532480\n"
    "v_mfma_f32_16x16x32_bf16 v[116:119], v[44:47], a[248:251], v[116:119]\n"
    "ds_read_b64_tr_b16 v[38:39], v15 offset:6144\n"
    "s_waitcnt lgkmcnt(8)\n"
    "v_mfma_f32_16x16x32_bf16 v[88:91], v[48:51], a[60:63], v[88:91]\n"
    "ds_read_b64_tr_b16 v[40:41], v14 offset:8192\n"
    "v_mfma_f32_16x16x32_bf16 v[96:99], v[48:51], a[124:127], v[96:99]\n"
    "ds_read_b64_tr_b16 v[42:43], v14 offset:10240\n"
    "buffer_store_dwordx4 v[228:231], v21, s[80:83], s99 offen offset:1024 sc1\n"
    "v_mfma_f32_16x16x32_bf16 v[104:107], v[48:51], a[188:191], v[104:107]\n"
    "v_mfma_f32_16x16x32_bf16 v[112:115], v[48:51], a[252:255], v[112:115]\n"
    "v_mfma_f32_16x16x32_bf16 v[92:95], v[52:55], a[60:63], v[92:95]\n"
    "ds_read_b64_tr_b16 v[44:45], v15 offset:8192\n"
    "v_mfma_f32_16x16x32_bf16 v[100:103], v[52:55], a[124:127], v[100:103]\n"
    "ds_read_b64_tr_b16 v[46:47], v15 offset:10240\n"
    "v_mfma_f32_16x16x32_bf16 v[108:111], v[52:55], a[188:191], v[108:111]\n"
    "v_mfma_f32_16x16x32_bf16 v[116:119], v[52:55], a[252:255], v[116:119]\n"
    "s_waitcnt lgkmcnt(8)\n"
    "v_mfma_f32_16x16x32_bf16 v[184:187], v[24:27], a[0:3], 0\n"
    "ds_read_b64_tr_b16 v[48:49], v14 offset:12288\n"
    "v_mfma_f32_16x16x32_bf16 v[192:195], v[24:27], a[64:67], 0\n"
    "ds_read_b64_tr_b16 v[50:51], v14 offset:14336\n"
    "v_mfma_f32_16x16x32_bf16 v[200:203], v[24:27], a[128:131], 0\n"
    "v_mfma_f32_16x16x32_bf16 v[208:211], v[24:27], a[192:195], 0\n"
    "v_mfma_f32_16x16x32_bf16 v[188:191], v[28:31], a[0:3], 0\n"
    "ds_read_b64_tr_b16 v[52:53], v15 offset:12288\n"
    "v_mfma_f32_16x16x32_bf16 v[196:199], v[28:31], a[64:67], 0\n"
    "ds_read_b64_tr_b16 v[54:55], v15 offset:14336\n"
    "v_mfma_f32_16x16x32_bf16 v[204:207], v[28:31], a[128:131], 0\n"
    "v_mfma_f32_16x16x32_bf16 v[212:215], v[28:31], a[192:195], 0\n"
    "s_waitcnt lgkmcnt(8)\n"
    "v_mfma_f32_16x16x32_bf16 v[184:187], v[32:35], a[4:7], v[184:187]\n"
    "s_mov_b32 s88, 0\n"
    "v_mfma_f32_16x16x32_bf16 v[192:195], v[32:35], a[68:71], v[192:195]\n"
    "buffer_load_dwordx4 a[0:3], v234, s[40:43], s88 offen offset:0 sc1\n"
    "v_mfma_f32_16x16x32_bf16 v[200:203], v[32:35], a[132:135], v[200:203]\n"
    "v_cvt_pk_bf16_f32 v216, v56, v57\n"
    "v_mfma_f32_16x16x32_bf16 v[208:211], v[32:35], a[196:199], v[208:211]\n"
    "v_mfma_f32_16x16x32_bf16 v[188:191], v[36:39], a[4:7], v[188:191]\n"
    "v_cvt_pk_bf16_f32 v217, v58, v59\n"
    "v_mfma_f32_16x16x32_bf16 v[196:199], v[36:39], a[68:71], v[196:199]\n"
    "buffer_load_dwordx4 a[64:67], v234, s[40:43], s88 offen offset:1024 sc1\n"
    "v_mfma_f32_16x16x32_bf16 v[204:207], v[36:39], a[132:135], v[204:207]\n"
    "v_mfma_f32_16x16x32_bf16 v[212:215], v[36:39], a[196:199], v[212:215]\n"
    "v_cvt_pk_bf16_f32 v218, v60, v61\n"
    "s_waitcnt vmcnt(20) lgkmcnt(0)\n"
    "s_barrier\n"
    "v_mfma_f32_16x16x32_bf16 v[184:187], v[40:43], a[8:11], v[184:187]\n"
    "ds_read_b64_tr_b16 v[24:25], v14 offset:16384\n"
    "v_mfma_f32_16x16x32_bf16 v[192:195], v[40:43], a[72:75], v[192:195]\n"
    "ds_read_b64_tr_b16 v[26:27], v14 offset:18432\n"
    "v_cvt_pk_bf16_f32 v219, v62, v63\n"
    "v_mfma_f32_16x16x32_bf16 v[200:203], v[40:43], a[136:139], v[200:203]\n"
    "ds_read_b64_tr_b16 v[28:29], v15 offset:16384\n"
    "buffer_load_dwordx4 a[128:131], v235, s[40:43], s88 offen offset:0 sc1\n"
    "v_mfma_f32_16x16x32_bf16 v[208:211], v[40:43], a[200:203], v[208:211]\n"
    "ds_read_b64_tr_b16 v[30:31], v15 offset:18432\n"
    "s_mov_b32 s99, 524288\n"
    "v_mfma_f32_16x16x32_bf16 v[188:191], v[44:47], a[8:11], v[188:191]\n"
    "ds_read_b64_tr_b16 v[32:33], v14 offset:20480\n"
    "v_mfma_f32_16x16x32_bf16 v[196:199], v[44:47], a[72:75], v[196:199]\n"
    "ds_read_b64_tr_b16 v[34:35], v14 offset:22528\n"
    "v_mfma_f32_16x16x32_bf16 v[204:207], v[44:47], a[136:139], v[204:207]\n"
    "ds_read_b64_tr_b16 v[36:37], v15 offset:20480\n"
    "buffer_load_dwordx4 a[192:195], v235, s[40:43], s88 offen offset:1024 sc1\n"
    "v_mfma_f32_16x16x32_bf16 v[212:215], v[44:47], a[200:203], v[212:215]\n"
    "ds_read_b64_tr_b16 v[38:39], v15 offset:22528\n"
    "s_mov_b32 s88, 8192\n"
    "v_mfma_f32_16x16x32_bf16 v[184:187], v[48:51], a[12:15], v[184:187]\n"
    "ds_read_b64_tr_b16 v[40:41], v14 offset:24576\n"
    "v_mfma_f32_16x16x32_bf16 v[192:195], v[48:51], a[76:79], v[192:195]\n"
    "ds_read_b64_tr_b16 v[42:43], v14 offset:26624\n"
    "v_mfma_f32_16x16x32_bf16 v[200:203], v[48:51], a[140:143], v[200:203]\n"
    "buffer_store_dwordx4 v[216:219], v21, s[80:83], s99 offen offset:2048 sc1\n"
    "v_cvt_pk_bf16_f32 v220, v64, v65\n"
    "v_cvt_pk_bf16_f32 v221, v66, v67\n"
    "v_mfma_f32_16x16x32_bf16 v[208:211], v[48:51], a[204:207], v[208:211]\n"
    "v_mfma_f32_16x16x32_bf16 v[188:191], v[52:55], a[12:15], v[188:191]\n"
    "ds_read_b64_tr_b16 v[44:45], v15 offset:24576\n"
    "v_mfma_f32_16x16x32_bf16 v[196:199], v[52:55], a[76:79], v[196:199]\n"
    "ds_read_b64_tr_b16 v[46:47], v15 offset:26624\n"
    "v_cvt_pk_bf16_f32 v222, v68, v69\n"
    "v_mfma_f32_16x16x32_bf16 v[204:207], v[52:55], a[140:143], v[204:207]\n"
    "v_mfma_f32_16x16x32_bf16 v[212:215], v[52:55], a[204:207], v[212:215]\n"
    "buffer_load_dwordx4 a[4:7], v234, s[40:43], s88 offen offset:0 sc1\n"
    "v_cvt_pk_bf16_f32 v223, v70, v71\n"
    "s_waitcnt lgkmcnt(8)\n"
    "v_mfma_f32_16x16x32_bf16 v[184:187], v[24:27], a[16:19], v[184:187]\n"
    "ds_read_b64_tr_b16 v[48:49], v14 offset:28672\n"
    "v_mfma_f32_16x16x32_bf16 v[192:195], v[24:27], a[80:83], v[192:195]\n"
    "ds_read_b64_tr_b16 v[50:51], v14 offset:30720\n"
    "v_mfma_f32_16x16x32_bf16 v[200:203], v[24:27], a[144:147], v[200:203]\n"
    "s_mov_b32 s99, 524288\n"
    "v_mfma_f32_16x16x32_bf16 v[208:211], v[24:27], a[208:211], v[208:211]\n"
    "buffer_load_dwordx4 a[68:71], v234, s[40:43], s88 offen offset:1024 sc1\n"
    "v_mfma_f32_16x16x32_bf16 v[188:191], v[28:31], a[16:19], v[188:191]\n"
    "ds_read_b64_tr_b16 v[52:53], v15 offset:28672\n"
    "v_mfma_f32_16x16x32_bf16 v[196:199], v[28:31], a[80:83], v[196:199]\n"
    "ds_read_b64_tr_b16 v[54:55], v15 offset:30720\n"
    "v_mfma_f32_16x16x32_bf16 v[204:207], v[28:31], a[144:147], v[204:207]\n"
    "v_mfma_f32_16x16x32_bf16 v[212:215], v[28:31], a[208:211], v[212:215]\n"
    "buffer_store_dwordx4 v[220:223], v21, s[80:83], s99 offen offset:3072 sc1\n"
    "v_cvt_pk_bf16_f32 v224, v72, v73\n"
    "s_waitcnt lgkmcnt(8)\n"
    "v_mfma_f32_16x16x32_bf16 v[184:187], v[32:35], a[20:23], v[184:187]\n"
    "v_mfma_f32_16x16x32_bf16 v[192:195], v[32:35], a[84:87], v[192:195]\n"
    "v_cvt_pk_bf16_f32 v225, v74, v75\n"
    "v_mfma_f32_16x16x32_bf16 v[200:203], v[32:35], a[148:151], v[200:203]\n"
    "v_mfma_f32_16x16x32_bf16 v[208:211], v[32:35], a[212:215], v[208:211]\n"
    "v_cvt_pk_bf16_f32 v226, v76, v77\n"
    "v_mfma_f32_16x16x32_bf16 v[188:191], v[36:39], a[20:23], v[188:191]\n"
    "buffer_load_dwordx4 a[132:135], v235, s[40:43], s88 offen offset:0 sc1\n"
    "v_mfma_f32_16x16x32_bf16 v[196:199], v[36:39], a[84:87], v[196:199]\n"
    "v_cvt_pk_bf16_f32 v227, v78, v79\n"
    "v_mfma_f32_16x16x32_bf16 v[204:207], v[36:39], a[148:151], v[204:207]\n"
    "v_mfma_f32_16x16x32_bf16 v[212:215], v[36:39], a[212:215], v[212:215]\n"
    "s_waitcnt vmcnt(27) lgkmcnt(0)\n"
    "s_barrier\n"
    "v_mfma_f32_16x16x32_bf16 v[184:187], v[40:43], a[24:27], v[184:187]\n"
    "ds_read_b64_tr_b16 v[24:25], v14 offset:32768\n"
    "buffer_load_dwordx4 a[196:199], v235, s[40:43], s88 offen offset:1024 sc1\n"
    "s_mov_b32 s99, 532480\n"
    "v_mfma_f32_16x16x32_bf16 v[192:195], v[40:43], a[88:91], v[192:195]\n"
    "ds_read_b64_tr_b16 v[26:27], v14 offset:34816\n"
    "s_mov_b32 s29, 2097152\n"
    "v_mfma_f32_16x16x32_bf16 v[200:203], v[40:43], a[152:155], v[200:203]\n"
    "ds_read_b64_tr_b16 v[28:29], v15 offset:32768\n"
    "s_add_u32 m0, s28, 65536\n"
    "v_mfma_f32_16x16x32_bf16 v[208:211], v[40:43], a[216:219], v[208:211]\n"
    "ds_read_b64_tr_b16 v[30:31], v15 offset:34816\n"
    "v_mfma_f32_16x16x32_bf16 v[188:191], v[44:47], a[24:27], v[188:191]\n"
    "ds_read_b64_tr_b16 v[32:33], v14 offset:36864\n"
    "buffer_store_dwordx4 v[224:227], v21, s[80:83], s99 offen offset:2048 sc1\n"
    "v_cvt_pk_bf16_f32 v228, v80, v81\n"
    "v_mfma_f32_16x16x32_bf16 v[196:199], v[44:47], a[88:91], v[196:199]\n"
    "ds_read_b64_tr_b16 v[34:35], v14 offset:38912\n"
    "v_mfma_f32_16x16x32_bf16 v[204:207], v[44:47], a[152:155], v[204:207]\n"
    "ds_read_b64_tr_b16 v[36:37], v15 offset:36864\n"
    "v_mfma_f32_16x16x32_bf16 v[212:215], v[44:47], a[216:219], v[212:215]\n"
    "ds_read_b64_tr_b16 v[38:39], v15 offset:38912\n"
    "v_cvt_pk_bf16_f32 v229, v82, v83\n"
    "v_mfma_f32_16x16x32_bf16 v[184:187], v[48:51], a[28:31], v[184:187]\n"
    "ds_read_b64_tr_b16 v[40:41], v14 offset:40960\n"
    "v_mfma_f32_16x16x32_bf16 v[192:195], v[48:51], a[92:95], v[192:195]\n"
    "ds_read_b64_tr_b16 v[42:43], v14 offset:43008\n"
    "buffer_load_dwordx4 v4, s[44:47], s29 offen offset:0 lds\n"
    "v_cvt_pk_bf16_f32 v230, v84, v85\n"
    "v_mfma_f32_16x16x32_bf16 v[200:203], v[48:51], a[156:159], v[200:203]\n"
    "v_mfma_f32_16x16x32_bf16 v[208:211], v[48:51], a[220:223], v[208:211]\n"
    "v_mfma_f32_16x16x32_bf16 v[188:191], v[52:55], a[28:31], v[188:191]\n"
    "ds_read_b64_tr_b16 v[44:45], v15 offset:40960\n"
    "v_cvt_pk_bf16_f32 v231, v86, v87\n"
    "v_mfma_f32_16x16x32_bf16 v[196:199], v[52:55], a[92:95], v[196:199]\n"
    "ds_read_b64_tr_b16 v[46:47], v15 offset:43008\n"
    "buffer_load_dwordx4 v5, s[44:47], s29 offen offset:1024 lds\n"
    "v_mfma_f32_16x16x32_bf16 v[204:207], v[52:55], a[156:159], v[204:207]\n"
    "s_mov_b32 s99, 532480\n"
    "v_mfma_f32_16x16x32_bf16 v[212:215], v[52:55], a[220:223], v[212:215]\n"
    "s_waitcnt lgkmcnt(8)\n"
    "v_mfma_f32_16x16x32_bf16 v[184:187], v[24:27], a[32:35], v[184:187]\n"
    "ds_read_b64_tr_b16 v[48:49], v14 offset:45056\n"
    "v_mfma_f32_16x16x32_bf16 v[192:195], v[24:27], a[96:99], v[192:195]\n"
    "ds_read_b64_tr_b16 v[50:51], v14 offset:47104\n"
    "buffer_store_dwordx4 v[228:231], v21, s[80:83], s99 offen offset:3072 sc1\n"
    "v_mfma_f32_16x16x32_bf16 v[200:203], v[24:27], a[160:163], v[200:203]\n"
    "v_mfma_f32_16x16x32_bf16 v[208:211], v[24:27], a[224:227], v[208:211]\n"
    "v_cvt_pk_bf16_f32 v216, v88, v89\n"
    "v_mfma_f32_16x16x32_bf16 v[188:191], v[28:31], a[32:35], v[188:191]\n"
    "ds_read_b64_tr_b16 v[52:53], v15 offset:45056\n"
    "v_mfma_f32_16x16x32_bf16 v[196:199], v[28:31], a[96:99], v[196:199]\n"
    "ds_read_b64_tr_b16 v[54:55], v15 offset:47104\n"
    "v_cvt_pk_bf16_f32 v217, v90, v91\n"
    "v_mfma_f32_16x16x32_bf16 v[204:207], v[28:31], a[160:163], v[204:207]\n"
    "buffer_load_dwordx4 v6, s[44:47], s29 offen offset:2048 lds\n"
    "v_mfma_f32_16x16x32_bf16 v[212:215], v[28:31], a[224:227], v[212:215]\n"
    "v_cvt_pk_bf16_f32 v218, v92, v93\n"
    "s_waitcnt lgkmcnt(8)\n"
    "v_mfma_f32_16x16x32_bf16 v[184:187], v[32:35], a[36:39], v[184:187]\n"
    "v_mfma_f32_16x16x32_bf16 v[192:195], v[32:35], a[100:103], v[192:195]\n"
    "v_mfma_f32_16x16x32_bf16 v[200:203], v[32:35], a[164:167], v[200:203]\n"
    "buffer_load_dwordx4 v7, s[44:47], s29 offen offset:3072 lds\n"
    "v_cvt_pk_bf16_f32 v219, v94, v95\n"
    "v_mfma_f32_16x16x32_bf16 v[208:211], v[32:35], a[228:231], v[208:211]\n"
    "s_mov_b32 s88, 16384\n"
    "v_mfma_f32_16x16x32_bf16 v[188:191], v[36:39], a[36:39], v[188:191]\n"
    "s_mov_b32 s99, 528384\n"
    "v_mfma_f32_16x16x32_bf16 v[196:199], v[36:39], a[100:103], v[196:199]\n"
    "v_mfma_f32_16x16x32_bf16 v[204:207], v[36:39], a[164:167], v[204:207]\n"
    "buffer_store_dwordx4 v[216:219], v21, s[80:83], s99 offen offset:0 sc1\n"
    "v_mfma_f32_16x16x32_bf16 v[212:215], v[36:39], a[228:231], v[212:215]\n"
    "s_waitcnt vmcnt(35) lgkmcnt(0)\n"
    "s_barrier\n"
    "v_mfma_f32_16x16x32_bf16 v[184:187], v[40:43], a[40:43], v[184:187]\n"
    "ds_read_b64_tr_b16 v[24:25], v14 offset:49152\n"
    "v_mfma_f32_16x16x32_bf16 v[192:195], v[40:43], a[104:107], v[192:195]\n"
    "ds_read_b64_tr_b16 v[26:27], v14 offset:51200\n"
    "v_cvt_pk_bf16_f32 v220, v96, v97\n"
    "v_mfma_f32_16x16x32_bf16 v[200:203], v[40:43], a[168:171], v[200:203]\n"
    "ds_read_b64_tr_b16 v[28:29], v15 offset:49152\n"
    "v_mfma_f32_16x16x32_bf16 v[208:211], v[40:43], a[232:235], v[208:211]\n"
    "ds_read_b64_tr_b16 v[30:31], v15 offset:51200\n"
    "buffer_load_dwordx4 a[8:11], v234, s[40:43], s88 offen offset:0 sc1\n"
    "v_cvt_pk_bf16_f32 v221, v98, v99\n"
    "v_mfma_f32_16x16x32_bf16 v[188:191], v[44:47], a[40:43], v[188:191]\n"
    "ds_read_b64_tr_b16 v[32:33], v14 offset:53248\n"
    "v_mfma_f32_16x16x32_bf16 v[196:199], v[44:47], a[104:107], v[196:199]\n"
    "ds_read_b64_tr_b16 v[34:35], v14 offset:55296\n"
    "v_mfma_f32_16x16x32_bf16 v[204:207], v[44:47], a[168:171], v[204:207]\n"
    "ds_read_b64_tr_b16 v[36:37], v15 offset:53248\n"
    "v_cvt_pk_bf16_f32 v222, v100, v101\n"
    "v_mfma_f32_16x16x32_bf16 v[212:215], v[44:47], a[232:235], v[212:215]\n"
    "ds_read_b64_tr_b16 v[38:39], v15 offset:55296\n"
    "buffer_load_dwordx4 a[72:75], v234, s[40:43], s88 offen offset:1024 sc1\n"
    "v_mfma_f32_16x16x32_bf16 v[184:187], v[48:51], a[44:47], v[184:187]\n"
    "ds_read_b64_tr_b16 v[40:41], v14 offset:57344\n"
    "v_cvt_pk_bf16_f32 v223, v102, v103\n"
    "v_mfma_f32_16x16x32_bf16 v[192:195], v[48:51], a[108:111], v[192:195]\n"
    "ds_read_b64_tr_b16 v[42:43], v14 offset:59392\n"
    "v_mfma_f32_16x16x32_bf16 v[200:203], v[48:51], a[172:175], v[200:203]\n"
    "s_mov_b32 s99, 528384\n"
    "v_mfma_f32_16x16x32_bf16 v[208:211], v[48:51], a[236:239], v[208:211]\n"
    "v_mfma_f32_16x16x32_bf16 v[188:191], v[52:55], a[44:47], v[188:191]\n"
    "ds_read_b64_tr_b16 v[44:45], v15 offset:57344\n"
    "buffer_load_dwordx4 a[136:139], v235, s[40:43], s88 offen offset:0 sc1\n"
    "v_mfma_f32_16x16x32_bf16 v[196:199], v[52:55], a[108:111], v[196:199]\n"
    "ds_read_b64_tr_b16 v[46:47], v15 offset:59392\n"
    "v_mfma_f32_16x16x32_bf16 v[204:207], v[52:55], a[172:175], v[204:207]\n"
    "v_mfma_f32_16x16x32_bf16 v[212:215], v[52:55], a[236:239], v[212:215]\n"
    "buffer_store_dwordx4 v[220:223], v21, s[80:83], s99 offen offset:1024 sc1\n"
    "v_cvt_pk_bf16_f32 v224, v104, v105\n"
    "s_waitcnt lgkmcnt(8)\n"
    "v_mfma_f32_16x16x32_bf16 v[184:187], v[24:27], a[48:51], v[184:187]\n"
    "ds_read_b64_tr_b16 v[48:49], v14 offset:61440\n"
    "v_mfma_f32_16x16x32_bf16 v[192:195], v[24:27], a[112:115], v[192:195]\n"
    "ds_read_b64_tr_b16 v[50:51], v14 offset:63488\n"
    "v_cvt_pk_bf16_f32 v225, v106, v107\n"
    "v_mfma_f32_16x16x32_bf16 v[200:203], v[24:27], a[176:179], v[200:203]\n"
    "v_mfma_f32_16x16x32_bf16 v[208:211], v[24:27], a[240:243], v[208:211]\n"
    "v_mfma_f32_16x16x32_bf16 v[188:191], v[28:31], a[48:51], v[188:191]\n"
    "ds_read_b64_tr_b16 v[52:53], v15 offset:61440\n"
    "buffer_load_dwordx4 a[200:203], v235, s[40:43], s88 offen offset:1024 sc1\n"
    "v_cvt_pk_bf16_f32 v226, v108, v109\n"
    "v_mfma_f32_16x16x32_bf16 v[196:199], v[28:31], a[112:115], v[196:199]\n"
    "ds_read_b64_tr_b16 v[54:55], v15 offset:63488\n"
    "s_mov_b32 s88, 24576\n"
    "v_mfma_f32_16x16x32_bf16 v[204:207], v[28:31], a[176:179], v[204:207]\n"
    "v_cvt_pk_bf16_f32 v227, v110, v111\n"
    "v_mfma_f32_16x16x32_bf16 v[212:215], v[28:31], a[240:243], v[212:215]\n"
    "s_waitcnt lgkmcnt(8)\n"
    "v_mfma_f32_16x16x32_bf16 v[184:187], v[32:35], a[52:55], v[184:187]\n"
    "buffer_load_dwordx4 a[12:15], v234, s[40:43], s88 offen offset:0 sc1\n"
    "v_mfma_f32_16x16x32_bf16 v[192:195], v[32:35], a[116:119], v[192:195]\n"
    "s_mov_b32 s99, 536576\n"
    "v_mfma_f32_16x16x32_bf16 v[200:203], v[32:35], a[180:183], v[200:203]\n"
    "v_mfma_f32_16x16x32_bf16 v[208:211], v[32:35], a[244:247], v[208:211]\n"
    "v_mfma_f32_16x16x32_bf16 v[188:191], v[36:39], a[52:55], v[188:191]\n"
    "buffer_store_dwordx4 v[224:227], v21, s[80:83], s99 offen offset:0 sc1\n"
    "v_mfma_f32_16x16x32_bf16 v[196:199], v[36:39], a[116:119], v[196:199]\n"
    "v_cvt_pk_bf16_f32 v228, v112, v113\n"
    "v_mfma_f32_16x16x32_bf16 v[204:207], v[36:39], a[180:183], v[204:207]\n"
    "v_mfma_f32_16x16x32_bf16 v[212:215], v[36:39], a[244:247], v[212:215]\n"
    "s_waitcnt vmcnt(42) lgkmcnt(0)\n"
    "s_barrier\n"
    "v_mfma_f32_16x16x32_bf16 v[184:187], v[40:43], a[56:59], v[184:187]\n"
    "ds_read_b64_tr_b16 v[24:25], v8 offset:0\n"
    "v_cvt_pk_bf16_f32 v229, v114, v115\n"
    "v_mfma_f32_16x16x32_bf16 v[192:195], v[40:43], a[120:123], v[192:195]\n"
    "ds_read_b64_tr_b16 v[26:27], v8 offset:2048\n"
    "buffer_load_dwordx4 a[76:79], v234, s[40:43], s88 offen offset:1024 sc1\n"
    "v_mfma_f32_16x16x32_bf16 v[200:203], v[40:43], a[184:187], v[200:203]\n"
    "ds_read_b64_tr_b16 v[28:29], v9 offset:0\n"
    "v_cvt_pk_bf16_f32 v230, v116, v117\n"
    "v_mfma_f32_16x16x32_bf16 v[208:211], v[40:43], a[248:251], v[208:211]\n"
    "ds_read_b64_tr_b16 v[30:31], v9 offset:2048\n"
    "v_mfma_f32_16x16x32_bf16 v[188:191], v[44:47], a[56:59], v[188:191]\n"
    "ds_read_b64_tr_b16 v[32:33], v10 offset:0\n"
    "v_cvt_pk_bf16_f32 v231, v118, v119\n"
    "v_mfma_f32_16x16x32_bf16 v[196:199], v[44:47], a[120:123], v[196:199]\n"
    "ds_read_b64_tr_b16 v[34:35], v10 offset:2048\n"
    "buffer_load_dwordx4 a[140:143], v235, s[40:43], s88 offen offset:0 sc1\n"
    "v_mfma_f32_16x16x32_bf16 v[204:207], v[44:47], a[184:187], v[204:207]\n"
    "ds_read_b64_tr_b16 v[36:37], v11 offset:0\n"
    "v_mfma_f32_16x16x32_bf16 v[212:215], v[44:47], a[248:251], v[212:215]\n"
    "ds_read_b64_tr_b16 v[38:39], v11 offset:2048\n"
    "s_mov_b32 s99, 536576\n"
    "v_mfma_f32_16x16x32_bf16 v[184:187], v[48:51], a[60:63], v[184:187]\n"
    "ds_read_b64_tr_b16 v[40:41], v8 offset:16384\n"
    "v_mfma_f32_16x16x32_bf16 v[192:195], v[48:51], a[124:127], v[192:195]\n"
    "ds_read_b64_tr_b16 v[42:43], v8 offset:18432\n"
    "buffer_store_dwordx4 v[228:231], v21, s[80:83], s99 offen offset:1024 sc1\n"
    "v_mfma_f32_16x16x32_bf16 v[200:203], v[48:51], a[188:191], v[200:203]\n"
    "v_mfma_f32_16x16x32_bf16 v[208:211], v[48:51], a[252:255], v[208:211]\n"
    "v_mfma_f32_16x16x32_bf16 v[188:191], v[52:55], a[60:63], v[188:191]\n"
    "ds_read_b64_tr_b16 v[44:45], v9 offset:16384\n"
    "v_mfma_f32_16x16x32_bf16 v[196:199], v[52:55], a[124:127], v[196:199]\n"
    "ds_read_b64_tr_b16 v[46:47], v9 offset:18432\n"
    "v_mfma_f32_16x16x32_bf16 v[204:207], v[52:55], a[188:191], v[204:207]\n"
    "buffer_load_dwordx4 a[204:207], v235, s[40:43], s88 offen offset:1024 sc1\n"
    "v_mfma_f32_16x16x32_bf16 v[212:215], v[52:55], a[252:255], v[212:215]\n"
    "s_mov_b32 s88, 32768\n"
    "s_waitcnt vmcnt(24)\n"
    "s_waitcnt lgkmcnt(8)\n"
    "v_mfma_f32_16x16x32_bf16 v[56:59], v[24:27], a[0:3], 0\n"
    "ds_read_b64_tr_b16 v[48:49], v10 offset:16384\n"
    "v_mfma_f32_16x16x32_bf16 v[64:67], v[24:27], a[64:67], 0\n"
    "ds_read_b64_tr_b16 v[50:51], v10 offset:18432\n"
    "s_lshl_b32 s30, s64, 2\n"
    "v_mfma_f32_16x16x32_bf16 v[72:75], v[24:27], a[128:131], 0\n"
    "buffer_load_dwordx4 a[16:19], v234, s[40:43], s88 offen offset:0 sc1\n"
    "v_mfma_f32_16x16x32_bf16 v[80:83], v[24:27], a[192:195], 0\n"
    "s_add_u32 s30, s30, s20\n"
    "v_mfma_f32_16x16x32_bf16 v[60:63], v[28:31], a[0:3], 0\n"
    "ds_read_b64_tr_b16 v[52:53], v11 offset:16384\n"
    "v_mfma_f32_16x16x32_bf16 v[68:71], v[28:31], a[64:67], 0\n"
    "ds_read_b64_tr_b16 v[54:55], v11 offset:18432\n"
    "v_lshrrev_b32 v240, 2, v16\n"
    "v_mfma_f32_16x16x32_bf16 v[76:79], v[28:31], a[128:131], 0\n"
    "v_mfma_f32_16x16x32_bf16 v[84:87], v[28:31], a[192:195], 0\n"
    "buffer_load_dwordx4 a[80:83], v234, s[40:43], s88 offen offset:1024 sc1\n"
    "v_mov_b32 v254, s16\n"
    "s_waitcnt lgkmcnt(8)\n"
    "v_mfma_f32_16x16x32_bf16 v[88:91], v[32:35], a[0:3], 0\n"
    "ds_read_b64_tr_b16 v[24:25], v8 offset:4096\n"
    "v_mfma_f32_16x16x32_bf16 v[96:99], v[32:35], a[64:67], 0\n"
    "ds_read_b64_tr_b16 v[26:27], v8 offset:6144\n"
    "v_mov_b32 v255, s17\n"
    "v_mfma_f32_16x16x32_bf16 v[104:107], v[32:35], a[128:131], 0\n"
    "v_mov_b32 v241, v240\n"
    "v_mfma_f32_16x16x32_bf16 v[112:115], v[32:35], a[192:195], 0\n"
    "buffer_load_dwordx4 a[144:147], v235, s[40:43], s88 offen offset:0 sc1\n"
    "v_cvt_pk_bf16_f32 v216, v184, v185\n"
    "v_mfma_f32_16x16x32_bf16 v[92:95], v[36:39], a[0:3], 0\n"
    "ds_read_b64_tr_b16 v[28:29], v9 offset:4096\n"
    "v_mov_b32 v251, 819200\n"
    "v_mfma_f32_16x16x32_bf16 v[100:103], v[36:39], a[64:67], 0\n"
    "ds_read_b64_tr_b16 v[30:31], v9 offset:6144\n"
    "v_mfma_f32_16x16x32_bf16 v[108:111], v[36:39], a[128:131], 0\n"
    "v_mov_b32 v242, 7\n"
    "v_mfma_f32_16x16x32_bf16 v[116:119], v[36:39], a[192:195], 0\n"
    "v_cvt_pk_bf16_f32 v217, v186, v187\n"
    "s_waitcnt lgkmcnt(8)\n"
    "v_mfma_f32_16x16x32_bf16 v[120:123], v[40:43], a[0:3], 0\n"
    "ds_read_b64_tr_b16 v[32:33], v10 offset:4096\n"
    "buffer_load_dwordx4 a[208:211], v235, s[40:43], s88 offen offset:1024 sc1\n"
    "v_cmp_le_u32 vcc, 7, v240\n"
    "v_mfma_f32_16x16x32_bf16 v[128:131], v[40:43], a[64:67], 0\n"
    "ds_read_b64_tr_b16 v[34:35], v10 offset:6144\n"
    "s_mov_b32 s88, 40960\n"
    "v_mfma_f32_16x16x32_bf16 v[136:139], v[40:43], a[128:131], 0\n"
    "v_mov_b32 v252, s10\n"
    "v_mfma_f32_16x16x32_bf16 v[144:147], v[40:43], a[192:195], 0\n"
    "v_cvt_pk_bf16_f32 v218, v188, v189\n"
    "v_mfma_f32_16x16x32_bf16 v[124:127], v[44:47], a[0:3], 0\n"
    "ds_read_b64_tr_b16 v[36:37], v11 offset:4096\n"
    "buffer_load_dwordx4 a[20:23], v234, s[40:43], s88 offen offset:0 sc1\n"
    "v_mov_b32 v253, s11\n"
    "v_mfma_f32_16x16x32_bf16 v[132:135], v[44:47], a[64:67], 0\n"
    "ds_read_b64_tr_b16 v[38:39], v11 offset:6144\n"
    "v_cndmask_b32 v254, v254, v252, vcc\n"
    "v_mfma_f32_16x16x32_bf16 v[140:143], v[44:47], a[128:131], 0\n"
    "v_mfma_f32_16x16x32_bf16 v[148:151], v[44:47], a[192:195], 0\n"
    "v_cndmask_b32 v255, v255, v253, vcc\n"
    "v_cvt_pk_bf16_f32 v219, v190, v191\n"
    "s_waitcnt lgkmcnt(8)\n"
    "v_mfma_f32_16x16x32_bf16 v[152:155], v[48:51], a[0:3], 0\n"
    "ds_read_b64_tr_b16 v[40:41], v8 offset:20480\n"
    "v_mfma_f32_16x16x32_bf16 v[160:163], v[48:51], a[64:67], 0\n"
    "ds_read_b64_tr_b16 v[42:43], v8 offset:22528\n"
    "buffer_load_dwordx4 a[84:87], v234, s[40:43], s88 offen offset:1024 sc1\n"
    "v_subrev_u32 v252, 7, v240\n"
    "v_mfma_f32_16x16x32_bf16 v[168:171], v[48:51], a[128:131], 0\n"
    "v_mfma_f32_16x16x32_bf16 v[176:179], v[48:51], a[192:195], 0\n"
    "v_cndmask_b32 v241, v241, v252, vcc\n"
    "s_mov_b32 s99, 528384\n"
    "v_mfma_f32_16x16x32_bf16 v[156:159], v[52:55], a[0:3], 0\n"
    "ds_read_b64_tr_b16 v[44:45], v9 offset:20480\n"
    "v_mfma_f32_16x16x32_bf16 v[164:167], v[52:55], a[64:67], 0\n"
    "ds_read_b64_tr_b16 v[46:47], v9 offset:22528\n"
    "buffer_load_dwordx4 a[148:151], v235, s[40:43], s88 offen offset:0 sc1\n"
    "v_mov_b32 v252, 360448\n"
    "v_mfma_f32_16x16x32_bf16 v[172:175], v[52:55], a[128:131], 0\n"
    "v_mfma_f32_16x16x32_bf16 v[180:183], v[52:55], a[192:195], 0\n"
    "v_cndmask_b32 v251, v251, v252, vcc\n"
    "s_waitcnt vmcnt(25)\n"
    "s_waitcnt lgkmcnt(8)\n"
    "v_mfma_f32_16x16x32_bf16 v[56:59], v[24:27], a[4:7], v[56:59]\n"
    "ds_read_b64_tr_b16 v[48:49], v10 offset:20480\n"
    "v_mov_b32 v252, 3\n"
    "v_mfma_f32_16x16x32_bf16 v[64:67], v[24:27], a[68:71], v[64:67]\n"
    "ds_read_b64_tr_b16 v[50:51], v10 offset:22528\n"
    "buffer_store_dwordx4 v[216:219], v21, s[80:83], s99 offen offset:2048 sc1\n"
    "v_mfma_f32_16x16x32_bf16 v[72:75], v[24:27], a[132:135], v[72:75]\n"
    "v_cndmask_b32 v242, v242, v252, vcc\n"
    "v_mfma_f32_16x16x32_bf16 v[80:83], v[24:27], a[196:199], v[80:83]\n"
    "v_cvt_pk_bf16_f32 v220, v192, v193\n"
    "v_mfma_f32_16x16x32_bf16 v[60:63], v[28:31], a[4:7], v[60:63]\n"
    "ds_read_b64_tr_b16 v[52:53], v11 offset:20480\n"
    "v_cmp_le_u32 vcc, 10, v240\n"
    "v_mfma_f32_16x16x32_bf16 v[68:71], v[28:31], a[68:71], v[68:71]\n"
    "ds_read_b64_tr_b16 v[54:55], v11 offset:22528\n"
    "v_mfma_f32_16x16x32_bf16 v[76:79], v[28:31], a[132:135], v[76:79]\n"
    "buffer_load_dwordx4 a[212:215], v235, s[40:43], s88 offen offset:1024 sc1\n"
    "v_mov_b32 v252, s12\n"
    "v_mfma_f32_16x16x32_bf16 v[84:87], v[28:31], a[196:199], v[84:87]\n"
    "s_mov_b32 s29, 2097280\n"
    "v_cvt_pk_bf16_f32 v221, v194, v195\n"
    "s_waitcnt lgkmcnt(8)\n"
    "v_mfma_f32_16x16x32_bf16 v[88:91], v[32:35], a[4:7], v[88:91]\n"
    "ds_read_b64_tr_b16 v[24:25], v8 offset:8192\n"
    "s_add_u32 m0, s28, 81920\n"
    "v_mov_b32 v253, s13\n"
    "v_mfma_f32_16x16x32_bf16 v[96:99], v[32:35], a[68:71], v[96:99]\n"
    "ds_read_b64_tr_b16 v[26:27], v8 offset:10240\n"
    "v_mfma_f32_16x16x32_bf16 v[104:107], v[32:35], a[132:135], v[104:107]\n"
    "buffer_load_dwordx4 v4, s[44:47], s29 offen offset:0 lds\n"
    "v_cndmask_b32 v254, v254, v252, vcc\n"
    "v_mfma_f32_16x16x32_bf16 v[112:115], v[32:35], a[196:199], v[112:115]\n"
    "v_cndmask_b32 v255, v255, v253, vcc\n"
    "v_cvt_pk_bf16_f32 v222, v196, v197\n"
    "v_mfma_f32_16x16x32_bf16 v[92:95], v[36:39], a[4:7], v[92:95]\n"
    "ds_read_b64_tr_b16 v[28:29], v9 offset:8192\n"
    "v_mfma_f32_16x16x32_bf16 v[100:103], v[36:39], a[68:71], v[100:103]\n"
    "ds_read_b64_tr_b16 v[30:31], v9 offset:10240\n"
    "v_subrev_u32 v252, 10, v240\n"
    "v_mfma_f32_16x16x32_bf16 v[108:111], v[36:39], a[132:135], v[108:111]\n"
    "v_mfma_f32_16x16x32_bf16 v[116:119], v[36:39], a[196:199], v[116:119]\n"
    "buffer_load_dwordx4 v5, s[44:47], s29 offen offset:1024 lds\n"
    "v_cndmask_b32 v241, v241, v252, vcc\n"
    "v_cvt_pk_bf16_f32 v223, v198, v199\n"
    "s_waitcnt lgkmcnt(8)\n"
    "v_mfma_f32_16x16x32_bf16 v[120:123], v[40:43], a[4:7], v[120:123]\n"
    "ds_read_b64_tr_b16 v[32:33], v10 offset:8192\n"
    "v_mfma_f32_16x16x32_bf16 v[128:131], v[40:43], a[68:71], v[128:131]\n"
    "ds_read_b64_tr_b16 v[34:35], v10 offset:10240\n"
    "v_cmp_le_u32 vcc, 13, v240\n"
    "v_mfma_f32_16x16x32_bf16 v[136:139], v[40:43], a[132:135], v[136:139]\n"
    "v_mfma_f32_16x16x32_bf16 v[144:147], v[40:43], a[196:199], v[144:147]\n"
    "buffer_load_dwordx4 v6, s[44:47], s29 offen offset:2048 lds\n"
    "v_mov_b32 v252, s14\n"
    "s_mov_b32 s99, 528384\n"
    "v_mfma_f32_16x16x32_bf16 v[124:127], v[44:47], a[4:7], v[124:127]\n"
    "ds_read_b64_tr_b16 v[36:37], v11 offset:8192\n"
    "v_mfma_f32_16x16x32_bf16 v[132:135], v[44:47], a[68:71], v[132:135]\n"
    "ds_read_b64_tr_b16 v[38:39], v11 offset:10240\n"
    "v_mov_b32 v253, s15\n"
    "v_mfma_f32_16x16x32_bf16 v[140:143], v[44:47], a[132:135], v[140:143]\n"
    "v_cndmask_b32 v254, v254, v252, vcc\n"
    "v_mfma_f32_16x16x32_bf16 v[148:151], v[44:47], a[196:199], v[148:151]\n"
    "buffer_store_dwordx4 v[220:223], v21, s[80:83], s99 offen offset:3072 sc1\n"
    "s_waitcnt lgkmcnt(8)\n"
    "v_mfma_f32_16x16x32_bf16 v[152:155], v[48:51], a[4:7], v[152:155]\n"
    "ds_read_b64_tr_b16 v[40:41], v8 offset:24576\n"
    "v_cndmask_b32 v255, v255, v253, vcc\n"
    "v_mfma_f32_16x16x32_bf16 v[160:163], v[48:51], a[68:71], v[160:163]\n"
    "ds_read_b64_tr_b16 v[42:43], v8 offset:26624\n"
    "v_mfma_f32_16x16x32_bf16 v[168:171], v[48:51], a[132:135], v[168:171]\n"
    "v_subrev_u32 v252, 13, v240\n"
    "v_mfma_f32_16x16x32_bf16 v[176:179], v[48:51], a[196:199], v[176:179]\n"
    "v_cvt_pk_bf16_f32 v224, v200, v201\n"
    "v_mfma_f32_16x16x32_bf16 v[156:159], v[52:55], a[4:7], v[156:159]\n"
    "ds_read_b64_tr_b16 v[44:45], v9 offset:24576\n"
    "buffer_load_dwordx4 v7, s[44:47], s29 offen offset:3072 lds\n"
    "v_cndmask_b32 v241, v241, v252, vcc\n"
    "v_mfma_f32_16x16x32_bf16 v[164:167], v[52:55], a[68:71], v[164:167]\n"
    "ds_read_b64_tr_b16 v[46:47], v9 offset:26624\n"
    "s_mov_b32 s88, 49152\n"
    "v_mfma_f32_16x16x32_bf16 v[172:175], v[52:55], a[132:135], v[172:175]\n"
    "v_mad_u32_u24 v241, v242, s30, v241\n"
    "v_mfma_f32_16x16x32_bf16 v[180:183], v[52:55], a[196:199], v[180:183]\n"
    "v_lshlrev_b32 v241, 7, v241\n"
    "v_cvt_pk_bf16_f32 v225, v202, v203\n"
    "s_waitcnt vmcnt(20)\n"
    "s_waitcnt lgkmcnt(8)\n"
    "v_mfma_f32_16x16x32_bf16 v[56:59], v[24:27], a[8:11], v[56:59]\n"
    "ds_read_b64_tr_b16 v[48:49], v10 offset:24576\n"
    "buffer_load_dwordx4 a[24:27], v234, s[40:43], s88 offen offset:0 sc1\n"
    "v_mfma_f32_16x16x32_bf16 v[64:67], v[24:27], a[72:75], v[64:67]\n"
    "ds_read_b64_tr_b16 v[50:51], v10 offset:26624\n"
    "v_add_co_u32 v254, vcc, v254, v241\n\ts_nop 1\n\tv_addc_co_u32 v255, vcc, 0, v255, vcc\n"
    "v_mfma_f32_16x16x32_bf16 v[72:75], v[24:27], a[136:139], v[72:75]\n"
    "v_mfma_f32_16x16x32_bf16 v[80:83], v[24:27], a[200:203], v[80:83]\n"
    "s_and_b32 s17, s17, 0xffff\n"
    "v_cvt_pk_bf16_f32 v226, v204, v205\n"
    "v_mfma_f32_16x16x32_bf16 v[60:63], v[28:31], a[8:11], v[60:63]\n"
    "ds_read_b64_tr_b16 v[52:53], v11 offset:24576\n"
    "v_mfma_f32_16x16x32_bf16 v[68:71], v[28:31], a[72:75], v[68:71]\n"
    "ds_read_b64_tr_b16 v[54:55], v11 offset:26624\n"
    "buffer_load_dwordx4 a[88:91], v234, s[40:43], s88 offen offset:1024 sc1\n"
    "s_and_b32 s30, s64, 7\n"
    "v_mfma_f32_16x16x32_bf16 v[76:79], v[28:31], a[136:139], v[76:79]\n"
    "v_cvt_pk_bf16_f32 v227, v206, v207\n"
    "v_mfma_f32_16x16x32_bf16 v[84:87], v[28:31], a[200:203], v[84:87]\n"
    "s_lshl_b32 s30, s30, 19\n"
    "s_waitcnt lgkmcnt(8)\n"
    "v_mfma_f32_16x16x32_bf16 v[88:91], v[32:35], a[8:11], v[88:91]\n"
    "ds_read_b64_tr_b16 v[24:25], v8 offset:12288\n"
    "v_mfma_f32_16x16x32_bf16 v[96:99], v[32:35], a[72:75], v[96:99]\n"
    "ds_read_b64_tr_b16 v[26:27], v8 offset:14336\n"
    "buffer_load_dwordx4 a[152:155], v235, s[40:43], s88 offen offset:0 sc1\n"
    "s_add_u32 s16, s16, s30\n\ts_addc_u32 s17, s17, 0\n"
    "v_mfma_f32_16x16x32_bf16 v[104:107], v[32:35], a[136:139], v[104:107]\n"
    "s_mov_b32 s18, 524288\n"
    "s_mov_b32 s99, 536576\n"
    "v_mfma_f32_16x16x32_bf16 v[112:115], v[32:35], a[200:203], v[112:115]\n"
    "v_mfma_f32_16x16x32_bf16 v[92:95], v[36:39], a[8:11], v[92:95]\n"
    "ds_read_b64_tr_b16 v[28:29], v9 offset:12288\n"
    "s_mov_b32 s19, 0x20000\n"
    "v_mfma_f32_16x16x32_bf16 v[100:103], v[36:39], a[72:75], v[100:103]\n"
    "ds_read_b64_tr_b16 v[30:31], v9 offset:14336\n"
    "v_mfma_f32_16x16x32_bf16 v[108:111], v[36:39], a[136:139], v[108:111]\n"
    "buffer_load_dwordx4 a[216:219], v235, s[40:43], s88 offen offset:1024 sc1\n"
    "s_lshr_b32 s30, s64, 3\n"
    "v_mfma_f32_16x16x32_bf16 v[116:119], v[36:39], a[200:203], v[116:119]\n"
    "s_mov_b32 s88, 57344\n"
    "s_waitcnt lgkmcnt(8)\n"
    "v_mfma_f32_16x16x32_bf16 v[120:123], v[40:43], a[8:11], v[120:123]\n"
    "ds_read_b64_tr_b16 v[32:33], v10 offset:12288\n"
    "s_lshl_b32 s30, s30, 2\n"
    "v_mfma_f32_16x16x32_bf16 v[128:131], v[40:43], a[72:75], v[128:131]\n"
    "ds_read_b64_tr_b16 v[34:35], v10 offset:14336\n"
    "buffer_store_dwordx4 v[224:227], v21, s[80:83], s99 offen offset:2048 sc1\n"
    "v_mfma_f32_16x16x32_bf16 v[136:139], v[40:43], a[136:139], v[136:139]\n"
    "s_add_u32 s30, s30, s20\n"
    "v_cvt_pk_bf16_f32 v228, v208, v209\n"
    "v_mfma_f32_16x16x32_bf16 v[144:147], v[40:43], a[200:203], v[144:147]\n"
    "v_mfma_f32_16x16x32_bf16 v[124:127], v[44:47], a[8:11], v[124:127]\n"
    "ds_read_b64_tr_b16 v[36:37], v11 offset:12288\n"
    "s_lshl_b32 s30, s30, 12\n"
    "v_mfma_f32_16x16x32_bf16 v[132:135], v[44:47], a[72:75], v[132:135]\n"
    "ds_read_b64_tr_b16 v[38:39], v11 offset:14336\n"
    "v_lshrrev_b32 v248, 1, v16\n"
    "v_mfma_f32_16x16x32_bf16 v[140:143], v[44:47], a[136:139], v[140:143]\n"
    "buffer_load_dwordx4 a[28:31], v234, s[40:43], s88 offen offset:0 sc1\n"
    "v_cvt_pk_bf16_f32 v229, v210, v211\n"
    "v_mfma_f32_16x16x32_bf16 v[148:151], v[44:47], a[200:203], v[148:151]\n"
    "v_lshlrev_b32 v248, 7, v248\n"
    "s_waitcnt lgkmcnt(8)\n"
    "v_mfma_f32_16x16x32_bf16 v[152:155], v[48:51], a[8:11], v[152:155]\n"
    "ds_read_b64_tr_b16 v[40:41], v8 offset:28672\n"
    "v_mfma_f32_16x16x32_bf16 v[160:163], v[48:51], a[72:75], v[160:163]\n"
    "ds_read_b64_tr_b16 v[42:43], v8 offset:30720\n"
    "v_add_u32 v248, s30, v248\n"
    "v_mfma_f32_16x16x32_bf16 v[168:171], v[48:51], a[136:139], v[168:171]\n"
    "buffer_load_dwordx4 a[92:95], v234, s[40:43], s88 offen offset:1024 sc1\n"
    "v_cvt_pk_bf16_f32 v230, v212, v213\n"
    "v_mfma_f32_16x16x32_bf16 v[176:179], v[48:51], a[200:203], v[176:179]\n"
    "v_mfma_f32_16x16x32_bf16 v[156:159], v[52:55], a[8:11], v[156:159]\n"
    "ds_read_b64_tr_b16 v[44:45], v9 offset:28672\n"
    "v_mfma_f32_16x16x32_bf16 v[164:167], v[52:55], a[72:75], v[164:167]\n"
    "ds_read_b64_tr_b16 v[46:47], v9 offset:30720\n"
    "v_mfma_f32_16x16x32_bf16 v[172:175], v[52:55], a[136:139], v[172:175]\n"
    "v_cvt_pk_bf16_f32 v231, v214, v215\n"
    "v_mfma_f32_16x16x32_bf16 v[180:183], v[52:55], a[200:203], v[180:183]\n"
    "buffer_load_dwordx4 a[156:159], v235, s[40:43], s88 offen offset:0 sc1\n"
    "s_waitcnt vmcnt(22)\n"
    "s_waitcnt lgkmcnt(8)\n"
    "v_mfma_f32_16x16x32_bf16 v[56:59], v[24:27], a[12:15], v[56:59]\n"
    "ds_read_b64_tr_b16 v[48:49], v10 offset:28672\n"
    "v_mfma_f32_16x16x32_bf16 v[64:67], v[24:27], a[76:79], v[64:67]\n"
    "ds_read_b64_tr_b16 v[50:51], v10 offset:30720\n"
    "v_mfma_f32_16x16x32_bf16 v[72:75], v[24:27], a[140:143], v[72:75]\n"
    "s_mov_b32 s99, 536576\n"
    "v_mfma_f32_16x16x32_bf16 v[80:83], v[24:27], a[204:207], v[80:83]\n"
    "buffer_load_dwordx4 a[220:223], v235, s[40:43], s88 offen offset:1024 sc1\n"
    "v_mfma_f32_16x16x32_bf16 v[60:63], v[28:31], a[12:15], v[60:63]\n"
    "ds_read_b64_tr_b16 v[52:53], v11 offset:28672\n"
    "s_mov_b32 s88, 65536\n"
    "v_mfma_f32_16x16x32_bf16 v[68:71], v[28:31], a[76:79], v[68:71]\n"
    "ds_read_b64_tr_b16 v[54:55], v11 offset:30720\n"
    "v_mfma_f32_16x16x32_bf16 v[76:79], v[28:31], a[140:143], v[76:79]\n"
    "v_mfma_f32_16x16x32_bf16 v[84:87], v[28:31], a[204:207], v[84:87]\n"
    "buffer_store_dwordx4 v[228:231], v21, s[80:83], s99 offen offset:3072 sc1\n"
    "s_waitcnt lgkmcnt(8)\n"
    "v_mfma_f32_16x16x32_bf16 v[88:91], v[32:35], a[12:15], v[88:91]\n"
    "v_mfma_f32_16x16x32_bf16 v[96:99], v[32:35], a[76:79], v[96:99]\n"
    "v_mfma_f32_16x16x32_bf16 v[104:107], v[32:35], a[140:143], v[104:107]\n"
    "v_mfma_f32_16x16x32_bf16 v[112:115], v[32:35], a[204:207], v[112:115]\n"
    "v_mfma_f32_16x16x32_bf16 v[92:95], v[36:39], a[12:15], v[92:95]\n"
    "buffer_load_dwordx4 a[32:35], v234, s[40:43], s88 offen offset:0 sc1\n"
    "v_mfma_f32_16x16x32_bf16 v[100:103], v[36:39], a[76:79], v[100:103]\n"
    "v_mfma_f32_16x16x32_bf16 v[108:111], v[36:39], a[140:143], v[108:111]\n"
    "v_mfma_f32_16x16x32_bf16 v[116:119], v[36:39], a[204:207], v[116:119]\n"
    "s_waitcnt vmcnt(25) lgkmcnt(0)\n"
    "s_barrier\n"
    "v_mfma_f32_16x16x32_bf16 v[120:123], v[40:43], a[12:15], v[120:123]\n"
    "ds_read_b64_tr_b16 v[24:25], v8 offset:32768\n"
    "buffer_load_dwordx4 a[96:99], v234, s[40:43], s88 offen offset:1024 sc1\n"
    "v_mfma_f32_16x16x32_bf16 v[128:131], v[40:43], a[76:79], v[128:131]\n"
    "ds_read_b64_tr_b16 v[26:27], v8 offset:34816\n"
    "v_mfma_f32_16x16x32_bf16 v[136:139], v[40:43], a[140:143], v[136:139]\n"
    "ds_read_b64_tr_b16 v[28:29], v9 offset:32768\n"
    "v_mfma_f32_16x16x32_bf16 v[144:147], v[40:43], a[204:207], v[144:147]\n"
    "ds_read_b64_tr_b16 v[30:31], v9 offset:34816\n"
    "v_mfma_f32_16x16x32_bf16 v[124:127], v[44:47], a[12:15], v[124:127]\n"
    "ds_read_b64_tr_b16 v[32:33], v10 offset:32768\n"
    "v_mfma_f32_16x16x32_bf16 v[132:135], v[44:47], a[76:79], v[132:135]\n"
    "ds_read_b64_tr_b16 v[34:35], v10 offset:34816\n"
    "buffer_load_dwordx4 a[160:163], v235, s[40:43], s88 offen offset:0 sc1\n"
    "v_mfma_f32_16x16x32_bf16 v[140:143], v[44:47], a[140:143], v[140:143]\n"
    "ds_read_b64_tr_b16 v[36:37], v11 offset:32768\n"
    "v_mfma_f32_16x16x32_bf16 v[148:151], v[44:47], a[204:207], v[148:151]\n"
    "ds_read_b64_tr_b16 v[38:39], v11 offset:34816\n"
    "v_mfma_f32_16x16x32_bf16 v[152:155], v[48:51], a[12:15], v[152:155]\n"
    "ds_read_b64_tr_b16 v[40:41], v8 offset:49152\n"
    "v_mfma_f32_16x16x32_bf16 v[160:163], v[48:51], a[76:79], v[160:163]\n"
    "ds_read_b64_tr_b16 v[42:43], v8 offset:51200\n"
    "buffer_load_dwordx4 a[224:227], v235, s[40:43], s88 offen offset:1024 sc1\n"
    "v_mfma_f32_16x16x32_bf16 v[168:171], v[48:51], a[140:143], v[168:171]\n"
    "s_mov_b32 s88, 73728\n"
    "v_mfma_f32_16x16x32_bf16 v[176:179], v[48:51], a[204:207], v[176:179]\n"
    "v_mfma_f32_16x16x32_bf16 v[156:159], v[52:55], a[12:15], v[156:159]\n"
    "ds_read_b64_tr_b16 v[44:45], v9 offset:49152\n"
    "v_mfma_f32_16x16x32_bf16 v[164:167], v[52:55], a[76:79], v[164:167]\n"
    "ds_read_b64_tr_b16 v[46:47], v9 offset:51200\n"
    "v_mfma_f32_16x16x32_bf16 v[172:175], v[52:55], a[140:143], v[172:175]\n"
    "buffer_load_dwordx4 a[36:39], v234, s[40:43], s88 offen offset:0 sc1\n"
    "v_mfma_f32_16x16x32_bf16 v[180:183], v[52:55], a[204:207], v[180:183]\n"
    "s_waitcnt vmcnt(25)\n"
    "s_waitcnt lgkmcnt(8)\n"
    "v_mfma_f32_16x16x32_bf16 v[56:59], v[24:27], a[16:19], v[56:59]\n"
    "ds_read_b64_tr_b16 v[48:49], v10 offset:49152\n"
    "v_mfma_f32_16x16x32_bf16 v[64:67], v[24:27], a[80:83], v[64:67]\n"
    "ds_read_b64_tr_b16 v[50:51], v10 offset:51200\n"
    "v_mfma_f32_16x16x32_bf16 v[72:75], v[24:27], a[144:147], v[72:75]\n"
    "buffer_load_dwordx4 a[100:103], v234, s[40:43], s88 offen offset:1024 sc1\n"
    "s_lshl_b32 s30, s20, 13\n"
    "v_mfma_f32_16x16x32_bf16 v[80:83], v[24:27], a[208:211], v[80:83]\n"
    "v_mfma_f32_16x16x32_bf16 v[60:63], v[28:31], a[16:19], v[60:63]\n"
    "ds_read_b64_tr_b16 v[52:53], v11 offset:49152\n"
    "s_add_u32 s30, s30, 131072\n"
    "v_mfma_f32_16x16x32_bf16 v[68:71], v[28:31], a[80:83], v[68:71]\n"
    "ds_read_b64_tr_b16 v[54:55], v11 offset:51200\n"
    "v_mfma_f32_16x16x32_bf16 v[76:79], v[28:31], a[144:147], v[76:79]\n"
    "v_add_u32 v17, s30, v2\n"
    "v_mfma_f32_16x16x32_bf16 v[84:87], v[28:31], a[208:211], v[84:87]\n"
    "buffer_load_dwordx4 a[164:167], v235, s[40:43], s88 offen offset:0 sc1\n"
    "s_waitcnt lgkmcnt(8)\n"
    "v_mfma_f32_16x16x32_bf16 v[88:91], v[32:35], a[16:19], v[88:91]\n"
    "ds_read_b64_tr_b16 v[24:25], v8 offset:36864\n"
    "v_add_u32 v20, s28, v2\n"
    "v_mfma_f32_16x16x32_bf16 v[96:99], v[32:35], a[80:83], v[96:99]\n"
    "ds_read_b64_tr_b16 v[26:27], v8 offset:38912\n"
    "v_mfma_f32_16x16x32_bf16 v[104:107], v[32:35], a[144:147], v[104:107]\n"
    "s_and_b32 s21, s20, 1\n"
    "v_mfma_f32_16x16x32_bf16 v[112:115], v[32:35], a[208:211], v[112:115]\n"
    "buffer_load_dwordx4 a[228:231], v235, s[40:43], s88 offen offset:1024 sc1\n"
    "v_mfma_f32_16x16x32_bf16 v[92:95], v[36:39], a[16:19], v[92:95]\n"
    "ds_read_b64_tr_b16 v[28:29], v9 offset:36864\n"
    "s_mov_b32 s29, 3145728\n"
    "s_lshl_b32 s23, s22, 1\n"
    "v_mfma_f32_16x16x32_bf16 v[100:103], v[36:39], a[80:83], v[100:103]\n"
    "ds_read_b64_tr_b16 v[30:31], v9 offset:38912\n"
    "s_add_u32 m0, s28, 98304\n"
    "v_mfma_f32_16x16x32_bf16 v[108:111], v[36:39], a[144:147], v[108:111]\n"
    "s_add_u32 s23, s23, 2\n"
    "v_mfma_f32_16x16x32_bf16 v[116:119], v[36:39], a[208:211], v[116:119]\n"
    "s_waitcnt lgkmcnt(8)\n"
    "v_mfma_f32_16x16x32_bf16 v[120:123], v[40:43], a[16:19], v[120:123]\n"
    "ds_read_b64_tr_b16 v[32:33], v10 offset:36864\n"
    "buffer_load_dwordx4 v4, s[44:47], s29 offen offset:0 lds\n"
    "s_mov_b32 s96, 0x3e0293ee\n"
    "v_mfma_f32_16x16x32_bf16 v[128:131], v[40:43], a[80:83], v[128:131]\n"
    "ds_read_b64_tr_b16 v[34:35], v10 offset:38912\n"
    "v_mfma_f32_16x16x32_bf16 v[136:139], v[40:43], a[144:147], v[136:139]\n"
    "s_mov_b32 s97, 0x7f800000\n"
    "v_mfma_f32_16x16x32_bf16 v[144:147], v[40:43], a[208:211], v[144:147]\n"
    "v_mfma_f32_16x16x32_bf16 v[124:127], v[44:47], a[16:19], v[124:127]\n"
    "ds_read_b64_tr_b16 v[36:37], v11 offset:36864\n"
    "buffer_load_dwordx4 v5, s[44:47], s29 offen offset:1024 lds\n"
    "s_mov_b32 s98, 0\n"
    "v_mfma_f32_16x16x32_bf16 v[132:135], v[44:47], a[80:83], v[132:135]\n"
    "ds_read_b64_tr_b16 v[38:39], v11 offset:38912\n"
    "v_mfma_f32_16x16x32_bf16 v[140:143], v[44:47], a[144:147], v[140:143]\n"
    "s_mov_b32 s86, 32768\n"
    "v_mfma_f32_16x16x32_bf16 v[148:151], v[44:47], a[208:211], v[148:151]\n"
    "s_waitcnt lgkmcnt(8)\n"
    "v_mfma_f32_16x16x32_bf16 v[152:155], v[48:51], a[16:19], v[152:155]\n"
    "ds_read_b64_tr_b16 v[40:41], v8 offset:53248\n"
    "s_mov_b32 s34, 16384\n"
    "v_mfma_f32_16x16x32_bf16 v[160:163], v[48:51], a[80:83], v[160:163]\n"
    "ds_read_b64_tr_b16 v[42:43], v8 offset:55296\n"
    "buffer_load_dwordx4 v6, s[44:47], s29 offen offset:2048 lds\n"
    "v_mfma_f32_16x16x32_bf16 v[168:171], v[48:51], a[144:147], v[168:171]\n"
    "s_mov_b32 s39, 16384\n"
    "v_mfma_f32_16x16x32_bf16 v[176:179], v[48:51], a[208:211], v[176:179]\n"
    "v_mfma_f32_16x16x32_bf16 v[156:159], v[52:55], a[16:19], v[156:159]\n"
    "ds_read_b64_tr_b16 v[44:45], v9 offset:53248\n"
    "s_mov_b32 s38, 16384\n"
    "v_mfma_f32_16x16x32_bf16 v[164:167], v[52:55], a[80:83], v[164:167]\n"
    "ds_read_b64_tr_b16 v[46:47], v9 offset:55296\n"
    "buffer_load_dwordx4 v7, s[44:47], s29 offen offset:3072 lds\n"
    "v_mfma_f32_16x16x32_bf16 v[172:175], v[52:55], a[144:147], v[172:175]\n"
    "s_mov_b32 s88, 81920\n"
    "s_add_u32 s36, s28, 32768\n"
    "v_mfma_f32_16x16x32_bf16 v[180:183], v[52:55], a[208:211], v[180:183]\n"
    "s_waitcnt vmcnt(27)\n"
    "s_waitcnt lgkmcnt(8)\n"
    "v_mfma_f32_16x16x32_bf16 v[56:59], v[24:27], a[20:23], v[56:59]\n"
    "ds_read_b64_tr_b16 v[48:49], v10 offset:53248\n"
    "s_add_u32 s37, s28, 98304\n"
    "v_mfma_f32_16x16x32_bf16 v[64:67], v[24:27], a[84:87], v[64:67]\n"
    "ds_read_b64_tr_b16 v[50:51], v10 offset:55296\n"
    "v_mfma_f32_16x16x32_bf16 v[72:75], v[24:27], a[148:151], v[72:75]\n"
    "buffer_load_dwordx4 a[40:43], v234, s[40:43], s88 offen offset:0 sc1\n"
    "s_mov_b32 s24, 0\n"
    "v_mfma_f32_16x16x32_bf16 v[80:83], v[24:27], a[212:215], v[80:83]\n"
    "v_mfma_f32_16x16x32_bf16 v[60:63], v[28:31], a[20:23], v[60:63]\n"
    "ds_read_b64_tr_b16 v[52:53], v11 offset:53248\n"
    "s_lshl_b32 s30, s20, 1\n"
    "v_mfma_f32_16x16x32_bf16 v[68:71], v[28:31], a[84:87], v[68:71]\n"
    "ds_read_b64_tr_b16 v[54:55], v11 offset:55296\n"
    "v_mfma_f32_16x16x32_bf16 v[76:79], v[28:31], a[148:151], v[76:79]\n"
    "buffer_load_dwordx4 a[104:107], v234, s[40:43], s88 offen offset:1024 sc1\n"
    "s_lshr_b32 s72, 129, s30\n"
    "v_mfma_f32_16x16x32_bf16 v[84:87], v[28:31], a[212:215], v[84:87]\n"
    "s_waitcnt lgkmcnt(8)\n"
    "v_mfma_f32_16x16x32_bf16 v[88:91], v[32:35], a[20:23], v[88:91]\n"
    "ds_read_b64_tr_b16 v[24:25], v8 offset:40960\n"
    "s_and_b32 s72, s72, 3\n"
    "v_mfma_f32_16x16x32_bf16 v[96:99], v[32:35], a[84:87], v[96:99]\n"
    "ds_read_b64_tr_b16 v[26:27], v8 offset:43008\n"
    "v_mfma_f32_16x16x32_bf16 v[104:107], v[32:35], a[148:151], v[104:107]\n"
    "s_mov_b32 s73, 0\n"
    "v_mfma_f32_16x16x32_bf16 v[112:115], v[32:35], a[212:215], v[112:115]\n"
    "buffer_load_dwordx4 a[168:171], v235, s[40:43], s88 offen offset:0 sc1\n"
    "v_mfma_f32_16x16x32_bf16 v[92:95], v[36:39], a[20:23], v[92:95]\n"
    "ds_read_b64_tr_b16 v[28:29], v9 offset:40960\n"
    "s_lshl_b64 s[72:73], s[72:73], 31\n"
    "v_mfma_f32_16x16x32_bf16 v[100:103], v[36:39], a[84:87], v[100:103]\n"
    "ds_read_b64_tr_b16 v[30:31], v9 offset:43008\n"
    "v_mfma_f32_16x16x32_bf16 v[108:111], v[36:39], a[148:151], v[108:111]\n"
    "s_lshr_b32 s74, 14, s30\n"
    "v_mfma_f32_16x16x32_bf16 v[116:119], v[36:39], a[212:215], v[116:119]\n"
    "buffer_load_dwordx4 a[232:235], v235, s[40:43], s88 offen offset:1024 sc1\n"
    "s_waitcnt lgkmcnt(8)\n"
    "v_mfma_f32_16x16x32_bf16 v[120:123], v[40:43], a[20:23], v[120:123]\n"
    "ds_read_b64_tr_b16 v[32:33], v10 offset:40960\n"
    "s_mov_b32 s88, 90112\n"
    "s_and_b32 s74, s74, 3\n"
    "v_mfma_f32_16x16x32_bf16 v[128:131], v[40:43], a[84:87], v[128:131]\n"
    "ds_read_b64_tr_b16 v[34:35], v10 offset:43008\n"
    "v_mfma_f32_16x16x32_bf16 v[136:139], v[40:43], a[148:151], v[136:139]\n"
    "s_mov_b32 s75, 0\n"
    "v_mfma_f32_16x16x32_bf16 v[144:147], v[40:43], a[212:215], v[144:147]\n"
    "v_mfma_f32_16x16x32_bf16 v[124:127], v[44:47], a[20:23], v[124:127]\n"
    "ds_read_b64_tr_b16 v[36:37], v11 offset:40960\n"
    "buffer_load_dwordx4 a[44:47], v234, s[40:43], s88 offen offset:0 sc1\n"
    "s_lshl_b64 s[74:75], s[74:75], 31\n"
    "v_mfma_f32_16x16x32_bf16 v[132:135], v[44:47], a[84:87], v[132:135]\n"
    "ds_read_b64_tr_b16 v[38:39], v11 offset:43008\n"
    "v_mfma_f32_16x16x32_bf16 v[140:143], v[44:47], a[148:151], v[140:143]\n"
    "s_lshr_b32 s76, 66, s30\n"
    "v_mfma_f32_16x16x32_bf16 v[148:151], v[44:47], a[212:215], v[148:151]\n"
    "s_waitcnt lgkmcnt(8)\n"
    "v_mfma_f32_16x16x32_bf16 v[152:155], v[48:51], a[20:23], v[152:155]\n"
    "ds_read_b64_tr_b16 v[40:41], v8 offset:57344\n"
    "buffer_load_dwordx4 a[108:111], v234, s[40:43], s88 offen offset:1024 sc1\n"
    "v_mfma_f32_16x16x32_bf16 v[160:163], v[48:51], a[84:87], v[160:163]\n"
    "ds_read_b64_tr_b16 v[42:43], v8 offset:59392\n"
    "s_and_b32 s76, s76, 3\n"
    "v_mfma_f32_16x16x32_bf16 v[168:171], v[48:51], a[148:151], v[168:171]\n"
    "v_mfma_f32_16x16x32_bf16 v[176:179], v[48:51], a[212:215], v[176:179]\n"
    "s_mov_b32 s77, 0\n"
    "v_mfma_f32_16x16x32_bf16 v[156:159], v[52:55], a[20:23], v[156:159]\n"
    "ds_read_b64_tr_b16 v[44:45], v9 offset:57344\n"
    "v_mfma_f32_16x16x32_bf16 v[164:167], v[52:55], a[84:87], v[164:167]\n"
    "ds_read_b64_tr_b16 v[46:47], v9 offset:59392\n"
    "buffer_load_dwordx4 a[172:175], v235, s[40:43], s88 offen offset:0 sc1\n"
    "s_lshl_b64 s[76:77], s[76:77], 31\n"
    "v_mfma_f32_16x16x32_bf16 v[172:175], v[52:55], a[148:151], v[172:175]\n"
    "v_mfma_f32_16x16x32_bf16 v[180:183], v[52:55], a[212:215], v[180:183]\n"
    "s_lshr_b32 s78, 140, s30\n"
    "s_waitcnt vmcnt(25)\n"
    "s_waitcnt lgkmcnt(8)\n"
    "v_mfma_f32_16x16x32_bf16 v[56:59], v[24:27], a[24:27], v[56:59]\n"
    "ds_read_b64_tr_b16 v[48:49], v10 offset:57344\n"
    "v_mfma_f32_16x16x32_bf16 v[64:67], v[24:27], a[88:91], v[64:67]\n"
    "ds_read_b64_tr_b16 v[50:51], v10 offset:59392\n"
    "buffer_load_dwordx4 a[236:239], v235, s[40:43], s88 offen offset:1024 sc1\n"
    "s_and_b32 s78, s78, 3\n"
    "v_mfma_f32_16x16x32_bf16 v[72:75], v[24:27], a[152:155], v[72:75]\n"
    "s_mov_b32 s88, 98304\n"
    "v_mfma_f32_16x16x32_bf16 v[80:83], v[24:27], a[216:219], v[80:83]\n"
    "s_mov_b32 s79, 0\n"
    "v_mfma_f32_16x16x32_bf16 v[60:63], v[28:31], a[24:27], v[60:63]\n"
    "ds_read_b64_tr_b16 v[52:53], v11 offset:57344\n"
    "v_mfma_f32_16x16x32_bf16 v[68:71], v[28:31], a[88:91], v[68:71]\n"
    "ds_read_b64_tr_b16 v[54:55], v11 offset:59392\n"
    "s_lshl_b64 s[78:79], s[78:79], 31\n"
    "v_mfma_f32_16x16x32_bf16 v[76:79], v[28:31], a[152:155], v[76:79]\n"
    "buffer_load_dwordx4 a[48:51], v234, s[40:43], s88 offen offset:0 sc1\n"
    "v_mfma_f32_16x16x32_bf16 v[84:87], v[28:31], a[216:219], v[84:87]\n"
    "s_lshl_b32 s30, s22, 2\n"
    "s_waitcnt lgkmcnt(8)\n"
    "v_mfma_f32_16x16x32_bf16 v[88:91], v[32:35], a[24:27], v[88:91]\n"
    "ds_read_b64_tr_b16 v[24:25], v8 offset:45056\n"
    "v_mfma_f32_16x16x32_bf16 v[96:99], v[32:35], a[88:91], v[96:99]\n"
    "ds_read_b64_tr_b16 v[26:27], v8 offset:47104\n"
    "s_add_u32 s31, s30, s20\n"
    "v_mfma_f32_16x16x32_bf16 v[104:107], v[32:35], a[152:155], v[104:107]\n"
    "buffer_load_dwordx4 a[112:115], v234, s[40:43], s88 offen offset:1024 sc1\n"
    "v_mfma_f32_16x16x32_bf16 v[112:115], v[32:35], a[216:219], v[112:115]\n"
    "s_lshl_b64 s[82:83], 1, s31\n"
    "v_mfma_f32_16x16x32_bf16 v[92:95], v[36:39], a[24:27], v[92:95]\n"
    "ds_read_b64_tr_b16 v[28:29], v9 offset:45056\n"
    "v_mfma_f32_16x16x32_bf16 v[100:103], v[36:39], a[88:91], v[100:103]\n"
    "ds_read_b64_tr_b16 v[30:31], v9 offset:47104\n"
    "s_lshr_b64 s[26:27], s[82:83], 1\n"
    "v_mfma_f32_16x16x32_bf16 v[108:111], v[36:39], a[152:155], v[108:111]\n"
    "v_mfma_f32_16x16x32_bf16 v[116:119], v[36:39], a[216:219], v[116:119]\n"
    "buffer_load_dwordx4 a[176:179], v235, s[40:43], s88 offen offset:0 sc1\n"
    "s_or_b64 s[76:77], s[76:77], s[26:27]\n"
    "s_waitcnt lgkmcnt(8)\n"
    "v_mfma_f32_16x16x32_bf16 v[120:123], v[40:43], a[24:27], v[120:123]\n"
    "ds_read_b64_tr_b16 v[32:33], v10 offset:45056\n"
    "v_mfma_f32_16x16x32_bf16 v[128:131], v[40:43], a[88:91], v[128:131]\n"
    "ds_read_b64_tr_b16 v[34:35], v10 offset:47104\n"
    "s_lshl_b64 s[26:27], 8, s30\n"
    "v_mfma_f32_16x16x32_bf16 v[136:139], v[40:43], a[152:155], v[136:139]\n"
    "v_mfma_f32_16x16x32_bf16 v[144:147], v[40:43], a[216:219], v[144:147]\n"
    "buffer_load_dwordx4 a[240:243], v235, s[40:43], s88 offen offset:1024 sc1\n"
    "s_sub_u32 s26, s26, s82\n\ts_subb_u32 s27, s27, s83\n"
    "v_mfma_f32_16x16x32_bf16 v[124:127], v[44:47], a[24:27], v[124:127]\n"
    "ds_read_b64_tr_b16 v[36:37], v11 offset:45056\n"
    "s_mov_b32 s88, 106496\n"
    "v_mfma_f32_16x16x32_bf16 v[132:135], v[44:47], a[88:91], v[132:135]\n"
    "ds_read_b64_tr_b16 v[38:39], v11 offset:47104\n"
    "s_or_b64 s[78:79], s[78:79], s[26:27]\n"
    "v_mfma_f32_16x16x32_bf16 v[140:143], v[44:47], a[152:155], v[140:143]\n"
    "v_mfma_f32_16x16x32_bf16 v[148:151], v[44:47], a[216:219], v[148:151]\n"
    "s_lshl_b64 s[26:27], 40, s30\n"
    "s_waitcnt lgkmcnt(8)\n"
    "v_mfma_f32_16x16x32_bf16 v[152:155], v[48:51], a[24:27], v[152:155]\n"
    "ds_read_b64_tr_b16 v[40:41], v8 offset:61440\n"
    "buffer_load_dwordx4 a[52:55], v234, s[40:43], s88 offen offset:0 sc1\n"
    "v_mfma_f32_16x16x32_bf16 v[160:163], v[48:51], a[88:91], v[160:163]\n"
    "ds_read_b64_tr_b16 v[42:43], v8 offset:63488\n"
    "s_lshl_b32 s80, s21, 14\n"
    "v_mfma_f32_16x16x32_bf16 v[168:171], v[48:51], a[152:155], v[168:171]\n"
    "v_mfma_f32_16x16x32_bf16 v[176:179], v[48:51], a[216:219], v[176:179]\n"
    "v_mfma_f32_16x16x32_bf16 v[156:159], v[52:55], a[24:27], v[156:159]\n"
    "ds_read_b64_tr_b16 v[44:45], v9 offset:61440\n"
    "buffer_load_dwordx4 a[116:119], v234, s[40:43], s88 offen offset:1024 sc1\n"
    "v_mfma_f32_16x16x32_bf16 v[164:167], v[52:55], a[88:91], v[164:167]\n"
    "ds_read_b64_tr_b16 v[46:47], v9 offset:63488\n"
    "v_mfma_f32_16x16x32_bf16 v[172:175], v[52:55], a[152:155], v[172:175]\n"
    "v_mfma_f32_16x16x32_bf16 v[180:183], v[52:55], a[216:219], v[180:183]\n"
    "s_waitcnt vmcnt(27)\n"
    "s_waitcnt lgkmcnt(8)\n"
    "v_mfma_f32_16x16x32_bf16 v[56:59], v[24:27], a[28:31], v[56:59]\n"
    "ds_read_b64_tr_b16 v[48:49], v10 offset:61440\n"
    "v_mfma_f32_16x16x32_bf16 v[64:67], v[24:27], a[92:95], v[64:67]\n"
    "ds_read_b64_tr_b16 v[50:51], v10 offset:63488\n"
    "buffer_load_dwordx4 a[180:183], v235, s[40:43], s88 offen offset:0 sc1\n"
    "v_mfma_f32_16x16x32_bf16 v[72:75], v[24:27], a[156:159], v[72:75]\n"
    "v_mfma_f32_16x16x32_bf16 v[80:83], v[24:27], a[220:223], v[80:83]\n"
    "v_mfma_f32_16x16x32_bf16 v[60:63], v[28:31], a[28:31], v[60:63]\n"
    "ds_read_b64_tr_b16 v[52:53], v11 offset:61440\n"
    "v_mfma_f32_16x16x32_bf16 v[68:71], v[28:31], a[92:95], v[68:71]\n"
    "ds_read_b64_tr_b16 v[54:55], v11 offset:63488\n"
    "buffer_load_dwordx4 a[244:247], v235, s[40:43], s88 offen offset:1024 sc1\n"
    "v_mfma_f32_16x16x32_bf16 v[76:79], v[28:31], a[156:159], v[76:79]\n"
    "s_mov_b32 s29, 3145856\n"
    "v_mfma_f32_16x16x32_bf16 v[84:87], v[28:31], a[220:223], v[84:87]\n"
    "s_add_u32 m0, s28, 114688\n"
    "s_waitcnt lgkmcnt(8)\n"
    "v_mfma_f32_16x16x32_bf16 v[88:91], v[32:35], a[28:31], v[88:91]\n"
    "v_mfma_f32_16x16x32_bf16 v[96:99], v[32:35], a[92:95], v[96:99]\n"
    "v_mfma_f32_16x16x32_bf16 v[104:107], v[32:35], a[156:159], v[104:107]\n"
    "buffer_load_dwordx4 v4, s[44:47], s29 offen offset:0 lds\n"
    "v_mfma_f32_16x16x32_bf16 v[112:115], v[32:35], a[220:223], v[112:115]\n"
    "v_mfma_f32_16x16x32_bf16 v[92:95], v[36:39], a[28:31], v[92:95]\n"
    "v_mfma_f32_16x16x32_bf16 v[100:103], v[36:39], a[92:95], v[100:103]\n"
    "v_mfma_f32_16x16x32_bf16 v[108:111], v[36:39], a[156:159], v[108:111]\n"
    "buffer_load_dwordx4 v5, s[44:47], s29 offen offset:1024 lds\n"
    "v_mfma_f32_16x16x32_bf16 v[116:119], v[36:39], a[220:223], v[116:119]\n"
    "s_waitcnt vmcnt(30) lgkmcnt(0)\n"
    "s_barrier\n"
    "s_cmp_lg_u32 s20, 0\n"
    "s_cbranch_scc1 L_skk_%=\n"
    "v_mov_b32 v18, s35\n"
    "v_mov_b32 v19, s94\n"
    "s_mov_b64 exec, 1\n"
    "global_atomic_swap v18, v19, s[60:61]\n"
    "s_mov_b64 exec, -1\n"
    "L_skk_%=:\n"
    "v_mfma_f32_16x16x32_bf16 v[120:123], v[40:43], a[28:31], v[120:123]\n"
    "ds_read_b64_tr_b16 v[24:25], v12 offset:0\n"
    "v_mfma_f32_16x16x32_bf16 v[128:131], v[40:43], a[92:95], v[128:131]\n"
    "ds_read_b64_tr_b16 v[26:27], v12 offset:2048\n"
    "v_mfma_f32_16x16x32_bf16 v[136:139], v[40:43], a[156:159], v[136:139]\n"
    "ds_read_b64_tr_b16 v[28:29], v13 offset:0\n"
    "v_mfma_f32_16x16x32_bf16 v[144:147], v[40:43], a[220:223], v[144:147]\n"
    "ds_read_b64_tr_b16 v[30:31], v13 offset:2048\n"
    "buffer_load_dwordx4 v6, s[44:47], s29 offen offset:2048 lds\n"
    "v_mfma_f32_16x16x32_bf16 v[124:127], v[44:47], a[28:31], v[124:127]\n"
    "ds_read_b64_tr_b16 v[32:33], v14 offset:0\n"
    "v_mfma_f32_16x16x32_bf16 v[132:135], v[44:47], a[92:95], v[132:135]\n"
    "ds_read_b64_tr_b16 v[34:35], v14 offset:2048\n"
    "v_mfma_f32_16x16x32_bf16 v[140:143], v[44:47], a[156:159], v[140:143]\n"
    "ds_read_b64_tr_b16 v[36:37], v15 offset:0\n"
    "v_mfma_f32_16x16x32_bf16 v[148:151], v[44:47], a[220:223], v[148:151]\n"
    "ds_read_b64_tr_b16 v[38:39], v15 offset:2048\n"
    "buffer_load_dwordx4 v7, s[44:47], s29 offen offset:3072 lds\n"
    "v_mfma_f32_16x16x32_bf16 v[152:155], v[48:51], a[28:31], v[152:155]\n"
    "ds_read_b64_tr_b16 v[40:41], v12 offset:16384\n"
    "s_mov_b32 s88, 114688\n"
    "v_mfma_f32_16x16x32_bf16 v[160:163], v[48:51], a[92:95], v[160:163]\n"
    "ds_read_b64_tr_b16 v[42:43], v12 offset:18432\n"
    "v_mfma_f32_16x16x32_bf16 v[168:171], v[48:51], a[156:159], v[168:171]\n"
    "v_mfma_f32_16x16x32_bf16 v[176:179], v[48:51], a[220:223], v[176:179]\n"
    "v_mfma_f32_16x16x32_bf16 v[156:159], v[52:55], a[28:31], v[156:159]\n"
    "ds_read_b64_tr_b16 v[44:45], v13 offset:16384\n"
    "buffer_load_dwordx4 a[56:59], v234, s[40:43], s88 offen offset:0 sc1\n"
    "v_mfma_f32_16x16x32_bf16 v[164:167], v[52:55], a[92:95], v[164:167]\n"
    "ds_read_b64_tr_b16 v[46:47], v13 offset:18432\n"
    "v_mfma_f32_16x16x32_bf16 v[172:175], v[52:55], a[156:159], v[172:175]\n"
    "v_mfma_f32_16x16x32_bf16 v[180:183], v[52:55], a[220:223], v[180:183]\n"
    "s_waitcnt vmcnt(29)\n"
    "s_waitcnt lgkmcnt(8)\n"
    "v_mfma_f32_16x16x32_bf16 v[56:59], v[24:27], a[32:35], v[56:59]\n"
    "ds_read_b64_tr_b16 v[48:49], v14 offset:16384\n"
    "buffer_load_dwordx4 a[120:123], v234, s[40:43], s88 offen offset:1024 sc1\n"
    "v_mov_b32 v184, 0\n"
    "v_mfma_f32_16x16x32_bf16 v[64:67], v[24:27], a[96:99], v[64:67]\n"
    "ds_read_b64_tr_b16 v[50:51], v14 offset:18432\n"
    "v_mov_b32 v185, 0\n"
    "v_mfma_f32_16x16x32_bf16 v[72:75], v[24:27], a[160:163], v[72:75]\n"
    "v_mov_b32 v186, 0\n"
    "v_mfma_f32_16x16x32_bf16 v[80:83], v[24:27], a[224:227], v[80:83]\n"
    "v_mov_b32 v187, 0\n"
    "v_mfma_f32_16x16x32_bf16 v[60:63], v[28:31], a[32:35], v[60:63]\n"
    "ds_read_b64_tr_b16 v[52:53], v15 offset:16384\n"
    "v_mov_b32 v188, 0\n"
    "v_mfma_f32_16x16x32_bf16 v[68:71], v[28:31], a[96:99], v[68:71]\n"
    "ds_read_b64_tr_b16 v[54:55], v15 offset:18432\n"
    "buffer_load_dwordx4 a[184:187], v235, s[40:43], s88 offen offset:0 sc1\n"
    "v_mov_b32 v189, 0\n"
    "v_mfma_f32_16x16x32_bf16 v[76:79], v[28:31], a[160:163], v[76:79]\n"
    "v_mov_b32 v190, 0\n"
    "v_mov_b32 v191, 0\n"
    "v_mfma_f32_16x16x32_bf16 v[84:87], v[28:31], a[224:227], v[84:87]\n"
    "v_mov_b32 v192, 0\n"
    "s_waitcnt lgkmcnt(8)\n"
    "v_mfma_f32_16x16x32_bf16 v[88:91], v[32:35], a[32:35], v[88:91]\n"
    "ds_read_b64_tr_b16 v[24:25], v12 offset:4096\n"
    "v_mov_b32 v193, 0\n"
    "v_mfma_f32_16x16x32_bf16 v[96:99], v[32:35], a[96:99], v[96:99]\n"
    "ds_read_b64_tr_b16 v[26:27], v12 offset:6144\n"
    "buffer_load_dwordx4 a[248:251], v235, s[40:43], s88 offen offset:1024 sc1\n"
    "v_mov_b32 v194, 0\n"
    "v_mfma_f32_16x16x32_bf16 v[104:107], v[32:35], a[160:163], v[104:107]\n"
    "s_mov_b32 s88, 122880\n"
    "v_mov_b32 v195, 0\n"
    "v_mfma_f32_16x16x32_bf16 v[112:115], v[32:35], a[224:227], v[112:115]\n"
    "v_mov_b32 v196, 0\n"
    "v_mfma_f32_16x16x32_bf16 v[92:95], v[36:39], a[32:35], v[92:95]\n"
    "ds_read_b64_tr_b16 v[28:29], v13 offset:4096\n"
    "v_mov_b32 v197, 0\n"
    "v_mfma_f32_16x16x32_bf16 v[100:103], v[36:39], a[96:99], v[100:103]\n"
    "ds_read_b64_tr_b16 v[30:31], v13 offset:6144\n"
    "v_mov_b32 v198, 0\n"
    "v_mov_b32 v199, 0\n"
    "v_mfma_f32_16x16x32_bf16 v[108:111], v[36:39], a[160:163], v[108:111]\n"
    "buffer_load_dwordx4 a[60:63], v234, s[40:43], s88 offen offset:0 sc1\n"
    "v_mov_b32 v200, 0\n"
    "v_mfma_f32_16x16x32_bf16 v[116:119], v[36:39], a[224:227], v[116:119]\n"
    "v_mov_b32 v201, 0\n"
    "s_waitcnt lgkmcnt(8)\n"
    "v_mfma_f32_16x16x32_bf16 v[120:123], v[40:43], a[32:35], v[120:123]\n"
    "ds_read_b64_tr_b16 v[32:33], v14 offset:4096\n"
    "v_mov_b32 v202, 0\n"
    "v_mfma_f32_16x16x32_bf16 v[128:131], v[40:43], a[96:99], v[128:131]\n"
    "ds_read_b64_tr_b16 v[34:35], v14 offset:6144\n"
    "v_mov_b32 v203, 0\n"
    "v_mfma_f32_16x16x32_bf16 v[136:139], v[40:43], a[160:163], v[136:139]\n"
    "buffer_load_dwordx4 a[124:127], v234, s[40:43], s88 offen offset:1024 sc1\n"
    "v_mov_b32 v204, 0\n"
    "v_mfma_f32_16x16x32_bf16 v[144:147], v[40:43], a[224:227], v[144:147]\n"
    "v_mov_b32 v205, 0\n"
    "v_mfma_f32_16x16x32_bf16 v[124:127], v[44:47], a[32:35], v[124:127]\n"
    "ds_read_b64_tr_b16 v[36:37], v15 offset:4096\n"
    "v_mov_b32 v206, 0\n"
    "v_mov_b32 v207, 0\n"
    "v_mfma_f32_16x16x32_bf16 v[132:135], v[44:47], a[96:99], v[132:135]\n"
    "ds_read_b64_tr_b16 v[38:39], v15 offset:6144\n"
    "v_mov_b32 v208, 0\n"
    "v_mfma_f32_16x16x32_bf16 v[140:143], v[44:47], a[160:163], v[140:143]\n"
    "v_mov_b32 v209, 0\n"
    "v_mfma_f32_16x16x32_bf16 v[148:151], v[44:47], a[224:227], v[148:151]\n"
    "buffer_load_dwordx4 a[188:191], v235, s[40:43], s88 offen offset:0 sc1\n"
    "v_mov_b32 v210, 0\n"
    "s_waitcnt lgkmcnt(8)\n"
    "v_mfma_f32_16x16x32_bf16 v[152:155], v[48:51], a[32:35], v[152:155]\n"
    "ds_read_b64_tr_b16 v[40:41], v12 offset:20480\n"
    "v_mov_b32 v211, 0\n"
    "v_mfma_f32_16x16x32_bf16 v[160:163], v[48:51], a[96:99], v[160:163]\n"
    "ds_read_b64_tr_b16 v[42:43], v12 offset:22528\n"
    "v_mov_b32 v212, 0\n"
    "v_mfma_f32_16x16x32_bf16 v[168:171], v[48:51], a[160:163], v[168:171]\n"
    "v_mov_b32 v213, 0\n"
    "v_mov_b32 v214, 0\n"
    "v_mfma_f32_16x16x32_bf16 v[176:179], v[48:51], a[224:227], v[176:179]\n"
    "buffer_load_dwordx4 a[252:255], v235, s[40:43], s88 offen offset:1024 sc1\n"
    "v_mov_b32 v215, 0\n"
    "v_mfma_f32_16x16x32_bf16 v[156:159], v[52:55], a[32:35], v[156:159]\n"
    "ds_read_b64_tr_b16 v[44:45], v13 offset:20480\n"
    "v_min_u32 v232, 15, v16\n"
    "v_and_b32 v216, 15, v16\n"
    "v_mfma_f32_16x16x32_bf16 v[164:167], v[52:55], a[96:99], v[164:167]\n"
    "ds_read_b64_tr_b16 v[46:47], v13 offset:22528\n"
    "v_lshlrev_b32 v232, 2, v232\n"
    "v_lshrrev_b32 v217, 4, v16\n"
    "v_mfma_f32_16x16x32_bf16 v[172:175], v[52:55], a[160:163], v[172:175]\n"
    "v_add_u32 v232, s85, v232\n"
    "v_lshlrev_b32 v217, 2, v217\n"
    "v_mfma_f32_16x16x32_bf16 v[180:183], v[52:55], a[224:227], v[180:183]\n"
    "s_nop 0\n"
    "v_sub_u32 v216, v216, v217\n"
    "s_waitcnt vmcnt(32)\n"
    "s_waitcnt lgkmcnt(8)\n"
    "v_mfma_f32_16x16x32_bf16 v[56:59], v[24:27], a[36:39], v[56:59]\n"
    "ds_read_b64_tr_b16 v[48:49], v14 offset:20480\n"
    "s_nop 0\n"
    "v_mov_b32 v217, 0xff800000\n"
    "v_mfma_f32_16x16x32_bf16 v[64:67], v[24:27], a[100:103], v[64:67]\n"
    "ds_read_b64_tr_b16 v[50:51], v14 offset:22528\n"
    "s_nop 0\n"
    "v_mov_b32 v218, 0x7f800000\n"
    "v_cmp_gt_i32 vcc, 0, v216\n"
    "v_mfma_f32_16x16x32_bf16 v[72:75], v[24:27], a[164:167], v[72:75]\n"
    "s_nop 0\n"
    "s_nop 1\n"
    "v_mfma_f32_16x16x32_bf16 v[80:83], v[24:27], a[228:231], v[80:83]\n"
    "s_nop 0\n"
    "v_cndmask_b32 v244, v218, v217, vcc\n"
    "v_mfma_f32_16x16x32_bf16 v[60:63], v[28:31], a[36:39], v[60:63]\n"
    "ds_read_b64_tr_b16 v[52:53], v15 offset:20480\n"
    "s_nop 0\n"
    "v_cmp_gt_i32 vcc, 1, v216\n"
    "v_mfma_f32_16x16x32_bf16 v[68:71], v[28:31], a[100:103], v[68:71]\n"
    "ds_read_b64_tr_b16 v[54:55], v15 offset:22528\n"
    "s_nop 0\n"
    "s_nop 1\n"
    "v_mfma_f32_16x16x32_bf16 v[76:79], v[28:31], a[164:167], v[76:79]\n"
    "s_nop 0\n"
    "v_cndmask_b32 v245, v218, v217, vcc\n"
    "v_mfma_f32_16x16x32_bf16 v[84:87], v[28:31], a[228:231], v[84:87]\n"
    "s_nop 0\n"
    "v_cmp_gt_i32 vcc, 2, v216\n"
    "s_waitcnt lgkmcnt(8)\n"
    "v_mfma_f32_16x16x32_bf16 v[88:91], v[32:35], a[36:39], v[88:91]\n"
    "ds_read_b64_tr_b16 v[24:25], v12 offset:8192\n"
    "s_nop 0\n"
    "s_nop 1\n"
    "v_cndmask_b32 v246, v218, v217, vcc\n"
    "v_mfma_f32_16x16x32_bf16 v[96:99], v[32:35], a[100:103], v[96:99]\n"
    "ds_read_b64_tr_b16 v[26:27], v12 offset:10240\n"
    "s_nop 0\n"
    "v_cmp_gt_i32 vcc, 3, v216\n"
    "v_mfma_f32_16x16x32_bf16 v[104:107], v[32:35], a[164:167], v[104:107]\n"
    "s_nop 0\n"
    "s_nop 1\n"
    "v_mfma_f32_16x16x32_bf16 v[112:115], v[32:35], a[228:231], v[112:115]\n"
    "s_nop 0\n"
    "v_cndmask_b32 v247, v218, v217, vcc\n"
    "v_mfma_f32_16x16x32_bf16 v[92:95], v[36:39], a[36:39], v[92:95]\n"
    "ds_read_b64_tr_b16 v[28:29], v13 offset:8192\n"
    "s_nop 0\n"
    "v_mov_b32 v216, 0x3f803f80\n"
    "v_mfma_f32_16x16x32_bf16 v[100:103], v[36:39], a[100:103], v[100:103]\n"
    "ds_read_b64_tr_b16 v[30:31], v13 offset:10240\n"
    "s_nop 0\n"
    "v_accvgpr_write_b32 a0, 0\n"
    "v_mfma_f32_16x16x32_bf16 v[108:111], v[36:39], a[164:167], v[108:111]\n"
    "s_nop 0\n"
    "v_accvgpr_write_b32 a1, 0\n"
    "v_accvgpr_write_b32 a2, 0\n"
    "v_mfma_f32_16x16x32_bf16 v[116:119], v[36:39], a[228:231], v[116:119]\n"
    "s_nop 0\n"
    "v_accvgpr_write_b32 a3, 0\n"
    "s_waitcnt lgkmcnt(8)\n"
    "v_mfma_f32_16x16x32_bf16 v[120:123], v[40:43], a[36:39], v[120:123]\n"
    "ds_read_b64_tr_b16 v[32:33], v14 offset:8192\n"
    "s_nop 0\n"
    "v_accvgpr_write_b32 a4, 0\n"
    "v_mfma_f32_16x16x32_bf16 v[128:131], v[40:43], a[100:103], v[128:131]\n"
    "ds_read_b64_tr_b16 v[34:35], v14 offset:10240\n"
    "s_nop 0\n"
    "v_accvgpr_write_b32 a5, 0\n"
    "v_mfma_f32_16x16x32_bf16 v[136:139], v[40:43], a[164:167], v[136:139]\n"
    "s_nop 0\n"
    "v_accvgpr_write_b32 a6, 0\n"
    "v_mfma_f32_16x16x32_bf16 v[144:147], v[40:43], a[228:231], v[144:147]\n"
    "s_nop 0\n"
    "v_accvgpr_write_b32 a7, 0\n"
    "v_mfma_f32_16x16x32_bf16 v[124:127], v[44:47], a[36:39], v[124:127]\n"
    "ds_read_b64_tr_b16 v[36:37], v15 offset:8192\n"
    "s_nop 0\n"
    "v_accvgpr_write_b32 a8, 0\n"
    "v_mfma_f32_16x16x32_bf16 v[132:135], v[44:47], a[100:103], v[132:135]\n"
    "ds_read_b64_tr_b16 v[38:39], v15 offset:10240\n"
    "s_nop 0\n"
    "v_accvgpr_write_b32 a9, 0\n"
    "v_accvgpr_write_b32 a10, 0\n"
    "v_mfma_f32_16x16x32_bf16 v[140:143], v[44:47], a[164:167], v[140:143]\n"
    "s_nop 0\n"
    "v_accvgpr_write_b32 a11, 0\n"
    "v_mfma_f32_16x16x32_bf16 v[148:151], v[44:47], a[228:231], v[148:151]\n"
    "s_nop 0\n"
    "v_accvgpr_write_b32 a12, 0\n"
    "s_waitcnt lgkmcnt(8)\n"
    "v_mfma_f32_16x16x32_bf16 v[152:155], v[48:51], a[36:39], v[152:155]\n"
    "ds_read_b64_tr_b16 v[40:41], v12 offset:24576\n"
    "s_nop 0\n"
    "v_accvgpr_write_b32 a13, 0\n"
    "v_mfma_f32_16x16x32_bf16 v[160:163], v[48:51], a[100:103], v[160:163]\n"
    "ds_read_b64_tr_b16 v[42:43], v12 offset:26624\n"
    "s_nop 0\n"
    "v_accvgpr_write_b32 a14, 0\n"
    "v_mfma_f32_16x16x32_bf16 v[168:171], v[48:51], a[164:167], v[168:171]\n"
    "s_nop 0\n"
    "v_accvgpr_write_b32 a15, 0\n"
    "v_mfma_f32_16x16x32_bf16 v[176:179], v[48:51], a[228:231], v[176:179]\n"
    "s_nop 0\n"
    "v_accvgpr_write_b32 a16, 0\n"
    "v_mfma_f32_16x16x32_bf16 v[156:159], v[52:55], a[36:39], v[156:159]\n"
    "ds_read_b64_tr_b16 v[44:45], v13 offset:24576\n"
    "s_nop 0\n"
    "v_accvgpr_write_b32 a17, 0\n"
    "v_accvgpr_write_b32 a18, 0\n"
    "v_mfma_f32_16x16x32_bf16 v[164:167], v[52:55], a[100:103], v[164:167]\n"
    "ds_read_b64_tr_b16 v[46:47], v13 offset:26624\n"
    "s_nop 0\n"
    "v_accvgpr_write_b32 a19, 0\n"
    "v_mfma_f32_16x16x32_bf16 v[172:175], v[52:55], a[164:167], v[172:175]\n"
    "s_nop 0\n"
    "v_accvgpr_write_b32 a20, 0\n"
    "v_mfma_f32_16x16x32_bf16 v[180:183], v[52:55], a[228:231], v[180:183]\n"
    "s_nop 0\n"
    "v_accvgpr_write_b32 a21, 0\n"
    "s_waitcnt vmcnt(24)\n"
    "s_waitcnt lgkmcnt(8)\n"
    "v_mfma_f32_16x16x32_bf16 v[56:59], v[24:27], a[40:43], v[56:59]\n"
    "ds_read_b64_tr_b16 v[48:49], v14 offset:24576\n"
    "s_nop 0\n"
    "v_accvgpr_write_b32 a22, 0\n"
    "v_mfma_f32_16x16x32_bf16 v[64:67], v[24:27], a[104:107], v[64:67]\n"
    "ds_read_b64_tr_b16 v[50:51], v14 offset:26624\n"
    "s_nop 0\n"
    "v_accvgpr_write_b32 a23, 0\n"
    "v_mfma_f32_16x16x32_bf16 v[72:75], v[24:27], a[168:171], v[72:75]\n"
    "s_nop 0\n"
    "v_accvgpr_write_b32 a24, 0\n"
    "v_accvgpr_write_b32 a25, 0\n"
    "v_mfma_f32_16x16x32_bf16 v[80:83], v[24:27], a[232:235], v[80:83]\n"
    "s_nop 0\n"
    "v_accvgpr_write_b32 a26, 0\n"
    "v_mfma_f32_16x16x32_bf16 v[60:63], v[28:31], a[40:43], v[60:63]\n"
    "ds_read_b64_tr_b16 v[52:53], v15 offset:24576\n"
    "s_nop 0\n"
    "v_accvgpr_write_b32 a27, 0\n"
    "v_mfma_f32_16x16x32_bf16 v[68:71], v[28:31], a[104:107], v[68:71]\n"
    "ds_read_b64_tr_b16 v[54:55], v15 offset:26624\n"
    "s_nop 0\n"
    "v_accvgpr_write_b32 a28, 0\n"
    "v_mfma_f32_16x16x32_bf16 v[76:79], v[28:31], a[168:171], v[76:79]\n"
    "s_nop 0\n"
    "v_accvgpr_write_b32 a29, 0\n"
    "v_mfma_f32_16x16x32_bf16 v[84:87], v[28:31], a[232:235], v[84:87]\n"
    "global_load_dword v233, v232, s[60:61] sc1\n"
    "v_accvgpr_write_b32 a30, 0\n"
    "s_waitcnt lgkmcnt(8)\n"
    "v_mfma_f32_16x16x32_bf16 v[88:91], v[32:35], a[40:43], v[88:91]\n"
    "ds_read_b64_tr_b16 v[24:25], v12 offset:12288\n"
    "v_accvgpr_write_b32 a31, 0\n"
    "v_mfma_f32_16x16x32_bf16 v[96:99], v[32:35], a[104:107], v[96:99]\n"
    "ds_read_b64_tr_b16 v[26:27], v12 offset:14336\n"
    "v_accvgpr_write_b32 a192, 0\n"
    "v_accvgpr_write_b32 a193, 0\n"
    "v_mfma_f32_16x16x32_bf16 v[104:107], v[32:35], a[168:171], v[104:107]\n"
    "v_accvgpr_write_b32 a194, 0\n"
    "v_mfma_f32_16x16x32_bf16 v[112:115], v[32:35], a[232:235], v[112:115]\n"
    "v_accvgpr_write_b32 a195, 0\n"
    "v_mfma_f32_16x16x32_bf16 v[92:95], v[36:39], a[40:43], v[92:95]\n"
    "ds_read_b64_tr_b16 v[28:29], v13 offset:12288\n"
    "v_accvgpr_write_b32 a196, 0\n"
    "v_mfma_f32_16x16x32_bf16 v[100:103], v[36:39], a[104:107], v[100:103]\n"
    "ds_read_b64_tr_b16 v[30:31], v13 offset:14336\n"
    "v_accvgpr_write_b32 a197, 0\n"
    "v_mfma_f32_16x16x32_bf16 v[108:111], v[36:39], a[168:171], v[108:111]\n"
    "v_accvgpr_write_b32 a198, 0\n"
    "v_mfma_f32_16x16x32_bf16 v[116:119], v[36:39], a[232:235], v[116:119]\n"
    "v_accvgpr_write_b32 a199, 0\n"
    "s_waitcnt lgkmcnt(8)\n"
    "v_mfma_f32_16x16x32_bf16 v[120:123], v[40:43], a[40:43], v[120:123]\n"
    "ds_read_b64_tr_b16 v[32:33], v14 offset:12288\n"
    "v_accvgpr_write_b32 a200, 0\n"
    "v_accvgpr_write_b32 a201, 0\n"
    "v_mfma_f32_16x16x32_bf16 v[128:131], v[40:43], a[104:107], v[128:131]\n"
    "ds_read_b64_tr_b16 v[34:35], v14 offset:14336\n"
    "v_accvgpr_write_b32 a202, 0\n"
    "v_mfma_f32_16x16x32_bf16 v[136:139], v[40:43], a[168:171], v[136:139]\n"
    "v_accvgpr_write_b32 a203, 0\n"
    "v_mfma_f32_16x16x32_bf16 v[144:147], v[40:43], a[232:235], v[144:147]\n"
    "v_accvgpr_write_b32 a204, 0\n"
    "v_mfma_f32_16x16x32_bf16 v[124:127], v[44:47], a[40:43], v[124:127]\n"
    "ds_read_b64_tr_b16 v[36:37], v15 offset:12288\n"
    "v_accvgpr_write_b32 a205, 0\n"
    "v_mfma_f32_16x16x32_bf16 v[132:135], v[44:47], a[104:107], v[132:135]\n"
    "ds_read_b64_tr_b16 v[38:39], v15 offset:14336\n"
    "v_accvgpr_write_b32 a206, 0\n"
    "v_mfma_f32_16x16x32_bf16 v[140:143], v[44:47], a[168:171], v[140:143]\n"
    "v_accvgpr_write_b32 a207, 0\n"
    "v_accvgpr_write_b32 a208, v216\n"
    "v_mfma_f32_16x16x32_bf16 v[148:151], v[44:47], a[232:235], v[148:151]\n"
    "v_accvgpr_write_b32 a209, v216\n"
    "s_waitcnt lgkmcnt(8)\n"
    "v_mfma_f32_16x16x32_bf16 v[152:155], v[48:51], a[40:43], v[152:155]\n"
    "ds_read_b64_tr_b16 v[40:41], v12 offset:28672\n"
    "v_accvgpr_write_b32 a210, v216\n"
    "v_mfma_f32_16x16x32_bf16 v[160:163], v[48:51], a[104:107], v[160:163]\n"
    "ds_read_b64_tr_b16 v[42:43], v12 offset:30720\n"
    "v_accvgpr_write_b32 a211, v216\n"
    "v_mfma_f32_16x16x32_bf16 v[168:171], v[48:51], a[168:171], v[168:171]\n"
    "v_mfma_f32_16x16x32_bf16 v[176:179], v[48:51], a[232:235], v[176:179]\n"
    "v_mfma_f32_16x16x32_bf16 v[156:159], v[52:55], a[40:43], v[156:159]\n"
    "ds_read_b64_tr_b16 v[44:45], v13 offset:28672\n"
    "v_mfma_f32_16x16x32_bf16 v[164:167], v[52:55], a[104:107], v[164:167]\n"
    "ds_read_b64_tr_b16 v[46:47], v13 offset:30720\n"
    "v_mfma_f32_16x16x32_bf16 v[172:175], v[52:55], a[168:171], v[172:175]\n"
    "v_mfma_f32_16x16x32_bf16 v[180:183], v[52:55], a[232:235], v[180:183]\n"
    "s_waitcnt vmcnt(21)\n"
    "s_waitcnt lgkmcnt(8)\n"
    "v_mfma_f32_16x16x32_bf16 v[56:59], v[24:27], a[44:47], v[56:59]\n"
    "ds_read_b64_tr_b16 v[48:49], v14 offset:28672\n"
    "v_mfma_f32_16x16x32_bf16 v[64:67], v[24:27], a[108:111], v[64:67]\n"
    "ds_read_b64_tr_b16 v[50:51], v14 offset:30720\n"
    "v_mfma_f32_16x16x32_bf16 v[72:75], v[24:27], a[172:175], v[72:75]\n"
    "v_mfma_f32_16x16x32_bf16 v[80:83], v[24:27], a[236:239], v[80:83]\n"
    "v_mfma_f32_16x16x32_bf16 v[60:63], v[28:31], a[44:47], v[60:63]\n"
    "ds_read_b64_tr_b16 v[52:53], v15 offset:28672\n"
    "v_mfma_f32_16x16x32_bf16 v[68:71], v[28:31], a[108:111], v[68:71]\n"
    "ds_read_b64_tr_b16 v[54:55], v15 offset:30720\n"
    "v_mfma_f32_16x16x32_bf16 v[76:79], v[28:31], a[172:175], v[76:79]\n"
    "v_mfma_f32_16x16x32_bf16 v[84:87], v[28:31], a[236:239], v[84:87]\n"
    "s_waitcnt lgkmcnt(8)\n"
    "v_mfma_f32_16x16x32_bf16 v[88:91], v[32:35], a[44:47], v[88:91]\n"
    "v_mfma_f32_16x16x32_bf16 v[96:99], v[32:35], a[108:111], v[96:99]\n"
    "v_mfma_f32_16x16x32_bf16 v[104:107], v[32:35], a[172:175], v[104:107]\n"
    "v_mfma_f32_16x16x32_bf16 v[112:115], v[32:35], a[236:239], v[112:115]\n"
    "v_mfma_f32_16x16x32_bf16 v[92:95], v[36:39], a[44:47], v[92:95]\n"
    "v_mfma_f32_16x16x32_bf16 v[100:103], v[36:39], a[108:111], v[100:103]\n"
    "v_mfma_f32_16x16x32_bf16 v[108:111], v[36:39], a[172:175], v[108:111]\n"
    "v_mfma_f32_16x16x32_bf16 v[116:119], v[36:39], a[236:239], v[116:119]\n"
    "s_waitcnt vmcnt(0)\n"
    "v_cmp_ne_u32 vcc, s94, v233\n"
    "s_mov_b32 s30, 0x400000\n"
    "s_nop 1\n"
    "s_cbranch_vccz L_pkk_%=_x\n"
    "L_pkk_%=:\n"
    "s_sleep 2\n"
    "global_load_dword v233, v232, s[60:61] sc1\n"
    "s_waitcnt vmcnt(0)\n"
    "v_cmp_ne_u32 vcc, s94, v233\n"
    "s_sub_u32 s30, s30, 1\n"
    "s_cbranch_scc1 L_pkk_%=_x\n"
    "s_nop 0\n"
    "s_cbranch_vccnz L_pkk_%=\n"
    "L_pkk_%=_x:\n"
    "s_waitcnt vmcnt(0) lgkmcnt(0)\n"
    "s_barrier\n"
    "v_mfma_f32_16x16x32_bf16 v[120:123], v[40:43], a[44:47], v[120:123]\n"
    "ds_read_b64_tr_b16 v[24:25], v12 offset:32768\n"
    "s_mov_b32 s99, 0\n"
    "v_mfma_f32_16x16x32_bf16 v[128:131], v[40:43], a[108:111], v[128:131]\n"
    "ds_read_b64_tr_b16 v[26:27], v12 offset:34816\n"
    "s_add_u32 m0, s28, 32768\n"
    "v_mfma_f32_16x16x32_bf16 v[136:139], v[40:43], a[172:175], v[136:139]\n"
    "ds_read_b64_tr_b16 v[28:29], v13 offset:32768\n"
    "buffer_load_dwordx4 v20, s[48:51], s99 offen offset:0 sc1 lds\n"
    "v_mfma_f32_16x16x32_bf16 v[144:147], v[40:43], a[236:239], v[144:147]\n"
    "ds_read_b64_tr_b16 v[30:31], v13 offset:34816\n"
    "v_mfma_f32_16x16x32_bf16 v[124:127], v[44:47], a[44:47], v[124:127]\n"
    "ds_read_b64_tr_b16 v[32:33], v14 offset:32768\n"
    "v_mfma_f32_16x16x32_bf16 v[132:135], v[44:47], a[108:111], v[132:135]\n"
    "ds_read_b64_tr_b16 v[34:35], v14 offset:34816\n"
    "v_mfma_f32_16x16x32_bf16 v[140:143], v[44:47], a[172:175], v[140:143]\n"
    "ds_read_b64_tr_b16 v[36:37], v15 offset:32768\n"
    "buffer_load_dwordx4 v20, s[48:51], s99 offen offset:1024 sc1 lds\n"
    "v_mfma_f32_16x16x32_bf16 v[148:151], v[44:47], a[236:239], v[148:151]\n"
    "ds_read_b64_tr_b16 v[38:39], v15 offset:34816\n"
    "v_mfma_f32_16x16x32_bf16 v[152:155], v[48:51], a[44:47], v[152:155]\n"
    "ds_read_b64_tr_b16 v[40:41], v12 offset:49152\n"
    "v_mfma_f32_16x16x32_bf16 v[160:163], v[48:51], a[108:111], v[160:163]\n"
    "ds_read_b64_tr_b16 v[42:43], v12 offset:51200\n"
    "v_mfma_f32_16x16x32_bf16 v[168:171], v[48:51], a[172:175], v[168:171]\n"
    "v_mfma_f32_16x16x32_bf16 v[176:179], v[48:51], a[236:239], v[176:179]\n"
    "buffer_load_dwordx4 v20, s[48:51], s99 offen offset:2048 sc1 lds\n"
    "v_mfma_f32_16x16x32_bf16 v[156:159], v[52:55], a[44:47], v[156:159]\n"
    "ds_read_b64_tr_b16 v[44:45], v13 offset:49152\n"
    "v_mfma_f32_16x16x32_bf16 v[164:167], v[52:55], a[108:111], v[164:167]\n"
    "ds_read_b64_tr_b16 v[46:47], v13 offset:51200\n"
    "v_mfma_f32_16x16x32_bf16 v[172:175], v[52:55], a[172:175], v[172:175]\n"
    "v_mfma_f32_16x16x32_bf16 v[180:183], v[52:55], a[236:239], v[180:183]\n"
    "buffer_load_dwordx4 v20, s[48:51], s99 offen offset:3072 sc1 lds\n"
    "s_waitcnt lgkmcnt(8)\n"
    "v_mfma_f32_16x16x32_bf16 v[56:59], v[24:27], a[48:51], v[56:59]\n"
    "ds_read_b64_tr_b16 v[48:49], v14 offset:49152\n"
    "s_mov_b32 s99, 16384\n"
    "v_mfma_f32_16x16x32_bf16 v[64:67], v[24:27], a[112:115], v[64:67]\n"
    "ds_read_b64_tr_b16 v[50:51], v14 offset:51200\n"
    "s_add_u32 m0, s28, 0\n"
    "v_mfma_f32_16x16x32_bf16 v[72:75], v[24:27], a[176:179], v[72:75]\n"
    "v_mfma_f32_16x16x32_bf16 v[80:83], v[24:27], a[240:243], v[80:83]\n"
    "v_mfma_f32_16x16x32_bf16 v[60:63], v[28:31], a[48:51], v[60:63]\n"
    "ds_read_b64_tr_b16 v[52:53], v15 offset:49152\n"
    "buffer_load_dwordx4 v20, s[48:51], s99 offen offset:0 sc1 lds\n"
    "v_mfma_f32_16x16x32_bf16 v[68:71], v[28:31], a[112:115], v[68:71]\n"
    "ds_read_b64_tr_b16 v[54:55], v15 offset:51200\n"
    "v_accvgpr_write_b32 a32, 0\n"
    "v_mfma_f32_16x16x32_bf16 v[76:79], v[28:31], a[176:179], v[76:79]\n"
    "v_mfma_f32_16x16x32_bf16 v[84:87], v[28:31], a[240:243], v[84:87]\n"
    "s_waitcnt lgkmcnt(8)\n"
    "v_mfma_f32_16x16x32_bf16 v[88:91], v[32:35], a[48:51], v[88:91]\n"
    "ds_read_b64_tr_b16 v[24:25], v12 offset:36864\n"
    "buffer_load_dwordx4 v20, s[48:51], s99 offen offset:1024 sc1 lds\n"
    "v_mfma_f32_16x16x32_bf16 v[96:99], v[32:35], a[112:115], v[96:99]\n"
    "ds_read_b64_tr_b16 v[26:27], v12 offset:38912\n"
    "v_mfma_f32_16x16x32_bf16 v[104:107], v[32:35], a[176:179], v[104:107]\n"
    "v_mfma_f32_16x16x32_bf16 v[112:115], v[32:35], a[240:243], v[112:115]\n"
    "v_accvgpr_write_b32 a33, 0\n"
    "v_mfma_f32_16x16x32_bf16 v[92:95], v[36:39], a[48:51], v[92:95]\n"
    "ds_read_b64_tr_b16 v[28:29], v13 offset:36864\n"
    "v_mfma_f32_16x16x32_bf16 v[100:103], v[36:39], a[112:115], v[100:103]\n"
    "ds_read_b64_tr_b16 v[30:31], v13 offset:38912\n"
    "buffer_load_dwordx4 v20, s[48:51], s99 offen offset:2048 sc1 lds\n"
    "v_mfma_f32_16x16x32_bf16 v[108:111], v[36:39], a[176:179], v[108:111]\n"
    "v_mfma_f32_16x16x32_bf16 v[116:119], v[36:39], a[240:243], v[116:119]\n"
    "s_waitcnt lgkmcnt(8)\n"
    "v_mfma_f32_16x16x32_bf16 v[120:123], v[40:43], a[48:51], v[120:123]\n"
    "ds_read_b64_tr_b16 v[32:33], v14 offset:36864\n"
    "v_accvgpr_write_b32 a34, 0\n"
    "v_mfma_f32_16x16x32_bf16 v[128:131], v[40:43], a[112:115], v[128:131]\n"
    "ds_read_b64_tr_b16 v[34:35], v14 offset:38912\n"
    "buffer_load_dwordx4 v20, s[48:51], s99 offen offset:3072 sc1 lds\n"
    "v_mfma_f32_16x16x32_bf16 v[136:139], v[40:43], a[176:179], v[136:139]\n"
    "s_mov_b32 s99, 0\n"
    "v_mfma_f32_16x16x32_bf16 v[144:147], v[40:43], a[240:243], v[144:147]\n"
    "s_add_u32 m0, s28, 65536\n"
    "v_mfma_f32_16x16x32_bf16 v[124:127], v[44:47], a[48:51], v[124:127]\n"
    "ds_read_b64_tr_b16 v[36:37], v15 offset:36864\n"
    "v_mfma_f32_16x16x32_bf16 v[132:135], v[44:47], a[112:115], v[132:135]\n"
    "ds_read_b64_tr_b16 v[38:39], v15 offset:38912\n"
    "v_mfma_f32_16x16x32_bf16 v[140:143], v[44:47], a[176:179], v[140:143]\n"
    "buffer_load_dwordx4 v20, s[52:55], s99 offen offset:0 sc1 lds\n"
    "v_accvgpr_write_b32 a35, 0\n"
    "v_mfma_f32_16x16x32_bf16 v[148:151], v[44:47], a[240:243], v[148:151]\n"
    "s_waitcnt lgkmcnt(8)\n"
    "v_mfma_f32_16x16x32_bf16 v[152:155], v[48:51], a[48:51], v[152:155]\n"
    "ds_read_b64_tr_b16 v[40:41], v12 offset:53248\n"
    "v_mfma_f32_16x16x32_bf16 v[160:163], v[48:51], a[112:115], v[160:163]\n"
    "ds_read_b64_tr_b16 v[42:43], v12 offset:55296\n"
    "v_mfma_f32_16x16x32_bf16 v[168:171], v[48:51], a[176:179], v[168:171]\n"
    "buffer_load_dwordx4 v20, s[52:55], s99 offen offset:1024 sc1 lds\n"
    "v_mfma_f32_16x16x32_bf16 v[176:179], v[48:51], a[240:243], v[176:179]\n"
    "v_accvgpr_write_b32 a36, 0\n"
    "v_mfma_f32_16x16x32_bf16 v[156:159], v[52:55], a[48:51], v[156:159]\n"
    "ds_read_b64_tr_b16 v[44:45], v13 offset:53248\n"
    "v_mfma_f32_16x16x32_bf16 v[164:167], v[52:55], a[112:115], v[164:167]\n"
    "ds_read_b64_tr_b16 v[46:47], v13 offset:55296\n"
    "v_mfma_f32_16x16x32_bf16 v[172:175], v[52:55], a[176:179], v[172:175]\n"
    "v_mfma_f32_16x16x32_bf16 v[180:183], v[52:55], a[240:243], v[180:183]\n"
    "buffer_load_dwordx4 v20, s[52:55], s99 offen offset:2048 sc1 lds\n"
    "s_waitcnt lgkmcnt(8)\n"
    "v_mfma_f32_16x16x32_bf16 v[56:59], v[24:27], a[52:55], v[56:59]\n"
    "ds_read_b64_tr_b16 v[48:49], v14 offset:53248\n"
    "v_mfma_f32_16x16x32_bf16 v[64:67], v[24:27], a[116:119], v[64:67]\n"
    "ds_read_b64_tr_b16 v[50:51], v14 offset:55296\n"
    "v_accvgpr_write_b32 a37, 0\n"
    "v_mfma_f32_16x16x32_bf16 v[72:75], v[24:27], a[180:183], v[72:75]\n"
    "v_mfma_f32_16x16x32_bf16 v[80:83], v[24:27], a[244:247], v[80:83]\n"
    "buffer_load_dwordx4 v20, s[52:55], s99 offen offset:3072 sc1 lds\n"
    "v_mfma_f32_16x16x32_bf16 v[60:63], v[28:31], a[52:55], v[60:63]\n"
    "ds_read_b64_tr_b16 v[52:53], v15 offset:53248\n"
    "v_mfma_f32_16x16x32_bf16 v[68:71], v[28:31], a[116:119], v[68:71]\n"
    "ds_read_b64_tr_b16 v[54:55], v15 offset:55296\n"
    "v_mfma_f32_16x16x32_bf16 v[76:79], v[28:31], a[180:183], v[76:79]\n"
    "v_mfma_f32_16x16x32_bf16 v[84:87], v[28:31], a[244:247], v[84:87]\n"
    "v_accvgpr_write_b32 a38, 0\n"
    "s_waitcnt lgkmcnt(8)\n"
    "v_mfma_f32_16x16x32_bf16 v[88:91], v[32:35], a[52:55], v[88:91]\n"
    "ds_read_b64_tr_b16 v[24:25], v12 offset:40960\n"
    "v_mfma_f32_16x16x32_bf16 v[96:99], v[32:35], a[116:119], v[96:99]\n"
    "ds_read_b64_tr_b16 v[26:27], v12 offset:43008\n"
    "v_mfma_f32_16x16x32_bf16 v[104:107], v[32:35], a[180:183], v[104:107]\n"
    "v_mfma_f32_16x16x32_bf16 v[112:115], v[32:35], a[244:247], v[112:115]\n"
    "v_mfma_f32_16x16x32_bf16 v[92:95], v[36:39], a[52:55], v[92:95]\n"
    "ds_read_b64_tr_b16 v[28:29], v13 offset:40960\n"
    "v_accvgpr_write_b32 a39, 0\n"
    "v_mfma_f32_16x16x32_bf16 v[100:103], v[36:39], a[116:119], v[100:103]\n"
    "ds_read_b64_tr_b16 v[30:31], v13 offset:43008\n"
    "v_mfma_f32_16x16x32_bf16 v[108:111], v[36:39], a[180:183], v[108:111]\n"
    "v_mfma_f32_16x16x32_bf16 v[116:119], v[36:39], a[244:247], v[116:119]\n"
    "s_waitcnt lgkmcnt(8)\n"
    "v_mfma_f32_16x16x32_bf16 v[120:123], v[40:43], a[52:55], v[120:123]\n"
    "ds_read_b64_tr_b16 v[32:33], v14 offset:40960\n"
    "v_mfma_f32_16x16x32_bf16 v[128:131], v[40:43], a[116:119], v[128:131]\n"
    "ds_read_b64_tr_b16 v[34:35], v14 offset:43008\n"
    "v_mfma_f32_16x16x32_bf16 v[136:139], v[40:43], a[180:183], v[136:139]\n"
    "v_accvgpr_write_b32 a40, 0\n"
    "v_mfma_f32_16x16x32_bf16 v[144:147], v[40:43], a[244:247], v[144:147]\n"
    "v_mfma_f32_16x16x32_bf16 v[124:127], v[44:47], a[52:55], v[124:127]\n"
    "ds_read_b64_tr_b16 v[36:37], v15 offset:40960\n"
    "v_mfma_f32_16x16x32_bf16 v[132:135], v[44:47], a[116:119], v[132:135]\n"
    "ds_read_b64_tr_b16 v[38:39], v15 offset:43008\n"
    "v_mfma_f32_16x16x32_bf16 v[140:143], v[44:47], a[180:183], v[140:143]\n"
    "v_mfma_f32_16x16x32_bf16 v[148:151], v[44:47], a[244:247], v[148:151]\n"
    "v_accvgpr_write_b32 a41, 0\n"
    "s_waitcnt lgkmcnt(8)\n"
    "v_mfma_f32_16x16x32_bf16 v[152:155], v[48:51], a[52:55], v[152:155]\n"
    "ds_read_b64_tr_b16 v[40:41], v12 offset:57344\n"
    "v_mfma_f32_16x16x32_bf16 v[160:163], v[48:51], a[116:119], v[160:163]\n"
    "ds_read_b64_tr_b16 v[42:43], v12 offset:59392\n"
    "v_mfma_f32_16x16x32_bf16 v[168:171], v[48:51], a[180:183], v[168:171]\n"
    "v_mfma_f32_16x16x32_bf16 v[176:179], v[48:51], a[244:247], v[176:179]\n"
    "v_mfma_f32_16x16x32_bf16 v[156:159], v[52:55], a[52:55], v[156:159]\n"
    "ds_read_b64_tr_b16 v[44:45], v13 offset:57344\n"
    "v_mfma_f32_16x16x32_bf16 v[164:167], v[52:55], a[116:119], v[164:167]\n"
    "ds_read_b64_tr_b16 v[46:47], v13 offset:59392\n"
    "v_accvgpr_write_b32 a42, 0\n"
    "v_mfma_f32_16x16x32_bf16 v[172:175], v[52:55], a[180:183], v[172:175]\n"
    "v_mfma_f32_16x16x32_bf16 v[180:183], v[52:55], a[244:247], v[180:183]\n"
    "s_waitcnt lgkmcnt(8)\n"
    "v_mfma_f32_16x16x32_bf16 v[56:59], v[24:27], a[56:59], v[56:59]\n"
    "ds_read_b64_tr_b16 v[48:49], v14 offset:57344\n"
    "v_mfma_f32_16x16x32_bf16 v[64:67], v[24:27], a[120:123], v[64:67]\n"
    "ds_read_b64_tr_b16 v[50:51], v14 offset:59392\n"
    "v_mfma_f32_16x16x32_bf16 v[72:75], v[24:27], a[184:187], v[72:75]\n"
    "v_mfma_f32_16x16x32_bf16 v[80:83], v[24:27], a[248:251], v[80:83]\n"
    "v_accvgpr_write_b32 a43, 0\n"
    "v_mfma_f32_16x16x32_bf16 v[60:63], v[28:31], a[56:59], v[60:63]\n"
    "ds_read_b64_tr_b16 v[52:53], v15 offset:57344\n"
    "v_mfma_f32_16x16x32_bf16 v[68:71], v[28:31], a[120:123], v[68:71]\n"
    "ds_read_b64_tr_b16 v[54:55], v15 offset:59392\n"
    "v_mfma_f32_16x16x32_bf16 v[76:79], v[28:31], a[184:187], v[76:79]\n"
    "v_mfma_f32_16x16x32_bf16 v[84:87], v[28:31], a[248:251], v[84:87]\n"
    "s_waitcnt lgkmcnt(8)\n"
    "v_mfma_f32_16x16x32_bf16 v[88:91], v[32:35], a[56:59], v[88:91]\n"
    "ds_read_b64_tr_b16 v[24:25], v12 offset:45056\n"
    "v_accvgpr_write_b32 a44, 0\n"
    "v_mfma_f32_16x16x32_bf16 v[96:99], v[32:35], a[120:123], v[96:99]\n"
    "ds_read_b64_tr_b16 v[26:27], v12 offset:47104\n"
    "v_mfma_f32_16x16x32_bf16 v[104:107], v[32:35], a[184:187], v[104:107]\n"
    "v_mfma_f32_16x16x32_bf16 v[112:115], v[32:35], a[248:251], v[112:115]\n"
    "v_mfma_f32_16x16x32_bf16 v[92:95], v[36:39], a[56:59], v[92:95]\n"
    "ds_read_b64_tr_b16 v[28:29], v13 offset:45056\n"
    "v_mfma_f32_16x16x32_bf16 v[100:103], v[36:39], a[120:123], v[100:103]\n"
    "ds_read_b64_tr_b16 v[30:31], v13 offset:47104\n"
    "v_mfma_f32_16x16x32_bf16 v[108:111], v[36:39], a[184:187], v[108:111]\n"
    "v_accvgpr_write_b32 a45, 0\n"
    "v_mfma_f32_16x16x32_bf16 v[116:119], v[36:39], a[248:251], v[116:119]\n"
    "s_waitcnt lgkmcnt(8)\n"
    "v_mfma_f32_16x16x32_bf16 v[120:123], v[40:43], a[56:59], v[120:123]\n"
    "ds_read_b64_tr_b16 v[32:33], v14 offset:45056\n"
    "v_mfma_f32_16x16x32_bf16 v[128:131], v[40:43], a[120:123], v[128:131]\n"
    "ds_read_b64_tr_b16 v[34:35], v14 offset:47104\n"
    "v_mfma_f32_16x16x32_bf16 v[136:139], v[40:43], a[184:187], v[136:139]\n"
    "v_mfma_f32_16x16x32_bf16 v[144:147], v[40:43], a[248:251], v[144:147]\n"
    "v_accvgpr_write_b32 a46, 0\n"
    "v_mfma_f32_16x16x32_bf16 v[124:127], v[44:47], a[56:59], v[124:127]\n"
    "ds_read_b64_tr_b16 v[36:37], v15 offset:45056\n"
    "v_mfma_f32_16x16x32_bf16 v[132:135], v[44:47], a[120:123], v[132:135]\n"
    "ds_read_b64_tr_b16 v[38:39], v15 offset:47104\n"
    "v_mfma_f32_16x16x32_bf16 v[140:143], v[44:47], a[184:187], v[140:143]\n"
    "v_mfma_f32_16x16x32_bf16 v[148:151], v[44:47], a[248:251], v[148:151]\n"
    "s_waitcnt lgkmcnt(8)\n"
    "v_mfma_f32_16x16x32_bf16 v[152:155], v[48:51], a[56:59], v[152:155]\n"
    "ds_read_b64_tr_b16 v[40:41], v12 offset:61440\n"
    "v_mfma_f32_16x16x32_bf16 v[160:163], v[48:51], a[120:123], v[160:163]\n"
    "ds_read_b64_tr_b16 v[42:43], v12 offset:63488\n"
    "v_accvgpr_write_b32 a47, 0\n"
    "v_mfma_f32_16x16x32_bf16 v[168:171], v[48:51], a[184:187], v[168:171]\n"
    "v_mfma_f32_16x16x32_bf16 v[176:179], v[48:51], a[248:251], v[176:179]\n"
    "v_mfma_f32_16x16x32_bf16 v[156:159], v[52:55], a[56:59], v[156:159]\n"
    "ds_read_b64_tr_b16 v[44:45], v13 offset:61440\n"
    "v_mfma_f32_16x16x32_bf16 v[164:167], v[52:55], a[120:123], v[164:167]\n"
    "ds_read_b64_tr_b16 v[46:47], v13 offset:63488\n"
    "v_mfma_f32_16x16x32_bf16 v[172:175], v[52:55], a[184:187], v[172:175]\n"
    "v_mfma_f32_16x16x32_bf16 v[180:183], v[52:55], a[248:251], v[180:183]\n"
    "s_waitcnt lgkmcnt(8)\n"
    "v_mfma_f32_16x16x32_bf16 v[56:59], v[24:27], a[60:63], v[56:59]\n"
    "ds_read_b64_tr_b16 v[48:49], v14 offset:61440\n"
    "v_mfma_f32_16x16x32_bf16 v[64:67], v[24:27], a[124:127], v[64:67]\n"
    "ds_read_b64_tr_b16 v[50:51], v14 offset:63488\n"
    "v_mfma_f32_16x16x32_bf16 v[72:75], v[24:27], a[188:191], v[72:75]\n"
    "v_mfma_f32_16x16x32_bf16 v[80:83], v[24:27], a[252:255], v[80:83]\n"
    "v_mfma_f32_16x16x32_bf16 v[60:63], v[28:31], a[60:63], v[60:63]\n"
    "ds_read_b64_tr_b16 v[52:53], v15 offset:61440\n"
    "v_mfma_f32_16x16x32_bf16 v[68:71], v[28:31], a[124:127], v[68:71]\n"
    "ds_read_b64_tr_b16 v[54:55], v15 offset:63488\n"
    "v_mfma_f32_16x16x32_bf16 v[76:79], v[28:31], a[188:191], v[76:79]\n"
    "v_mfma_f32_16x16x32_bf16 v[84:87], v[28:31], a[252:255], v[84:87]\n"
    "s_waitcnt lgkmcnt(8)\n"
    "v_mfma_f32_16x16x32_bf16 v[88:91], v[32:35], a[60:63], v[88:91]\n"
    "v_mfma_f32_16x16x32_bf16 v[96:99], v[32:35], a[124:127], v[96:99]\n"
    "v_mfma_f32_16x16x32_bf16 v[104:107], v[32:35], a[188:191], v[104:107]\n"
    "v_mfma_f32_16x16x32_bf16 v[112:115], v[32:35], a[252:255], v[112:115]\n"
    "v_mfma_f32_16x16x32_bf16 v[92:95], v[36:39], a[60:63], v[92:95]\n"
    "v_mfma_f32_16x16x32_bf16 v[100:103], v[36:39], a[124:127], v[100:103]\n"
    "v_mfma_f32_16x16x32_bf16 v[108:111], v[36:39], a[188:191], v[108:111]\n"
    "v_mfma_f32_16x16x32_bf16 v[116:119], v[36:39], a[252:255], v[116:119]\n"
    "s_waitcnt lgkmcnt(4)\n"
    "v_mfma_f32_16x16x32_bf16 v[120:123], v[40:43], a[60:63], v[120:123]\n"
    "v_mfma_f32_16x16x32_bf16 v[128:131], v[40:43], a[124:127], v[128:131]\n"
    "v_mfma_f32_16x16x32_bf16 v[136:139], v[40:43], a[188:191], v[136:139]\n"
    "v_mfma_f32_16x16x32_bf16 v[144:147], v[40:43], a[252:255], v[144:147]\n"
    "v_mfma_f32_16x16x32_bf16 v[124:127], v[44:47], a[60:63], v[124:127]\n"
    "v_mfma_f32_16x16x32_bf16 v[132:135], v[44:47], a[124:127], v[132:135]\n"
    "v_mfma_f32_16x16x32_bf16 v[140:143], v[44:47], a[188:191], v[140:143]\n"
    "v_mfma_f32_16x16x32_bf16 v[148:151], v[44:47], a[252:255], v[148:151]\n"
    "s_waitcnt lgkmcnt(0)\n"
    "v_mfma_f32_16x16x32_bf16 v[152:155], v[48:51], a[60:63], v[152:155]\n"
    "v_mfma_f32_16x16x32_bf16 v[160:163], v[48:51], a[124:127], v[160:163]\n"
    "v_mfma_f32_16x16x32_bf16 v[168:171], v[48:51], a[188:191], v[168:171]\n"
    "v_mfma_f32_16x16x32_bf16 v[176:179], v[48:51], a[252:255], v[176:179]\n"
    "v_mfma_f32_16x16x32_bf16 v[156:159], v[52:55], a[60:63], v[156:159]\n"
    "v_mfma_f32_16x16x32_bf16 v[164:167], v[52:55], a[124:127], v[164:167]\n"
    "v_mfma_f32_16x16x32_bf16 v[172:175], v[52:55], a[188:191], v[172:175]\n"
    "v_mfma_f32_16x16x32_bf16 v[180:183], v[52:55], a[252:255], v[180:183]\n"
    "s_branch L_k1_end_%=\n"
    "L_k1_v_%=:\n"
    "ds_read_b64_tr_b16 v[24:25], v8 offset:0\n"
    "ds_read_b64_tr_b16 v[26:27], v8 offset:2048\n"
    "ds_read_b64_tr_b16 v[28:29], v9 offset:0\n"
    "ds_read_b64_tr_b16 v[30:31], v9 offset:2048\n"
    "ds_read_b64_tr_b16 v[32:33], v10 offset:0\n"
    "ds_read_b64_tr_b16 v[34:35], v10 offset:2048\n"
    "ds_read_b64_tr_b16 v[36:37], v11 offset:0\n"
    "ds_read_b64_tr_b16 v[38:39], v11 offset:2048\n"
    "ds_read_b64_tr_b16 v[40:41], v8 offset:16384\n"
    "ds_read_b64_tr_b16 v[42:43], v8 offset:18432\n"
    "ds_read_b64_tr_b16 v[44:45], v9 offset:16384\n"
    "ds_read_b64_tr_b16 v[46:47], v9 offset:18432\n"
    "s_waitcnt vmcnt(4)\n"
    "s_waitcnt lgkmcnt(8)\n"
    "v_mfma_f32_16x16x32_bf16 v[56:59], a[0:3], v[24:27], 0\n"
    "ds_read_b64_tr_b16 v[48:49], v10 offset:16384\n"
    "s_mov_b32 s88, 16384\n"
    "v_mfma_f32_16x16x32_bf16 v[64:67], a[64:67], v[24:27], 0\n"
    "ds_read_b64_tr_b16 v[50:51], v10 offset:18432\n"
    "buffer_load_dwordx4 a[8:11], v0, s[72:75], s88 offen offset:0 sc1\n"
    "v_mfma_f32_16x16x32_bf16 v[72:75], a[128:131], v[24:27], 0\n"
    "v_mfma_f32_16x16x32_bf16 v[80:83], a[192:195], v[24:27], 0\n"
    "v_mfma_f32_16x16x32_bf16 v[60:63], a[0:3], v[28:31], 0\n"
    "ds_read_b64_tr_b16 v[52:53], v11 offset:16384\n"
    "v_mfma_f32_16x16x32_bf16 v[68:71], a[64:67], v[28:31], 0\n"
    "ds_read_b64_tr_b16 v[54:55], v11 offset:18432\n"
    "buffer_load_dwordx4 a[72:75], v0, s[72:75], s88 offen offset:1024 sc1\n"
    "v_mfma_f32_16x16x32_bf16 v[76:79], a[128:131], v[28:31], 0\n"
    "v_mfma_f32_16x16x32_bf16 v[84:87], a[192:195], v[28:31], 0\n"
    "s_waitcnt lgkmcnt(8)\n"
    "v_mfma_f32_16x16x32_bf16 v[88:91], a[0:3], v[32:35], 0\n"
    "ds_read_b64_tr_b16 v[24:25], v8 offset:4096\n"
    "v_mfma_f32_16x16x32_bf16 v[96:99], a[64:67], v[32:35], 0\n"
    "ds_read_b64_tr_b16 v[26:27], v8 offset:6144\n"
    "v_mfma_f32_16x16x32_bf16 v[104:107], a[128:131], v[32:35], 0\n"
    "buffer_load_dwordx4 a[136:139], v1, s[72:75], s88 offen offset:0 sc1\n"
    "v_mfma_f32_16x16x32_bf16 v[112:115], a[192:195], v[32:35], 0\n"
    "v_mfma_f32_16x16x32_bf16 v[92:95], a[0:3], v[36:39], 0\n"
    "ds_read_b64_tr_b16 v[28:29], v9 offset:4096\n"
    "v_mfma_f32_16x16x32_bf16 v[100:103], a[64:67], v[36:39], 0\n"
    "ds_read_b64_tr_b16 v[30:31], v9 offset:6144\n"
    "v_mfma_f32_16x16x32_bf16 v[108:111], a[128:131], v[36:39], 0\n"
    "buffer_load_dwordx4 a[200:203], v1, s[72:75], s88 offen offset:1024 sc1\n"
    "v_mfma_f32_16x16x32_bf16 v[116:119], a[192:195], v[36:39], 0\n"
    "s_mov_b32 s88, 24576\n"
    "s_waitcnt lgkmcnt(8)\n"
    "v_mfma_f32_16x16x32_bf16 v[120:123], a[0:3], v[40:43], 0\n"
    "ds_read_b64_tr_b16 v[32:33], v10 offset:4096\n"
    "v_mfma_f32_16x16x32_bf16 v[128:131], a[64:67], v[40:43], 0\n"
    "ds_read_b64_tr_b16 v[34:35], v10 offset:6144\n"
    "v_mfma_f32_16x16x32_bf16 v[136:139], a[128:131], v[40:43], 0\n"
    "v_mfma_f32_16x16x32_bf16 v[144:147], a[192:195], v[40:43], 0\n"
    "buffer_load_dwordx4 a[12:15], v0, s[72:75], s88 offen offset:0 sc1\n"
    "v_mfma_f32_16x16x32_bf16 v[124:127], a[0:3], v[44:47], 0\n"
    "ds_read_b64_tr_b16 v[36:37], v11 offset:4096\n"
    "v_mfma_f32_16x16x32_bf16 v[132:135], a[64:67], v[44:47], 0\n"
    "ds_read_b64_tr_b16 v[38:39], v11 offset:6144\n"
    "v_mfma_f32_16x16x32_bf16 v[140:143], a[128:131], v[44:47], 0\n"
    "v_mfma_f32_16x16x32_bf16 v[148:151], a[192:195], v[44:47], 0\n"
    "buffer_load_dwordx4 a[76:79], v0, s[72:75], s88 offen offset:1024 sc1\n"
    "s_waitcnt lgkmcnt(8)\n"
    "v_mfma_f32_16x16x32_bf16 v[152:155], a[0:3], v[48:51], 0\n"
    "ds_read_b64_tr_b16 v[40:41], v8 offset:20480\n"
    "v_mfma_f32_16x16x32_bf16 v[160:163], a[64:67], v[48:51], 0\n"
    "ds_read_b64_tr_b16 v[42:43], v8 offset:22528\n"
    "v_mfma_f32_16x16x32_bf16 v[168:171], a[128:131], v[48:51], 0\n"
    "v_mfma_f32_16x16x32_bf16 v[176:179], a[192:195], v[48:51], 0\n"
    "v_mfma_f32_16x16x32_bf16 v[156:159], a[0:3], v[52:55], 0\n"
    "ds_read_b64_tr_b16 v[44:45], v9 offset:20480\n"
    "buffer_load_dwordx4 a[140:143], v1, s[72:75], s88 offen offset:0 sc1\n"
    "v_mfma_f32_16x16x32_bf16 v[164:167], a[64:67], v[52:55], 0\n"
    "ds_read_b64_tr_b16 v[46:47], v9 offset:22528\n"
    "v_mfma_f32_16x16x32_bf16 v[172:175], a[128:131], v[52:55], 0\n"
    "v_mfma_f32_16x16x32_bf16 v[180:183], a[192:195], v[52:55], 0\n"
    "s_waitcnt vmcnt(7)\n"
    "s_waitcnt lgkmcnt(8)\n"
    "v_mfma_f32_16x16x32_bf16 v[56:59], a[4:7], v[24:27], v[56:59]\n"
    "ds_read_b64_tr_b16 v[48:49], v10 offset:20480\n"
    "buffer_load_dwordx4 a[204:207], v1, s[72:75], s88 offen offset:1024 sc1\n"
    "v_mfma_f32_16x16x32_bf16 v[64:67], a[68:71], v[24:27], v[64:67]\n"
    "ds_read_b64_tr_b16 v[50:51], v10 offset:22528\n"
    "s_mov_b32 s88, 32768\n"
    "v_mfma_f32_16x16x32_bf16 v[72:75], a[132:135], v[24:27], v[72:75]\n"
    "v_mfma_f32_16x16x32_bf16 v[80:83], a[196:199], v[24:27], v[80:83]\n"
    "v_mfma_f32_16x16x32_bf16 v[60:63], a[4:7], v[28:31], v[60:63]\n"
    "ds_read_b64_tr_b16 v[52:53], v11 offset:20480\n"
    "v_mfma_f32_16x16x32_bf16 v[68:71], a[68:71], v[28:31], v[68:71]\n"
    "ds_read_b64_tr_b16 v[54:55], v11 offset:22528\n"
    "buffer_load_dwordx4 a[16:19], v0, s[72:75], s88 offen offset:0 sc1\n"
    "v_mfma_f32_16x16x32_bf16 v[76:79], a[132:135], v[28:31], v[76:79]\n"
    "v_mfma_f32_16x16x32_bf16 v[84:87], a[196:199], v[28:31], v[84:87]\n"
    "s_waitcnt lgkmcnt(8)\n"
    "v_mfma_f32_16x16x32_bf16 v[88:91], a[4:7], v[32:35], v[88:91]\n"
    "ds_read_b64_tr_b16 v[24:25], v8 offset:8192\n"
    "v_mfma_f32_16x16x32_bf16 v[96:99], a[68:71], v[32:35], v[96:99]\n"
    "ds_read_b64_tr_b16 v[26:27], v8 offset:10240\n"
    "buffer_load_dwordx4 a[80:83], v0, s[72:75], s88 offen offset:1024 sc1\n"
    "v_mfma_f32_16x16x32_bf16 v[104:107], a[132:135], v[32:35], v[104:107]\n"
    "v_mfma_f32_16x16x32_bf16 v[112:115], a[196:199], v[32:35], v[112:115]\n"
    "v_mfma_f32_16x16x32_bf16 v[92:95], a[4:7], v[36:39], v[92:95]\n"
    "ds_read_b64_tr_b16 v[28:29], v9 offset:8192\n"
    "v_mfma_f32_16x16x32_bf16 v[100:103], a[68:71], v[36:39], v[100:103]\n"
    "ds_read_b64_tr_b16 v[30:31], v9 offset:10240\n"
    "v_mfma_f32_16x16x32_bf16 v[108:111], a[132:135], v[36:39], v[108:111]\n"
    "buffer_load_dwordx4 a[144:147], v1, s[72:75], s88 offen offset:0 sc1\n"
    "v_mfma_f32_16x16x32_bf16 v[116:119], a[196:199], v[36:39], v[116:119]\n"
    "s_waitcnt lgkmcnt(8)\n"
    "v_mfma_f32_16x16x32_bf16 v[120:123], a[4:7], v[40:43], v[120:123]\n"
    "ds_read_b64_tr_b16 v[32:33], v10 offset:8192\n"
    "v_mfma_f32_16x16x32_bf16 v[128:131], a[68:71], v[40:43], v[128:131]\n"
    "ds_read_b64_tr_b16 v[34:35], v10 offset:10240\n"
    "v_mfma_f32_16x16x32_bf16 v[136:139], a[132:135], v[40:43], v[136:139]\n"
    "buffer_load_dwordx4 a[208:211], v1, s[72:75], s88 offen offset:1024 sc1\n"
    "v_mfma_f32_16x16x32_bf16 v[144:147], a[196:199], v[40:43], v[144:147]\n"
    "s_mov_b32 s88, 40960\n"
    "v_mfma_f32_16x16x32_bf16 v[124:127], a[4:7], v[44:47], v[124:127]\n"
    "ds_read_b64_tr_b16 v[36:37], v11 offset:8192\n"
    "v_mfma_f32_16x16x32_bf16 v[132:135], a[68:71], v[44:47], v[132:135]\n"
    "ds_read_b64_tr_b16 v[38:39], v11 offset:10240\n"
    "v_mfma_f32_16x16x32_bf16 v[140:143], a[132:135], v[44:47], v[140:143]\n"
    "v_mfma_f32_16x16x32_bf16 v[148:151], a[196:199], v[44:47], v[148:151]\n"
    "buffer_load_dwordx4 a[20:23], v0, s[72:75], s88 offen offset:0 sc1\n"
    "s_waitcnt lgkmcnt(8)\n"
    "v_mfma_f32_16x16x32_bf16 v[152:155], a[4:7], v[48:51], v[152:155]\n"
    "ds_read_b64_tr_b16 v[40:41], v8 offset:24576\n"
    "v_mfma_f32_16x16x32_bf16 v[160:163], a[68:71], v[48:51], v[160:163]\n"
    "ds_read_b64_tr_b16 v[42:43], v8 offset:26624\n"
    "v_mfma_f32_16x16x32_bf16 v[168:171], a[132:135], v[48:51], v[168:171]\n"
    "v_mfma_f32_16x16x32_bf16 v[176:179], a[196:199], v[48:51], v[176:179]\n"
    "buffer_load_dwordx4 a[84:87], v0, s[72:75], s88 offen offset:1024 sc1\n"
    "v_mfma_f32_16x16x32_bf16 v[156:159], a[4:7], v[52:55], v[156:159]\n"
    "ds_read_b64_tr_b16 v[44:45], v9 offset:24576\n"
    "v_mfma_f32_16x16x32_bf16 v[164:167], a[68:71], v[52:55], v[164:167]\n"
    "ds_read_b64_tr_b16 v[46:47], v9 offset:26624\n"
    "v_mfma_f32_16x16x32_bf16 v[172:175], a[132:135], v[52:55], v[172:175]\n"
    "v_mfma_f32_16x16x32_bf16 v[180:183], a[196:199], v[52:55], v[180:183]\n"
    "s_waitcnt vmcnt(10)\n"
    "s_waitcnt lgkmcnt(8)\n"
    "v_mfma_f32_16x16x32_bf16 v[56:59], a[8:11], v[24:27], v[56:59]\n"
    "ds_read_b64_tr_b16 v[48:49], v10 offset:24576\n"
    "buffer_load_dwordx4 a[148:151], v1, s[72:75], s88 offen offset:0 sc1\n"
    "v_mfma_f32_16x16x32_bf16 v[64:67], a[72:75], v[24:27], v[64:67]\n"
    "ds_read_b64_tr_b16 v[50:51], v10 offset:26624\n"
    "v_mfma_f32_16x16x32_bf16 v[72:75], a[136:139], v[24:27], v[72:75]\n"
    "v_mfma_f32_16x16x32_bf16 v[80:83], a[200:203], v[24:27], v[80:83]\n"
    "v_mfma_f32_16x16x32_bf16 v[60:63], a[8:11], v[28:31], v[60:63]\n"
    "ds_read_b64_tr_b16 v[52:53], v11 offset:24576\n"
    "buffer_load_dwordx4 a[212:215], v1, s[72:75], s88 offen offset:1024 sc1\n"
    "v_mfma_f32_16x16x32_bf16 v[68:71], a[72:75], v[28:31], v[68:71]\n"
    "ds_read_b64_tr_b16 v[54:55], v11 offset:26624\n"
    "s_mov_b32 s88, 49152\n"
    "v_mfma_f32_16x16x32_bf16 v[76:79], a[136:139], v[28:31], v[76:79]\n"
    "v_mfma_f32_16x16x32_bf16 v[84:87], a[200:203], v[28:31], v[84:87]\n"
    "s_waitcnt lgkmcnt(8)\n"
    "v_mfma_f32_16x16x32_bf16 v[88:91], a[8:11], v[32:35], v[88:91]\n"
    "ds_read_b64_tr_b16 v[24:25], v8 offset:12288\n"
    "v_mfma_f32_16x16x32_bf16 v[96:99], a[72:75], v[32:35], v[96:99]\n"
    "ds_read_b64_tr_b16 v[26:27], v8 offset:14336\n"
    "buffer_load_dwordx4 a[24:27], v0, s[72:75], s88 offen offset:0 sc1\n"
    "v_mfma_f32_16x16x32_bf16 v[104:107], a[136:139], v[32:35], v[104:107]\n"
    "v_mfma_f32_16x16x32_bf16 v[112:115], a[200:203], v[32:35], v[112:115]\n"
    "v_mfma_f32_16x16x32_bf16 v[92:95], a[8:11], v[36:39], v[92:95]\n"
    "ds_read_b64_tr_b16 v[28:29], v9 offset:12288\n"
    "v_mfma_f32_16x16x32_bf16 v[100:103], a[72:75], v[36:39], v[100:103]\n"
    "ds_read_b64_tr_b16 v[30:31], v9 offset:14336\n"
    "buffer_load_dwordx4 a[88:91], v0, s[72:75], s88 offen offset:1024 sc1\n"
    "v_mfma_f32_16x16x32_bf16 v[108:111], a[136:139], v[36:39], v[108:111]\n"
    "v_mfma_f32_16x16x32_bf16 v[116:119], a[200:203], v[36:39], v[116:119]\n"
    "s_waitcnt lgkmcnt(8)\n"
    "v_mfma_f32_16x16x32_bf16 v[120:123], a[8:11], v[40:43], v[120:123]\n"
    "ds_read_b64_tr_b16 v[32:33], v10 offset:12288\n"
    "v_mfma_f32_16x16x32_bf16 v[128:131], a[72:75], v[40:43], v[128:131]\n"
    "ds_read_b64_tr_b16 v[34:35], v10 offset:14336\n"
    "v_mfma_f32_16x16x32_bf16 v[136:139], a[136:139], v[40:43], v[136:139]\n"
    "buffer_load_dwordx4 a[152:155], v1, s[72:75], s88 offen offset:0 sc1\n"
    "v_mfma_f32_16x16x32_bf16 v[144:147], a[200:203], v[40:43], v[144:147]\n"
    "v_mfma_f32_16x16x32_bf16 v[124:127], a[8:11], v[44:47], v[124:127]\n"
    "ds_read_b64_tr_b16 v[36:37], v11 offset:12288\n"
    "v_mfma_f32_16x16x32_bf16 v[132:135], a[72:75], v[44:47], v[132:135]\n"
    "ds_read_b64_tr_b16 v[38:39], v11 offset:14336\n"
    "v_mfma_f32_16x16x32_bf16 v[140:143], a[136:139], v[44:47], v[140:143]\n"
    "buffer_load_dwordx4 a[216:219], v1, s[72:75], s88 offen offset:1024 sc1\n"
    "v_mfma_f32_16x16x32_bf16 v[148:151], a[200:203], v[44:47], v[148:151]\n"
    "s_mov_b32 s29, 2097152\n"
    "s_waitcnt lgkmcnt(8)\n"
    "v_mfma_f32_16x16x32_bf16 v[152:155], a[8:11], v[48:51], v[152:155]\n"
    "ds_read_b64_tr_b16 v[40:41], v8 offset:28672\n"
    "s_add_u32 m0, s28, 65536\n"
    "v_mfma_f32_16x16x32_bf16 v[160:163], a[72:75], v[48:51], v[160:163]\n"
    "ds_read_b64_tr_b16 v[42:43], v8 offset:30720\n"
    "v_mfma_f32_16x16x32_bf16 v[168:171], a[136:139], v[48:51], v[168:171]\n"
    "v_mfma_f32_16x16x32_bf16 v[176:179], a[200:203], v[48:51], v[176:179]\n"
    "buffer_load_dwordx4 v4, s[76:79], s29 offen offset:0 lds\n"
    "v_mfma_f32_16x16x32_bf16 v[156:159], a[8:11], v[52:55], v[156:159]\n"
    "ds_read_b64_tr_b16 v[44:45], v9 offset:28672\n"
    "v_mfma_f32_16x16x32_bf16 v[164:167], a[72:75], v[52:55], v[164:167]\n"
    "ds_read_b64_tr_b16 v[46:47], v9 offset:30720\n"
    "v_mfma_f32_16x16x32_bf16 v[172:175], a[136:139], v[52:55], v[172:175]\n"
    "v_mfma_f32_16x16x32_bf16 v[180:183], a[200:203], v[52:55], v[180:183]\n"
    "buffer_load_dwordx4 v5, s[76:79], s29 offen offset:1024 lds\n"
    "s_waitcnt vmcnt(14)\n"
    "s_waitcnt lgkmcnt(8)\n"
    "v_mfma_f32_16x16x32_bf16 v[56:59], a[12:15], v[24:27], v[56:59]\n"
    "ds_read_b64_tr_b16 v[48:49], v10 offset:28672\n"
    "v_mfma_f32_16x16x32_bf16 v[64:67], a[76:79], v[24:27], v[64:67]\n"
    "ds_read_b64_tr_b16 v[50:51], v10 offset:30720\n"
    "v_mfma_f32_16x16x32_bf16 v[72:75], a[140:143], v[24:27], v[72:75]\n"
    "v_mfma_f32_16x16x32_bf16 v[80:83], a[204:207], v[24:27], v[80:83]\n"
    "v_mfma_f32_16x16x32_bf16 v[60:63], a[12:15], v[28:31], v[60:63]\n"
    "ds_read_b64_tr_b16 v[52:53], v11 offset:28672\n"
    "buffer_load_dwordx4 v6, s[76:79], s29 offen offset:2048 lds\n"
    "v_mfma_f32_16x16x32_bf16 v[68:71], a[76:79], v[28:31], v[68:71]\n"
    "ds_read_b64_tr_b16 v[54:55], v11 offset:30720\n"
    "v_mfma_f32_16x16x32_bf16 v[76:79], a[140:143], v[28:31], v[76:79]\n"
    "v_mfma_f32_16x16x32_bf16 v[84:87], a[204:207], v[28:31], v[84:87]\n"
    "s_waitcnt lgkmcnt(8)\n"
    "v_mfma_f32_16x16x32_bf16 v[88:91], a[12:15], v[32:35], v[88:91]\n"
    "buffer_load_dwordx4 v7, s[76:79], s29 offen offset:3072 lds\n"
    "v_mfma_f32_16x16x32_bf16 v[96:99], a[76:79], v[32:35], v[96:99]\n"
    "s_mov_b32 s29, 2097280\n"
    "v_mfma_f32_16x16x32_bf16 v[104:107], a[140:143], v[32:35], v[104:107]\n"
    "s_add_u32 m0, s28, 81920\n"
    "v_mfma_f32_16x16x32_bf16 v[112:115], a[204:207], v[32:35], v[112:115]\n"
    "v_mfma_f32_16x16x32_bf16 v[92:95], a[12:15], v[36:39], v[92:95]\n"
    "v_mfma_f32_16x16x32_bf16 v[100:103], a[76:79], v[36:39], v[100:103]\n"
    "buffer_load_dwordx4 v4, s[76:79], s29 offen offset:0 lds\n"
    "v_mfma_f32_16x16x32_bf16 v[108:111], a[140:143], v[36:39], v[108:111]\n"
    "v_mfma_f32_16x16x32_bf16 v[116:119], a[204:207], v[36:39], v[116:119]\n"
    "s_waitcnt vmcnt(17) lgkmcnt(0)\n"
    "s_barrier\n"
    "v_mfma_f32_16x16x32_bf16 v[120:123], a[12:15], v[40:43], v[120:123]\n"
    "ds_read_b64_tr_b16 v[24:25], v8 offset:32768\n"
    "v_mfma_f32_16x16x32_bf16 v[128:131], a[76:79], v[40:43], v[128:131]\n"
    "ds_read_b64_tr_b16 v[26:27], v8 offset:34816\n"
    "buffer_load_dwordx4 v5, s[76:79], s29 offen offset:1024 lds\n"
    "v_mfma_f32_16x16x32_bf16 v[136:139], a[140:143], v[40:43], v[136:139]\n"
    "ds_read_b64_tr_b16 v[28:29], v9 offset:32768\n"
    "v_mfma_f32_16x16x32_bf16 v[144:147], a[204:207], v[40:43], v[144:147]\n"
    "ds_read_b64_tr_b16 v[30:31], v9 offset:34816\n"
    "v_mfma_f32_16x16x32_bf16 v[124:127], a[12:15], v[44:47], v[124:127]\n"
    "ds_read_b64_tr_b16 v[32:33], v10 offset:32768\n"
    "v_mfma_f32_16x16x32_bf16 v[132:135], a[76:79], v[44:47], v[132:135]\n"
    "ds_read_b64_tr_b16 v[34:35], v10 offset:34816\n"
    "v_mfma_f32_16x16x32_bf16 v[140:143], a[140:143], v[44:47], v[140:143]\n"
    "ds_read_b64_tr_b16 v[36:37], v11 offset:32768\n"
    "buffer_load_dwordx4 v6, s[76:79], s29 offen offset:2048 lds\n"
    "v_mfma_f32_16x16x32_bf16 v[148:151], a[204:207], v[44:47], v[148:151]\n"
    "ds_read_b64_tr_b16 v[38:39], v11 offset:34816\n"
    "v_mfma_f32_16x16x32_bf16 v[152:155], a[12:15], v[48:51], v[152:155]\n"
    "ds_read_b64_tr_b16 v[40:41], v8 offset:49152\n"
    "v_mfma_f32_16x16x32_bf16 v[160:163], a[76:79], v[48:51], v[160:163]\n"
    "ds_read_b64_tr_b16 v[42:43], v8 offset:51200\n"
    "v_mfma_f32_16x16x32_bf16 v[168:171], a[140:143], v[48:51], v[168:171]\n"
    "buffer_load_dwordx4 v7, s[76:79], s29 offen offset:3072 lds\n"
    "v_mfma_f32_16x16x32_bf16 v[176:179], a[204:207], v[48:51], v[176:179]\n"
    "s_mov_b32 s88, 57344\n"
    "v_mfma_f32_16x16x32_bf16 v[156:159], a[12:15], v[52:55], v[156:159]\n"
    "ds_read_b64_tr_b16 v[44:45], v9 offset:49152\n"
    "v_mfma_f32_16x16x32_bf16 v[164:167], a[76:79], v[52:55], v[164:167]\n"
    "ds_read_b64_tr_b16 v[46:47], v9 offset:51200\n"
    "v_mfma_f32_16x16x32_bf16 v[172:175], a[140:143], v[52:55], v[172:175]\n"
    "v_mfma_f32_16x16x32_bf16 v[180:183], a[204:207], v[52:55], v[180:183]\n"
    "buffer_load_dwordx4 a[28:31], v0, s[72:75], s88 offen offset:0 sc1\n"
    "s_waitcnt vmcnt(17)\n"
    "s_waitcnt lgkmcnt(8)\n"
    "v_mfma_f32_16x16x32_bf16 v[56:59], a[16:19], v[24:27], v[56:59]\n"
    "ds_read_b64_tr_b16 v[48:49], v10 offset:49152\n"
    "v_mfma_f32_16x16x32_bf16 v[64:67], a[80:83], v[24:27], v[64:67]\n"
    "ds_read_b64_tr_b16 v[50:51], v10 offset:51200\n"
    "v_mfma_f32_16x16x32_bf16 v[72:75], a[144:147], v[24:27], v[72:75]\n"
    "v_mfma_f32_16x16x32_bf16 v[80:83], a[208:211], v[24:27], v[80:83]\n"
    "buffer_load_dwordx4 a[92:95], v0, s[72:75], s88 offen offset:1024 sc1\n"
    "v_mfma_f32_16x16x32_bf16 v[60:63], a[16:19], v[28:31], v[60:63]\n"
    "ds_read_b64_tr_b16 v[52:53], v11 offset:49152\n"
    "v_mfma_f32_16x16x32_bf16 v[68:71], a[80:83], v[28:31], v[68:71]\n"
    "ds_read_b64_tr_b16 v[54:55], v11 offset:51200\n"
    "v_mfma_f32_16x16x32_bf16 v[76:79], a[144:147], v[28:31], v[76:79]\n"
    "v_mfma_f32_16x16x32_bf16 v[84:87], a[208:211], v[28:31], v[84:87]\n"
    "s_waitcnt lgkmcnt(8)\n"
    "v_mfma_f32_16x16x32_bf16 v[88:91], a[16:19], v[32:35], v[88:91]\n"
    "ds_read_b64_tr_b16 v[24:25], v8 offset:36864\n"
    "buffer_load_dwordx4 a[156:159], v1, s[72:75], s88 offen offset:0 sc1\n"
    "v_mfma_f32_16x16x32_bf16 v[96:99], a[80:83], v[32:35], v[96:99]\n"
    "ds_read_b64_tr_b16 v[26:27], v8 offset:38912\n"
    "v_mfma_f32_16x16x32_bf16 v[104:107], a[144:147], v[32:35], v[104:107]\n"
    "v_mfma_f32_16x16x32_bf16 v[112:115], a[208:211], v[32:35], v[112:115]\n"
    "v_mfma_f32_16x16x32_bf16 v[92:95], a[16:19], v[36:39], v[92:95]\n"
    "ds_read_b64_tr_b16 v[28:29], v9 offset:36864\n"
    "buffer_load_dwordx4 a[220:223], v1, s[72:75], s88 offen offset:1024 sc1\n"
    "v_mfma_f32_16x16x32_bf16 v[100:103], a[80:83], v[36:39], v[100:103]\n"
    "ds_read_b64_tr_b16 v[30:31], v9 offset:38912\n"
    "v_mfma_f32_16x16x32_bf16 v[108:111], a[144:147], v[36:39], v[108:111]\n"
    "v_mfma_f32_16x16x32_bf16 v[116:119], a[208:211], v[36:39], v[116:119]\n"
    "s_waitcnt lgkmcnt(8)\n"
    "v_mfma_f32_16x16x32_bf16 v[120:123], a[16:19], v[40:43], v[120:123]\n"
    "ds_read_b64_tr_b16 v[32:33], v10 offset:36864\n"
    "v_mfma_f32_16x16x32_bf16 v[128:131], a[80:83], v[40:43], v[128:131]\n"
    "ds_read_b64_tr_b16 v[34:35], v10 offset:38912\n"
    "global_load_dword v231, v230, s[60:61] sc1\n"
    "v_mfma_f32_16x16x32_bf16 v[136:139], a[144:147], v[40:43], v[136:139]\n"
    "s_mov_b32 s88, 65536\n"
    "v_mfma_f32_16x16x32_bf16 v[144:147], a[208:211], v[40:43], v[144:147]\n"
    "v_mfma_f32_16x16x32_bf16 v[124:127], a[16:19], v[44:47], v[124:127]\n"
    "ds_read_b64_tr_b16 v[36:37], v11 offset:36864\n"
    "v_mfma_f32_16x16x32_bf16 v[132:135], a[80:83], v[44:47], v[132:135]\n"
    "ds_read_b64_tr_b16 v[38:39], v11 offset:38912\n"
    "buffer_load_dwordx4 a[32:35], v0, s[72:75], s88 offen offset:0 sc1\n"
    "v_mfma_f32_16x16x32_bf16 v[140:143], a[144:147], v[44:47], v[140:143]\n"
    "v_mfma_f32_16x16x32_bf16 v[148:151], a[208:211], v[44:47], v[148:151]\n"
    "s_waitcnt lgkmcnt(8)\n"
    "v_mfma_f32_16x16x32_bf16 v[152:155], a[16:19], v[48:51], v[152:155]\n"
    "ds_read_b64_tr_b16 v[40:41], v8 offset:53248\n"
    "v_mfma_f32_16x16x32_bf16 v[160:163], a[80:83], v[48:51], v[160:163]\n"
    "ds_read_b64_tr_b16 v[42:43], v8 offset:55296\n"
    "v_mfma_f32_16x16x32_bf16 v[168:171], a[144:147], v[48:51], v[168:171]\n"
    "buffer_load_dwordx4 a[96:99], v0, s[72:75], s88 offen offset:1024 sc1\n"
    "v_mfma_f32_16x16x32_bf16 v[176:179], a[208:211], v[48:51], v[176:179]\n"
    "v_mfma_f32_16x16x32_bf16 v[156:159], a[16:19], v[52:55], v[156:159]\n"
    "ds_read_b64_tr_b16 v[44:45], v9 offset:53248\n"
    "v_mfma_f32_16x16x32_bf16 v[164:167], a[80:83], v[52:55], v[164:167]\n"
    "ds_read_b64_tr_b16 v[46:47], v9 offset:55296\n"
    "v_mfma_f32_16x16x32_bf16 v[172:175], a[144:147], v[52:55], v[172:175]\n"
    "buffer_load_dwordx4 a[160:163], v1, s[72:75], s88 offen offset:0 sc1\n"
    "v_mfma_f32_16x16x32_bf16 v[180:183], a[208:211], v[52:55], v[180:183]\n"
    "s_waitcnt vmcnt(20)\n"
    "s_waitcnt lgkmcnt(8)\n"
    "v_mfma_f32_16x16x32_bf16 v[56:59], a[20:23], v[24:27], v[56:59]\n"
    "ds_read_b64_tr_b16 v[48:49], v10 offset:53248\n"
    "v_mfma_f32_16x16x32_bf16 v[64:67], a[84:87], v[24:27], v[64:67]\n"
    "ds_read_b64_tr_b16 v[50:51], v10 offset:55296\n"
    "v_mfma_f32_16x16x32_bf16 v[72:75], a[148:151], v[24:27], v[72:75]\n"
    "v_mfma_f32_16x16x32_bf16 v[80:83], a[212:215], v[24:27], v[80:83]\n"
    "buffer_load_dwordx4 a[224:227], v1, s[72:75], s88 offen offset:1024 sc1\n"
    "v_mfma_f32_16x16x32_bf16 v[60:63], a[20:23], v[28:31], v[60:63]\n"
    "ds_read_b64_tr_b16 v[52:53], v11 offset:53248\n"
    "s_mov_b32 s88, 73728\n"
    "v_mfma_f32_16x16x32_bf16 v[68:71], a[84:87], v[28:31], v[68:71]\n"
    "ds_read_b64_tr_b16 v[54:55], v11 offset:55296\n"
    "v_mfma_f32_16x16x32_bf16 v[76:79], a[148:151], v[28:31], v[76:79]\n"
    "v_mfma_f32_16x16x32_bf16 v[84:87], a[212:215], v[28:31], v[84:87]\n"
    "buffer_load_dwordx4 a[36:39], v0, s[72:75], s88 offen offset:0 sc1\n"
    "s_waitcnt lgkmcnt(8)\n"
    "v_mfma_f32_16x16x32_bf16 v[88:91], a[20:23], v[32:35], v[88:91]\n"
    "ds_read_b64_tr_b16 v[24:25], v8 offset:40960\n"
    "v_mfma_f32_16x16x32_bf16 v[96:99], a[84:87], v[32:35], v[96:99]\n"
    "ds_read_b64_tr_b16 v[26:27], v8 offset:43008\n"
    "v_mfma_f32_16x16x32_bf16 v[104:107], a[148:151], v[32:35], v[104:107]\n"
    "v_mfma_f32_16x16x32_bf16 v[112:115], a[212:215], v[32:35], v[112:115]\n"
    "v_mfma_f32_16x16x32_bf16 v[92:95], a[20:23], v[36:39], v[92:95]\n"
    "ds_read_b64_tr_b16 v[28:29], v9 offset:40960\n"
    "buffer_load_dwordx4 a[100:103], v0, s[72:75], s88 offen offset:1024 sc1\n"
    "v_mfma_f32_16x16x32_bf16 v[100:103], a[84:87], v[36:39], v[100:103]\n"
    "ds_read_b64_tr_b16 v[30:31], v9 offset:43008\n"
    "v_mfma_f32_16x16x32_bf16 v[108:111], a[148:151], v[36:39], v[108:111]\n"
    "v_mfma_f32_16x16x32_bf16 v[116:119], a[212:215], v[36:39], v[116:119]\n"
    "s_waitcnt lgkmcnt(8)\n"
    "v_mfma_f32_16x16x32_bf16 v[120:123], a[20:23], v[40:43], v[120:123]\n"
    "ds_read_b64_tr_b16 v[32:33], v10 offset:40960\n"
    "buffer_load_dwordx4 a[164:167], v1, s[72:75], s88 offen offset:0 sc1\n"
    "v_mfma_f32_16x16x32_bf16 v[128:131], a[84:87], v[40:43], v[128:131]\n"
    "ds_read_b64_tr_b16 v[34:35], v10 offset:43008\n"
    "v_mfma_f32_16x16x32_bf16 v[136:139], a[148:151], v[40:43], v[136:139]\n"
    "v_mfma_f32_16x16x32_bf16 v[144:147], a[212:215], v[40:43], v[144:147]\n"
    "v_mfma_f32_16x16x32_bf16 v[124:127], a[20:23], v[44:47], v[124:127]\n"
    "ds_read_b64_tr_b16 v[36:37], v11 offset:40960\n"
    "v_mfma_f32_16x16x32_bf16 v[132:135], a[84:87], v[44:47], v[132:135]\n"
    "ds_read_b64_tr_b16 v[38:39], v11 offset:43008\n"
    "buffer_load_dwordx4 a[228:231], v1, s[72:75], s88 offen offset:1024 sc1\n"
    "v_mfma_f32_16x16x32_bf16 v[140:143], a[148:151], v[44:47], v[140:143]\n"
    "s_mov_b32 s88, 81920\n"
    "v_mfma_f32_16x16x32_bf16 v[148:151], a[212:215], v[44:47], v[148:151]\n"
    "s_waitcnt lgkmcnt(8)\n"
    "v_mfma_f32_16x16x32_bf16 v[152:155], a[20:23], v[48:51], v[152:155]\n"
    "ds_read_b64_tr_b16 v[40:41], v8 offset:57344\n"
    "v_mfma_f32_16x16x32_bf16 v[160:163], a[84:87], v[48:51], v[160:163]\n"
    "ds_read_b64_tr_b16 v[42:43], v8 offset:59392\n"
    "buffer_load_dwordx4 a[40:43], v0, s[72:75], s88 offen offset:0 sc1\n"
    "v_mfma_f32_16x16x32_bf16 v[168:171], a[148:151], v[48:51], v[168:171]\n"
    "v_mfma_f32_16x16x32_bf16 v[176:179], a[212:215], v[48:51], v[176:179]\n"
    "v_mfma_f32_16x16x32_bf16 v[156:159], a[20:23], v[52:55], v[156:159]\n"
    "ds_read_b64_tr_b16 v[44:45], v9 offset:57344\n"
    "v_mfma_f32_16x16x32_bf16 v[164:167], a[84:87], v[52:55], v[164:167]\n"
    "ds_read_b64_tr_b16 v[46:47], v9 offset:59392\n"
    "v_mfma_f32_16x16x32_bf16 v[172:175], a[148:151], v[52:55], v[172:175]\n"
    "buffer_load_dwordx4 a[104:107], v0, s[72:75], s88 offen offset:1024 sc1\n"
    "v_mfma_f32_16x16x32_bf16 v[180:183], a[212:215], v[52:55], v[180:183]\n"
    "s_waitcnt vmcnt(23)\n"
    "s_waitcnt lgkmcnt(8)\n"
    "v_mfma_f32_16x16x32_bf16 v[56:59], a[24:27], v[24:27], v[56:59]\n"
    "ds_read_b64_tr_b16 v[48:49], v10 offset:57344\n"
    "v_mfma_f32_16x16x32_bf16 v[64:67], a[88:91], v[24:27], v[64:67]\n"
    "ds_read_b64_tr_b16 v[50:51], v10 offset:59392\n"
    "v_mfma_f32_16x16x32_bf16 v[72:75], a[152:155], v[24:27], v[72:75]\n"
    "buffer_load_dwordx4 a[168:171], v1, s[72:75], s88 offen offset:0 sc1\n"
    "v_mfma_f32_16x16x32_bf16 v[80:83], a[216:219], v[24:27], v[80:83]\n"
    "v_mfma_f32_16x16x32_bf16 v[60:63], a[24:27], v[28:31], v[60:63]\n"
    "ds_read_b64_tr_b16 v[52:53], v11 offset:57344\n"
    "v_mfma_f32_16x16x32_bf16 v[68:71], a[88:91], v[28:31], v[68:71]\n"
    "ds_read_b64_tr_b16 v[54:55], v11 offset:59392\n"
    "v_mfma_f32_16x16x32_bf16 v[76:79], a[152:155], v[28:31], v[76:79]\n"
    "v_mfma_f32_16x16x32_bf16 v[84:87], a[216:219], v[28:31], v[84:87]\n"
    "buffer_load_dwordx4 a[232:235], v1, s[72:75], s88 offen offset:1024 sc1\n"
    "s_waitcnt lgkmcnt(8)\n"
    "v_mfma_f32_16x16x32_bf16 v[88:91], a[24:27], v[32:35], v[88:91]\n"
    "ds_read_b64_tr_b16 v[24:25], v8 offset:45056\n"
    "s_mov_b32 s29, 3145728\n"
    "v_mfma_f32_16x16x32_bf16 v[96:99], a[88:91], v[32:35], v[96:99]\n"
    "ds_read_b64_tr_b16 v[26:27], v8 offset:47104\n"
    "s_add_u32 m0, s28, 98304\n"
    "v_mfma_f32_16x16x32_bf16 v[104:107], a[152:155], v[32:35], v[104:107]\n"
    "v_mfma_f32_16x16x32_bf16 v[112:115], a[216:219], v[32:35], v[112:115]\n"
    "buffer_load_dwordx4 v4, s[76:79], s29 offen offset:0 lds\n"
    "v_mfma_f32_16x16x32_bf16 v[92:95], a[24:27], v[36:39], v[92:95]\n"
    "ds_read_b64_tr_b16 v[28:29], v9 offset:45056\n"
    "v_mfma_f32_16x16x32_bf16 v[100:103], a[88:91], v[36:39], v[100:103]\n"
    "ds_read_b64_tr_b16 v[30:31], v9 offset:47104\n"
    "v_mfma_f32_16x16x32_bf16 v[108:111], a[152:155], v[36:39], v[108:111]\n"
    "v_mfma_f32_16x16x32_bf16 v[116:119], a[216:219], v[36:39], v[116:119]\n"
    "s_waitcnt lgkmcnt(8)\n"
    "v_mfma_f32_16x16x32_bf16 v[120:123], a[24:27], v[40:43], v[120:123]\n"
    "ds_read_b64_tr_b16 v[32:33], v10 offset:45056\n"
    "buffer_load_dwordx4 v5, s[76:79], s29 offen offset:1024 lds\n"
    "v_mfma_f32_16x16x32_bf16 v[128:131], a[88:91], v[40:43], v[128:131]\n"
    "ds_read_b64_tr_b16 v[34:35], v10 offset:47104\n"
    "v_mfma_f32_16x16x32_bf16 v[136:139], a[152:155], v[40:43], v[136:139]\n"
    "v_mfma_f32_16x16x32_bf16 v[144:147], a[216:219], v[40:43], v[144:147]\n"
    "v_mfma_f32_16x16x32_bf16 v[124:127], a[24:27], v[44:47], v[124:127]\n"
    "ds_read_b64_tr_b16 v[36:37], v11 offset:45056\n"
    "buffer_load_dwordx4 v6, s[76:79], s29 offen offset:2048 lds\n"
    "v_mfma_f32_16x16x32_bf16 v[132:135], a[88:91], v[44:47], v[132:135]\n"
    "ds_read_b64_tr_b16 v[38:39], v11 offset:47104\n"
    "v_mfma_f32_16x16x32_bf16 v[140:143], a[152:155], v[44:47], v[140:143]\n"
    "v_mfma_f32_16x16x32_bf16 v[148:151], a[216:219], v[44:47], v[148:151]\n"
    "s_waitcnt lgkmcnt(8)\n"
    "v_mfma_f32_16x16x32_bf16 v[152:155], a[24:27], v[48:51], v[152:155]\n"
    "ds_read_b64_tr_b16 v[40:41], v8 offset:61440\n"
    "v_mfma_f32_16x16x32_bf16 v[160:163], a[88:91], v[48:51], v[160:163]\n"
    "ds_read_b64_tr_b16 v[42:43], v8 offset:63488\n"
    "buffer_load_dwordx4 v7, s[76:79], s29 offen offset:3072 lds\n"
    "v_mfma_f32_16x16x32_bf16 v[168:171], a[152:155], v[48:51], v[168:171]\n"
    "s_mov_b32 s29, 3145856\n"
    "v_mfma_f32_16x16x32_bf16 v[176:179], a[216:219], v[48:51], v[176:179]\n"
    "s_add_u32 m0, s28, 114688\n"
    "v_mfma_f32_16x16x32_bf16 v[156:159], a[24:27], v[52:55], v[156:159]\n"
    "ds_read_b64_tr_b16 v[44:45], v9 offset:61440\n"
    "v_mfma_f32_16x16x32_bf16 v[164:167], a[88:91], v[52:55], v[164:167]\n"
    "ds_read_b64_tr_b16 v[46:47], v9 offset:63488\n"
    "buffer_load_dwordx4 v4, s[76:79], s29 offen offset:0 lds\n"
    "v_mfma_f32_16x16x32_bf16 v[172:175], a[152:155], v[52:55], v[172:175]\n"
    "v_mfma_f32_16x16x32_bf16 v[180:183], a[216:219], v[52:55], v[180:183]\n"
    "s_waitcnt vmcnt(18)\n"
    "s_waitcnt lgkmcnt(8)\n"
    "v_mfma_f32_16x16x32_bf16 v[56:59], a[28:31], v[24:27], v[56:59]\n"
    "ds_read_b64_tr_b16 v[48:49], v10 offset:61440\n"
    "v_mfma_f32_16x16x32_bf16 v[64:67], a[92:95], v[24:27], v[64:67]\n"
    "ds_read_b64_tr_b16 v[50:51], v10 offset:63488\n"
    "v_mfma_f32_16x16x32_bf16 v[72:75], a[156:159], v[24:27], v[72:75]\n"
    "buffer_load_dwordx4 v5, s[76:79], s29 offen offset:1024 lds\n"
    "v_mfma_f32_16x16x32_bf16 v[80:83], a[220:223], v[24:27], v[80:83]\n"
    "v_mfma_f32_16x16x32_bf16 v[60:63], a[28:31], v[28:31], v[60:63]\n"
    "ds_read_b64_tr_b16 v[52:53], v11 offset:61440\n"
    "v_mfma_f32_16x16x32_bf16 v[68:71], a[92:95], v[28:31], v[68:71]\n"
    "ds_read_b64_tr_b16 v[54:55], v11 offset:63488\n"
    "v_mfma_f32_16x16x32_bf16 v[76:79], a[156:159], v[28:31], v[76:79]\n"
    "buffer_load_dwordx4 v6, s[76:79], s29 offen offset:2048 lds\n"
    "v_mfma_f32_16x16x32_bf16 v[84:87], a[220:223], v[28:31], v[84:87]\n"
    "s_waitcnt lgkmcnt(8)\n"
    "v_mfma_f32_16x16x32_bf16 v[88:91], a[28:31], v[32:35], v[88:91]\n"
    "v_mfma_f32_16x16x32_bf16 v[96:99], a[92:95], v[32:35], v[96:99]\n"
    "v_mfma_f32_16x16x32_bf16 v[104:107], a[156:159], v[32:35], v[104:107]\n"
    "v_mfma_f32_16x16x32_bf16 v[112:115], a[220:223], v[32:35], v[112:115]\n"
    "buffer_load_dwordx4 v7, s[76:79], s29 offen offset:3072 lds\n"
    "v_mfma_f32_16x16x32_bf16 v[92:95], a[28:31], v[36:39], v[92:95]\n"
    "s_mov_b32 s88, 90112\n"
    "v_mfma_f32_16x16x32_bf16 v[100:103], a[92:95], v[36:39], v[100:103]\n"
    "v_mfma_f32_16x16x32_bf16 v[108:111], a[156:159], v[36:39], v[108:111]\n"
    "v_mfma_f32_16x16x32_bf16 v[116:119], a[220:223], v[36:39], v[116:119]\n"
    "buffer_load_dwordx4 a[44:47], v0, s[72:75], s88 offen offset:0 sc1\n"
    "s_waitcnt vmcnt(22) lgkmcnt(0)\n"
    "s_barrier\n"
    "v_mfma_f32_16x16x32_bf16 v[120:123], a[28:31], v[40:43], v[120:123]\n"
    "ds_read_b64_tr_b16 v[24:25], v12 offset:0\n"
    "v_mfma_f32_16x16x32_bf16 v[128:131], a[92:95], v[40:43], v[128:131]\n"
    "ds_read_b64_tr_b16 v[26:27], v12 offset:2048\n"
    "v_mfma_f32_16x16x32_bf16 v[136:139], a[156:159], v[40:43], v[136:139]\n"
    "ds_read_b64_tr_b16 v[28:29], v13 offset:0\n"
    "v_mfma_f32_16x16x32_bf16 v[144:147], a[220:223], v[40:43], v[144:147]\n"
    "ds_read_b64_tr_b16 v[30:31], v13 offset:2048\n"
    "v_mfma_f32_16x16x32_bf16 v[124:127], a[28:31], v[44:47], v[124:127]\n"
    "ds_read_b64_tr_b16 v[32:33], v14 offset:0\n"
    "buffer_load_dwordx4 a[108:111], v0, s[72:75], s88 offen offset:1024 sc1\n"
    "v_mfma_f32_16x16x32_bf16 v[132:135], a[92:95], v[44:47], v[132:135]\n"
    "ds_read_b64_tr_b16 v[34:35], v14 offset:2048\n"
    "v_mfma_f32_16x16x32_bf16 v[140:143], a[156:159], v[44:47], v[140:143]\n"
    "ds_read_b64_tr_b16 v[36:37], v15 offset:0\n"
    "v_mfma_f32_16x16x32_bf16 v[148:151], a[220:223], v[44:47], v[148:151]\n"
    "ds_read_b64_tr_b16 v[38:39], v15 offset:2048\n"
    "v_mfma_f32_16x16x32_bf16 v[152:155], a[28:31], v[48:51], v[152:155]\n"
    "ds_read_b64_tr_b16 v[40:41], v12 offset:16384\n"
    "buffer_load_dwordx4 a[172:175], v1, s[72:75], s88 offen offset:0 sc1\n"
    "v_mfma_f32_16x16x32_bf16 v[160:163], a[92:95], v[48:51], v[160:163]\n"
    "ds_read_b64_tr_b16 v[42:43], v12 offset:18432\n"
    "v_mfma_f32_16x16x32_bf16 v[168:171], a[156:159], v[48:51], v[168:171]\n"
    "v_mfma_f32_16x16x32_bf16 v[176:179], a[220:223], v[48:51], v[176:179]\n"
    "v_mfma_f32_16x16x32_bf16 v[156:159], a[28:31], v[52:55], v[156:159]\n"
    "ds_read_b64_tr_b16 v[44:45], v13 offset:16384\n"
    "v_mfma_f32_16x16x32_bf16 v[164:167], a[92:95], v[52:55], v[164:167]\n"
    "ds_read_b64_tr_b16 v[46:47], v13 offset:18432\n"
    "buffer_load_dwordx4 a[236:239], v1, s[72:75], s88 offen offset:1024 sc1\n"
    "v_mfma_f32_16x16x32_bf16 v[172:175], a[156:159], v[52:55], v[172:175]\n"
    "s_waitcnt vmcnt(24)\n"
    "v_cmp_ne_u32 vcc, s94, v231\n"
    "s_mov_b32 s30, 0x400000\n"
    "s_nop 1\n"
    "s_cbranch_vccz L_pmv_%=_x\n"
    "L_pmv_%=:\n"
    "s_sleep 2\n"
    "global_load_dword v231, v230, s[60:61] sc1\n"
    "s_waitcnt vmcnt(0)\n"
    "v_cmp_ne_u32 vcc, s94, v231\n"
    "s_sub_u32 s30, s30, 1\n"
    "s_cbranch_scc1 L_pmv_%=_x\n"
    "s_nop 0\n"
    "s_cbranch_vccnz L_pmv_%=\n"
    "L_pmv_%=_x:\n"
    "v_mfma_f32_16x16x32_bf16 v[180:183], a[220:223], v[52:55], v[180:183]\n"
    "s_waitcnt vmcnt(20)\n"
    "s_waitcnt lgkmcnt(8)\n"
    "v_mfma_f32_16x16x32_bf16 v[56:59], a[32:35], v[24:27], v[56:59]\n"
    "ds_read_b64_tr_b16 v[48:49], v14 offset:16384\n"
    "v_mfma_f32_16x16x32_bf16 v[64:67], a[96:99], v[24:27], v[64:67]\n"
    "ds_read_b64_tr_b16 v[50:51], v14 offset:18432\n"
    "global_load_dwordx4 v[216:219], v23, s[60:61] sc1\n"
    "v_mfma_f32_16x16x32_bf16 v[72:75], a[160:163], v[24:27], v[72:75]\n"
    "v_add_u32 v23, 0x2000, v23\n"
    "v_mfma_f32_16x16x32_bf16 v[80:83], a[224:227], v[24:27], v[80:83]\n"
    "v_mfma_f32_16x16x32_bf16 v[60:63], a[32:35], v[28:31], v[60:63]\n"
    "ds_read_b64_tr_b16 v[52:53], v15 offset:16384\n"
    "v_mfma_f32_16x16x32_bf16 v[68:71], a[96:99], v[28:31], v[68:71]\n"
    "ds_read_b64_tr_b16 v[54:55], v15 offset:18432\n"
    "v_mfma_f32_16x16x32_bf16 v[76:79], a[160:163], v[28:31], v[76:79]\n"
    "global_load_dwordx4 v[220:223], v23, s[60:61] sc1\n"
    "v_mfma_f32_16x16x32_bf16 v[84:87], a[224:227], v[28:31], v[84:87]\n"
    "s_waitcnt lgkmcnt(8)\n"
    "v_mfma_f32_16x16x32_bf16 v[88:91], a[32:35], v[32:35], v[88:91]\n"
    "ds_read_b64_tr_b16 v[24:25], v12 offset:4096\n"
    "v_mfma_f32_16x16x32_bf16 v[96:99], a[96:99], v[32:35], v[96:99]\n"
    "ds_read_b64_tr_b16 v[26:27], v12 offset:6144\n"
    "v_mfma_f32_16x16x32_bf16 v[104:107], a[160:163], v[32:35], v[104:107]\n"
    "global_load_dwordx4 v[224:227], v228, s[60:61] sc1\n"
    "v_mfma_f32_16x16x32_bf16 v[112:115], a[224:227], v[32:35], v[112:115]\n"
    "v_mfma_f32_16x16x32_bf16 v[92:95], a[32:35], v[36:39], v[92:95]\n"
    "ds_read_b64_tr_b16 v[28:29], v13 offset:4096\n"
    "v_mfma_f32_16x16x32_bf16 v[100:103], a[96:99], v[36:39], v[100:103]\n"
    "ds_read_b64_tr_b16 v[30:31], v13 offset:6144\n"
    "v_mfma_f32_16x16x32_bf16 v[108:111], a[160:163], v[36:39], v[108:111]\n"
    "v_mfma_f32_16x16x32_bf16 v[116:119], a[224:227], v[36:39], v[116:119]\n"
    "global_load_dwordx2 v[232:233], v22, s[62:63] offset:0\n"
    "s_waitcnt lgkmcnt(8)\n"
    "v_mfma_f32_16x16x32_bf16 v[120:123], a[32:35], v[40:43], v[120:123]\n"
    "ds_read_b64_tr_b16 v[32:33], v14 offset:4096\n"
    "v_mfma_f32_16x16x32_bf16 v[128:131], a[96:99], v[40:43], v[128:131]\n"
    "ds_read_b64_tr_b16 v[34:35], v14 offset:6144\n"
    "v_mfma_f32_16x16x32_bf16 v[136:139], a[160:163], v[40:43], v[136:139]\n"
    "v_mfma_f32_16x16x32_bf16 v[144:147], a[224:227], v[40:43], v[144:147]\n"
    "global_load_dwordx2 v[234:235], v22, s[62:63] offset:32\n"
    "v_mfma_f32_16x16x32_bf16 v[124:127], a[32:35], v[44:47], v[124:127]\n"
    "ds_read_b64_tr_b16 v[36:37], v15 offset:4096\n"
    "v_mfma_f32_16x16x32_bf16 v[132:135], a[96:99], v[44:47], v[132:135]\n"
    "ds_read_b64_tr_b16 v[38:39], v15 offset:6144\n"
    "v_mfma_f32_16x16x32_bf16 v[140:143], a[160:163], v[44:47], v[140:143]\n"
    "v_mfma_f32_16x16x32_bf16 v[148:151], a[224:227], v[44:47], v[148:151]\n"
    "s_waitcnt lgkmcnt(8)\n"
    "v_mfma_f32_16x16x32_bf16 v[152:155], a[32:35], v[48:51], v[152:155]\n"
    "ds_read_b64_tr_b16 v[40:41], v12 offset:20480\n"
    "global_load_dwordx2 v[236:237], v22, s[62:63] offset:64\n"
    "v_mfma_f32_16x16x32_bf16 v[160:163], a[96:99], v[48:51], v[160:163]\n"
    "ds_read_b64_tr_b16 v[42:43], v12 offset:22528\n"
    "v_mfma_f32_16x16x32_bf16 v[168:171], a[160:163], v[48:51], v[168:171]\n"
    "v_mfma_f32_16x16x32_bf16 v[176:179], a[224:227], v[48:51], v[176:179]\n"
    "v_mfma_f32_16x16x32_bf16 v[156:159], a[32:35], v[52:55], v[156:159]\n"
    "ds_read_b64_tr_b16 v[44:45], v13 offset:20480\n"
    "global_load_dwordx2 v[238:239], v22, s[62:63] offset:96\n"
    "v_mfma_f32_16x16x32_bf16 v[164:167], a[96:99], v[52:55], v[164:167]\n"
    "ds_read_b64_tr_b16 v[46:47], v13 offset:22528\n"
    "s_mov_b32 s88, 98304\n"
    "v_mfma_f32_16x16x32_bf16 v[172:175], a[160:163], v[52:55], v[172:175]\n"
    "v_mfma_f32_16x16x32_bf16 v[180:183], a[224:227], v[52:55], v[180:183]\n"
    "s_waitcnt vmcnt(23)\n"
    "s_waitcnt lgkmcnt(8)\n"
    "v_mfma_f32_16x16x32_bf16 v[56:59], a[36:39], v[24:27], v[56:59]\n"
    "ds_read_b64_tr_b16 v[48:49], v14 offset:20480\n"
    "v_mfma_f32_16x16x32_bf16 v[64:67], a[100:103], v[24:27], v[64:67]\n"
    "ds_read_b64_tr_b16 v[50:51], v14 offset:22528\n"
    "buffer_load_dwordx4 a[48:51], v0, s[72:75], s88 offen offset:0 sc1\n"
    "v_mfma_f32_16x16x32_bf16 v[72:75], a[164:167], v[24:27], v[72:75]\n"
    "v_mfma_f32_16x16x32_bf16 v[80:83], a[228:231], v[24:27], v[80:83]\n"
    "v_mfma_f32_16x16x32_bf16 v[60:63], a[36:39], v[28:31], v[60:63]\n"
    "ds_read_b64_tr_b16 v[52:53], v15 offset:20480\n"
    "v_mfma_f32_16x16x32_bf16 v[68:71], a[100:103], v[28:31], v[68:71]\n"
    "ds_read_b64_tr_b16 v[54:55], v15 offset:22528\n"
    "buffer_load_dwordx4 a[112:115], v0, s[72:75], s88 offen offset:1024 sc1\n"
    "v_mfma_f32_16x16x32_bf16 v[76:79], a[164:167], v[28:31], v[76:79]\n"
    "v_mfma_f32_16x16x32_bf16 v[84:87], a[228:231], v[28:31], v[84:87]\n"
    "s_waitcnt lgkmcnt(8)\n"
    "v_mfma_f32_16x16x32_bf16 v[88:91], a[36:39], v[32:35], v[88:91]\n"
    "ds_read_b64_tr_b16 v[24:25], v12 offset:8192\n"
    "v_mfma_f32_16x16x32_bf16 v[96:99], a[100:103], v[32:35], v[96:99]\n"
    "ds_read_b64_tr_b16 v[26:27], v12 offset:10240\n"
    "v_mfma_f32_16x16x32_bf16 v[104:107], a[164:167], v[32:35], v[104:107]\n"
    "buffer_load_dwordx4 a[176:179], v1, s[72:75], s88 offen offset:0 sc1\n"
    "v_mfma_f32_16x16x32_bf16 v[112:115], a[228:231], v[32:35], v[112:115]\n"
    "v_mfma_f32_16x16x32_bf16 v[92:95], a[36:39], v[36:39], v[92:95]\n"
    "ds_read_b64_tr_b16 v[28:29], v13 offset:8192\n"
    "v_mfma_f32_16x16x32_bf16 v[100:103], a[100:103], v[36:39], v[100:103]\n"
    "ds_read_b64_tr_b16 v[30:31], v13 offset:10240\n"
    "v_mfma_f32_16x16x32_bf16 v[108:111], a[164:167], v[36:39], v[108:111]\n"
    "buffer_load_dwordx4 a[240:243], v1, s[72:75], s88 offen offset:1024 sc1\n"
    "v_mfma_f32_16x16x32_bf16 v[116:119], a[228:231], v[36:39], v[116:119]\n"
    "s_mov_b32 s88, 106496\n"
    "s_waitcnt lgkmcnt(8)\n"
    "v_mfma_f32_16x16x32_bf16 v[120:123], a[36:39], v[40:43], v[120:123]\n"
    "ds_read_b64_tr_b16 v[32:33], v14 offset:8192\n"
    "v_mfma_f32_16x16x32_bf16 v[128:131], a[100:103], v[40:43], v[128:131]\n"
    "ds_read_b64_tr_b16 v[34:35], v14 offset:10240\n"
    "v_mfma_f32_16x16x32_bf16 v[136:139], a[164:167], v[40:43], v[136:139]\n"
    "v_mfma_f32_16x16x32_bf16 v[144:147], a[228:231], v[40:43], v[144:147]\n"
    "buffer_load_dwordx4 a[52:55], v0, s[72:75], s88 offen offset:0 sc1\n"
    "v_mfma_f32_16x16x32_bf16 v[124:127], a[36:39], v[44:47], v[124:127]\n"
    "ds_read_b64_tr_b16 v[36:37], v15 offset:8192\n"
    "v_mfma_f32_16x16x32_bf16 v[132:135], a[100:103], v[44:47], v[132:135]\n"
    "ds_read_b64_tr_b16 v[38:39], v15 offset:10240\n"
    "v_mfma_f32_16x16x32_bf16 v[140:143], a[164:167], v[44:47], v[140:143]\n"
    "v_mfma_f32_16x16x32_bf16 v[148:151], a[228:231], v[44:47], v[148:151]\n"
    "buffer_load_dwordx4 a[116:119], v0, s[72:75], s88 offen offset:1024 sc1\n"
    "s_waitcnt lgkmcnt(8)\n"
    "v_mfma_f32_16x16x32_bf16 v[152:155], a[36:39], v[48:51], v[152:155]\n"
    "ds_read_b64_tr_b16 v[40:41], v12 offset:24576\n"
    "v_mfma_f32_16x16x32_bf16 v[160:163], a[100:103], v[48:51], v[160:163]\n"
    "ds_read_b64_tr_b16 v[42:43], v12 offset:26624\n"
    "v_mfma_f32_16x16x32_bf16 v[168:171], a[164:167], v[48:51], v[168:171]\n"
    "v_mfma_f32_16x16x32_bf16 v[176:179], a[228:231], v[48:51], v[176:179]\n"
    "v_mfma_f32_16x16x32_bf16 v[156:159], a[36:39], v[52:55], v[156:159]\n"
    "ds_read_b64_tr_b16 v[44:45], v13 offset:24576\n"
    "buffer_load_dwordx4 a[180:183], v1, s[72:75], s88 offen offset:0 sc1\n"
    "v_mfma_f32_16x16x32_bf16 v[164:167], a[100:103], v[52:55], v[164:167]\n"
    "ds_read_b64_tr_b16 v[46:47], v13 offset:26624\n"
    "v_mfma_f32_16x16x32_bf16 v[172:175], a[164:167], v[52:55], v[172:175]\n"
    "v_mfma_f32_16x16x32_bf16 v[180:183], a[228:231], v[52:55], v[180:183]\n"
    "s_waitcnt vmcnt(26)\n"
    "s_waitcnt lgkmcnt(8)\n"
    "v_mfma_f32_16x16x32_bf16 v[56:59], a[40:43], v[24:27], v[56:59]\n"
    "ds_read_b64_tr_b16 v[48:49], v14 offset:24576\n"
    "buffer_load_dwordx4 a[244:247], v1, s[72:75], s88 offen offset:1024 sc1\n"
    "v_mfma_f32_16x16x32_bf16 v[64:67], a[104:107], v[24:27], v[64:67]\n"
    "ds_read_b64_tr_b16 v[50:51], v14 offset:26624\n"
    "s_mov_b32 s88, 114688\n"
    "v_mfma_f32_16x16x32_bf16 v[72:75], a[168:171], v[24:27], v[72:75]\n"
    "v_mfma_f32_16x16x32_bf16 v[80:83], a[232:235], v[24:27], v[80:83]\n"
    "v_mfma_f32_16x16x32_bf16 v[60:63], a[40:43], v[28:31], v[60:63]\n"
    "ds_read_b64_tr_b16 v[52:53], v15 offset:24576\n"
    "s_waitcnt vmcnt(8)\n"
    "v_add_f32 v216, v216, v217\n"
    "v_mfma_f32_16x16x32_bf16 v[68:71], a[104:107], v[28:31], v[68:71]\n"
    "ds_read_b64_tr_b16 v[54:55], v15 offset:26624\n"
    "buffer_load_dwordx4 a[56:59], v0, s[72:75], s88 offen offset:0 sc1\n"
    "v_add_f32 v218, v218, v219\n"
    "v_add_f32 v216, v216, v218\n"
    "v_mfma_f32_16x16x32_bf16 v[76:79], a[168:171], v[28:31], v[76:79]\n"
    "v_cvt_pk_bf16_f32 v216, 0, v216\n"
    "v_add_f32 v220, v220, v221\n"
    "v_mfma_f32_16x16x32_bf16 v[84:87], a[232:235], v[28:31], v[84:87]\n"
    "v_add_f32 v222, v222, v223\n"
    "v_add_f32 v220, v220, v222\n"
    "s_waitcnt lgkmcnt(8)\n"
    "v_mfma_f32_16x16x32_bf16 v[88:91], a[40:43], v[32:35], v[88:91]\n"
    "ds_read_b64_tr_b16 v[24:25], v12 offset:12288\n"
    "v_cvt_pk_bf16_f32 v220, 0, v220\n"
    "v_add_f32 v224, v224, v225\n"
    "v_mfma_f32_16x16x32_bf16 v[96:99], a[104:107], v[32:35], v[96:99]\n"
    "ds_read_b64_tr_b16 v[26:27], v12 offset:14336\n"
    "buffer_load_dwordx4 a[120:123], v0, s[72:75], s88 offen offset:1024 sc1\n"
    "v_add_f32 v226, v226, v227\n"
    "v_add_f32 v224, v224, v226\n"
    "v_mfma_f32_16x16x32_bf16 v[104:107], a[168:171], v[32:35], v[104:107]\n"
    "v_cvt_pk_bf16_f32 v224, 0, v224\n"
    "v_add_f32 v220, 1.0, v220\n"
    "v_mfma_f32_16x16x32_bf16 v[112:115], a[232:235], v[32:35], v[112:115]\n"
    "v_cvt_pk_bf16_f32 v220, 0, v220\n"
    "v_add_f32 v224, s67, v224\n"
    "v_mfma_f32_16x16x32_bf16 v[92:95], a[40:43], v[36:39], v[92:95]\n"
    "ds_read_b64_tr_b16 v[28:29], v13 offset:12288\n"
    "v_cvt_pk_bf16_f32 v224, 0, v224\n"
    "s_lshl_b32 s30, s20, 8\n"
    "v_mfma_f32_16x16x32_bf16 v[100:103], a[104:107], v[36:39], v[100:103]\n"
    "ds_read_b64_tr_b16 v[30:31], v13 offset:14336\n"
    "v_lshlrev_b32 v17, 2, v16\n"
    "v_add_u32 v17, s30, v17\n"
    "v_mfma_f32_16x16x32_bf16 v[108:111], a[168:171], v[36:39], v[108:111]\n"
    "buffer_load_dwordx4 a[184:187], v1, s[72:75], s88 offen offset:0 sc1\n"
    "v_add_u32 v17, 0x20000, v17\n"
    "v_mfma_f32_16x16x32_bf16 v[116:119], a[232:235], v[36:39], v[116:119]\n"
    "s_waitcnt lgkmcnt(8)\n"
    "v_mfma_f32_16x16x32_bf16 v[120:123], a[40:43], v[40:43], v[120:123]\n"
    "ds_read_b64_tr_b16 v[32:33], v14 offset:12288\n"
    "v_mfma_f32_16x16x32_bf16 v[128:131], a[104:107], v[40:43], v[128:131]\n"
    "ds_read_b64_tr_b16 v[34:35], v14 offset:14336\n"
    "v_mfma_f32_16x16x32_bf16 v[136:139], a[168:171], v[40:43], v[136:139]\n"
    "buffer_load_dwordx4 a[248:251], v1, s[72:75], s88 offen offset:1024 sc1\n"
    "ds_write_b32 v17, v216 offset:0\n"
    "v_mfma_f32_16x16x32_bf16 v[144:147], a[232:235], v[40:43], v[144:147]\n"
    "s_mov_b32 s88, 122880\n"
    "v_mfma_f32_16x16x32_bf16 v[124:127], a[40:43], v[44:47], v[124:127]\n"
    "ds_read_b64_tr_b16 v[36:37], v15 offset:12288\n"
    "v_mfma_f32_16x16x32_bf16 v[132:135], a[104:107], v[44:47], v[132:135]\n"
    "ds_read_b64_tr_b16 v[38:39], v15 offset:14336\n"
    "ds_write_b32 v17, v220 offset:1024\n"
    "v_mfma_f32_16x16x32_bf16 v[140:143], a[168:171], v[44:47], v[140:143]\n"
    "v_mfma_f32_16x16x32_bf16 v[148:151], a[232:235], v[44:47], v[148:151]\n"
    "buffer_load_dwordx4 a[60:63], v0, s[72:75], s88 offen offset:0 sc1\n"
    "s_waitcnt lgkmcnt(10)\n"
    "v_mfma_f32_16x16x32_bf16 v[152:155], a[40:43], v[48:51], v[152:155]\n"
    "ds_read_b64_tr_b16 v[40:41], v12 offset:28672\n"
    "v_mfma_f32_16x16x32_bf16 v[160:163], a[104:107], v[48:51], v[160:163]\n"
    "ds_read_b64_tr_b16 v[42:43], v12 offset:30720\n"
    "s_mov_b64 exec, 0xff\n\tglobal_store_dword v229, v224, s[60:61] sc1\n\ts_mov_b64 exec, -1\n"
    "v_mfma_f32_16x16x32_bf16 v[168:171], a[168:171], v[48:51], v[168:171]\n"
    "v_mfma_f32_16x16x32_bf16 v[176:179], a[232:235], v[48:51], v[176:179]\n"
    "buffer_load_dwordx4 a[124:127], v0, s[72:75], s88 offen offset:1024 sc1\n"
    "v_mfma_f32_16x16x32_bf16 v[156:159], a[40:43], v[52:55], v[156:159]\n"
    "ds_read_b64_tr_b16 v[44:45], v13 offset:28672\n"
    "v_mfma_f32_16x16x32_bf16 v[164:167], a[104:107], v[52:55], v[164:167]\n"
    "ds_read_b64_tr_b16 v[46:47], v13 offset:30720\n"
    "v_mfma_f32_16x16x32_bf16 v[172:175], a[168:171], v[52:55], v[172:175]\n"
    "v_mfma_f32_16x16x32_bf16 v[180:183], a[232:235], v[52:55], v[180:183]\n"
    "s_waitcnt lgkmcnt(10)\n"
    "v_mfma_f32_16x16x32_bf16 v[56:59], a[44:47], v[24:27], v[56:59]\n"
    "ds_read_b64_tr_b16 v[48:49], v14 offset:28672\n"
    "buffer_load_dwordx4 a[188:191], v1, s[72:75], s88 offen offset:0 sc1\n"
    "v_mfma_f32_16x16x32_bf16 v[64:67], a[108:111], v[24:27], v[64:67]\n"
    "ds_read_b64_tr_b16 v[50:51], v14 offset:30720\n"
    "v_mfma_f32_16x16x32_bf16 v[72:75], a[172:175], v[24:27], v[72:75]\n"
    "v_mfma_f32_16x16x32_bf16 v[80:83], a[236:239], v[24:27], v[80:83]\n"
    "v_mfma_f32_16x16x32_bf16 v[60:63], a[44:47], v[28:31], v[60:63]\n"
    "ds_read_b64_tr_b16 v[52:53], v15 offset:28672\n"
    "buffer_load_dwordx4 a[252:255], v1, s[72:75], s88 offen offset:1024 sc1\n"
    "v_mfma_f32_16x16x32_bf16 v[68:71], a[108:111], v[28:31], v[68:71]\n"
    "ds_read_b64_tr_b16 v[54:55], v15 offset:30720\n"
    "s_mov_b32 s29, 256\n"
    "v_mfma_f32_16x16x32_bf16 v[76:79], a[172:175], v[28:31], v[76:79]\n"
    "s_add_u32 m0, s28, 0\n"
    "v_mfma_f32_16x16x32_bf16 v[84:87], a[236:239], v[28:31], v[84:87]\n"
    "s_waitcnt lgkmcnt(9)\n"
    "v_mfma_f32_16x16x32_bf16 v[88:91], a[44:47], v[32:35], v[88:91]\n"
    "v_mfma_f32_16x16x32_bf16 v[96:99], a[108:111], v[32:35], v[96:99]\n"
    "buffer_load_dwordx4 v4, s[76:79], s29 offen offset:0 lds\n"
    "v_mfma_f32_16x16x32_bf16 v[104:107], a[172:175], v[32:35], v[104:107]\n"
    "v_mfma_f32_16x16x32_bf16 v[112:115], a[236:239], v[32:35], v[112:115]\n"
    "v_mfma_f32_16x16x32_bf16 v[92:95], a[44:47], v[36:39], v[92:95]\n"
    "v_mfma_f32_16x16x32_bf16 v[100:103], a[108:111], v[36:39], v[100:103]\n"
    "buffer_load_dwordx4 v5, s[76:79], s29 offen offset:1024 lds\n"
    "v_mfma_f32_16x16x32_bf16 v[108:111], a[172:175], v[36:39], v[108:111]\n"
    "v_mfma_f32_16x16x32_bf16 v[116:119], a[236:239], v[36:39], v[116:119]\n"
    "s_waitcnt vmcnt(19) lgkmcnt(0)\n"
    "s_barrier\n"
    "v_mfma_f32_16x16x32_bf16 v[120:123], a[44:47], v[40:43], v[120:123]\n"
    "ds_read_b64_tr_b16 v[24:25], v12 offset:32768\n"
    "v_mfma_f32_16x16x32_bf16 v[128:131], a[108:111], v[40:43], v[128:131]\n"
    "ds_read_b64_tr_b16 v[26:27], v12 offset:34816\n"
    "v_mfma_f32_16x16x32_bf16 v[136:139], a[172:175], v[40:43], v[136:139]\n"
    "ds_read_b64_tr_b16 v[28:29], v13 offset:32768\n"
    "buffer_load_dwordx4 v6, s[76:79], s29 offen offset:2048 lds\n"
    "v_mfma_f32_16x16x32_bf16 v[144:147], a[236:239], v[40:43], v[144:147]\n"
    "ds_read_b64_tr_b16 v[30:31], v13 offset:34816\n"
    "v_mfma_f32_16x16x32_bf16 v[124:127], a[44:47], v[44:47], v[124:127]\n"
    "ds_read_b64_tr_b16 v[32:33], v14 offset:32768\n"
    "v_mfma_f32_16x16x32_bf16 v[132:135], a[108:111], v[44:47], v[132:135]\n"
    "ds_read_b64_tr_b16 v[34:35], v14 offset:34816\n"
    "v_mfma_f32_16x16x32_bf16 v[140:143], a[172:175], v[44:47], v[140:143]\n"
    "ds_read_b64_tr_b16 v[36:37], v15 offset:32768\n"
    "buffer_load_dwordx4 v7, s[76:79], s29 offen offset:3072 lds\n"
    "v_mfma_f32_16x16x32_bf16 v[148:151], a[236:239], v[44:47], v[148:151]\n"
    "ds_read_b64_tr_b16 v[38:39], v15 offset:34816\n"
    "s_mov_b32 s29, 1048832\n"
    "v_mfma_f32_16x16x32_bf16 v[152:155], a[44:47], v[48:51], v[152:155]\n"
    "ds_read_b64_tr_b16 v[40:41], v12 offset:49152\n"
    "s_add_u32 m0, s28, 16384\n"
    "v_mfma_f32_16x16x32_bf16 v[160:163], a[108:111], v[48:51], v[160:163]\n"
    "ds_read_b64_tr_b16 v[42:43], v12 offset:51200\n"
    "v_mfma_f32_16x16x32_bf16 v[168:171], a[172:175], v[48:51], v[168:171]\n"
    "v_mfma_f32_16x16x32_bf16 v[176:179], a[236:239], v[48:51], v[176:179]\n"
    "buffer_load_dwordx4 v4, s[76:79], s29 offen offset:0 lds\n"
    "v_mfma_f32_16x16x32_bf16 v[156:159], a[44:47], v[52:55], v[156:159]\n"
    "ds_read_b64_tr_b16 v[44:45], v13 offset:49152\n"
    "v_mfma_f32_16x16x32_bf16 v[164:167], a[108:111], v[52:55], v[164:167]\n"
    "ds_read_b64_tr_b16 v[46:47], v13 offset:51200\n"
    "v_mfma_f32_16x16x32_bf16 v[172:175], a[172:175], v[52:55], v[172:175]\n"
    "v_mfma_f32_16x16x32_bf16 v[180:183], a[236:239], v[52:55], v[180:183]\n"
    "buffer_load_dwordx4 v5, s[76:79], s29 offen offset:1024 lds\n"
    "s_waitcnt vmcnt(19)\n"
    "s_waitcnt lgkmcnt(8)\n"
    "v_mfma_f32_16x16x32_bf16 v[56:59], a[48:51], v[24:27], v[56:59]\n"
    "ds_read_b64_tr_b16 v[48:49], v14 offset:49152\n"
    "v_mfma_f32_16x16x32_bf16 v[64:67], a[112:115], v[24:27], v[64:67]\n"
    "ds_read_b64_tr_b16 v[50:51], v14 offset:51200\n"
    "v_lshlrev_b32 v22, 4, v16\n"
    "v_mfma_f32_16x16x32_bf16 v[72:75], a[176:179], v[24:27], v[72:75]\n"
    "v_and_b32 v240, 15, v16\n"
    "v_mfma_f32_16x16x32_bf16 v[80:83], a[240:243], v[24:27], v[80:83]\n"
    "v_lshlrev_b32 v240, 3, v240\n"
    "v_mfma_f32_16x16x32_bf16 v[60:63], a[48:51], v[28:31], v[60:63]\n"
    "ds_read_b64_tr_b16 v[52:53], v15 offset:49152\n"
    "buffer_load_dwordx4 v6, s[76:79], s29 offen offset:2048 lds\n"
    "v_add_u32 v240, 0x20c00, v240\n"
    "v_mfma_f32_16x16x32_bf16 v[68:71], a[112:115], v[28:31], v[68:71]\n"
    "ds_read_b64_tr_b16 v[54:55], v15 offset:51200\n"
    "ds_read_b64 v[240:241], v240\n"
    "v_mfma_f32_16x16x32_bf16 v[76:79], a[176:179], v[28:31], v[76:79]\n"
    "s_waitcnt lgkmcnt(0)\n"
    "v_mfma_f32_16x16x32_bf16 v[84:87], a[240:243], v[28:31], v[84:87]\n"
    "s_lshl_b32 s30, s20, 8\n"
    "v_mfma_f32_16x16x32_bf16 v[88:91], a[48:51], v[32:35], v[88:91]\n"
    "ds_read_b64_tr_b16 v[24:25], v12 offset:36864\n"
    "buffer_load_dwordx4 v7, s[76:79], s29 offen offset:3072 lds\n"
    "v_lshrrev_b32 v17, 4, v16\n"
    "v_mfma_f32_16x16x32_bf16 v[96:99], a[112:115], v[32:35], v[96:99]\n"
    "ds_read_b64_tr_b16 v[26:27], v12 offset:38912\n"
    "s_mov_b32 s29, 2097408\n"
    "v_lshlrev_b32 v17, 4, v17\n"
    "v_mfma_f32_16x16x32_bf16 v[104:107], a[176:179], v[32:35], v[104:107]\n"
    "s_add_u32 m0, s28, 32768\n"
    "v_add_u32 v17, s30, v17\n"
    "v_mfma_f32_16x16x32_bf16 v[112:115], a[240:243], v[32:35], v[112:115]\n"
    "v_mfma_f32_16x16x32_bf16 v[92:95], a[48:51], v[36:39], v[92:95]\n"
    "ds_read_b64_tr_b16 v[28:29], v13 offset:36864\n"
    "v_add_u32 v17, 0x20000, v17\n"
    "v_mfma_f32_16x16x32_bf16 v[100:103], a[112:115], v[36:39], v[100:103]\n"
    "ds_read_b64_tr_b16 v[30:31], v13 offset:38912\n"
    "buffer_load_dwordx4 v4, s[76:79], s29 offen offset:0 lds\n"
    "ds_read_b128 v[242:245], v17 offset:0\n"
    "v_mfma_f32_16x16x32_bf16 v[108:111], a[176:179], v[36:39], v[108:111]\n"
    "ds_read_b128 v[246:249], v17 offset:1024\n"
    "v_mfma_f32_16x16x32_bf16 v[116:119], a[240:243], v[36:39], v[116:119]\n"
    "v_lshlrev_b32 v250, 16, v232\n"
    "v_mfma_f32_16x16x32_bf16 v[120:123], a[48:51], v[40:43], v[120:123]\n"
    "ds_read_b64_tr_b16 v[32:33], v14 offset:36864\n"
    "v_and_b32 v232, 0xffff0000, v232\n"
    "v_mfma_f32_16x16x32_bf16 v[128:131], a[112:115], v[40:43], v[128:131]\n"
    "ds_read_b64_tr_b16 v[34:35], v14 offset:38912\n"
    "buffer_load_dwordx4 v5, s[76:79], s29 offen offset:1024 lds\n"
    "v_lshlrev_b32 v251, 16, v233\n"
    "v_mfma_f32_16x16x32_bf16 v[136:139], a[176:179], v[40:43], v[136:139]\n"
    "v_and_b32 v233, 0xffff0000, v233\n"
    "v_mfma_f32_16x16x32_bf16 v[144:147], a[240:243], v[40:43], v[144:147]\n"
    "v_fma_f32 v250, v250, v240, v241\n"
    "v_mfma_f32_16x16x32_bf16 v[124:127], a[48:51], v[44:47], v[124:127]\n"
    "ds_read_b64_tr_b16 v[36:37], v15 offset:36864\n"
    "v_fma_f32 v232, v232, v240, v241\n"
    "v_mfma_f32_16x16x32_bf16 v[132:135], a[112:115], v[44:47], v[132:135]\n"
    "ds_read_b64_tr_b16 v[38:39], v15 offset:38912\n"
    "v_fma_f32 v251, v251, v240, v241\n"
    "v_mfma_f32_16x16x32_bf16 v[140:143], a[176:179], v[44:47], v[140:143]\n"
    "buffer_load_dwordx4 v6, s[76:79], s29 offen offset:2048 lds\n"
    "v_fma_f32 v233, v233, v240, v241\n"
    "v_mfma_f32_16x16x32_bf16 v[148:151], a[240:243], v[44:47], v[148:151]\n"
    "v_mfma_f32_16x16x32_bf16 v[152:155], a[48:51], v[48:51], v[152:155]\n"
    "ds_read_b64_tr_b16 v[40:41], v12 offset:53248\n"
    "s_waitcnt lgkmcnt(5)\n"
    "v_mfma_f32_16x16x32_bf16 v[160:163], a[112:115], v[48:51], v[160:163]\n"
    "ds_read_b64_tr_b16 v[42:43], v12 offset:55296\n"
    "v_fma_f32 v250, v250, v246, v242\n"
    "v_mfma_f32_16x16x32_bf16 v[168:171], a[176:179], v[48:51], v[168:171]\n"
    "buffer_load_dwordx4 v7, s[76:79], s29 offen offset:3072 lds\n"
    "v_fma_f32 v232, v232, v247, v243\n"
    "v_mfma_f32_16x16x32_bf16 v[176:179], a[240:243], v[48:51], v[176:179]\n"
    "s_mov_b32 s29, 3145984\n"
    "v_fma_f32 v251, v251, v248, v244\n"
    "v_mfma_f32_16x16x32_bf16 v[156:159], a[48:51], v[52:55], v[156:159]\n"
    "ds_read_b64_tr_b16 v[44:45], v13 offset:53248\n"
    "s_add_u32 m0, s28, 49152\n"
    "v_fma_f32 v233, v233, v249, v245\n"
    "v_mfma_f32_16x16x32_bf16 v[164:167], a[112:115], v[52:55], v[164:167]\n"
    "ds_read_b64_tr_b16 v[46:47], v13 offset:55296\n"
    "v_cvt_pk_bf16_f32 v252, v250, v232\n"
    "v_mfma_f32_16x16x32_bf16 v[172:175], a[176:179], v[52:55], v[172:175]\n"
    "v_cvt_pk_bf16_f32 v253, v251, v233\n"
    "v_mfma_f32_16x16x32_bf16 v[180:183], a[240:243], v[52:55], v[180:183]\n"
    "buffer_load_dwordx4 v4, s[76:79], s29 offen offset:0 lds\n"
    "ds_read_b128 v[242:245], v17 offset:64\n"
    "s_waitcnt vmcnt(22)\n"
    "v_mfma_f32_16x16x32_bf16 v[56:59], a[52:55], v[24:27], v[56:59]\n"
    "ds_read_b64_tr_b16 v[48:49], v14 offset:53248\n"
    "ds_read_b128 v[246:249], v17 offset:1088\n"
    "v_mfma_f32_16x16x32_bf16 v[64:67], a[116:119], v[24:27], v[64:67]\n"
    "ds_read_b64_tr_b16 v[50:51], v14 offset:55296\n"
    "v_lshlrev_b32 v250, 16, v234\n"
    "v_mfma_f32_16x16x32_bf16 v[72:75], a[180:183], v[24:27], v[72:75]\n"
    "v_and_b32 v234, 0xffff0000, v234\n"
    "v_mfma_f32_16x16x32_bf16 v[80:83], a[244:247], v[24:27], v[80:83]\n"
    "buffer_load_dwordx4 v5, s[76:79], s29 offen offset:1024 lds\n"
    "v_mfma_f32_16x16x32_bf16 v[60:63], a[52:55], v[28:31], v[60:63]\n"
    "ds_read_b64_tr_b16 v[52:53], v15 offset:53248\n"
    "v_lshlrev_b32 v251, 16, v235\n"
    "v_mfma_f32_16x16x32_bf16 v[68:71], a[116:119], v[28:31], v[68:71]\n"
    "ds_read_b64_tr_b16 v[54:55], v15 offset:55296\n"
    "v_and_b32 v235, 0xffff0000, v235\n"
    "v_mfma_f32_16x16x32_bf16 v[76:79], a[180:183], v[28:31], v[76:79]\n"
    "v_fma_f32 v250, v250, v240, v241\n"
    "v_mfma_f32_16x16x32_bf16 v[84:87], a[244:247], v[28:31], v[84:87]\n"
    "v_fma_f32 v234, v234, v240, v241\n"
    "s_waitcnt lgkmcnt(10)\n"
    "v_mfma_f32_16x16x32_bf16 v[88:91], a[52:55], v[32:35], v[88:91]\n"
    "ds_read_b64_tr_b16 v[24:25], v12 offset:40960\n"
    "buffer_load_dwordx4 v6, s[76:79], s29 offen offset:2048 lds\n"
    "v_fma_f32 v251, v251, v240, v241\n"
    "v_mfma_f32_16x16x32_bf16 v[96:99], a[116:119], v[32:35], v[96:99]\n"
    "ds_read_b64_tr_b16 v[26:27], v12 offset:43008\n"
    "v_fma_f32 v235, v235, v240, v241\n"
    "v_mfma_f32_16x16x32_bf16 v[104:107], a[180:183], v[32:35], v[104:107]\n"
    "s_waitcnt lgkmcnt(5)\n"
    "v_mfma_f32_16x16x32_bf16 v[112:115], a[244:247], v[32:35], v[112:115]\n"
    "v_fma_f32 v250, v250, v246, v242\n"
    "v_mfma_f32_16x16x32_bf16 v[92:95], a[52:55], v[36:39], v[92:95]\n"
    "ds_read_b64_tr_b16 v[28:29], v13 offset:40960\n"
    "buffer_load_dwordx4 v7, s[76:79], s29 offen offset:3072 lds\n"
    "v_fma_f32 v234, v234, v247, v243\n"
    "v_mfma_f32_16x16x32_bf16 v[100:103], a[116:119], v[36:39], v[100:103]\n"
    "ds_read_b64_tr_b16 v[30:31], v13 offset:43008\n"
    "s_mov_b32 s29, 384\n"
    "v_fma_f32 v251, v251, v248, v244\n"
    "v_mfma_f32_16x16x32_bf16 v[108:111], a[180:183], v[36:39], v[108:111]\n"
    "s_add_u32 m0, s28, 65536\n"
    "v_fma_f32 v235, v235, v249, v245\n"
    "v_mfma_f32_16x16x32_bf16 v[116:119], a[244:247], v[36:39], v[116:119]\n"
    "v_mfma_f32_16x16x32_bf16 v[120:123], a[52:55], v[40:43], v[120:123]\n"
    "ds_read_b64_tr_b16 v[32:33], v14 offset:40960\n"
    "v_cvt_pk_bf16_f32 v254, v250, v234\n"
    "v_mfma_f32_16x16x32_bf16 v[128:131], a[116:119], v[40:43], v[128:131]\n"
    "ds_read_b64_tr_b16 v[34:35], v14 offset:43008\n"
    "buffer_load_dwordx4 v4, s[76:79], s29 offen offset:0 lds\n"
    "v_cvt_pk_bf16_f32 v255, v251, v235\n"
    "v_mfma_f32_16x16x32_bf16 v[136:139], a[180:183], v[40:43], v[136:139]\n"
    "global_store_dwordx4 v22, v[252:255], s[68:69] sc1\n\ts_nop 1\n"
    "v_mfma_f32_16x16x32_bf16 v[144:147], a[244:247], v[40:43], v[144:147]\n"
    "v_add_u32 v22, 0x2000, v22\n"
    "v_mfma_f32_16x16x32_bf16 v[124:127], a[52:55], v[44:47], v[124:127]\n"
    "ds_read_b64_tr_b16 v[36:37], v15 offset:40960\n"
    "ds_read_b128 v[242:245], v17 offset:128\n"
    "v_mfma_f32_16x16x32_bf16 v[132:135], a[116:119], v[44:47], v[132:135]\n"
    "ds_read_b64_tr_b16 v[38:39], v15 offset:43008\n"
    "buffer_load_dwordx4 v5, s[76:79], s29 offen offset:1024 lds\n"
    "ds_read_b128 v[246:249], v17 offset:1152\n"
    "v_mfma_f32_16x16x32_bf16 v[140:143], a[180:183], v[44:47], v[140:143]\n"
    "v_lshlrev_b32 v250, 16, v236\n"
    "v_mfma_f32_16x16x32_bf16 v[148:151], a[244:247], v[44:47], v[148:151]\n"
    "v_and_b32 v236, 0xffff0000, v236\n"
    "s_waitcnt lgkmcnt(10)\n"
    "v_mfma_f32_16x16x32_bf16 v[152:155], a[52:55], v[48:51], v[152:155]\n"
    "ds_read_b64_tr_b16 v[40:41], v12 offset:57344\n"
    "v_lshlrev_b32 v251, 16, v237\n"
    "v_mfma_f32_16x16x32_bf16 v[160:163], a[116:119], v[48:51], v[160:163]\n"
    "ds_read_b64_tr_b16 v[42:43], v12 offset:59392\n"
    "v_and_b32 v237, 0xffff0000, v237\n"
    "v_mfma_f32_16x16x32_bf16 v[168:171], a[180:183], v[48:51], v[168:171]\n"
    "buffer_load_dwordx4 v6, s[76:79], s29 offen offset:2048 lds\n"
    "v_mfma_f32_16x16x32_bf16 v[176:179], a[244:247], v[48:51], v[176:179]\n"
    "v_fma_f32 v250, v250, v240, v241\n"
    "v_mfma_f32_16x16x32_bf16 v[156:159], a[52:55], v[52:55], v[156:159]\n"
    "ds_read_b64_tr_b16 v[44:45], v13 offset:57344\n"
    "v_fma_f32 v236, v236, v240, v241\n"
    "v_mfma_f32_16x16x32_bf16 v[164:167], a[116:119], v[52:55], v[164:167]\n"
    "ds_read_b64_tr_b16 v[46:47], v13 offset:59392\n"
    "v_fma_f32 v251, v251, v240, v241\n"
    "v_mfma_f32_16x16x32_bf16 v[172:175], a[180:183], v[52:55], v[172:175]\n"
    "buffer_load_dwordx4 v7, s[76:79], s29 offen offset:3072 lds\n"
    "v_fma_f32 v237, v237, v240, v241\n"
    "v_mfma_f32_16x16x32_bf16 v[180:183], a[244:247], v[52:55], v[180:183]\n"
    "s_mov_b32 s29, 1048960\n"
    "s_waitcnt lgkmcnt(4)\n"
    "s_waitcnt vmcnt(26)\n"
    "v_mfma_f32_16x16x32_bf16 v[56:59], a[56:59], v[24:27], v[56:59]\n"
    "ds_read_b64_tr_b16 v[48:49], v14 offset:57344\n"
    "s_add_u32 m0, s28, 81920\n"
    "v_fma_f32 v250, v250, v246, v242\n"
    "v_mfma_f32_16x16x32_bf16 v[64:67], a[120:123], v[24:27], v[64:67]\n"
    "ds_read_b64_tr_b16 v[50:51], v14 offset:59392\n"
    "v_fma_f32 v236, v236, v247, v243\n"
    "v_mfma_f32_16x16x32_bf16 v[72:75], a[184:187], v[24:27], v[72:75]\n"
    "v_fma_f32 v251, v251, v248, v244\n"
    "v_mfma_f32_16x16x32_bf16 v[80:83], a[248:251], v[24:27], v[80:83]\n"
    "buffer_load_dwordx4 v4, s[76:79], s29 offen offset:0 lds\n"
    "v_fma_f32 v237, v237, v249, v245\n"
    "v_mfma_f32_16x16x32_bf16 v[60:63], a[56:59], v[28:31], v[60:63]\n"
    "ds_read_b64_tr_b16 v[52:53], v15 offset:57344\n"
    "v_cvt_pk_bf16_f32 v252, v250, v236\n"
    "v_mfma_f32_16x16x32_bf16 v[68:71], a[120:123], v[28:31], v[68:71]\n"
    "ds_read_b64_tr_b16 v[54:55], v15 offset:59392\n"
    "v_cvt_pk_bf16_f32 v253, v251, v237\n"
    "v_mfma_f32_16x16x32_bf16 v[76:79], a[184:187], v[28:31], v[76:79]\n"
    "v_mfma_f32_16x16x32_bf16 v[84:87], a[248:251], v[28:31], v[84:87]\n"
    "buffer_load_dwordx4 v5, s[76:79], s29 offen offset:1024 lds\n"
    "ds_read_b128 v[242:245], v17 offset:192\n"
    "v_mfma_f32_16x16x32_bf16 v[88:91], a[56:59], v[32:35], v[88:91]\n"
    "ds_read_b64_tr_b16 v[24:25], v12 offset:45056\n"
    "ds_read_b128 v[246:249], v17 offset:1216\n"
    "v_mfma_f32_16x16x32_bf16 v[96:99], a[120:123], v[32:35], v[96:99]\n"
    "ds_read_b64_tr_b16 v[26:27], v12 offset:47104\n"
    "v_lshlrev_b32 v250, 16, v238\n"
    "v_mfma_f32_16x16x32_bf16 v[104:107], a[184:187], v[32:35], v[104:107]\n"
    "v_and_b32 v238, 0xffff0000, v238\n"
    "v_mfma_f32_16x16x32_bf16 v[112:115], a[248:251], v[32:35], v[112:115]\n"
    "v_lshlrev_b32 v251, 16, v239\n"
    "v_mfma_f32_16x16x32_bf16 v[92:95], a[56:59], v[36:39], v[92:95]\n"
    "ds_read_b64_tr_b16 v[28:29], v13 offset:45056\n"
    "buffer_load_dwordx4 v6, s[76:79], s29 offen offset:2048 lds\n"
    "v_and_b32 v239, 0xffff0000, v239\n"
    "v_mfma_f32_16x16x32_bf16 v[100:103], a[120:123], v[36:39], v[100:103]\n"
    "ds_read_b64_tr_b16 v[30:31], v13 offset:47104\n"
    "v_fma_f32 v250, v250, v240, v241\n"
    "v_mfma_f32_16x16x32_bf16 v[108:111], a[184:187], v[36:39], v[108:111]\n"
    "v_fma_f32 v238, v238, v240, v241\n"
    "v_mfma_f32_16x16x32_bf16 v[116:119], a[248:251], v[36:39], v[116:119]\n"
    "v_fma_f32 v251, v251, v240, v241\n"
    "s_waitcnt lgkmcnt(10)\n"
    "v_mfma_f32_16x16x32_bf16 v[120:123], a[56:59], v[40:43], v[120:123]\n"
    "ds_read_b64_tr_b16 v[32:33], v14 offset:45056\n"
    "buffer_load_dwordx4 v7, s[76:79], s29 offen offset:3072 lds\n"
    "v_fma_f32 v239, v239, v240, v241\n"
    "v_mfma_f32_16x16x32_bf16 v[128:131], a[120:123], v[40:43], v[128:131]\n"
    "ds_read_b64_tr_b16 v[34:35], v14 offset:47104\n"
    "s_waitcnt lgkmcnt(5)\n"
    "v_mfma_f32_16x16x32_bf16 v[136:139], a[184:187], v[40:43], v[136:139]\n"
    "v_mfma_f32_16x16x32_bf16 v[144:147], a[248:251], v[40:43], v[144:147]\n"
    "v_fma_f32 v250, v250, v246, v242\n"
    "v_mfma_f32_16x16x32_bf16 v[124:127], a[56:59], v[44:47], v[124:127]\n"
    "ds_read_b64_tr_b16 v[36:37], v15 offset:45056\n"
    "v_fma_f32 v238, v238, v247, v243\n"
    "v_mfma_f32_16x16x32_bf16 v[132:135], a[120:123], v[44:47], v[132:135]\n"
    "ds_read_b64_tr_b16 v[38:39], v15 offset:47104\n"
    "v_fma_f32 v251, v251, v248, v244\n"
    "v_mfma_f32_16x16x32_bf16 v[140:143], a[184:187], v[44:47], v[140:143]\n"
    "v_fma_f32 v239, v239, v249, v245\n"
    "v_mfma_f32_16x16x32_bf16 v[148:151], a[248:251], v[44:47], v[148:151]\n"
    "v_cvt_pk_bf16_f32 v254, v250, v238\n"
    "v_mfma_f32_16x16x32_bf16 v[152:155], a[56:59], v[48:51], v[152:155]\n"
    "ds_read_b64_tr_b16 v[40:41], v12 offset:61440\n"
    "v_cvt_pk_bf16_f32 v255, v251, v239\n"
    "v_mfma_f32_16x16x32_bf16 v[160:163], a[120:123], v[48:51], v[160:163]\n"
    "ds_read_b64_tr_b16 v[42:43], v12 offset:63488\n"
    "global_store_dwordx4 v22, v[252:255], s[68:69] sc1\n\ts_nop 1\n"
    "v_mfma_f32_16x16x32_bf16 v[168:171], a[184:187], v[48:51], v[168:171]\n"
    "v_mfma_f32_16x16x32_bf16 v[176:179], a[248:251], v[48:51], v[176:179]\n"
    "v_mfma_f32_16x16x32_bf16 v[156:159], a[56:59], v[52:55], v[156:159]\n"
    "ds_read_b64_tr_b16 v[44:45], v13 offset:61440\n"
    "v_mfma_f32_16x16x32_bf16 v[164:167], a[120:123], v[52:55], v[164:167]\n"
    "ds_read_b64_tr_b16 v[46:47], v13 offset:63488\n"
    "v_mfma_f32_16x16x32_bf16 v[172:175], a[184:187], v[52:55], v[172:175]\n"
    "v_mfma_f32_16x16x32_bf16 v[180:183], a[248:251], v[52:55], v[180:183]\n"
    "s_waitcnt vmcnt(26)\n"
    "s_waitcnt lgkmcnt(8)\n"
    "v_mfma_f32_16x16x32_bf16 v[56:59], a[60:63], v[24:27], v[56:59]\n"
    "ds_read_b64_tr_b16 v[48:49], v14 offset:61440\n"
    "v_mfma_f32_16x16x32_bf16 v[64:67], a[124:127], v[24:27], v[64:67]\n"
    "ds_read_b64_tr_b16 v[50:51], v14 offset:63488\n"
    "v_mfma_f32_16x16x32_bf16 v[72:75], a[188:191], v[24:27], v[72:75]\n"
    "v_mfma_f32_16x16x32_bf16 v[80:83], a[252:255], v[24:27], v[80:83]\n"
    "v_mfma_f32_16x16x32_bf16 v[60:63], a[60:63], v[28:31], v[60:63]\n"
    "ds_read_b64_tr_b16 v[52:53], v15 offset:61440\n"
    "v_mfma_f32_16x16x32_bf16 v[68:71], a[124:127], v[28:31], v[68:71]\n"
    "ds_read_b64_tr_b16 v[54:55], v15 offset:63488\n"
    "v_mfma_f32_16x16x32_bf16 v[76:79], a[188:191], v[28:31], v[76:79]\n"
    "v_mfma_f32_16x16x32_bf16 v[84:87], a[252:255], v[28:31], v[84:87]\n"
    "s_waitcnt lgkmcnt(8)\n"
    "v_mfma_f32_16x16x32_bf16 v[88:91], a[60:63], v[32:35], v[88:91]\n"
    "v_mfma_f32_16x16x32_bf16 v[96:99], a[124:127], v[32:35], v[96:99]\n"
    "v_mfma_f32_16x16x32_bf16 v[104:107], a[188:191], v[32:35], v[104:107]\n"
    "v_mfma_f32_16x16x32_bf16 v[112:115], a[252:255], v[32:35], v[112:115]\n"
    "v_mfma_f32_16x16x32_bf16 v[92:95], a[60:63], v[36:39], v[92:95]\n"
    "v_mfma_f32_16x16x32_bf16 v[100:103], a[124:127], v[36:39], v[100:103]\n"
    "v_mfma_f32_16x16x32_bf16 v[108:111], a[188:191], v[36:39], v[108:111]\n"
    "v_mfma_f32_16x16x32_bf16 v[116:119], a[252:255], v[36:39], v[116:119]\n"
    "s_waitcnt vmcnt(22) lgkmcnt(0)\n"
    "s_barrier\n"
    "v_mfma_f32_16x16x32_bf16 v[120:123], a[60:63], v[40:43], v[120:123]\n"
    "ds_read_b64_tr_b16 v[24:25], v8 offset:0\n"
    "s_mov_b32 s29, 2097536\n"
    "v_mfma_f32_16x16x32_bf16 v[128:131], a[124:127], v[40:43], v[128:131]\n"
    "ds_read_b64_tr_b16 v[26:27], v8 offset:2048\n"
    "s_add_u32 m0, s28, 98304\n"
    "v_mfma_f32_16x16x32_bf16 v[136:139], a[188:191], v[40:43], v[136:139]\n"
    "ds_read_b64_tr_b16 v[28:29], v9 offset:0\n"
    "buffer_load_dwordx4 v4, s[76:79], s29 offen offset:0 lds\n"
    "v_mfma_f32_16x16x32_bf16 v[144:147], a[252:255], v[40:43], v[144:147]\n"
    "ds_read_b64_tr_b16 v[30:31], v9 offset:2048\n"
    "v_mfma_f32_16x16x32_bf16 v[124:127], a[60:63], v[44:47], v[124:127]\n"
    "ds_read_b64_tr_b16 v[32:33], v8 offset:4096\n"
    "v_mfma_f32_16x16x32_bf16 v[132:135], a[124:127], v[44:47], v[132:135]\n"
    "ds_read_b64_tr_b16 v[34:35], v8 offset:6144\n"
    "v_mfma_f32_16x16x32_bf16 v[140:143], a[188:191], v[44:47], v[140:143]\n"
    "ds_read_b64_tr_b16 v[36:37], v9 offset:4096\n"
    "buffer_load_dwordx4 v5, s[76:79], s29 offen offset:1024 lds\n"
    "v_mfma_f32_16x16x32_bf16 v[148:151], a[252:255], v[44:47], v[148:151]\n"
    "ds_read_b64_tr_b16 v[38:39], v9 offset:6144\n"
    "v_mfma_f32_16x16x32_bf16 v[152:155], a[60:63], v[48:51], v[152:155]\n"
    "ds_read_b64_tr_b16 v[40:41], v8 offset:8192\n"
    "v_mfma_f32_16x16x32_bf16 v[160:163], a[124:127], v[48:51], v[160:163]\n"
    "ds_read_b64_tr_b16 v[42:43], v8 offset:10240\n"
    "v_mfma_f32_16x16x32_bf16 v[168:171], a[188:191], v[48:51], v[168:171]\n"
    "v_mfma_f32_16x16x32_bf16 v[176:179], a[252:255], v[48:51], v[176:179]\n"
    "buffer_load_dwordx4 v6, s[76:79], s29 offen offset:2048 lds\n"
    "v_mfma_f32_16x16x32_bf16 v[156:159], a[60:63], v[52:55], v[156:159]\n"
    "ds_read_b64_tr_b16 v[44:45], v9 offset:8192\n"
    "v_mfma_f32_16x16x32_bf16 v[164:167], a[124:127], v[52:55], v[164:167]\n"
    "ds_read_b64_tr_b16 v[46:47], v9 offset:10240\n"
    "v_mfma_f32_16x16x32_bf16 v[172:175], a[188:191], v[52:55], v[172:175]\n"
    "v_mfma_f32_16x16x32_bf16 v[180:183], a[252:255], v[52:55], v[180:183]\n"
    "buffer_load_dwordx4 v7, s[76:79], s29 offen offset:3072 lds\n"
    "s_waitcnt lgkmcnt(8)\n"
    "v_mfma_f32_16x16x32_bf16 v[184:187], a[0:3], v[24:27], 0\n"
    "ds_read_b64_tr_b16 v[48:49], v8 offset:12288\n"
    "s_mov_b32 s29, 3146112\n"
    "v_mfma_f32_16x16x32_bf16 v[192:195], a[64:67], v[24:27], 0\n"
    "ds_read_b64_tr_b16 v[50:51], v8 offset:14336\n"
    "s_add_u32 m0, s28, 114688\n"
    "v_mfma_f32_16x16x32_bf16 v[200:203], a[128:131], v[24:27], 0\n"
    "v_mfma_f32_16x16x32_bf16 v[208:211], a[192:195], v[24:27], 0\n"
    "v_mfma_f32_16x16x32_bf16 v[188:191], a[0:3], v[28:31], 0\n"
    "ds_read_b64_tr_b16 v[52:53], v9 offset:12288\n"
    "buffer_load_dwordx4 v4, s[76:79], s29 offen offset:0 lds\n"
    "v_mfma_f32_16x16x32_bf16 v[196:199], a[64:67], v[28:31], 0\n"
    "ds_read_b64_tr_b16 v[54:55], v9 offset:14336\n"
    "v_mfma_f32_16x16x32_bf16 v[204:207], a[128:131], v[28:31], 0\n"
    "v_mfma_f32_16x16x32_bf16 v[212:215], a[192:195], v[28:31], 0\n"
    "s_waitcnt lgkmcnt(8)\n"
    "v_mfma_f32_16x16x32_bf16 v[184:187], a[4:7], v[32:35], v[184:187]\n"
    "buffer_load_dwordx4 v5, s[76:79], s29 offen offset:1024 lds\n"
    "v_mfma_f32_16x16x32_bf16 v[192:195], a[68:71], v[32:35], v[192:195]\n"
    "v_mfma_f32_16x16x32_bf16 v[200:203], a[132:135], v[32:35], v[200:203]\n"
    "v_cvt_pk_bf16_f32 v216, v56, v57\n"
    "v_mfma_f32_16x16x32_bf16 v[208:211], a[196:199], v[32:35], v[208:211]\n"
    "v_mfma_f32_16x16x32_bf16 v[188:191], a[4:7], v[36:39], v[188:191]\n"
    "v_cvt_pk_bf16_f32 v217, v58, v59\n"
    "v_mfma_f32_16x16x32_bf16 v[196:199], a[68:71], v[36:39], v[196:199]\n"
    "buffer_load_dwordx4 v6, s[76:79], s29 offen offset:2048 lds\n"
    "v_mfma_f32_16x16x32_bf16 v[204:207], a[132:135], v[36:39], v[204:207]\n"
    "v_mfma_f32_16x16x32_bf16 v[212:215], a[196:199], v[36:39], v[212:215]\n"
    "v_cvt_pk_bf16_f32 v218, v64, v65\n"
    "s_waitcnt vmcnt(7) lgkmcnt(0)\n"
    "s_barrier\n"
    "s_cmp_lg_u32 s20, 0\n"
    "s_cbranch_scc1 L_shv_%=\n"
    "v_mov_b32 v18, s34\n"
    "v_mov_b32 v19, s94\n"
    "s_mov_b64 exec, 1\n"
    "global_atomic_swap v18, v19, s[60:61]\n"
    "s_mov_b64 exec, -1\n"
    "L_shv_%=:\n"
    "v_mfma_f32_16x16x32_bf16 v[184:187], a[8:11], v[40:43], v[184:187]\n"
    "ds_read_b64_tr_b16 v[24:25], v8 offset:16384\n"
    "v_mfma_f32_16x16x32_bf16 v[192:195], a[72:75], v[40:43], v[192:195]\n"
    "ds_read_b64_tr_b16 v[26:27], v8 offset:18432\n"
    "buffer_load_dwordx4 v7, s[76:79], s29 offen offset:3072 lds\n"
    "v_cvt_pk_bf16_f32 v219, v66, v67\n"
    "v_mfma_f32_16x16x32_bf16 v[200:203], a[136:139], v[40:43], v[200:203]\n"
    "ds_read_b64_tr_b16 v[28:29], v9 offset:16384\n"
    "v_mfma_f32_16x16x32_bf16 v[208:211], a[200:203], v[40:43], v[208:211]\n"
    "ds_read_b64_tr_b16 v[30:31], v9 offset:18432\n"
    "s_mov_b32 s99, 0\n"
    "v_mfma_f32_16x16x32_bf16 v[188:191], a[8:11], v[44:47], v[188:191]\n"
    "ds_read_b64_tr_b16 v[32:33], v8 offset:20480\n"
    "v_mfma_f32_16x16x32_bf16 v[196:199], a[72:75], v[44:47], v[196:199]\n"
    "ds_read_b64_tr_b16 v[34:35], v8 offset:22528\n"
    "v_mfma_f32_16x16x32_bf16 v[204:207], a[136:139], v[44:47], v[204:207]\n"
    "ds_read_b64_tr_b16 v[36:37], v9 offset:20480\n"
    "buffer_store_dwordx4 v[216:219], v21, s[80:83], s99 offen offset:0 sc1\n"
    "v_mfma_f32_16x16x32_bf16 v[212:215], a[200:203], v[44:47], v[212:215]\n"
    "ds_read_b64_tr_b16 v[38:39], v9 offset:22528\n"
    "v_mfma_f32_16x16x32_bf16 v[184:187], a[12:15], v[48:51], v[184:187]\n"
    "ds_read_b64_tr_b16 v[40:41], v8 offset:24576\n"
    "v_cvt_pk_bf16_f32 v220, v72, v73\n"
    "v_mfma_f32_16x16x32_bf16 v[192:195], a[76:79], v[48:51], v[192:195]\n"
    "ds_read_b64_tr_b16 v[42:43], v8 offset:26624\n"
    "v_mfma_f32_16x16x32_bf16 v[200:203], a[140:143], v[48:51], v[200:203]\n"
    "v_cvt_pk_bf16_f32 v221, v74, v75\n"
    "v_mfma_f32_16x16x32_bf16 v[208:211], a[204:207], v[48:51], v[208:211]\n"
    "v_mfma_f32_16x16x32_bf16 v[188:191], a[12:15], v[52:55], v[188:191]\n"
    "ds_read_b64_tr_b16 v[44:45], v9 offset:24576\n"
    "v_mfma_f32_16x16x32_bf16 v[196:199], a[76:79], v[52:55], v[196:199]\n"
    "ds_read_b64_tr_b16 v[46:47], v9 offset:26624\n"
    "v_cvt_pk_bf16_f32 v222, v80, v81\n"
    "v_mfma_f32_16x16x32_bf16 v[204:207], a[140:143], v[52:55], v[204:207]\n"
    "v_mfma_f32_16x16x32_bf16 v[212:215], a[204:207], v[52:55], v[212:215]\n"
    "v_cvt_pk_bf16_f32 v223, v82, v83\n"
    "s_waitcnt lgkmcnt(8)\n"
    "v_mfma_f32_16x16x32_bf16 v[184:187], a[16:19], v[24:27], v[184:187]\n"
    "ds_read_b64_tr_b16 v[48:49], v8 offset:28672\n"
    "v_mfma_f32_16x16x32_bf16 v[192:195], a[80:83], v[24:27], v[192:195]\n"
    "ds_read_b64_tr_b16 v[50:51], v8 offset:30720\n"
    "v_mfma_f32_16x16x32_bf16 v[200:203], a[144:147], v[24:27], v[200:203]\n"
    "s_mov_b32 s99, 8192\n"
    "v_mfma_f32_16x16x32_bf16 v[208:211], a[208:211], v[24:27], v[208:211]\n"
    "v_mfma_f32_16x16x32_bf16 v[188:191], a[16:19], v[28:31], v[188:191]\n"
    "ds_read_b64_tr_b16 v[52:53], v9 offset:28672\n"
    "buffer_store_dwordx4 v[220:223], v21, s[80:83], s99 offen offset:0 sc1\n"
    "v_mfma_f32_16x16x32_bf16 v[196:199], a[80:83], v[28:31], v[196:199]\n"
    "ds_read_b64_tr_b16 v[54:55], v9 offset:30720\n"
    "v_mfma_f32_16x16x32_bf16 v[204:207], a[144:147], v[28:31], v[204:207]\n"
    "v_cvt_pk_bf16_f32 v224, v60, v61\n"
    "v_mfma_f32_16x16x32_bf16 v[212:215], a[208:211], v[28:31], v[212:215]\n"
    "s_waitcnt lgkmcnt(8)\n"
    "v_mfma_f32_16x16x32_bf16 v[184:187], a[20:23], v[32:35], v[184:187]\n"
    "v_mfma_f32_16x16x32_bf16 v[192:195], a[84:87], v[32:35], v[192:195]\n"
    "v_cvt_pk_bf16_f32 v225, v62, v63\n"
    "v_mfma_f32_16x16x32_bf16 v[200:203], a[148:151], v[32:35], v[200:203]\n"
    "v_mfma_f32_16x16x32_bf16 v[208:211], a[212:215], v[32:35], v[208:211]\n"
    "v_cvt_pk_bf16_f32 v226, v68, v69\n"
    "v_mfma_f32_16x16x32_bf16 v[188:191], a[20:23], v[36:39], v[188:191]\n"
    "v_mfma_f32_16x16x32_bf16 v[196:199], a[84:87], v[36:39], v[196:199]\n"
    "v_cvt_pk_bf16_f32 v227, v70, v71\n"
    "v_mfma_f32_16x16x32_bf16 v[204:207], a[148:151], v[36:39], v[204:207]\n"
    "v_mfma_f32_16x16x32_bf16 v[212:215], a[212:215], v[36:39], v[212:215]\n"
    "s_waitcnt lgkmcnt(4)\n"
    "v_mfma_f32_16x16x32_bf16 v[184:187], a[24:27], v[40:43], v[184:187]\n"
    "ds_read_b64_tr_b16 v[24:25], v8 offset:32768\n"
    "s_mov_b32 s99, 0\n"
    "v_mfma_f32_16x16x32_bf16 v[192:195], a[88:91], v[40:43], v[192:195]\n"
    "ds_read_b64_tr_b16 v[26:27], v8 offset:34816\n"
    "v_mfma_f32_16x16x32_bf16 v[200:203], a[152:155], v[40:43], v[200:203]\n"
    "ds_read_b64_tr_b16 v[28:29], v9 offset:32768\n"
    "buffer_store_dwordx4 v[224:227], v21, s[80:83], s99 offen offset:1024 sc1\n"
    "v_mfma_f32_16x16x32_bf16 v[208:211], a[216:219], v[40:43], v[208:211]\n"
    "ds_read_b64_tr_b16 v[30:31], v9 offset:34816\n"
    "v_mfma_f32_16x16x32_bf16 v[188:191], a[24:27], v[44:47], v[188:191]\n"
    "ds_read_b64_tr_b16 v[32:33], v8 offset:36864\n"
    "v_cvt_pk_bf16_f32 v228, v76, v77\n"
    "v_mfma_f32_16x16x32_bf16 v[196:199], a[88:91], v[44:47], v[196:199]\n"
    "ds_read_b64_tr_b16 v[34:35], v8 offset:38912\n"
    "v_mfma_f32_16x16x32_bf16 v[204:207], a[152:155], v[44:47], v[204:207]\n"
    "ds_read_b64_tr_b16 v[36:37], v9 offset:36864\n"
    "v_mfma_f32_16x16x32_bf16 v[212:215], a[216:219], v[44:47], v[212:215]\n"
    "ds_read_b64_tr_b16 v[38:39], v9 offset:38912\n"
    "v_cvt_pk_bf16_f32 v229, v78, v79\n"
    "s_waitcnt lgkmcnt(8)\n"
    "v_mfma_f32_16x16x32_bf16 v[184:187], a[28:31], v[48:51], v[184:187]\n"
    "ds_read_b64_tr_b16 v[40:41], v8 offset:40960\n"
    "v_mfma_f32_16x16x32_bf16 v[192:195], a[92:95], v[48:51], v[192:195]\n"
    "ds_read_b64_tr_b16 v[42:43], v8 offset:43008\n"
    "v_cvt_pk_bf16_f32 v230, v84, v85\n"
    "v_mfma_f32_16x16x32_bf16 v[200:203], a[156:159], v[48:51], v[200:203]\n"
    "v_mfma_f32_16x16x32_bf16 v[208:211], a[220:223], v[48:51], v[208:211]\n"
    "v_mfma_f32_16x16x32_bf16 v[188:191], a[28:31], v[52:55], v[188:191]\n"
    "ds_read_b64_tr_b16 v[44:45], v9 offset:40960\n"
    "v_cvt_pk_bf16_f32 v231, v86, v87\n"
    "v_mfma_f32_16x16x32_bf16 v[196:199], a[92:95], v[52:55], v[196:199]\n"
    "ds_read_b64_tr_b16 v[46:47], v9 offset:43008\n"
    "v_mfma_f32_16x16x32_bf16 v[204:207], a[156:159], v[52:55], v[204:207]\n"
    "s_mov_b32 s99, 8192\n"
    "v_mfma_f32_16x16x32_bf16 v[212:215], a[220:223], v[52:55], v[212:215]\n"
    "s_waitcnt lgkmcnt(8)\n"
    "v_mfma_f32_16x16x32_bf16 v[184:187], a[32:35], v[24:27], v[184:187]\n"
    "ds_read_b64_tr_b16 v[48:49], v8 offset:45056\n"
    "buffer_store_dwordx4 v[228:231], v21, s[80:83], s99 offen offset:1024 sc1\n"
    "v_mfma_f32_16x16x32_bf16 v[192:195], a[96:99], v[24:27], v[192:195]\n"
    "ds_read_b64_tr_b16 v[50:51], v8 offset:47104\n"
    "v_mfma_f32_16x16x32_bf16 v[200:203], a[160:163], v[24:27], v[200:203]\n"
    "v_mfma_f32_16x16x32_bf16 v[208:211], a[224:227], v[24:27], v[208:211]\n"
    "v_cvt_pk_bf16_f32 v216, v88, v89\n"
    "v_mfma_f32_16x16x32_bf16 v[188:191], a[32:35], v[28:31], v[188:191]\n"
    "ds_read_b64_tr_b16 v[52:53], v9 offset:45056\n"
    "v_mfma_f32_16x16x32_bf16 v[196:199], a[96:99], v[28:31], v[196:199]\n"
    "ds_read_b64_tr_b16 v[54:55], v9 offset:47104\n"
    "v_cvt_pk_bf16_f32 v217, v90, v91\n"
    "v_mfma_f32_16x16x32_bf16 v[204:207], a[160:163], v[28:31], v[204:207]\n"
    "v_mfma_f32_16x16x32_bf16 v[212:215], a[224:227], v[28:31], v[212:215]\n"
    "v_cvt_pk_bf16_f32 v218, v96, v97\n"
    "s_waitcnt lgkmcnt(8)\n"
    "v_mfma_f32_16x16x32_bf16 v[184:187], a[36:39], v[32:35], v[184:187]\n"
    "v_mfma_f32_16x16x32_bf16 v[192:195], a[100:103], v[32:35], v[192:195]\n"
    "v_mfma_f32_16x16x32_bf16 v[200:203], a[164:167], v[32:35], v[200:203]\n"
    "v_cvt_pk_bf16_f32 v219, v98, v99\n"
    "v_mfma_f32_16x16x32_bf16 v[208:211], a[228:231], v[32:35], v[208:211]\n"
    "v_mfma_f32_16x16x32_bf16 v[188:191], a[36:39], v[36:39], v[188:191]\n"
    "s_mov_b32 s99, 0\n"
    "v_mfma_f32_16x16x32_bf16 v[196:199], a[100:103], v[36:39], v[196:199]\n"
    "v_mfma_f32_16x16x32_bf16 v[204:207], a[164:167], v[36:39], v[204:207]\n"
    "buffer_store_dwordx4 v[216:219], v21, s[80:83], s99 offen offset:2048 sc1\n"
    "v_mfma_f32_16x16x32_bf16 v[212:215], a[228:231], v[36:39], v[212:215]\n"
    "s_waitcnt lgkmcnt(4)\n"
    "v_mfma_f32_16x16x32_bf16 v[184:187], a[40:43], v[40:43], v[184:187]\n"
    "ds_read_b64_tr_b16 v[24:25], v8 offset:49152\n"
    "v_mfma_f32_16x16x32_bf16 v[192:195], a[104:107], v[40:43], v[192:195]\n"
    "ds_read_b64_tr_b16 v[26:27], v8 offset:51200\n"
    "v_cvt_pk_bf16_f32 v220, v104, v105\n"
    "v_mfma_f32_16x16x32_bf16 v[200:203], a[168:171], v[40:43], v[200:203]\n"
    "ds_read_b64_tr_b16 v[28:29], v9 offset:49152\n"
    "v_mfma_f32_16x16x32_bf16 v[208:211], a[232:235], v[40:43], v[208:211]\n"
    "ds_read_b64_tr_b16 v[30:31], v9 offset:51200\n"
    "v_cvt_pk_bf16_f32 v221, v106, v107\n"
    "v_mfma_f32_16x16x32_bf16 v[188:191], a[40:43], v[44:47], v[188:191]\n"
    "ds_read_b64_tr_b16 v[32:33], v8 offset:53248\n"
    "v_mfma_f32_16x16x32_bf16 v[196:199], a[104:107], v[44:47], v[196:199]\n"
    "ds_read_b64_tr_b16 v[34:35], v8 offset:55296\n"
    "v_mfma_f32_16x16x32_bf16 v[204:207], a[168:171], v[44:47], v[204:207]\n"
    "ds_read_b64_tr_b16 v[36:37], v9 offset:53248\n"
    "v_cvt_pk_bf16_f32 v222, v112, v113\n"
    "v_mfma_f32_16x16x32_bf16 v[212:215], a[232:235], v[44:47], v[212:215]\n"
    "ds_read_b64_tr_b16 v[38:39], v9 offset:55296\n"
    "s_waitcnt lgkmcnt(8)\n"
    "v_mfma_f32_16x16x32_bf16 v[184:187], a[44:47], v[48:51], v[184:187]\n"
    "ds_read_b64_tr_b16 v[40:41], v8 offset:57344\n"
    "v_cvt_pk_bf16_f32 v223, v114, v115\n"
    "v_mfma_f32_16x16x32_bf16 v[192:195], a[108:111], v[48:51], v[192:195]\n"
    "ds_read_b64_tr_b16 v[42:43], v8 offset:59392\n"
    "v_mfma_f32_16x16x32_bf16 v[200:203], a[172:175], v[48:51], v[200:203]\n"
    "s_mov_b32 s99, 8192\n"
    "v_mfma_f32_16x16x32_bf16 v[208:211], a[236:239], v[48:51], v[208:211]\n"
    "v_mfma_f32_16x16x32_bf16 v[188:191], a[44:47], v[52:55], v[188:191]\n"
    "ds_read_b64_tr_b16 v[44:45], v9 offset:57344\n"
    "v_mfma_f32_16x16x32_bf16 v[196:199], a[108:111], v[52:55], v[196:199]\n"
    "ds_read_b64_tr_b16 v[46:47], v9 offset:59392\n"
    "buffer_store_dwordx4 v[220:223], v21, s[80:83], s99 offen offset:2048 sc1\n"
    "v_mfma_f32_16x16x32_bf16 v[204:207], a[172:175], v[52:55], v[204:207]\n"
    "v_mfma_f32_16x16x32_bf16 v[212:215], a[236:239], v[52:55], v[212:215]\n"
    "v_cvt_pk_bf16_f32 v224, v92, v93\n"
    "s_waitcnt lgkmcnt(8)\n"
    "v_mfma_f32_16x16x32_bf16 v[184:187], a[48:51], v[24:27], v[184:187]\n"
    "ds_read_b64_tr_b16 v[48:49], v8 offset:61440\n"
    "v_mfma_f32_16x16x32_bf16 v[192:195], a[112:115], v[24:27], v[192:195]\n"
    "ds_read_b64_tr_b16 v[50:51], v8 offset:63488\n"
    "v_cvt_pk_bf16_f32 v225, v94, v95\n"
    "v_mfma_f32_16x16x32_bf16 v[200:203], a[176:179], v[24:27], v[200:203]\n"
    "v_mfma_f32_16x16x32_bf16 v[208:211], a[240:243], v[24:27], v[208:211]\n"
    "v_mfma_f32_16x16x32_bf16 v[188:191], a[48:51], v[28:31], v[188:191]\n"
    "ds_read_b64_tr_b16 v[52:53], v9 offset:61440\n"
    "v_cvt_pk_bf16_f32 v226, v100, v101\n"
    "v_mfma_f32_16x16x32_bf16 v[196:199], a[112:115], v[28:31], v[196:199]\n"
    "ds_read_b64_tr_b16 v[54:55], v9 offset:63488\n"
    "v_mfma_f32_16x16x32_bf16 v[204:207], a[176:179], v[28:31], v[204:207]\n"
    "v_cvt_pk_bf16_f32 v227, v102, v103\n"
    "v_mfma_f32_16x16x32_bf16 v[212:215], a[240:243], v[28:31], v[212:215]\n"
    "s_waitcnt lgkmcnt(8)\n"
    "v_mfma_f32_16x16x32_bf16 v[184:187], a[52:55], v[32:35], v[184:187]\n"
    "v_mfma_f32_16x16x32_bf16 v[192:195], a[116:119], v[32:35], v[192:195]\n"
    "s_mov_b32 s99, 0\n"
    "v_mfma_f32_16x16x32_bf16 v[200:203], a[180:183], v[32:35], v[200:203]\n"
    "v_mfma_f32_16x16x32_bf16 v[208:211], a[244:247], v[32:35], v[208:211]\n"
    "buffer_store_dwordx4 v[224:227], v21, s[80:83], s99 offen offset:3072 sc1\n"
    "v_mfma_f32_16x16x32_bf16 v[188:191], a[52:55], v[36:39], v[188:191]\n"
    "v_mfma_f32_16x16x32_bf16 v[196:199], a[116:119], v[36:39], v[196:199]\n"
    "v_cvt_pk_bf16_f32 v228, v108, v109\n"
    "v_mfma_f32_16x16x32_bf16 v[204:207], a[180:183], v[36:39], v[204:207]\n"
    "v_mfma_f32_16x16x32_bf16 v[212:215], a[244:247], v[36:39], v[212:215]\n"
    "s_waitcnt lgkmcnt(4)\n"
    "v_mfma_f32_16x16x32_bf16 v[184:187], a[56:59], v[40:43], v[184:187]\n"
    "ds_read_b64_tr_b16 v[24:25], v10 offset:0\n"
    "v_cvt_pk_bf16_f32 v229, v110, v111\n"
    "v_mfma_f32_16x16x32_bf16 v[192:195], a[120:123], v[40:43], v[192:195]\n"
    "ds_read_b64_tr_b16 v[26:27], v10 offset:2048\n"
    "v_mfma_f32_16x16x32_bf16 v[200:203], a[184:187], v[40:43], v[200:203]\n"
    "ds_read_b64_tr_b16 v[28:29], v11 offset:0\n"
    "v_cvt_pk_bf16_f32 v230, v116, v117\n"
    "v_mfma_f32_16x16x32_bf16 v[208:211], a[248:251], v[40:43], v[208:211]\n"
    "ds_read_b64_tr_b16 v[30:31], v11 offset:2048\n"
    "v_mfma_f32_16x16x32_bf16 v[188:191], a[56:59], v[44:47], v[188:191]\n"
    "ds_read_b64_tr_b16 v[32:33], v10 offset:4096\n"
    "v_cvt_pk_bf16_f32 v231, v118, v119\n"
    "v_mfma_f32_16x16x32_bf16 v[196:199], a[120:123], v[44:47], v[196:199]\n"
    "ds_read_b64_tr_b16 v[34:35], v10 offset:6144\n"
    "v_mfma_f32_16x16x32_bf16 v[204:207], a[184:187], v[44:47], v[204:207]\n"
    "ds_read_b64_tr_b16 v[36:37], v11 offset:4096\n"
    "v_mfma_f32_16x16x32_bf16 v[212:215], a[248:251], v[44:47], v[212:215]\n"
    "ds_read_b64_tr_b16 v[38:39], v11 offset:6144\n"
    "s_mov_b32 s99, 8192\n"
    "s_waitcnt lgkmcnt(8)\n"
    "v_mfma_f32_16x16x32_bf16 v[184:187], a[60:63], v[48:51], v[184:187]\n"
    "ds_read_b64_tr_b16 v[40:41], v10 offset:8192\n"
    "v_mfma_f32_16x16x32_bf16 v[192:195], a[124:127], v[48:51], v[192:195]\n"
    "ds_read_b64_tr_b16 v[42:43], v10 offset:10240\n"
    "buffer_store_dwordx4 v[228:231], v21, s[80:83], s99 offen offset:3072 sc1\n"
    "v_mfma_f32_16x16x32_bf16 v[200:203], a[188:191], v[48:51], v[200:203]\n"
    "v_mfma_f32_16x16x32_bf16 v[208:211], a[252:255], v[48:51], v[208:211]\n"
    "v_mfma_f32_16x16x32_bf16 v[188:191], a[60:63], v[52:55], v[188:191]\n"
    "ds_read_b64_tr_b16 v[44:45], v11 offset:8192\n"
    "v_mfma_f32_16x16x32_bf16 v[196:199], a[124:127], v[52:55], v[196:199]\n"
    "ds_read_b64_tr_b16 v[46:47], v11 offset:10240\n"
    "v_mfma_f32_16x16x32_bf16 v[204:207], a[188:191], v[52:55], v[204:207]\n"
    "v_mfma_f32_16x16x32_bf16 v[212:215], a[252:255], v[52:55], v[212:215]\n"
    "s_waitcnt lgkmcnt(8)\n"
    "v_mfma_f32_16x16x32_bf16 v[56:59], a[0:3], v[24:27], 0\n"
    "ds_read_b64_tr_b16 v[48:49], v10 offset:12288\n"
    "v_mfma_f32_16x16x32_bf16 v[64:67], a[64:67], v[24:27], 0\n"
    "ds_read_b64_tr_b16 v[50:51], v10 offset:14336\n"
    "v_mfma_f32_16x16x32_bf16 v[72:75], a[128:131], v[24:27], 0\n"
    "v_mfma_f32_16x16x32_bf16 v[80:83], a[192:195], v[24:27], 0\n"
    "s_lshl_b32 s31, s20, 11\n"
    "v_mfma_f32_16x16x32_bf16 v[60:63], a[0:3], v[28:31], 0\n"
    "ds_read_b64_tr_b16 v[52:53], v11 offset:12288\n"
    "v_mfma_f32_16x16x32_bf16 v[68:71], a[64:67], v[28:31], 0\n"
    "ds_read_b64_tr_b16 v[54:55], v11 offset:14336\n"
    "v_mfma_f32_16x16x32_bf16 v[76:79], a[128:131], v[28:31], 0\n"
    "v_add_u32 v235, s31, v2\n"
    "v_mfma_f32_16x16x32_bf16 v[84:87], a[192:195], v[28:31], 0\n"
    "s_waitcnt lgkmcnt(8)\n"
    "v_mfma_f32_16x16x32_bf16 v[56:59], a[4:7], v[32:35], v[56:59]\n"
    "v_mfma_f32_16x16x32_bf16 v[64:67], a[68:71], v[32:35], v[64:67]\n"
    "s_sub_u32 s30, 15, s22\n"
    "v_mfma_f32_16x16x32_bf16 v[72:75], a[132:135], v[32:35], v[72:75]\n"
    "v_mfma_f32_16x16x32_bf16 v[80:83], a[196:199], v[32:35], v[80:83]\n"
    "v_cvt_pk_bf16_f32 v216, v120, v121\n"
    "v_mfma_f32_16x16x32_bf16 v[60:63], a[4:7], v[36:39], v[60:63]\n"
    "s_lshl_b32 s30, s30, 17\n"
    "v_mfma_f32_16x16x32_bf16 v[68:71], a[68:71], v[36:39], v[68:71]\n"
    "v_mfma_f32_16x16x32_bf16 v[76:79], a[132:135], v[36:39], v[76:79]\n"
    "v_cvt_pk_bf16_f32 v217, v122, v123\n"
    "v_mfma_f32_16x16x32_bf16 v[84:87], a[196:199], v[36:39], v[84:87]\n"
    "v_add_u32 v234, s30, v235\n"
    "s_waitcnt vmcnt(16) lgkmcnt(0)\n"
    "s_barrier\n"
    "v_mfma_f32_16x16x32_bf16 v[56:59], a[8:11], v[40:43], v[56:59]\n"
    "ds_read_b64_tr_b16 v[24:25], v10 offset:16384\n"
    "s_mov_b32 s29, 0\n"
    "v_mfma_f32_16x16x32_bf16 v[64:67], a[72:75], v[40:43], v[64:67]\n"
    "ds_read_b64_tr_b16 v[26:27], v10 offset:18432\n"
    "s_add_u32 m0, s28, 0\n"
    "v_cvt_pk_bf16_f32 v218, v128, v129\n"
    "v_mfma_f32_16x16x32_bf16 v[72:75], a[136:139], v[40:43], v[72:75]\n"
    "ds_read_b64_tr_b16 v[28:29], v11 offset:16384\n"
    "buffer_load_dwordx4 v4, s[44:47], s29 offen offset:0 lds\n"
    "v_mfma_f32_16x16x32_bf16 v[80:83], a[200:203], v[40:43], v[80:83]\n"
    "ds_read_b64_tr_b16 v[30:31], v11 offset:18432\n"
    "s_lshl_b32 s30, s22, 17\n"
    "v_mfma_f32_16x16x32_bf16 v[60:63], a[8:11], v[44:47], v[60:63]\n"
    "ds_read_b64_tr_b16 v[32:33], v10 offset:20480\n"
    "v_cvt_pk_bf16_f32 v219, v130, v131\n"
    "v_mfma_f32_16x16x32_bf16 v[68:71], a[72:75], v[44:47], v[68:71]\n"
    "ds_read_b64_tr_b16 v[34:35], v10 offset:22528\n"
    "v_mfma_f32_16x16x32_bf16 v[76:79], a[136:139], v[44:47], v[76:79]\n"
    "ds_read_b64_tr_b16 v[36:37], v11 offset:20480\n"
    "buffer_load_dwordx4 v5, s[44:47], s29 offen offset:1024 lds\n"
    "v_add_u32 v235, s30, v235\n"
    "v_mfma_f32_16x16x32_bf16 v[84:87], a[200:203], v[44:47], v[84:87]\n"
    "ds_read_b64_tr_b16 v[38:39], v11 offset:22528\n"
    "s_mov_b32 s99, 4096\n"
    "v_mfma_f32_16x16x32_bf16 v[56:59], a[12:15], v[48:51], v[56:59]\n"
    "ds_read_b64_tr_b16 v[40:41], v10 offset:24576\n"
    "v_mfma_f32_16x16x32_bf16 v[64:67], a[76:79], v[48:51], v[64:67]\n"
    "ds_read_b64_tr_b16 v[42:43], v10 offset:26624\n"
    "v_mfma_f32_16x16x32_bf16 v[72:75], a[140:143], v[48:51], v[72:75]\n"
    "buffer_store_dwordx4 v[216:219], v21, s[80:83], s99 offen offset:0 sc1\n"
    "v_mfma_f32_16x16x32_bf16 v[80:83], a[204:207], v[48:51], v[80:83]\n"
    "v_mfma_f32_16x16x32_bf16 v[60:63], a[12:15], v[52:55], v[60:63]\n"
    "ds_read_b64_tr_b16 v[44:45], v11 offset:24576\n"
    "v_mfma_f32_16x16x32_bf16 v[68:71], a[76:79], v[52:55], v[68:71]\n"
    "ds_read_b64_tr_b16 v[46:47], v11 offset:26624\n"
    "v_mfma_f32_16x16x32_bf16 v[76:79], a[140:143], v[52:55], v[76:79]\n"
    "v_cvt_pk_bf16_f32 v220, v136, v137\n"
    "v_mfma_f32_16x16x32_bf16 v[84:87], a[204:207], v[52:55], v[84:87]\n"
    "buffer_load_dwordx4 v6, s[44:47], s29 offen offset:2048 lds\n"
    "s_waitcnt lgkmcnt(8)\n"
    "v_mfma_f32_16x16x32_bf16 v[56:59], a[16:19], v[24:27], v[56:59]\n"
    "ds_read_b64_tr_b16 v[48:49], v10 offset:28672\n"
    "v_mfma_f32_16x16x32_bf16 v[64:67], a[80:83], v[24:27], v[64:67]\n"
    "ds_read_b64_tr_b16 v[50:51], v10 offset:30720\n"
    "v_cvt_pk_bf16_f32 v221, v138, v139\n"
    "v_mfma_f32_16x16x32_bf16 v[72:75], a[144:147], v[24:27], v[72:75]\n"
    "v_mfma_f32_16x16x32_bf16 v[80:83], a[208:211], v[24:27], v[80:83]\n"
    "buffer_load_dwordx4 v7, s[44:47], s29 offen offset:3072 lds\n"
    "v_mfma_f32_16x16x32_bf16 v[60:63], a[16:19], v[28:31], v[60:63]\n"
    "ds_read_b64_tr_b16 v[52:53], v11 offset:28672\n"
    "v_bfe_u32 v232, v16, 4, 1\n"
    "v_cvt_pk_bf16_f32 v222, v144, v145\n"
    "v_mfma_f32_16x16x32_bf16 v[68:71], a[80:83], v[28:31], v[68:71]\n"
    "ds_read_b64_tr_b16 v[54:55], v11 offset:30720\n"
    "v_mul_u32_u24 v232, s71, v232\n"
    "v_mfma_f32_16x16x32_bf16 v[76:79], a[144:147], v[28:31], v[76:79]\n"
    "v_and_b32 v233, 15, v16\n"
    "v_mfma_f32_16x16x32_bf16 v[84:87], a[208:211], v[28:31], v[84:87]\n"
    "v_lshl_add_u32 v232, v233, 2, v232\n"
    "v_cvt_pk_bf16_f32 v223, v146, v147\n"
    "s_waitcnt lgkmcnt(8)\n"
    "v_mfma_f32_16x16x32_bf16 v[56:59], a[20:23], v[32:35], v[56:59]\n"
    "v_add_u32 v232, s70, v232\n"
    "v_mfma_f32_16x16x32_bf16 v[64:67], a[84:87], v[32:35], v[64:67]\n"
    "global_load_dword v233, v232, s[60:61] sc1\n"
    "v_mfma_f32_16x16x32_bf16 v[72:75], a[148:151], v[32:35], v[72:75]\n"
    "s_mov_b32 s99, 12288\n"
    "v_mfma_f32_16x16x32_bf16 v[80:83], a[212:215], v[32:35], v[80:83]\n"
    "v_mfma_f32_16x16x32_bf16 v[60:63], a[20:23], v[36:39], v[60:63]\n"
    "v_mfma_f32_16x16x32_bf16 v[68:71], a[84:87], v[36:39], v[68:71]\n"
    "buffer_store_dwordx4 v[220:223], v21, s[80:83], s99 offen offset:0 sc1\n"
    "v_mfma_f32_16x16x32_bf16 v[76:79], a[148:151], v[36:39], v[76:79]\n"
    "v_mfma_f32_16x16x32_bf16 v[84:87], a[212:215], v[36:39], v[84:87]\n"
    "s_waitcnt vmcnt(23) lgkmcnt(0)\n"
    "s_barrier\n"
    "v_mfma_f32_16x16x32_bf16 v[56:59], a[24:27], v[40:43], v[56:59]\n"
    "ds_read_b64_tr_b16 v[24:25], v10 offset:32768\n"
    "s_mov_b32 s29, 128\n"
    "v_mfma_f32_16x16x32_bf16 v[64:67], a[88:91], v[40:43], v[64:67]\n"
    "ds_read_b64_tr_b16 v[26:27], v10 offset:34816\n"
    "s_add_u32 m0, s28, 16384\n"
    "v_cvt_pk_bf16_f32 v224, v124, v125\n"
    "v_mfma_f32_16x16x32_bf16 v[72:75], a[152:155], v[40:43], v[72:75]\n"
    "ds_read_b64_tr_b16 v[28:29], v11 offset:32768\n"
    "buffer_load_dwordx4 v4, s[44:47], s29 offen offset:0 lds\n"
    "v_mfma_f32_16x16x32_bf16 v[80:83], a[216:219], v[40:43], v[80:83]\n"
    "ds_read_b64_tr_b16 v[30:31], v11 offset:34816\n"
    "v_mfma_f32_16x16x32_bf16 v[60:63], a[24:27], v[44:47], v[60:63]\n"
    "ds_read_b64_tr_b16 v[32:33], v10 offset:36864\n"
    "v_cvt_pk_bf16_f32 v225, v126, v127\n"
    "v_mfma_f32_16x16x32_bf16 v[68:71], a[88:91], v[44:47], v[68:71]\n"
    "ds_read_b64_tr_b16 v[34:35], v10 offset:38912\n"
    "v_mfma_f32_16x16x32_bf16 v[76:79], a[152:155], v[44:47], v[76:79]\n"
    "ds_read_b64_tr_b16 v[36:37], v11 offset:36864\n"
    "buffer_load_dwordx4 v5, s[44:47], s29 offen offset:1024 lds\n"
    "v_mfma_f32_16x16x32_bf16 v[84:87], a[216:219], v[44:47], v[84:87]\n"
    "ds_read_b64_tr_b16 v[38:39], v11 offset:38912\n"
    "v_cvt_pk_bf16_f32 v226, v132, v133\n"
    "v_mfma_f32_16x16x32_bf16 v[56:59], a[28:31], v[48:51], v[56:59]\n"
    "ds_read_b64_tr_b16 v[40:41], v10 offset:40960\n"
    "v_mfma_f32_16x16x32_bf16 v[64:67], a[92:95], v[48:51], v[64:67]\n"
    "ds_read_b64_tr_b16 v[42:43], v10 offset:43008\n"
    "v_mfma_f32_16x16x32_bf16 v[72:75], a[156:159], v[48:51], v[72:75]\n"
    "v_cvt_pk_bf16_f32 v227, v134, v135\n"
    "v_mfma_f32_16x16x32_bf16 v[80:83], a[220:223], v[48:51], v[80:83]\n"
    "buffer_load_dwordx4 v6, s[44:47], s29 offen offset:2048 lds\n"
    "v_mfma_f32_16x16x32_bf16 v[60:63], a[28:31], v[52:55], v[60:63]\n"
    "ds_read_b64_tr_b16 v[44:45], v11 offset:40960\n"
    "v_mfma_f32_16x16x32_bf16 v[68:71], a[92:95], v[52:55], v[68:71]\n"
    "ds_read_b64_tr_b16 v[46:47], v11 offset:43008\n"
    "s_mov_b32 s99, 4096\n"
    "v_mfma_f32_16x16x32_bf16 v[76:79], a[156:159], v[52:55], v[76:79]\n"
    "v_mfma_f32_16x16x32_bf16 v[84:87], a[220:223], v[52:55], v[84:87]\n"
    "buffer_load_dwordx4 v7, s[44:47], s29 offen offset:3072 lds\n"
    "s_waitcnt lgkmcnt(8)\n"
    "v_mfma_f32_16x16x32_bf16 v[56:59], a[32:35], v[24:27], v[56:59]\n"
    "ds_read_b64_tr_b16 v[48:49], v10 offset:45056\n"
    "v_mfma_f32_16x16x32_bf16 v[64:67], a[96:99], v[24:27], v[64:67]\n"
    "ds_read_b64_tr_b16 v[50:51], v10 offset:47104\n"
    "v_mfma_f32_16x16x32_bf16 v[72:75], a[160:163], v[24:27], v[72:75]\n"
    "v_mfma_f32_16x16x32_bf16 v[80:83], a[224:227], v[24:27], v[80:83]\n"
    "buffer_store_dwordx4 v[224:227], v21, s[80:83], s99 offen offset:1024 sc1\n"
    "v_cvt_pk_bf16_f32 v228, v140, v141\n"
    "v_mfma_f32_16x16x32_bf16 v[60:63], a[32:35], v[28:31], v[60:63]\n"
    "ds_read_b64_tr_b16 v[52:53], v11 offset:45056\n"
    "v_mfma_f32_16x16x32_bf16 v[68:71], a[96:99], v[28:31], v[68:71]\n"
    "ds_read_b64_tr_b16 v[54:55], v11 offset:47104\n"
    "v_mfma_f32_16x16x32_bf16 v[76:79], a[160:163], v[28:31], v[76:79]\n"
    "v_mfma_f32_16x16x32_bf16 v[84:87], a[224:227], v[28:31], v[84:87]\n"
    "v_cvt_pk_bf16_f32 v229, v142, v143\n"
    "s_waitcnt lgkmcnt(8)\n"
    "v_mfma_f32_16x16x32_bf16 v[56:59], a[36:39], v[32:35], v[56:59]\n"
    "v_mfma_f32_16x16x32_bf16 v[64:67], a[100:103], v[32:35], v[64:67]\n"
    "v_mfma_f32_16x16x32_bf16 v[72:75], a[164:167], v[32:35], v[72:75]\n"
    "v_cvt_pk_bf16_f32 v230, v148, v149\n"
    "v_mfma_f32_16x16x32_bf16 v[80:83], a[228:231], v[32:35], v[80:83]\n"
    "v_mfma_f32_16x16x32_bf16 v[60:63], a[36:39], v[36:39], v[60:63]\n"
    "v_mfma_f32_16x16x32_bf16 v[68:71], a[100:103], v[36:39], v[68:71]\n"
    "v_cvt_pk_bf16_f32 v231, v150, v151\n"
    "v_mfma_f32_16x16x32_bf16 v[76:79], a[164:167], v[36:39], v[76:79]\n"
    "v_mfma_f32_16x16x32_bf16 v[84:87], a[228:231], v[36:39], v[84:87]\n"
    "s_waitcnt vmcnt(28) lgkmcnt(0)\n"
    "s_barrier\n"
    "v_mfma_f32_16x16x32_bf16 v[56:59], a[40:43], v[40:43], v[56:59]\n"
    "ds_read_b64_tr_b16 v[24:25], v10 offset:49152\n"
    "s_mov_b32 s29, 1048576\n"
    "s_mov_b32 s99, 12288\n"
    "v_mfma_f32_16x16x32_bf16 v[64:67], a[104:107], v[40:43], v[64:67]\n"
    "ds_read_b64_tr_b16 v[26:27], v10 offset:51200\n"
    "s_add_u32 m0, s28, 32768\n"
    "v_mfma_f32_16x16x32_bf16 v[72:75], a[168:171], v[40:43], v[72:75]\n"
    "ds_read_b64_tr_b16 v[28:29], v11 offset:49152\n"
    "buffer_load_dwordx4 v4, s[44:47], s29 offen offset:0 lds\n"
    "v_mfma_f32_16x16x32_bf16 v[80:83], a[232:235], v[40:43], v[80:83]\n"
    "ds_read_b64_tr_b16 v[30:31], v11 offset:51200\n"
    "v_mfma_f32_16x16x32_bf16 v[60:63], a[40:43], v[44:47], v[60:63]\n"
    "ds_read_b64_tr_b16 v[32:33], v10 offset:53248\n"
    "v_mfma_f32_16x16x32_bf16 v[68:71], a[104:107], v[44:47], v[68:71]\n"
    "ds_read_b64_tr_b16 v[34:35], v10 offset:55296\n"
    "buffer_store_dwordx4 v[228:231], v21, s[80:83], s99 offen offset:1024 sc1\n"
    "v_mfma_f32_16x16x32_bf16 v[76:79], a[168:171], v[44:47], v[76:79]\n"
    "ds_read_b64_tr_b16 v[36:37], v11 offset:53248\n"
    "v_cvt_pk_bf16_f32 v216, v152, v153\n"
    "v_mfma_f32_16x16x32_bf16 v[84:87], a[232:235], v[44:47], v[84:87]\n"
    "ds_read_b64_tr_b16 v[38:39], v11 offset:55296\n"
    "v_mfma_f32_16x16x32_bf16 v[56:59], a[44:47], v[48:51], v[56:59]\n"
    "ds_read_b64_tr_b16 v[40:41], v10 offset:57344\n"
    "v_mfma_f32_16x16x32_bf16 v[64:67], a[108:111], v[48:51], v[64:67]\n"
    "ds_read_b64_tr_b16 v[42:43], v10 offset:59392\n"
    "v_mfma_f32_16x16x32_bf16 v[72:75], a[172:175], v[48:51], v[72:75]\n"
    "buffer_load_dwordx4 v5, s[44:47], s29 offen offset:1024 lds\n"
    "v_cvt_pk_bf16_f32 v217, v154, v155\n"
    "v_mfma_f32_16x16x32_bf16 v[80:83], a[236:239], v[48:51], v[80:83]\n"
    "v_mfma_f32_16x16x32_bf16 v[60:63], a[44:47], v[52:55], v[60:63]\n"
    "ds_read_b64_tr_b16 v[44:45], v11 offset:57344\n"
    "v_mfma_f32_16x16x32_bf16 v[68:71], a[108:111], v[52:55], v[68:71]\n"
    "ds_read_b64_tr_b16 v[46:47], v11 offset:59392\n"
    "v_cvt_pk_bf16_f32 v218, v160, v161\n"
    "v_mfma_f32_16x16x32_bf16 v[76:79], a[172:175], v[52:55], v[76:79]\n"
    "buffer_load_dwordx4 v6, s[44:47], s29 offen offset:2048 lds\n"
    "v_mfma_f32_16x16x32_bf16 v[84:87], a[236:239], v[52:55], v[84:87]\n"
    "s_waitcnt lgkmcnt(8)\n"
    "v_mfma_f32_16x16x32_bf16 v[56:59], a[48:51], v[24:27], v[56:59]\n"
    "ds_read_b64_tr_b16 v[48:49], v10 offset:61440\n"
    "v_cvt_pk_bf16_f32 v219, v162, v163\n"
    "v_mfma_f32_16x16x32_bf16 v[64:67], a[112:115], v[24:27], v[64:67]\n"
    "ds_read_b64_tr_b16 v[50:51], v10 offset:63488\n"
    "v_mfma_f32_16x16x32_bf16 v[72:75], a[176:179], v[24:27], v[72:75]\n"
    "v_mfma_f32_16x16x32_bf16 v[80:83], a[240:243], v[24:27], v[80:83]\n"
    "buffer_load_dwordx4 v7, s[44:47], s29 offen offset:3072 lds\n"
    "s_mov_b32 s99, 4096\n"
    "v_mfma_f32_16x16x32_bf16 v[60:63], a[48:51], v[28:31], v[60:63]\n"
    "ds_read_b64_tr_b16 v[52:53], v11 offset:61440\n"
    "v_mfma_f32_16x16x32_bf16 v[68:71], a[112:115], v[28:31], v[68:71]\n"
    "ds_read_b64_tr_b16 v[54:55], v11 offset:63488\n"
    "v_mfma_f32_16x16x32_bf16 v[76:79], a[176:179], v[28:31], v[76:79]\n"
    "buffer_store_dwordx4 v[216:219], v21, s[80:83], s99 offen offset:2048 sc1\n"
    "v_mfma_f32_16x16x32_bf16 v[84:87], a[240:243], v[28:31], v[84:87]\n"
    "s_waitcnt lgkmcnt(8)\n"
    "v_mfma_f32_16x16x32_bf16 v[56:59], a[52:55], v[32:35], v[56:59]\n"
    "v_mfma_f32_16x16x32_bf16 v[64:67], a[116:119], v[32:35], v[64:67]\n"
    "v_cvt_pk_bf16_f32 v220, v168, v169\n"
    "v_mfma_f32_16x16x32_bf16 v[72:75], a[180:183], v[32:35], v[72:75]\n"
    "v_mfma_f32_16x16x32_bf16 v[80:83], a[244:247], v[32:35], v[80:83]\n"
    "v_mfma_f32_16x16x32_bf16 v[60:63], a[52:55], v[36:39], v[60:63]\n"
    "v_cvt_pk_bf16_f32 v221, v170, v171\n"
    "v_mfma_f32_16x16x32_bf16 v[68:71], a[116:119], v[36:39], v[68:71]\n"
    "v_mfma_f32_16x16x32_bf16 v[76:79], a[180:183], v[36:39], v[76:79]\n"
    "v_mfma_f32_16x16x32_bf16 v[84:87], a[244:247], v[36:39], v[84:87]\n"
    "s_waitcnt vmcnt(34) lgkmcnt(0)\n"
    "s_barrier\n"
    "v_mfma_f32_16x16x32_bf16 v[56:59], a[56:59], v[40:43], v[56:59]\n"
    "ds_read_b64_tr_b16 v[24:25], v12 offset:0\n"
    "s_mov_b32 s29, 1048704\n"
    "v_cvt_pk_bf16_f32 v222, v176, v177\n"
    "v_mfma_f32_16x16x32_bf16 v[64:67], a[120:123], v[40:43], v[64:67]\n"
    "ds_read_b64_tr_b16 v[26:27], v12 offset:2048\n"
    "s_add_u32 m0, s28, 49152\n"
    "v_mfma_f32_16x16x32_bf16 v[72:75], a[184:187], v[40:43], v[72:75]\n"
    "ds_read_b64_tr_b16 v[28:29], v13 offset:0\n"
    "buffer_load_dwordx4 v4, s[44:47], s29 offen offset:0 lds\n"
    "v_mfma_f32_16x16x32_bf16 v[80:83], a[248:251], v[40:43], v[80:83]\n"
    "ds_read_b64_tr_b16 v[30:31], v13 offset:2048\n"
    "v_cvt_pk_bf16_f32 v223, v178, v179\n"
    "v_mfma_f32_16x16x32_bf16 v[60:63], a[56:59], v[44:47], v[60:63]\n"
    "ds_read_b64_tr_b16 v[32:33], v12 offset:4096\n"
    "v_mfma_f32_16x16x32_bf16 v[68:71], a[120:123], v[44:47], v[68:71]\n"
    "ds_read_b64_tr_b16 v[34:35], v12 offset:6144\n"
    "v_mfma_f32_16x16x32_bf16 v[76:79], a[184:187], v[44:47], v[76:79]\n"
    "ds_read_b64_tr_b16 v[36:37], v13 offset:4096\n"
    "buffer_load_dwordx4 v5, s[44:47], s29 offen offset:1024 lds\n"
    "s_mov_b32 s99, 12288\n"
    "v_mfma_f32_16x16x32_bf16 v[84:87], a[248:251], v[44:47], v[84:87]\n"
    "ds_read_b64_tr_b16 v[38:39], v13 offset:6144\n"
    "v_mfma_f32_16x16x32_bf16 v[56:59], a[60:63], v[48:51], v[56:59]\n"
    "ds_read_b64_tr_b16 v[40:41], v12 offset:8192\n"
    "v_mfma_f32_16x16x32_bf16 v[64:67], a[124:127], v[48:51], v[64:67]\n"
    "ds_read_b64_tr_b16 v[42:43], v12 offset:10240\n"
    "v_mfma_f32_16x16x32_bf16 v[72:75], a[188:191], v[48:51], v[72:75]\n"
    "buffer_store_dwordx4 v[220:223], v21, s[80:83], s99 offen offset:2048 sc1\n"
    "v_mfma_f32_16x16x32_bf16 v[80:83], a[252:255], v[48:51], v[80:83]\n"
    "v_mfma_f32_16x16x32_bf16 v[60:63], a[60:63], v[52:55], v[60:63]\n"
    "ds_read_b64_tr_b16 v[44:45], v13 offset:8192\n"
    "v_mfma_f32_16x16x32_bf16 v[68:71], a[124:127], v[52:55], v[68:71]\n"
    "ds_read_b64_tr_b16 v[46:47], v13 offset:10240\n"
    "v_mfma_f32_16x16x32_bf16 v[76:79], a[188:191], v[52:55], v[76:79]\n"
    "v_mfma_f32_16x16x32_bf16 v[84:87], a[252:255], v[52:55], v[84:87]\n"
    "buffer_load_dwordx4 v6, s[44:47], s29 offen offset:2048 lds\n"
    "s_waitcnt lgkmcnt(8)\n"
    "v_mfma_f32_16x16x32_bf16 v[88:91], a[0:3], v[24:27], 0\n"
    "ds_read_b64_tr_b16 v[48:49], v12 offset:12288\n"
    "v_mfma_f32_16x16x32_bf16 v[96:99], a[64:67], v[24:27], 0\n"
    "ds_read_b64_tr_b16 v[50:51], v12 offset:14336\n"
    "v_mfma_f32_16x16x32_bf16 v[104:107], a[128:131], v[24:27], 0\n"
    "v_mfma_f32_16x16x32_bf16 v[112:115], a[192:195], v[24:27], 0\n"
    "buffer_load_dwordx4 v7, s[44:47], s29 offen offset:3072 lds\n"
    "v_mfma_f32_16x16x32_bf16 v[92:95], a[0:3], v[28:31], 0\n"
    "ds_read_b64_tr_b16 v[52:53], v13 offset:12288\n"
    "v_mfma_f32_16x16x32_bf16 v[100:103], a[64:67], v[28:31], 0\n"
    "ds_read_b64_tr_b16 v[54:55], v13 offset:14336\n"
    "v_mfma_f32_16x16x32_bf16 v[108:111], a[128:131], v[28:31], 0\n"
    "v_mfma_f32_16x16x32_bf16 v[116:119], a[192:195], v[28:31], 0\n"
    "s_waitcnt lgkmcnt(8)\n"
    "v_mfma_f32_16x16x32_bf16 v[88:91], a[4:7], v[32:35], v[88:91]\n"
    "v_mfma_f32_16x16x32_bf16 v[96:99], a[68:71], v[32:35], v[96:99]\n"
    "v_mfma_f32_16x16x32_bf16 v[104:107], a[132:135], v[32:35], v[104:107]\n"
    "v_mfma_f32_16x16x32_bf16 v[112:115], a[196:199], v[32:35], v[112:115]\n"
    "v_cvt_pk_bf16_f32 v224, v156, v157\n"
    "v_mfma_f32_16x16x32_bf16 v[92:95], a[4:7], v[36:39], v[92:95]\n"
    "v_mfma_f32_16x16x32_bf16 v[100:103], a[68:71], v[36:39], v[100:103]\n"
    "v_mfma_f32_16x16x32_bf16 v[108:111], a[132:135], v[36:39], v[108:111]\n"
    "v_cvt_pk_bf16_f32 v225, v158, v159\n"
    "v_mfma_f32_16x16x32_bf16 v[116:119], a[196:199], v[36:39], v[116:119]\n"
    "s_waitcnt lgkmcnt(4)\n"
    "v_mfma_f32_16x16x32_bf16 v[88:91], a[8:11], v[40:43], v[88:91]\n"
    "ds_read_b64_tr_b16 v[24:25], v12 offset:16384\n"
    "v_mfma_f32_16x16x32_bf16 v[96:99], a[72:75], v[40:43], v[96:99]\n"
    "ds_read_b64_tr_b16 v[26:27], v12 offset:18432\n"
    "v_cvt_pk_bf16_f32 v226, v164, v165\n"
    "v_mfma_f32_16x16x32_bf16 v[104:107], a[136:139], v[40:43], v[104:107]\n"
    "ds_read_b64_tr_b16 v[28:29], v13 offset:16384\n"
    "v_mfma_f32_16x16x32_bf16 v[112:115], a[200:203], v[40:43], v[112:115]\n"
    "ds_read_b64_tr_b16 v[30:31], v13 offset:18432\n"
    "v_mfma_f32_16x16x32_bf16 v[92:95], a[8:11], v[44:47], v[92:95]\n"
    "ds_read_b64_tr_b16 v[32:33], v12 offset:20480\n"
    "v_cvt_pk_bf16_f32 v227, v166, v167\n"
    "v_mfma_f32_16x16x32_bf16 v[100:103], a[72:75], v[44:47], v[100:103]\n"
    "ds_read_b64_tr_b16 v[34:35], v12 offset:22528\n"
    "v_mfma_f32_16x16x32_bf16 v[108:111], a[136:139], v[44:47], v[108:111]\n"
    "ds_read_b64_tr_b16 v[36:37], v13 offset:20480\n"
    "v_mfma_f32_16x16x32_bf16 v[116:119], a[200:203], v[44:47], v[116:119]\n"
    "ds_read_b64_tr_b16 v[38:39], v13 offset:22528\n"
    "s_mov_b32 s99, 4096\n"
    "s_waitcnt lgkmcnt(8)\n"
    "v_mfma_f32_16x16x32_bf16 v[88:91], a[12:15], v[48:51], v[88:91]\n"
    "ds_read_b64_tr_b16 v[40:41], v12 offset:24576\n"
    "v_mfma_f32_16x16x32_bf16 v[96:99], a[76:79], v[48:51], v[96:99]\n"
    "ds_read_b64_tr_b16 v[42:43], v12 offset:26624\n"
    "v_mfma_f32_16x16x32_bf16 v[104:107], a[140:143], v[48:51], v[104:107]\n"
    "buffer_store_dwordx4 v[224:227], v21, s[80:83], s99 offen offset:3072 sc1\n"
    "v_mfma_f32_16x16x32_bf16 v[112:115], a[204:207], v[48:51], v[112:115]\n"
    "v_mfma_f32_16x16x32_bf16 v[92:95], a[12:15], v[52:55], v[92:95]\n"
    "ds_read_b64_tr_b16 v[44:45], v13 offset:24576\n"
    "v_mfma_f32_16x16x32_bf16 v[100:103], a[76:79], v[52:55], v[100:103]\n"
    "ds_read_b64_tr_b16 v[46:47], v13 offset:26624\n"
    "v_mfma_f32_16x16x32_bf16 v[108:111], a[140:143], v[52:55], v[108:111]\n"
    "v_cvt_pk_bf16_f32 v228, v172, v173\n"
    "v_mfma_f32_16x16x32_bf16 v[116:119], a[204:207], v[52:55], v[116:119]\n"
    "s_waitcnt lgkmcnt(8)\n"
    "v_mfma_f32_16x16x32_bf16 v[88:91], a[16:19], v[24:27], v[88:91]\n"
    "ds_read_b64_tr_b16 v[48:49], v12 offset:28672\n"
    "v_mfma_f32_16x16x32_bf16 v[96:99], a[80:83], v[24:27], v[96:99]\n"
    "ds_read_b64_tr_b16 v[50:51], v12 offset:30720\n"
    "v_cvt_pk_bf16_f32 v229, v174, v175\n"
    "v_mfma_f32_16x16x32_bf16 v[104:107], a[144:147], v[24:27], v[104:107]\n"
    "v_mfma_f32_16x16x32_bf16 v[112:115], a[208:211], v[24:27], v[112:115]\n"
    "v_mfma_f32_16x16x32_bf16 v[92:95], a[16:19], v[28:31], v[92:95]\n"
    "ds_read_b64_tr_b16 v[52:53], v13 offset:28672\n"
    "v_cvt_pk_bf16_f32 v230, v180, v181\n"
    "v_mfma_f32_16x16x32_bf16 v[100:103], a[80:83], v[28:31], v[100:103]\n"
    "ds_read_b64_tr_b16 v[54:55], v13 offset:30720\n"
    "v_mfma_f32_16x16x32_bf16 v[108:111], a[144:147], v[28:31], v[108:111]\n"
    "v_mfma_f32_16x16x32_bf16 v[116:119], a[208:211], v[28:31], v[116:119]\n"
    "v_cvt_pk_bf16_f32 v231, v182, v183\n"
    "s_waitcnt lgkmcnt(8)\n"
    "v_mfma_f32_16x16x32_bf16 v[88:91], a[20:23], v[32:35], v[88:91]\n"
    "v_mfma_f32_16x16x32_bf16 v[96:99], a[84:87], v[32:35], v[96:99]\n"
    "v_mfma_f32_16x16x32_bf16 v[104:107], a[148:151], v[32:35], v[104:107]\n"
    "s_mov_b32 s99, 12288\n"
    "v_mfma_f32_16x16x32_bf16 v[112:115], a[212:215], v[32:35], v[112:115]\n"
    "v_mfma_f32_16x16x32_bf16 v[92:95], a[20:23], v[36:39], v[92:95]\n"
    "v_mfma_f32_16x16x32_bf16 v[100:103], a[84:87], v[36:39], v[100:103]\n"
    "buffer_store_dwordx4 v[228:231], v21, s[80:83], s99 offen offset:3072 sc1\n"
    "v_mfma_f32_16x16x32_bf16 v[108:111], a[148:151], v[36:39], v[108:111]\n"
    "v_mfma_f32_16x16x32_bf16 v[116:119], a[212:215], v[36:39], v[116:119]\n"
    "s_waitcnt vmcnt(14) lgkmcnt(0)\n"
    "s_barrier\n"
    "v_mfma_f32_16x16x32_bf16 v[88:91], a[24:27], v[40:43], v[88:91]\n"
    "ds_read_b64_tr_b16 v[24:25], v12 offset:32768\n"
    "v_mfma_f32_16x16x32_bf16 v[96:99], a[88:91], v[40:43], v[96:99]\n"
    "ds_read_b64_tr_b16 v[26:27], v12 offset:34816\n"
    "v_cvt_pk_bf16_f32 v216, v184, v185\n"
    "v_mfma_f32_16x16x32_bf16 v[104:107], a[152:155], v[40:43], v[104:107]\n"
    "ds_read_b64_tr_b16 v[28:29], v13 offset:32768\n"
    "v_mfma_f32_16x16x32_bf16 v[112:115], a[216:219], v[40:43], v[112:115]\n"
    "ds_read_b64_tr_b16 v[30:31], v13 offset:34816\n"
    "v_mfma_f32_16x16x32_bf16 v[92:95], a[24:27], v[44:47], v[92:95]\n"
    "ds_read_b64_tr_b16 v[32:33], v12 offset:36864\n"
    "v_cvt_pk_bf16_f32 v217, v186, v187\n"
    "v_mfma_f32_16x16x32_bf16 v[100:103], a[88:91], v[44:47], v[100:103]\n"
    "ds_read_b64_tr_b16 v[34:35], v12 offset:38912\n"
    "v_mfma_f32_16x16x32_bf16 v[108:111], a[152:155], v[44:47], v[108:111]\n"
    "ds_read_b64_tr_b16 v[36:37], v13 offset:36864\n"
    "v_mfma_f32_16x16x32_bf16 v[116:119], a[216:219], v[44:47], v[116:119]\n"
    "ds_read_b64_tr_b16 v[38:39], v13 offset:38912\n"
    "v_cvt_pk_bf16_f32 v218, v192, v193\n"
    "v_mfma_f32_16x16x32_bf16 v[88:91], a[28:31], v[48:51], v[88:91]\n"
    "ds_read_b64_tr_b16 v[40:41], v12 offset:40960\n"
    "v_mfma_f32_16x16x32_bf16 v[96:99], a[92:95], v[48:51], v[96:99]\n"
    "ds_read_b64_tr_b16 v[42:43], v12 offset:43008\n"
    "v_mfma_f32_16x16x32_bf16 v[104:107], a[156:159], v[48:51], v[104:107]\n"
    "v_cvt_pk_bf16_f32 v219, v194, v195\n"
    "v_mfma_f32_16x16x32_bf16 v[112:115], a[220:223], v[48:51], v[112:115]\n"
    "v_mfma_f32_16x16x32_bf16 v[92:95], a[28:31], v[52:55], v[92:95]\n"
    "ds_read_b64_tr_b16 v[44:45], v13 offset:40960\n"
    "v_mfma_f32_16x16x32_bf16 v[100:103], a[92:95], v[52:55], v[100:103]\n"
    "ds_read_b64_tr_b16 v[46:47], v13 offset:43008\n"
    "s_mov_b32 s99, 524288\n"
    "v_mfma_f32_16x16x32_bf16 v[108:111], a[156:159], v[52:55], v[108:111]\n"
    "v_mfma_f32_16x16x32_bf16 v[116:119], a[220:223], v[52:55], v[116:119]\n"
    "s_waitcnt lgkmcnt(8)\n"
    "v_mfma_f32_16x16x32_bf16 v[88:91], a[32:35], v[24:27], v[88:91]\n"
    "ds_read_b64_tr_b16 v[48:49], v12 offset:45056\n"
    "buffer_store_dwordx4 v[216:219], v21, s[80:83], s99 offen offset:0 sc1\n"
    "v_mfma_f32_16x16x32_bf16 v[96:99], a[96:99], v[24:27], v[96:99]\n"
    "ds_read_b64_tr_b16 v[50:51], v12 offset:47104\n"
    "v_mfma_f32_16x16x32_bf16 v[104:107], a[160:163], v[24:27], v[104:107]\n"
    "v_mfma_f32_16x16x32_bf16 v[112:115], a[224:227], v[24:27], v[112:115]\n"
    "v_cvt_pk_bf16_f32 v220, v200, v201\n"
    "v_mfma_f32_16x16x32_bf16 v[92:95], a[32:35], v[28:31], v[92:95]\n"
    "ds_read_b64_tr_b16 v[52:53], v13 offset:45056\n"
    "v_mfma_f32_16x16x32_bf16 v[100:103], a[96:99], v[28:31], v[100:103]\n"
    "ds_read_b64_tr_b16 v[54:55], v13 offset:47104\n"
    "v_mfma_f32_16x16x32_bf16 v[108:111], a[160:163], v[28:31], v[108:111]\n"
    "v_mfma_f32_16x16x32_bf16 v[116:119], a[224:227], v[28:31], v[116:119]\n"
    "v_cvt_pk_bf16_f32 v221, v202, v203\n"
    "s_waitcnt lgkmcnt(8)\n"
    "v_mfma_f32_16x16x32_bf16 v[88:91], a[36:39], v[32:35], v[88:91]\n"
    "v_mfma_f32_16x16x32_bf16 v[96:99], a[100:103], v[32:35], v[96:99]\n"
    "v_mfma_f32_16x16x32_bf16 v[104:107], a[164:167], v[32:35], v[104:107]\n"
    "v_cvt_pk_bf16_f32 v222, v208, v209\n"
    "v_mfma_f32_16x16x32_bf16 v[112:115], a[228:231], v[32:35], v[112:115]\n"
    "v_mfma_f32_16x16x32_bf16 v[92:95], a[36:39], v[36:39], v[92:95]\n"
    "v_mfma_f32_16x16x32_bf16 v[100:103], a[100:103], v[36:39], v[100:103]\n"
    "v_cvt_pk_bf16_f32 v223, v210, v211\n"
    "v_mfma_f32_16x16x32_bf16 v[108:111], a[164:167], v[36:39], v[108:111]\n"
    "v_mfma_f32_16x16x32_bf16 v[116:119], a[228:231], v[36:39], v[116:119]\n"
    "s_waitcnt lgkmcnt(4)\n"
    "v_mfma_f32_16x16x32_bf16 v[88:91], a[40:43], v[40:43], v[88:91]\n"
    "ds_read_b64_tr_b16 v[24:25], v12 offset:49152\n"
    "s_waitcnt vmcnt(15)\n"
    "v_cmp_ne_u32 vcc, s94, v233\n"
    "s_mov_b32 s30, 0x400000\n"
    "s_nop 1\n"
    "s_cbranch_vccz L_phv_%=_x\n"
    "L_phv_%=:\n"
    "s_sleep 2\n"
    "global_load_dword v233, v232, s[60:61] sc1\n"
    "s_waitcnt vmcnt(0)\n"
    "v_cmp_ne_u32 vcc, s94, v233\n"
    "s_sub_u32 s30, s30, 1\n"
    "s_cbranch_scc1 L_phv_%=_x\n"
    "s_nop 0\n"
    "s_cbranch_vccnz L_phv_%=\n"
    "L_phv_%=_x:\n"
    "s_mov_b32 s99, 532480\n"
    "v_mfma_f32_16x16x32_bf16 v[96:99], a[104:107], v[40:43], v[96:99]\n"
    "ds_read_b64_tr_b16 v[26:27], v12 offset:51200\n"
    "v_mfma_f32_16x16x32_bf16 v[104:107], a[168:171], v[40:43], v[104:107]\n"
    "ds_read_b64_tr_b16 v[28:29], v13 offset:49152\n"
    "v_mfma_f32_16x16x32_bf16 v[112:115], a[232:235], v[40:43], v[112:115]\n"
    "ds_read_b64_tr_b16 v[30:31], v13 offset:51200\n"
    "buffer_store_dwordx4 v[220:223], v21, s[80:83], s99 offen offset:0 sc1\n"
    "v_mfma_f32_16x16x32_bf16 v[92:95], a[40:43], v[44:47], v[92:95]\n"
    "ds_read_b64_tr_b16 v[32:33], v12 offset:53248\n"
    "v_mfma_f32_16x16x32_bf16 v[100:103], a[104:107], v[44:47], v[100:103]\n"
    "ds_read_b64_tr_b16 v[34:35], v12 offset:55296\n"
    "v_mfma_f32_16x16x32_bf16 v[108:111], a[168:171], v[44:47], v[108:111]\n"
    "ds_read_b64_tr_b16 v[36:37], v13 offset:53248\n"
    "v_cvt_pk_bf16_f32 v224, v188, v189\n"
    "v_mfma_f32_16x16x32_bf16 v[116:119], a[232:235], v[44:47], v[116:119]\n"
    "ds_read_b64_tr_b16 v[38:39], v13 offset:55296\n"
    "s_waitcnt lgkmcnt(8)\n"
    "v_mfma_f32_16x16x32_bf16 v[88:91], a[44:47], v[48:51], v[88:91]\n"
    "ds_read_b64_tr_b16 v[40:41], v12 offset:57344\n"
    "v_mfma_f32_16x16x32_bf16 v[96:99], a[108:111], v[48:51], v[96:99]\n"
    "ds_read_b64_tr_b16 v[42:43], v12 offset:59392\n"
    "v_mfma_f32_16x16x32_bf16 v[104:107], a[172:175], v[48:51], v[104:107]\n"
    "v_cvt_pk_bf16_f32 v225, v190, v191\n"
    "v_mfma_f32_16x16x32_bf16 v[112:115], a[236:239], v[48:51], v[112:115]\n"
    "v_mfma_f32_16x16x32_bf16 v[92:95], a[44:47], v[52:55], v[92:95]\n"
    "ds_read_b64_tr_b16 v[44:45], v13 offset:57344\n"
    "v_mfma_f32_16x16x32_bf16 v[100:103], a[108:111], v[52:55], v[100:103]\n"
    "ds_read_b64_tr_b16 v[46:47], v13 offset:59392\n"
    "v_cvt_pk_bf16_f32 v226, v196, v197\n"
    "v_mfma_f32_16x16x32_bf16 v[108:111], a[172:175], v[52:55], v[108:111]\n"
    "v_mfma_f32_16x16x32_bf16 v[116:119], a[236:239], v[52:55], v[116:119]\n"
    "s_waitcnt lgkmcnt(8)\n"
    "v_mfma_f32_16x16x32_bf16 v[88:91], a[48:51], v[24:27], v[88:91]\n"
    "ds_read_b64_tr_b16 v[48:49], v12 offset:61440\n"
    "v_cvt_pk_bf16_f32 v227, v198, v199\n"
    "v_mfma_f32_16x16x32_bf16 v[96:99], a[112:115], v[24:27], v[96:99]\n"
    "ds_read_b64_tr_b16 v[50:51], v12 offset:63488\n"
    "v_mfma_f32_16x16x32_bf16 v[104:107], a[176:179], v[24:27], v[104:107]\n"
    "v_mfma_f32_16x16x32_bf16 v[112:115], a[240:243], v[24:27], v[112:115]\n"
    "s_mov_b32 s99, 524288\n"
    "v_mfma_f32_16x16x32_bf16 v[92:95], a[48:51], v[28:31], v[92:95]\n"
    "ds_read_b64_tr_b16 v[52:53], v13 offset:61440\n"
    "v_mfma_f32_16x16x32_bf16 v[100:103], a[112:115], v[28:31], v[100:103]\n"
    "ds_read_b64_tr_b16 v[54:55], v13 offset:63488\n"
    "v_mfma_f32_16x16x32_bf16 v[108:111], a[176:179], v[28:31], v[108:111]\n"
    "buffer_store_dwordx4 v[224:227], v21, s[80:83], s99 offen offset:1024 sc1\n"
    "v_mfma_f32_16x16x32_bf16 v[116:119], a[240:243], v[28:31], v[116:119]\n"
    "s_waitcnt lgkmcnt(8)\n"
    "v_mfma_f32_16x16x32_bf16 v[88:91], a[52:55], v[32:35], v[88:91]\n"
    "v_mfma_f32_16x16x32_bf16 v[96:99], a[116:119], v[32:35], v[96:99]\n"
    "v_cvt_pk_bf16_f32 v228, v204, v205\n"
    "v_mfma_f32_16x16x32_bf16 v[104:107], a[180:183], v[32:35], v[104:107]\n"
    "v_mfma_f32_16x16x32_bf16 v[112:115], a[244:247], v[32:35], v[112:115]\n"
    "v_mfma_f32_16x16x32_bf16 v[92:95], a[52:55], v[36:39], v[92:95]\n"
    "v_cvt_pk_bf16_f32 v229, v206, v207\n"
    "v_mfma_f32_16x16x32_bf16 v[100:103], a[116:119], v[36:39], v[100:103]\n"
    "v_mfma_f32_16x16x32_bf16 v[108:111], a[180:183], v[36:39], v[108:111]\n"
    "v_mfma_f32_16x16x32_bf16 v[116:119], a[244:247], v[36:39], v[116:119]\n"
    "s_waitcnt lgkmcnt(4)\n"
    "v_mfma_f32_16x16x32_bf16 v[88:91], a[56:59], v[40:43], v[88:91]\n"
    "ds_read_b64_tr_b16 v[24:25], v14 offset:0\n"
    "v_cvt_pk_bf16_f32 v230, v212, v213\n"
    "v_mfma_f32_16x16x32_bf16 v[96:99], a[120:123], v[40:43], v[96:99]\n"
    "ds_read_b64_tr_b16 v[26:27], v14 offset:2048\n"
    "v_mfma_f32_16x16x32_bf16 v[104:107], a[184:187], v[40:43], v[104:107]\n"
    "ds_read_b64_tr_b16 v[28:29], v15 offset:0\n"
    "v_mfma_f32_16x16x32_bf16 v[112:115], a[248:251], v[40:43], v[112:115]\n"
    "ds_read_b64_tr_b16 v[30:31], v15 offset:2048\n"
    "v_cvt_pk_bf16_f32 v231, v214, v215\n"
    "v_mfma_f32_16x16x32_bf16 v[92:95], a[56:59], v[44:47], v[92:95]\n"
    "ds_read_b64_tr_b16 v[32:33], v14 offset:4096\n"
    "v_mfma_f32_16x16x32_bf16 v[100:103], a[120:123], v[44:47], v[100:103]\n"
    "ds_read_b64_tr_b16 v[34:35], v14 offset:6144\n"
    "v_mfma_f32_16x16x32_bf16 v[108:111], a[184:187], v[44:47], v[108:111]\n"
    "ds_read_b64_tr_b16 v[36:37], v15 offset:4096\n"
    "s_mov_b32 s99, 532480\n"
    "v_mfma_f32_16x16x32_bf16 v[116:119], a[248:251], v[44:47], v[116:119]\n"
    "ds_read_b64_tr_b16 v[38:39], v15 offset:6144\n"
    "s_waitcnt lgkmcnt(8)\n"
    "v_mfma_f32_16x16x32_bf16 v[88:91], a[60:63], v[48:51], v[88:91]\n"
    "ds_read_b64_tr_b16 v[40:41], v14 offset:8192\n"
    "v_mfma_f32_16x16x32_bf16 v[96:99], a[124:127], v[48:51], v[96:99]\n"
    "ds_read_b64_tr_b16 v[42:43], v14 offset:10240\n"
    "buffer_store_dwordx4 v[228:231], v21, s[80:83], s99 offen offset:1024 sc1\n"
    "v_mfma_f32_16x16x32_bf16 v[104:107], a[188:191], v[48:51], v[104:107]\n"
    "v_mfma_f32_16x16x32_bf16 v[112:115], a[252:255], v[48:51], v[112:115]\n"
    "v_mfma_f32_16x16x32_bf16 v[92:95], a[60:63], v[52:55], v[92:95]\n"
    "ds_read_b64_tr_b16 v[44:45], v15 offset:8192\n"
    "v_mfma_f32_16x16x32_bf16 v[100:103], a[124:127], v[52:55], v[100:103]\n"
    "ds_read_b64_tr_b16 v[46:47], v15 offset:10240\n"
    "v_mfma_f32_16x16x32_bf16 v[108:111], a[188:191], v[52:55], v[108:111]\n"
    "v_mfma_f32_16x16x32_bf16 v[116:119], a[252:255], v[52:55], v[116:119]\n"
    "s_waitcnt lgkmcnt(8)\n"
    "v_mfma_f32_16x16x32_bf16 v[184:187], a[0:3], v[24:27], 0\n"
    "ds_read_b64_tr_b16 v[48:49], v14 offset:12288\n"
    "v_mfma_f32_16x16x32_bf16 v[192:195], a[64:67], v[24:27], 0\n"
    "ds_read_b64_tr_b16 v[50:51], v14 offset:14336\n"
    "v_mfma_f32_16x16x32_bf16 v[200:203], a[128:131], v[24:27], 0\n"
    "v_mfma_f32_16x16x32_bf16 v[208:211], a[192:195], v[24:27], 0\n"
    "v_mfma_f32_16x16x32_bf16 v[188:191], a[0:3], v[28:31], 0\n"
    "ds_read_b64_tr_b16 v[52:53], v15 offset:12288\n"
    "v_mfma_f32_16x16x32_bf16 v[196:199], a[64:67], v[28:31], 0\n"
    "ds_read_b64_tr_b16 v[54:55], v15 offset:14336\n"
    "v_mfma_f32_16x16x32_bf16 v[204:207], a[128:131], v[28:31], 0\n"
    "v_mfma_f32_16x16x32_bf16 v[212:215], a[192:195], v[28:31], 0\n"
    "s_waitcnt lgkmcnt(8)\n"
    "v_mfma_f32_16x16x32_bf16 v[184:187], a[4:7], v[32:35], v[184:187]\n"
    "s_mov_b32 s88, 0\n"
    "v_mfma_f32_16x16x32_bf16 v[192:195], a[68:71], v[32:35], v[192:195]\n"
    "buffer_load_dwordx4 a[0:3], v234, s[40:43], s88 offen offset:0 sc1\n"
    "v_mfma_f32_16x16x32_bf16 v[200:203], a[132:135], v[32:35], v[200:203]\n"
    "v_cvt_pk_bf16_f32 v216, v56, v57\n"
    "v_mfma_f32_16x16x32_bf16 v[208:211], a[196:199], v[32:35], v[208:211]\n"
    "v_mfma_f32_16x16x32_bf16 v[188:191], a[4:7], v[36:39], v[188:191]\n"
    "v_cvt_pk_bf16_f32 v217, v58, v59\n"
    "v_mfma_f32_16x16x32_bf16 v[196:199], a[68:71], v[36:39], v[196:199]\n"
    "buffer_load_dwordx4 a[64:67], v234, s[40:43], s88 offen offset:1024 sc1\n"
    "v_mfma_f32_16x16x32_bf16 v[204:207], a[132:135], v[36:39], v[204:207]\n"
    "v_mfma_f32_16x16x32_bf16 v[212:215], a[196:199], v[36:39], v[212:215]\n"
    "v_cvt_pk_bf16_f32 v218, v64, v65\n"
    "s_waitcnt vmcnt(20) lgkmcnt(0)\n"
    "s_barrier\n"
    "v_mfma_f32_16x16x32_bf16 v[184:187], a[8:11], v[40:43], v[184:187]\n"
    "ds_read_b64_tr_b16 v[24:25], v14 offset:16384\n"
    "v_mfma_f32_16x16x32_bf16 v[192:195], a[72:75], v[40:43], v[192:195]\n"
    "ds_read_b64_tr_b16 v[26:27], v14 offset:18432\n"
    "v_cvt_pk_bf16_f32 v219, v66, v67\n"
    "v_mfma_f32_16x16x32_bf16 v[200:203], a[136:139], v[40:43], v[200:203]\n"
    "ds_read_b64_tr_b16 v[28:29], v15 offset:16384\n"
    "buffer_load_dwordx4 a[128:131], v235, s[40:43], s88 offen offset:0 sc1\n"
    "v_mfma_f32_16x16x32_bf16 v[208:211], a[200:203], v[40:43], v[208:211]\n"
    "ds_read_b64_tr_b16 v[30:31], v15 offset:18432\n"
    "s_mov_b32 s99, 524288\n"
    "v_mfma_f32_16x16x32_bf16 v[188:191], a[8:11], v[44:47], v[188:191]\n"
    "ds_read_b64_tr_b16 v[32:33], v14 offset:20480\n"
    "v_mfma_f32_16x16x32_bf16 v[196:199], a[72:75], v[44:47], v[196:199]\n"
    "ds_read_b64_tr_b16 v[34:35], v14 offset:22528\n"
    "v_mfma_f32_16x16x32_bf16 v[204:207], a[136:139], v[44:47], v[204:207]\n"
    "ds_read_b64_tr_b16 v[36:37], v15 offset:20480\n"
    "buffer_load_dwordx4 a[192:195], v235, s[40:43], s88 offen offset:1024 sc1\n"
    "v_mfma_f32_16x16x32_bf16 v[212:215], a[200:203], v[44:47], v[212:215]\n"
    "ds_read_b64_tr_b16 v[38:39], v15 offset:22528\n"
    "s_mov_b32 s88, 8192\n"
    "v_mfma_f32_16x16x32_bf16 v[184:187], a[12:15], v[48:51], v[184:187]\n"
    "ds_read_b64_tr_b16 v[40:41], v14 offset:24576\n"
    "v_mfma_f32_16x16x32_bf16 v[192:195], a[76:79], v[48:51], v[192:195]\n"
    "ds_read_b64_tr_b16 v[42:43], v14 offset:26624\n"
    "v_mfma_f32_16x16x32_bf16 v[200:203], a[140:143], v[48:51], v[200:203]\n"
    "buffer_store_dwordx4 v[216:219], v21, s[80:83], s99 offen offset:2048 sc1\n"
    "v_cvt_pk_bf16_f32 v220, v72, v73\n"
    "v_cvt_pk_bf16_f32 v221, v74, v75\n"
    "v_mfma_f32_16x16x32_bf16 v[208:211], a[204:207], v[48:51], v[208:211]\n"
    "v_mfma_f32_16x16x32_bf16 v[188:191], a[12:15], v[52:55], v[188:191]\n"
    "ds_read_b64_tr_b16 v[44:45], v15 offset:24576\n"
    "v_mfma_f32_16x16x32_bf16 v[196:199], a[76:79], v[52:55], v[196:199]\n"
    "ds_read_b64_tr_b16 v[46:47], v15 offset:26624\n"
    "v_cvt_pk_bf16_f32 v222, v80, v81\n"
    "v_mfma_f32_16x16x32_bf16 v[204:207], a[140:143], v[52:55], v[204:207]\n"
    "v_mfma_f32_16x16x32_bf16 v[212:215], a[204:207], v[52:55], v[212:215]\n"
    "buffer_load_dwordx4 a[4:7], v234, s[40:43], s88 offen offset:0 sc1\n"
    "v_cvt_pk_bf16_f32 v223, v82, v83\n"
    "s_waitcnt lgkmcnt(8)\n"
    "v_mfma_f32_16x16x32_bf16 v[184:187], a[16:19], v[24:27], v[184:187]\n"
    "ds_read_b64_tr_b16 v[48:49], v14 offset:28672\n"
    "v_mfma_f32_16x16x32_bf16 v[192:195], a[80:83], v[24:27], v[192:195]\n"
    "ds_read_b64_tr_b16 v[50:51], v14 offset:30720\n"
    "v_mfma_f32_16x16x32_bf16 v[200:203], a[144:147], v[24:27], v[200:203]\n"
    "s_mov_b32 s99, 532480\n"
    "v_mfma_f32_16x16x32_bf16 v[208:211], a[208:211], v[24:27], v[208:211]\n"
    "buffer_load_dwordx4 a[68:71], v234, s[40:43], s88 offen offset:1024 sc1\n"
    "v_mfma_f32_16x16x32_bf16 v[188:191], a[16:19], v[28:31], v[188:191]\n"
    "ds_read_b64_tr_b16 v[52:53], v15 offset:28672\n"
    "v_mfma_f32_16x16x32_bf16 v[196:199], a[80:83], v[28:31], v[196:199]\n"
    "ds_read_b64_tr_b16 v[54:55], v15 offset:30720\n"
    "v_mfma_f32_16x16x32_bf16 v[204:207], a[144:147], v[28:31], v[204:207]\n"
    "v_mfma_f32_16x16x32_bf16 v[212:215], a[208:211], v[28:31], v[212:215]\n"
    "buffer_store_dwordx4 v[220:223], v21, s[80:83], s99 offen offset:2048 sc1\n"
    "v_cvt_pk_bf16_f32 v224, v60, v61\n"
    "s_waitcnt lgkmcnt(8)\n"
    "v_mfma_f32_16x16x32_bf16 v[184:187], a[20:23], v[32:35], v[184:187]\n"
    "v_mfma_f32_16x16x32_bf16 v[192:195], a[84:87], v[32:35], v[192:195]\n"
    "v_cvt_pk_bf16_f32 v225, v62, v63\n"
    "v_mfma_f32_16x16x32_bf16 v[200:203], a[148:151], v[32:35], v[200:203]\n"
    "v_mfma_f32_16x16x32_bf16 v[208:211], a[212:215], v[32:35], v[208:211]\n"
    "v_cvt_pk_bf16_f32 v226, v68, v69\n"
    "v_mfma_f32_16x16x32_bf16 v[188:191], a[20:23], v[36:39], v[188:191]\n"
    "buffer_load_dwordx4 a[132:135], v235, s[40:43], s88 offen offset:0 sc1\n"
    "v_mfma_f32_16x16x32_bf16 v[196:199], a[84:87], v[36:39], v[196:199]\n"
    "v_cvt_pk_bf16_f32 v227, v70, v71\n"
    "v_mfma_f32_16x16x32_bf16 v[204:207], a[148:151], v[36:39], v[204:207]\n"
    "v_mfma_f32_16x16x32_bf16 v[212:215], a[212:215], v[36:39], v[212:215]\n"
    "s_waitcnt vmcnt(27) lgkmcnt(0)\n"
    "s_barrier\n"
    "v_mfma_f32_16x16x32_bf16 v[184:187], a[24:27], v[40:43], v[184:187]\n"
    "ds_read_b64_tr_b16 v[24:25], v14 offset:32768\n"
    "buffer_load_dwordx4 a[196:199], v235, s[40:43], s88 offen offset:1024 sc1\n"
    "s_mov_b32 s99, 524288\n"
    "v_mfma_f32_16x16x32_bf16 v[192:195], a[88:91], v[40:43], v[192:195]\n"
    "ds_read_b64_tr_b16 v[26:27], v14 offset:34816\n"
    "s_mov_b32 s29, 2097152\n"
    "v_mfma_f32_16x16x32_bf16 v[200:203], a[152:155], v[40:43], v[200:203]\n"
    "ds_read_b64_tr_b16 v[28:29], v15 offset:32768\n"
    "s_add_u32 m0, s28, 65536\n"
    "v_mfma_f32_16x16x32_bf16 v[208:211], a[216:219], v[40:43], v[208:211]\n"
    "ds_read_b64_tr_b16 v[30:31], v15 offset:34816\n"
    "v_mfma_f32_16x16x32_bf16 v[188:191], a[24:27], v[44:47], v[188:191]\n"
    "ds_read_b64_tr_b16 v[32:33], v14 offset:36864\n"
    "buffer_store_dwordx4 v[224:227], v21, s[80:83], s99 offen offset:3072 sc1\n"
    "v_cvt_pk_bf16_f32 v228, v76, v77\n"
    "v_mfma_f32_16x16x32_bf16 v[196:199], a[88:91], v[44:47], v[196:199]\n"
    "ds_read_b64_tr_b16 v[34:35], v14 offset:38912\n"
    "v_mfma_f32_16x16x32_bf16 v[204:207], a[152:155], v[44:47], v[204:207]\n"
    "ds_read_b64_tr_b16 v[36:37], v15 offset:36864\n"
    "v_mfma_f32_16x16x32_bf16 v[212:215], a[216:219], v[44:47], v[212:215]\n"
    "ds_read_b64_tr_b16 v[38:39], v15 offset:38912\n"
    "v_cvt_pk_bf16_f32 v229, v78, v79\n"
    "v_mfma_f32_16x16x32_bf16 v[184:187], a[28:31], v[48:51], v[184:187]\n"
    "ds_read_b64_tr_b16 v[40:41], v14 offset:40960\n"
    "v_mfma_f32_16x16x32_bf16 v[192:195], a[92:95], v[48:51], v[192:195]\n"
    "ds_read_b64_tr_b16 v[42:43], v14 offset:43008\n"
    "buffer_load_dwordx4 v4, s[44:47], s29 offen offset:0 lds\n"
    "v_cvt_pk_bf16_f32 v230, v84, v85\n"
    "v_mfma_f32_16x16x32_bf16 v[200:203], a[156:159], v[48:51], v[200:203]\n"
    "v_mfma_f32_16x16x32_bf16 v[208:211], a[220:223], v[48:51], v[208:211]\n"
    "v_mfma_f32_16x16x32_bf16 v[188:191], a[28:31], v[52:55], v[188:191]\n"
    "ds_read_b64_tr_b16 v[44:45], v15 offset:40960\n"
    "v_cvt_pk_bf16_f32 v231, v86, v87\n"
    "v_mfma_f32_16x16x32_bf16 v[196:199], a[92:95], v[52:55], v[196:199]\n"
    "ds_read_b64_tr_b16 v[46:47], v15 offset:43008\n"
    "buffer_load_dwordx4 v5, s[44:47], s29 offen offset:1024 lds\n"
    "v_mfma_f32_16x16x32_bf16 v[204:207], a[156:159], v[52:55], v[204:207]\n"
    "s_mov_b32 s99, 532480\n"
    "v_mfma_f32_16x16x32_bf16 v[212:215], a[220:223], v[52:55], v[212:215]\n"
    "s_waitcnt lgkmcnt(8)\n"
    "v_mfma_f32_16x16x32_bf16 v[184:187], a[32:35], v[24:27], v[184:187]\n"
    "ds_read_b64_tr_b16 v[48:49], v14 offset:45056\n"
    "v_mfma_f32_16x16x32_bf16 v[192:195], a[96:99], v[24:27], v[192:195]\n"
    "ds_read_b64_tr_b16 v[50:51], v14 offset:47104\n"
    "buffer_store_dwordx4 v[228:231], v21, s[80:83], s99 offen offset:3072 sc1\n"
    "v_mfma_f32_16x16x32_bf16 v[200:203], a[160:163], v[24:27], v[200:203]\n"
    "v_mfma_f32_16x16x32_bf16 v[208:211], a[224:227], v[24:27], v[208:211]\n"
    "v_cvt_pk_bf16_f32 v216, v88, v89\n"
    "v_mfma_f32_16x16x32_bf16 v[188:191], a[32:35], v[28:31], v[188:191]\n"
    "ds_read_b64_tr_b16 v[52:53], v15 offset:45056\n"
    "v_mfma_f32_16x16x32_bf16 v[196:199], a[96:99], v[28:31], v[196:199]\n"
    "ds_read_b64_tr_b16 v[54:55], v15 offset:47104\n"
    "v_cvt_pk_bf16_f32 v217, v90, v91\n"
    "v_mfma_f32_16x16x32_bf16 v[204:207], a[160:163], v[28:31], v[204:207]\n"
    "buffer_load_dwordx4 v6, s[44:47], s29 offen offset:2048 lds\n"
    "v_mfma_f32_16x16x32_bf16 v[212:215], a[224:227], v[28:31], v[212:215]\n"
    "v_cvt_pk_bf16_f32 v218, v96, v97\n"
    "s_waitcnt lgkmcnt(8)\n"
    "v_mfma_f32_16x16x32_bf16 v[184:187], a[36:39], v[32:35], v[184:187]\n"
    "v_mfma_f32_16x16x32_bf16 v[192:195], a[100:103], v[32:35], v[192:195]\n"
    "v_mfma_f32_16x16x32_bf16 v[200:203], a[164:167], v[32:35], v[200:203]\n"
    "buffer_load_dwordx4 v7, s[44:47], s29 offen offset:3072 lds\n"
    "v_cvt_pk_bf16_f32 v219, v98, v99\n"
    "v_mfma_f32_16x16x32_bf16 v[208:211], a[228:231], v[32:35], v[208:211]\n"
    "s_mov_b32 s88, 16384\n"
    "v_mfma_f32_16x16x32_bf16 v[188:191], a[36:39], v[36:39], v[188:191]\n"
    "s_mov_b32 s99, 528384\n"
    "v_mfma_f32_16x16x32_bf16 v[196:199], a[100:103], v[36:39], v[196:199]\n"
    "v_mfma_f32_16x16x32_bf16 v[204:207], a[164:167], v[36:39], v[204:207]\n"
    "buffer_store_dwordx4 v[216:219], v21, s[80:83], s99 offen offset:0 sc1\n"
    "v_mfma_f32_16x16x32_bf16 v[212:215], a[228:231], v[36:39], v[212:215]\n"
    "s_waitcnt vmcnt(35) lgkmcnt(0)\n"
    "s_barrier\n"
    "v_mfma_f32_16x16x32_bf16 v[184:187], a[40:43], v[40:43], v[184:187]\n"
    "ds_read_b64_tr_b16 v[24:25], v14 offset:49152\n"
    "v_mfma_f32_16x16x32_bf16 v[192:195], a[104:107], v[40:43], v[192:195]\n"
    "ds_read_b64_tr_b16 v[26:27], v14 offset:51200\n"
    "v_cvt_pk_bf16_f32 v220, v104, v105\n"
    "v_mfma_f32_16x16x32_bf16 v[200:203], a[168:171], v[40:43], v[200:203]\n"
    "ds_read_b64_tr_b16 v[28:29], v15 offset:49152\n"
    "v_mfma_f32_16x16x32_bf16 v[208:211], a[232:235], v[40:43], v[208:211]\n"
    "ds_read_b64_tr_b16 v[30:31], v15 offset:51200\n"
    "buffer_load_dwordx4 a[8:11], v234, s[40:43], s88 offen offset:0 sc1\n"
    "v_cvt_pk_bf16_f32 v221, v106, v107\n"
    "v_mfma_f32_16x16x32_bf16 v[188:191], a[40:43], v[44:47], v[188:191]\n"
    "ds_read_b64_tr_b16 v[32:33], v14 offset:53248\n"
    "v_mfma_f32_16x16x32_bf16 v[196:199], a[104:107], v[44:47], v[196:199]\n"
    "ds_read_b64_tr_b16 v[34:35], v14 offset:55296\n"
    "v_mfma_f32_16x16x32_bf16 v[204:207], a[168:171], v[44:47], v[204:207]\n"
    "ds_read_b64_tr_b16 v[36:37], v15 offset:53248\n"
    "v_cvt_pk_bf16_f32 v222, v112, v113\n"
    "v_mfma_f32_16x16x32_bf16 v[212:215], a[232:235], v[44:47], v[212:215]\n"
    "ds_read_b64_tr_b16 v[38:39], v15 offset:55296\n"
    "buffer_load_dwordx4 a[72:75], v234, s[40:43], s88 offen offset:1024 sc1\n"
    "v_mfma_f32_16x16x32_bf16 v[184:187], a[44:47], v[48:51], v[184:187]\n"
    "ds_read_b64_tr_b16 v[40:41], v14 offset:57344\n"
    "v_cvt_pk_bf16_f32 v223, v114, v115\n"
    "v_mfma_f32_16x16x32_bf16 v[192:195], a[108:111], v[48:51], v[192:195]\n"
    "ds_read_b64_tr_b16 v[42:43], v14 offset:59392\n"
    "v_mfma_f32_16x16x32_bf16 v[200:203], a[172:175], v[48:51], v[200:203]\n"
    "s_mov_b32 s99, 536576\n"
    "v_mfma_f32_16x16x32_bf16 v[208:211], a[236:239], v[48:51], v[208:211]\n"
    "v_mfma_f32_16x16x32_bf16 v[188:191], a[44:47], v[52:55], v[188:191]\n"
    "ds_read_b64_tr_b16 v[44:45], v15 offset:57344\n"
    "buffer_load_dwordx4 a[136:139], v235, s[40:43], s88 offen offset:0 sc1\n"
    "v_mfma_f32_16x16x32_bf16 v[196:199], a[108:111], v[52:55], v[196:199]\n"
    "ds_read_b64_tr_b16 v[46:47], v15 offset:59392\n"
    "v_mfma_f32_16x16x32_bf16 v[204:207], a[172:175], v[52:55], v[204:207]\n"
    "v_mfma_f32_16x16x32_bf16 v[212:215], a[236:239], v[52:55], v[212:215]\n"
    "buffer_store_dwordx4 v[220:223], v21, s[80:83], s99 offen offset:0 sc1\n"
    "v_cvt_pk_bf16_f32 v224, v92, v93\n"
    "s_waitcnt lgkmcnt(8)\n"
    "v_mfma_f32_16x16x32_bf16 v[184:187], a[48:51], v[24:27], v[184:187]\n"
    "ds_read_b64_tr_b16 v[48:49], v14 offset:61440\n"
    "v_mfma_f32_16x16x32_bf16 v[192:195], a[112:115], v[24:27], v[192:195]\n"
    "ds_read_b64_tr_b16 v[50:51], v14 offset:63488\n"
    "v_cvt_pk_bf16_f32 v225, v94, v95\n"
    "v_mfma_f32_16x16x32_bf16 v[200:203], a[176:179], v[24:27], v[200:203]\n"
    "v_mfma_f32_16x16x32_bf16 v[208:211], a[240:243], v[24:27], v[208:211]\n"
    "v_mfma_f32_16x16x32_bf16 v[188:191], a[48:51], v[28:31], v[188:191]\n"
    "ds_read_b64_tr_b16 v[52:53], v15 offset:61440\n"
    "buffer_load_dwordx4 a[200:203], v235, s[40:43], s88 offen offset:1024 sc1\n"
    "v_cvt_pk_bf16_f32 v226, v100, v101\n"
    "v_mfma_f32_16x16x32_bf16 v[196:199], a[112:115], v[28:31], v[196:199]\n"
    "ds_read_b64_tr_b16 v[54:55], v15 offset:63488\n"
    "s_mov_b32 s88, 24576\n"
    "v_mfma_f32_16x16x32_bf16 v[204:207], a[176:179], v[28:31], v[204:207]\n"
    "v_cvt_pk_bf16_f32 v227, v102, v103\n"
    "v_mfma_f32_16x16x32_bf16 v[212:215], a[240:243], v[28:31], v[212:215]\n"
    "s_waitcnt lgkmcnt(8)\n"
    "v_mfma_f32_16x16x32_bf16 v[184:187], a[52:55], v[32:35], v[184:187]\n"
    "buffer_load_dwordx4 a[12:15], v234, s[40:43], s88 offen offset:0 sc1\n"
    "v_mfma_f32_16x16x32_bf16 v[192:195], a[116:119], v[32:35], v[192:195]\n"
    "s_mov_b32 s99, 528384\n"
    "v_mfma_f32_16x16x32_bf16 v[200:203], a[180:183], v[32:35], v[200:203]\n"
    "v_mfma_f32_16x16x32_bf16 v[208:211], a[244:247], v[32:35], v[208:211]\n"
    "v_mfma_f32_16x16x32_bf16 v[188:191], a[52:55], v[36:39], v[188:191]\n"
    "buffer_store_dwordx4 v[224:227], v21, s[80:83], s99 offen offset:1024 sc1\n"
    "v_mfma_f32_16x16x32_bf16 v[196:199], a[116:119], v[36:39], v[196:199]\n"
    "v_cvt_pk_bf16_f32 v228, v108, v109\n"
    "v_mfma_f32_16x16x32_bf16 v[204:207], a[180:183], v[36:39], v[204:207]\n"
    "v_mfma_f32_16x16x32_bf16 v[212:215], a[244:247], v[36:39], v[212:215]\n"
    "s_waitcnt vmcnt(42) lgkmcnt(0)\n"
    "s_barrier\n"
    "v_mfma_f32_16x16x32_bf16 v[184:187], a[56:59], v[40:43], v[184:187]\n"
    "ds_read_b64_tr_b16 v[24:25], v8 offset:0\n"
    "v_cvt_pk_bf16_f32 v229, v110, v111\n"
    "v_mfma_f32_16x16x32_bf16 v[192:195], a[120:123], v[40:43], v[192:195]\n"
    "ds_read_b64_tr_b16 v[26:27], v8 offset:2048\n"
    "buffer_load_dwordx4 a[76:79], v234, s[40:43], s88 offen offset:1024 sc1\n"
    "v_mfma_f32_16x16x32_bf16 v[200:203], a[184:187], v[40:43], v[200:203]\n"
    "ds_read_b64_tr_b16 v[28:29], v9 offset:0\n"
    "v_cvt_pk_bf16_f32 v230, v116, v117\n"
    "v_mfma_f32_16x16x32_bf16 v[208:211], a[248:251], v[40:43], v[208:211]\n"
    "ds_read_b64_tr_b16 v[30:31], v9 offset:2048\n"
    "v_mfma_f32_16x16x32_bf16 v[188:191], a[56:59], v[44:47], v[188:191]\n"
    "ds_read_b64_tr_b16 v[32:33], v10 offset:0\n"
    "v_cvt_pk_bf16_f32 v231, v118, v119\n"
    "v_mfma_f32_16x16x32_bf16 v[196:199], a[120:123], v[44:47], v[196:199]\n"
    "ds_read_b64_tr_b16 v[34:35], v10 offset:2048\n"
    "buffer_load_dwordx4 a[140:143], v235, s[40:43], s88 offen offset:0 sc1\n"
    "v_mfma_f32_16x16x32_bf16 v[204:207], a[184:187], v[44:47], v[204:207]\n"
    "ds_read_b64_tr_b16 v[36:37], v11 offset:0\n"
    "v_mfma_f32_16x16x32_bf16 v[212:215], a[248:251], v[44:47], v[212:215]\n"
    "ds_read_b64_tr_b16 v[38:39], v11 offset:2048\n"
    "s_mov_b32 s99, 536576\n"
    "v_mfma_f32_16x16x32_bf16 v[184:187], a[60:63], v[48:51], v[184:187]\n"
    "ds_read_b64_tr_b16 v[40:41], v8 offset:16384\n"
    "v_mfma_f32_16x16x32_bf16 v[192:195], a[124:127], v[48:51], v[192:195]\n"
    "ds_read_b64_tr_b16 v[42:43], v8 offset:18432\n"
    "buffer_store_dwordx4 v[228:231], v21, s[80:83], s99 offen offset:1024 sc1\n"
    "v_mfma_f32_16x16x32_bf16 v[200:203], a[188:191], v[48:51], v[200:203]\n"
    "v_mfma_f32_16x16x32_bf16 v[208:211], a[252:255], v[48:51], v[208:211]\n"
    "v_mfma_f32_16x16x32_bf16 v[188:191], a[60:63], v[52:55], v[188:191]\n"
    "ds_read_b64_tr_b16 v[44:45], v9 offset:16384\n"
    "v_mfma_f32_16x16x32_bf16 v[196:199], a[124:127], v[52:55], v[196:199]\n"
    "ds_read_b64_tr_b16 v[46:47], v9 offset:18432\n"
    "v_mfma_f32_16x16x32_bf16 v[204:207], a[188:191], v[52:55], v[204:207]\n"
    "buffer_load_dwordx4 a[204:207], v235, s[40:43], s88 offen offset:1024 sc1\n"
    "v_mfma_f32_16x16x32_bf16 v[212:215], a[252:255], v[52:55], v[212:215]\n"
    "s_mov_b32 s88, 32768\n"
    "s_waitcnt vmcnt(24)\n"
    "s_waitcnt lgkmcnt(8)\n"
    "v_mfma_f32_16x16x32_bf16 v[56:59], v[24:27], a[0:3], 0\n"
    "ds_read_b64_tr_b16 v[48:49], v10 offset:16384\n"
    "v_mfma_f32_16x16x32_bf16 v[64:67], v[24:27], a[64:67], 0\n"
    "ds_read_b64_tr_b16 v[50:51], v10 offset:18432\n"
    "s_lshl_b32 s30, s64, 2\n"
    "v_mfma_f32_16x16x32_bf16 v[72:75], v[24:27], a[128:131], 0\n"
    "buffer_load_dwordx4 a[16:19], v234, s[40:43], s88 offen offset:0 sc1\n"
    "v_mfma_f32_16x16x32_bf16 v[80:83], v[24:27], a[192:195], 0\n"
    "s_add_u32 s30, s30, s20\n"
    "v_mfma_f32_16x16x32_bf16 v[60:63], v[28:31], a[0:3], 0\n"
    "ds_read_b64_tr_b16 v[52:53], v11 offset:16384\n"
    "v_mfma_f32_16x16x32_bf16 v[68:71], v[28:31], a[64:67], 0\n"
    "ds_read_b64_tr_b16 v[54:55], v11 offset:18432\n"
    "v_lshrrev_b32 v240, 2, v16\n"
    "v_mfma_f32_16x16x32_bf16 v[76:79], v[28:31], a[128:131], 0\n"
    "v_mfma_f32_16x16x32_bf16 v[84:87], v[28:31], a[192:195], 0\n"
    "buffer_load_dwordx4 a[80:83], v234, s[40:43], s88 offen offset:1024 sc1\n"
    "v_mov_b32 v254, s16\n"
    "s_waitcnt lgkmcnt(8)\n"
    "v_mfma_f32_16x16x32_bf16 v[88:91], v[32:35], a[0:3], 0\n"
    "ds_read_b64_tr_b16 v[24:25], v8 offset:4096\n"
    "v_mfma_f32_16x16x32_bf16 v[96:99], v[32:35], a[64:67], 0\n"
    "ds_read_b64_tr_b16 v[26:27], v8 offset:6144\n"
    "v_mov_b32 v255, s17\n"
    "v_mfma_f32_16x16x32_bf16 v[104:107], v[32:35], a[128:131], 0\n"
    "v_mov_b32 v241, v240\n"
    "v_mfma_f32_16x16x32_bf16 v[112:115], v[32:35], a[192:195], 0\n"
    "buffer_load_dwordx4 a[144:147], v235, s[40:43], s88 offen offset:0 sc1\n"
    "v_cvt_pk_bf16_f32 v216, v184, v185\n"
    "v_mfma_f32_16x16x32_bf16 v[92:95], v[36:39], a[0:3], 0\n"
    "ds_read_b64_tr_b16 v[28:29], v9 offset:4096\n"
    "v_mov_b32 v251, 819200\n"
    "v_mfma_f32_16x16x32_bf16 v[100:103], v[36:39], a[64:67], 0\n"
    "ds_read_b64_tr_b16 v[30:31], v9 offset:6144\n"
    "v_mfma_f32_16x16x32_bf16 v[108:111], v[36:39], a[128:131], 0\n"
    "v_mov_b32 v242, 7\n"
    "v_mfma_f32_16x16x32_bf16 v[116:119], v[36:39], a[192:195], 0\n"
    "v_cvt_pk_bf16_f32 v217, v186, v187\n"
    "s_waitcnt lgkmcnt(8)\n"
    "v_mfma_f32_16x16x32_bf16 v[120:123], v[40:43], a[0:3], 0\n"
    "ds_read_b64_tr_b16 v[32:33], v10 offset:4096\n"
    "buffer_load_dwordx4 a[208:211], v235, s[40:43], s88 offen offset:1024 sc1\n"
    "v_cmp_le_u32 vcc, 7, v240\n"
    "v_mfma_f32_16x16x32_bf16 v[128:131], v[40:43], a[64:67], 0\n"
    "ds_read_b64_tr_b16 v[34:35], v10 offset:6144\n"
    "s_mov_b32 s88, 40960\n"
    "v_mfma_f32_16x16x32_bf16 v[136:139], v[40:43], a[128:131], 0\n"
    "v_mov_b32 v252, s10\n"
    "v_mfma_f32_16x16x32_bf16 v[144:147], v[40:43], a[192:195], 0\n"
    "v_cvt_pk_bf16_f32 v218, v192, v193\n"
    "v_mfma_f32_16x16x32_bf16 v[124:127], v[44:47], a[0:3], 0\n"
    "ds_read_b64_tr_b16 v[36:37], v11 offset:4096\n"
    "buffer_load_dwordx4 a[20:23], v234, s[40:43], s88 offen offset:0 sc1\n"
    "v_mov_b32 v253, s11\n"
    "v_mfma_f32_16x16x32_bf16 v[132:135], v[44:47], a[64:67], 0\n"
    "ds_read_b64_tr_b16 v[38:39], v11 offset:6144\n"
    "v_cndmask_b32 v254, v254, v252, vcc\n"
    "v_mfma_f32_16x16x32_bf16 v[140:143], v[44:47], a[128:131], 0\n"
    "v_mfma_f32_16x16x32_bf16 v[148:151], v[44:47], a[192:195], 0\n"
    "v_cndmask_b32 v255, v255, v253, vcc\n"
    "v_cvt_pk_bf16_f32 v219, v194, v195\n"
    "s_waitcnt lgkmcnt(8)\n"
    "v_mfma_f32_16x16x32_bf16 v[152:155], v[48:51], a[0:3], 0\n"
    "ds_read_b64_tr_b16 v[40:41], v8 offset:20480\n"
    "v_mfma_f32_16x16x32_bf16 v[160:163], v[48:51], a[64:67], 0\n"
    "ds_read_b64_tr_b16 v[42:43], v8 offset:22528\n"
    "buffer_load_dwordx4 a[84:87], v234, s[40:43], s88 offen offset:1024 sc1\n"
    "v_subrev_u32 v252, 7, v240\n"
    "v_mfma_f32_16x16x32_bf16 v[168:171], v[48:51], a[128:131], 0\n"
    "v_mfma_f32_16x16x32_bf16 v[176:179], v[48:51], a[192:195], 0\n"
    "v_cndmask_b32 v241, v241, v252, vcc\n"
    "s_mov_b32 s99, 528384\n"
    "v_mfma_f32_16x16x32_bf16 v[156:159], v[52:55], a[0:3], 0\n"
    "ds_read_b64_tr_b16 v[44:45], v9 offset:20480\n"
    "v_mfma_f32_16x16x32_bf16 v[164:167], v[52:55], a[64:67], 0\n"
    "ds_read_b64_tr_b16 v[46:47], v9 offset:22528\n"
    "buffer_load_dwordx4 a[148:151], v235, s[40:43], s88 offen offset:0 sc1\n"
    "v_mov_b32 v252, 360448\n"
    "v_mfma_f32_16x16x32_bf16 v[172:175], v[52:55], a[128:131], 0\n"
    "v_mfma_f32_16x16x32_bf16 v[180:183], v[52:55], a[192:195], 0\n"
    "v_cndmask_b32 v251, v251, v252, vcc\n"
    "s_waitcnt vmcnt(25)\n"
    "s_waitcnt lgkmcnt(8)\n"
    "v_mfma_f32_16x16x32_bf16 v[56:59], v[24:27], a[4:7], v[56:59]\n"
    "ds_read_b64_tr_b16 v[48:49], v10 offset:20480\n"
    "v_mov_b32 v252, 3\n"
    "v_mfma_f32_16x16x32_bf16 v[64:67], v[24:27], a[68:71], v[64:67]\n"
    "ds_read_b64_tr_b16 v[50:51], v10 offset:22528\n"
    "buffer_store_dwordx4 v[216:219], v21, s[80:83], s99 offen offset:2048 sc1\n"
    "v_mfma_f32_16x16x32_bf16 v[72:75], v[24:27], a[132:135], v[72:75]\n"
    "v_cndmask_b32 v242, v242, v252, vcc\n"
    "v_mfma_f32_16x16x32_bf16 v[80:83], v[24:27], a[196:199], v[80:83]\n"
    "v_cvt_pk_bf16_f32 v220, v200, v201\n"
    "v_mfma_f32_16x16x32_bf16 v[60:63], v[28:31], a[4:7], v[60:63]\n"
    "ds_read_b64_tr_b16 v[52:53], v11 offset:20480\n"
    "v_cmp_le_u32 vcc, 10, v240\n"
    "v_mfma_f32_16x16x32_bf16 v[68:71], v[28:31], a[68:71], v[68:71]\n"
    "ds_read_b64_tr_b16 v[54:55], v11 offset:22528\n"
    "v_mfma_f32_16x16x32_bf16 v[76:79], v[28:31], a[132:135], v[76:79]\n"
    "buffer_load_dwordx4 a[212:215], v235, s[40:43], s88 offen offset:1024 sc1\n"
    "v_mov_b32 v252, s12\n"
    "v_mfma_f32_16x16x32_bf16 v[84:87], v[28:31], a[196:199], v[84:87]\n"
    "s_mov_b32 s29, 2097280\n"
    "v_cvt_pk_bf16_f32 v221, v202, v203\n"
    "s_waitcnt lgkmcnt(8)\n"
    "v_mfma_f32_16x16x32_bf16 v[88:91], v[32:35], a[4:7], v[88:91]\n"
    "ds_read_b64_tr_b16 v[24:25], v8 offset:8192\n"
    "s_add_u32 m0, s28, 81920\n"
    "v_mov_b32 v253, s13\n"
    "v_mfma_f32_16x16x32_bf16 v[96:99], v[32:35], a[68:71], v[96:99]\n"
    "ds_read_b64_tr_b16 v[26:27], v8 offset:10240\n"
    "v_mfma_f32_16x16x32_bf16 v[104:107], v[32:35], a[132:135], v[104:107]\n"
    "buffer_load_dwordx4 v4, s[44:47], s29 offen offset:0 lds\n"
    "v_cndmask_b32 v254, v254, v252, vcc\n"
    "v_mfma_f32_16x16x32_bf16 v[112:115], v[32:35], a[196:199], v[112:115]\n"
    "v_cndmask_b32 v255, v255, v253, vcc\n"
    "v_cvt_pk_bf16_f32 v222, v208, v209\n"
    "v_mfma_f32_16x16x32_bf16 v[92:95], v[36:39], a[4:7], v[92:95]\n"
    "ds_read_b64_tr_b16 v[28:29], v9 offset:8192\n"
    "v_mfma_f32_16x16x32_bf16 v[100:103], v[36:39], a[68:71], v[100:103]\n"
    "ds_read_b64_tr_b16 v[30:31], v9 offset:10240\n"
    "v_subrev_u32 v252, 10, v240\n"
    "v_mfma_f32_16x16x32_bf16 v[108:111], v[36:39], a[132:135], v[108:111]\n"
    "v_mfma_f32_16x16x32_bf16 v[116:119], v[36:39], a[196:199], v[116:119]\n"
    "buffer_load_dwordx4 v5, s[44:47], s29 offen offset:1024 lds\n"
    "v_cndmask_b32 v241, v241, v252, vcc\n"
    "v_cvt_pk_bf16_f32 v223, v210, v211\n"
    "s_waitcnt lgkmcnt(8)\n"
    "v_mfma_f32_16x16x32_bf16 v[120:123], v[40:43], a[4:7], v[120:123]\n"
    "ds_read_b64_tr_b16 v[32:33], v10 offset:8192\n"
    "v_mfma_f32_16x16x32_bf16 v[128:131], v[40:43], a[68:71], v[128:131]\n"
    "ds_read_b64_tr_b16 v[34:35], v10 offset:10240\n"
    "v_cmp_le_u32 vcc, 13, v240\n"
    "v_mfma_f32_16x16x32_bf16 v[136:139], v[40:43], a[132:135], v[136:139]\n"
    "v_mfma_f32_16x16x32_bf16 v[144:147], v[40:43], a[196:199], v[144:147]\n"
    "buffer_load_dwordx4 v6, s[44:47], s29 offen offset:2048 lds\n"
    "v_mov_b32 v252, s14\n"
    "s_mov_b32 s99, 536576\n"
    "v_mfma_f32_16x16x32_bf16 v[124:127], v[44:47], a[4:7], v[124:127]\n"
    "ds_read_b64_tr_b16 v[36:37], v11 offset:8192\n"
    "v_mfma_f32_16x16x32_bf16 v[132:135], v[44:47], a[68:71], v[132:135]\n"
    "ds_read_b64_tr_b16 v[38:39], v11 offset:10240\n"
    "v_mov_b32 v253, s15\n"
    "v_mfma_f32_16x16x32_bf16 v[140:143], v[44:47], a[132:135], v[140:143]\n"
    "v_cndmask_b32 v254, v254, v252, vcc\n"
    "v_mfma_f32_16x16x32_bf16 v[148:151], v[44:47], a[196:199], v[148:151]\n"
    "buffer_store_dwordx4 v[220:223], v21, s[80:83], s99 offen offset:2048 sc1\n"
    "s_waitcnt lgkmcnt(8)\n"
    "v_mfma_f32_16x16x32_bf16 v[152:155], v[48:51], a[4:7], v[152:155]\n"
    "ds_read_b64_tr_b16 v[40:41], v8 offset:24576\n"
    "v_cndmask_b32 v255, v255, v253, vcc\n"
    "v_mfma_f32_16x16x32_bf16 v[160:163], v[48:51], a[68:71], v[160:163]\n"
    "ds_read_b64_tr_b16 v[42:43], v8 offset:26624\n"
    "v_mfma_f32_16x16x32_bf16 v[168:171], v[48:51], a[132:135], v[168:171]\n"
    "v_subrev_u32 v252, 13, v240\n"
    "v_mfma_f32_16x16x32_bf16 v[176:179], v[48:51], a[196:199], v[176:179]\n"
    "v_cvt_pk_bf16_f32 v224, v188, v189\n"
    "v_mfma_f32_16x16x32_bf16 v[156:159], v[52:55], a[4:7], v[156:159]\n"
    "ds_read_b64_tr_b16 v[44:45], v9 offset:24576\n"
    "buffer_load_dwordx4 v7, s[44:47], s29 offen offset:3072 lds\n"
    "v_cndmask_b32 v241, v241, v252, vcc\n"
    "v_mfma_f32_16x16x32_bf16 v[164:167], v[52:55], a[68:71], v[164:167]\n"
    "ds_read_b64_tr_b16 v[46:47], v9 offset:26624\n"
    "s_mov_b32 s88, 49152\n"
    "v_mfma_f32_16x16x32_bf16 v[172:175], v[52:55], a[132:135], v[172:175]\n"
    "v_mad_u32_u24 v241, v242, s30, v241\n"
    "v_mfma_f32_16x16x32_bf16 v[180:183], v[52:55], a[196:199], v[180:183]\n"
    "v_lshlrev_b32 v241, 7, v241\n"
    "v_cvt_pk_bf16_f32 v225, v190, v191\n"
    "s_waitcnt vmcnt(20)\n"
    "s_waitcnt lgkmcnt(8)\n"
    "v_mfma_f32_16x16x32_bf16 v[56:59], v[24:27], a[8:11], v[56:59]\n"
    "ds_read_b64_tr_b16 v[48:49], v10 offset:24576\n"
    "buffer_load_dwordx4 a[24:27], v234, s[40:43], s88 offen offset:0 sc1\n"
    "v_mfma_f32_16x16x32_bf16 v[64:67], v[24:27], a[72:75], v[64:67]\n"
    "ds_read_b64_tr_b16 v[50:51], v10 offset:26624\n"
    "v_add_co_u32 v254, vcc, v254, v241\n\ts_nop 1\n\tv_addc_co_u32 v255, vcc, 0, v255, vcc\n"
    "v_mfma_f32_16x16x32_bf16 v[72:75], v[24:27], a[136:139], v[72:75]\n"
    "v_mfma_f32_16x16x32_bf16 v[80:83], v[24:27], a[200:203], v[80:83]\n"
    "s_and_b32 s17, s17, 0xffff\n"
    "v_cvt_pk_bf16_f32 v226, v196, v197\n"
    "v_mfma_f32_16x16x32_bf16 v[60:63], v[28:31], a[8:11], v[60:63]\n"
    "ds_read_b64_tr_b16 v[52:53], v11 offset:24576\n"
    "v_mfma_f32_16x16x32_bf16 v[68:71], v[28:31], a[72:75], v[68:71]\n"
    "ds_read_b64_tr_b16 v[54:55], v11 offset:26624\n"
    "buffer_load_dwordx4 a[88:91], v234, s[40:43], s88 offen offset:1024 sc1\n"
    "s_and_b32 s30, s64, 7\n"
    "v_mfma_f32_16x16x32_bf16 v[76:79], v[28:31], a[136:139], v[76:79]\n"
    "v_cvt_pk_bf16_f32 v227, v198, v199\n"
    "v_mfma_f32_16x16x32_bf16 v[84:87], v[28:31], a[200:203], v[84:87]\n"
    "s_lshl_b32 s30, s30, 19\n"
    "s_waitcnt lgkmcnt(8)\n"
    "v_mfma_f32_16x16x32_bf16 v[88:91], v[32:35], a[8:11], v[88:91]\n"
    "ds_read_b64_tr_b16 v[24:25], v8 offset:12288\n"
    "v_mfma_f32_16x16x32_bf16 v[96:99], v[32:35], a[72:75], v[96:99]\n"
    "ds_read_b64_tr_b16 v[26:27], v8 offset:14336\n"
    "buffer_load_dwordx4 a[152:155], v235, s[40:43], s88 offen offset:0 sc1\n"
    "s_add_u32 s16, s16, s30\n\ts_addc_u32 s17, s17, 0\n"
    "v_mfma_f32_16x16x32_bf16 v[104:107], v[32:35], a[136:139], v[104:107]\n"
    "s_mov_b32 s18, 524288\n"
    "s_mov_b32 s99, 528384\n"
    "v_mfma_f32_16x16x32_bf16 v[112:115], v[32:35], a[200:203], v[112:115]\n"
    "v_mfma_f32_16x16x32_bf16 v[92:95], v[36:39], a[8:11], v[92:95]\n"
    "ds_read_b64_tr_b16 v[28:29], v9 offset:12288\n"
    "s_mov_b32 s19, 0x20000\n"
    "v_mfma_f32_16x16x32_bf16 v[100:103], v[36:39], a[72:75], v[100:103]\n"
    "ds_read_b64_tr_b16 v[30:31], v9 offset:14336\n"
    "v_mfma_f32_16x16x32_bf16 v[108:111], v[36:39], a[136:139], v[108:111]\n"
    "buffer_load_dwordx4 a[216:219], v235, s[40:43], s88 offen offset:1024 sc1\n"
    "s_lshr_b32 s30, s64, 3\n"
    "v_mfma_f32_16x16x32_bf16 v[116:119], v[36:39], a[200:203], v[116:119]\n"
    "s_mov_b32 s88, 57344\n"
    "s_waitcnt lgkmcnt(8)\n"
    "v_mfma_f32_16x16x32_bf16 v[120:123], v[40:43], a[8:11], v[120:123]\n"
    "ds_read_b64_tr_b16 v[32:33], v10 offset:12288\n"
    "s_lshl_b32 s30, s30, 2\n"
    "v_mfma_f32_16x16x32_bf16 v[128:131], v[40:43], a[72:75], v[128:131]\n"
    "ds_read_b64_tr_b16 v[34:35], v10 offset:14336\n"
    "buffer_store_dwordx4 v[224:227], v21, s[80:83], s99 offen offset:3072 sc1\n"
    "v_mfma_f32_16x16x32_bf16 v[136:139], v[40:43], a[136:139], v[136:139]\n"
    "s_add_u32 s30, s30, s20\n"
    "v_cvt_pk_bf16_f32 v228, v204, v205\n"
    "v_mfma_f32_16x16x32_bf16 v[144:147], v[40:43], a[200:203], v[144:147]\n"
    "v_mfma_f32_16x16x32_bf16 v[124:127], v[44:47], a[8:11], v[124:127]\n"
    "ds_read_b64_tr_b16 v[36:37], v11 offset:12288\n"
    "s_lshl_b32 s30, s30, 12\n"
    "v_mfma_f32_16x16x32_bf16 v[132:135], v[44:47], a[72:75], v[132:135]\n"
    "ds_read_b64_tr_b16 v[38:39], v11 offset:14336\n"
    "v_lshrrev_b32 v248, 1, v16\n"
    "v_mfma_f32_16x16x32_bf16 v[140:143], v[44:47], a[136:139], v[140:143]\n"
    "buffer_load_dwordx4 a[28:31], v234, s[40:43], s88 offen offset:0 sc1\n"
    "v_cvt_pk_bf16_f32 v229, v206, v207\n"
    "v_mfma_f32_16x16x32_bf16 v[148:151], v[44:47], a[200:203], v[148:151]\n"
    "v_lshlrev_b32 v248, 7, v248\n"
    "s_waitcnt lgkmcnt(8)\n"
    "v_mfma_f32_16x16x32_bf16 v[152:155], v[48:51], a[8:11], v[152:155]\n"
    "ds_read_b64_tr_b16 v[40:41], v8 offset:28672\n"
    "v_mfma_f32_16x16x32_bf16 v[160:163], v[48:51], a[72:75], v[160:163]\n"
    "ds_read_b64_tr_b16 v[42:43], v8 offset:30720\n"
    "v_add_u32 v248, s30, v248\n"
    "v_mfma_f32_16x16x32_bf16 v[168:171], v[48:51], a[136:139], v[168:171]\n"
    "buffer_load_dwordx4 a[92:95], v234, s[40:43], s88 offen offset:1024 sc1\n"
    "v_cvt_pk_bf16_f32 v230, v212, v213\n"
    "v_mfma_f32_16x16x32_bf16 v[176:179], v[48:51], a[200:203], v[176:179]\n"
    "v_mfma_f32_16x16x32_bf16 v[156:159], v[52:55], a[8:11], v[156:159]\n"
    "ds_read_b64_tr_b16 v[44:45], v9 offset:28672\n"
    "v_mfma_f32_16x16x32_bf16 v[164:167], v[52:55], a[72:75], v[164:167]\n"
    "ds_read_b64_tr_b16 v[46:47], v9 offset:30720\n"
    "v_mfma_f32_16x16x32_bf16 v[172:175], v[52:55], a[136:139], v[172:175]\n"
    "v_cvt_pk_bf16_f32 v231, v214, v215\n"
    "v_mfma_f32_16x16x32_bf16 v[180:183], v[52:55], a[200:203], v[180:183]\n"
    "buffer_load_dwordx4 a[156:159], v235, s[40:43], s88 offen offset:0 sc1\n"
    "s_waitcnt vmcnt(22)\n"
    "s_waitcnt lgkmcnt(8)\n"
    "v_mfma_f32_16x16x32_bf16 v[56:59], v[24:27], a[12:15], v[56:59]\n"
    "ds_read_b64_tr_b16 v[48:49], v10 offset:28672\n"
    "v_mfma_f32_16x16x32_bf16 v[64:67], v[24:27], a[76:79], v[64:67]\n"
    "ds_read_b64_tr_b16 v[50:51], v10 offset:30720\n"
    "v_mfma_f32_16x16x32_bf16 v[72:75], v[24:27], a[140:143], v[72:75]\n"
    "s_mov_b32 s99, 536576\n"
    "v_mfma_f32_16x16x32_bf16 v[80:83], v[24:27], a[204:207], v[80:83]\n"
    "buffer_load_dwordx4 a[220:223], v235, s[40:43], s88 offen offset:1024 sc1\n"
    "v_mfma_f32_16x16x32_bf16 v[60:63], v[28:31], a[12:15], v[60:63]\n"
    "ds_read_b64_tr_b16 v[52:53], v11 offset:28672\n"
    "s_mov_b32 s88, 65536\n"
    "v_mfma_f32_16x16x32_bf16 v[68:71], v[28:31], a[76:79], v[68:71]\n"
    "ds_read_b64_tr_b16 v[54:55], v11 offset:30720\n"
    "v_mfma_f32_16x16x32_bf16 v[76:79], v[28:31], a[140:143], v[76:79]\n"
    "v_mfma_f32_16x16x32_bf16 v[84:87], v[28:31], a[204:207], v[84:87]\n"
    "buffer_store_dwordx4 v[228:231], v21, s[80:83], s99 offen offset:3072 sc1\n"
    "s_waitcnt lgkmcnt(8)\n"
    "v_mfma_f32_16x16x32_bf16 v[88:91], v[32:35], a[12:15], v[88:91]\n"
    "v_mfma_f32_16x16x32_bf16 v[96:99], v[32:35], a[76:79], v[96:99]\n"
    "v_mfma_f32_16x16x32_bf16 v[104:107], v[32:35], a[140:143], v[104:107]\n"
    "v_mfma_f32_16x16x32_bf16 v[112:115], v[32:35], a[204:207], v[112:115]\n"
    "v_mfma_f32_16x16x32_bf16 v[92:95], v[36:39], a[12:15], v[92:95]\n"
    "buffer_load_dwordx4 a[32:35], v234, s[40:43], s88 offen offset:0 sc1\n"
    "v_mfma_f32_16x16x32_bf16 v[100:103], v[36:39], a[76:79], v[100:103]\n"
    "v_mfma_f32_16x16x32_bf16 v[108:111], v[36:39], a[140:143], v[108:111]\n"
    "v_mfma_f32_16x16x32_bf16 v[116:119], v[36:39], a[204:207], v[116:119]\n"
    "s_waitcnt vmcnt(25) lgkmcnt(0)\n"
    "s_barrier\n"
    "v_mfma_f32_16x16x32_bf16 v[120:123], v[40:43], a[12:15], v[120:123]\n"
    "ds_read_b64_tr_b16 v[24:25], v8 offset:32768\n"
    "buffer_load_dwordx4 a[96:99], v234, s[40:43], s88 offen offset:1024 sc1\n"
    "v_mfma_f32_16x16x32_bf16 v[128:131], v[40:43], a[76:79], v[128:131]\n"
    "ds_read_b64_tr_b16 v[26:27], v8 offset:34816\n"
    "v_mfma_f32_16x16x32_bf16 v[136:139], v[40:43], a[140:143], v[136:139]\n"
    "ds_read_b64_tr_b16 v[28:29], v9 offset:32768\n"
    "v_mfma_f32_16x16x32_bf16 v[144:147], v[40:43], a[204:207], v[144:147]\n"
    "ds_read_b64_tr_b16 v[30:31], v9 offset:34816\n"
    "v_mfma_f32_16x16x32_bf16 v[124:127], v[44:47], a[12:15], v[124:127]\n"
    "ds_read_b64_tr_b16 v[32:33], v10 offset:32768\n"
    "v_mfma_f32_16x16x32_bf16 v[132:135], v[44:47], a[76:79], v[132:135]\n"
    "ds_read_b64_tr_b16 v[34:35], v10 offset:34816\n"
    "buffer_load_dwordx4 a[160:163], v235, s[40:43], s88 offen offset:0 sc1\n"
    "v_mfma_f32_16x16x32_bf16 v[140:143], v[44:47], a[140:143], v[140:143]\n"
    "ds_read_b64_tr_b16 v[36:37], v11 offset:32768\n"
    "v_mfma_f32_16x16x32_bf16 v[148:151], v[44:47], a[204:207], v[148:151]\n"
    "ds_read_b64_tr_b16 v[38:39], v11 offset:34816\n"
    "v_mfma_f32_16x16x32_bf16 v[152:155], v[48:51], a[12:15], v[152:155]\n"
    "ds_read_b64_tr_b16 v[40:41], v8 offset:49152\n"
    "v_mfma_f32_16x16x32_bf16 v[160:163], v[48:51], a[76:79], v[160:163]\n"
    "ds_read_b64_tr_b16 v[42:43], v8 offset:51200\n"
    "buffer_load_dwordx4 a[224:227], v235, s[40:43], s88 offen offset:1024 sc1\n"
    "v_mfma_f32_16x16x32_bf16 v[168:171], v[48:51], a[140:143], v[168:171]\n"
    "s_mov_b32 s88, 73728\n"
    "v_mfma_f32_16x16x32_bf16 v[176:179], v[48:51], a[204:207], v[176:179]\n"
    "v_mfma_f32_16x16x32_bf16 v[156:159], v[52:55], a[12:15], v[156:159]\n"
    "ds_read_b64_tr_b16 v[44:45], v9 offset:49152\n"
    "v_mfma_f32_16x16x32_bf16 v[164:167], v[52:55], a[76:79], v[164:167]\n"
    "ds_read_b64_tr_b16 v[46:47], v9 offset:51200\n"
    "v_mfma_f32_16x16x32_bf16 v[172:175], v[52:55], a[140:143], v[172:175]\n"
    "buffer_load_dwordx4 a[36:39], v234, s[40:43], s88 offen offset:0 sc1\n"
    "v_mfma_f32_16x16x32_bf16 v[180:183], v[52:55], a[204:207], v[180:183]\n"
    "s_waitcnt vmcnt(25)\n"
    "s_waitcnt lgkmcnt(8)\n"
    "v_mfma_f32_16x16x32_bf16 v[56:59], v[24:27], a[16:19], v[56:59]\n"
    "ds_read_b64_tr_b16 v[48:49], v10 offset:49152\n"
    "v_mfma_f32_16x16x32_bf16 v[64:67], v[24:27], a[80:83], v[64:67]\n"
    "ds_read_b64_tr_b16 v[50:51], v10 offset:51200\n"
    "v_mfma_f32_16x16x32_bf16 v[72:75], v[24:27], a[144:147], v[72:75]\n"
    "buffer_load_dwordx4 a[100:103], v234, s[40:43], s88 offen offset:1024 sc1\n"
    "s_lshl_b32 s30, s20, 13\n"
    "v_mfma_f32_16x16x32_bf16 v[80:83], v[24:27], a[208:211], v[80:83]\n"
    "v_mfma_f32_16x16x32_bf16 v[60:63], v[28:31], a[16:19], v[60:63]\n"
    "ds_read_b64_tr_b16 v[52:53], v11 offset:49152\n"
    "s_add_u32 s30, s30, 131072\n"
    "v_mfma_f32_16x16x32_bf16 v[68:71], v[28:31], a[80:83], v[68:71]\n"
    "ds_read_b64_tr_b16 v[54:55], v11 offset:51200\n"
    "v_mfma_f32_16x16x32_bf16 v[76:79], v[28:31], a[144:147], v[76:79]\n"
    "v_add_u32 v17, s30, v2\n"
    "v_mfma_f32_16x16x32_bf16 v[84:87], v[28:31], a[208:211], v[84:87]\n"
    "buffer_load_dwordx4 a[164:167], v235, s[40:43], s88 offen offset:0 sc1\n"
    "s_waitcnt lgkmcnt(8)\n"
    "v_mfma_f32_16x16x32_bf16 v[88:91], v[32:35], a[16:19], v[88:91]\n"
    "ds_read_b64_tr_b16 v[24:25], v8 offset:36864\n"
    "v_add_u32 v20, s28, v2\n"
    "v_mfma_f32_16x16x32_bf16 v[96:99], v[32:35], a[80:83], v[96:99]\n"
    "ds_read_b64_tr_b16 v[26:27], v8 offset:38912\n"
    "v_mfma_f32_16x16x32_bf16 v[104:107], v[32:35], a[144:147], v[104:107]\n"
    "s_and_b32 s21, s20, 1\n"
    "v_mfma_f32_16x16x32_bf16 v[112:115], v[32:35], a[208:211], v[112:115]\n"
    "buffer_load_dwordx4 a[228:231], v235, s[40:43], s88 offen offset:1024 sc1\n"
    "v_mfma_f32_16x16x32_bf16 v[92:95], v[36:39], a[16:19], v[92:95]\n"
    "ds_read_b64_tr_b16 v[28:29], v9 offset:36864\n"
    "s_mov_b32 s29, 3145728\n"
    "s_lshl_b32 s23, s22, 1\n"
    "v_mfma_f32_16x16x32_bf16 v[100:103], v[36:39], a[80:83], v[100:103]\n"
    "ds_read_b64_tr_b16 v[30:31], v9 offset:38912\n"
    "s_add_u32 m0, s28, 98304\n"
    "v_mfma_f32_16x16x32_bf16 v[108:111], v[36:39], a[144:147], v[108:111]\n"
    "s_add_u32 s23, s23, 2\n"
    "v_mfma_f32_16x16x32_bf16 v[116:119], v[36:39], a[208:211], v[116:119]\n"
    "s_waitcnt lgkmcnt(8)\n"
    "v_mfma_f32_16x16x32_bf16 v[120:123], v[40:43], a[16:19], v[120:123]\n"
    "ds_read_b64_tr_b16 v[32:33], v10 offset:36864\n"
    "buffer_load_dwordx4 v4, s[44:47], s29 offen offset:0 lds\n"
    "s_mov_b32 s96, 0x3e0293ee\n"
    "v_mfma_f32_16x16x32_bf16 v[128:131], v[40:43], a[80:83], v[128:131]\n"
    "ds_read_b64_tr_b16 v[34:35], v10 offset:38912\n"
    "v_mfma_f32_16x16x32_bf16 v[136:139], v[40:43], a[144:147], v[136:139]\n"
    "s_mov_b32 s97, 0x7f800000\n"
    "v_mfma_f32_16x16x32_bf16 v[144:147], v[40:43], a[208:211], v[144:147]\n"
    "v_mfma_f32_16x16x32_bf16 v[124:127], v[44:47], a[16:19], v[124:127]\n"
    "ds_read_b64_tr_b16 v[36:37], v11 offset:36864\n"
    "buffer_load_dwordx4 v5, s[44:47], s29 offen offset:1024 lds\n"
    "s_mov_b32 s98, 0\n"
    "v_mfma_f32_16x16x32_bf16 v[132:135], v[44:47], a[80:83], v[132:135]\n"
    "ds_read_b64_tr_b16 v[38:39], v11 offset:38912\n"
    "v_mfma_f32_16x16x32_bf16 v[140:143], v[44:47], a[144:147], v[140:143]\n"
    "s_mov_b32 s86, 32768\n"
    "v_mfma_f32_16x16x32_bf16 v[148:151], v[44:47], a[208:211], v[148:151]\n"
    "s_waitcnt lgkmcnt(8)\n"
    "v_mfma_f32_16x16x32_bf16 v[152:155], v[48:51], a[16:19], v[152:155]\n"
    "ds_read_b64_tr_b16 v[40:41], v8 offset:53248\n"
    "s_mov_b32 s34, 16384\n"
    "v_mfma_f32_16x16x32_bf16 v[160:163], v[48:51], a[80:83], v[160:163]\n"
    "ds_read_b64_tr_b16 v[42:43], v8 offset:55296\n"
    "buffer_load_dwordx4 v6, s[44:47], s29 offen offset:2048 lds\n"
    "v_mfma_f32_16x16x32_bf16 v[168:171], v[48:51], a[144:147], v[168:171]\n"
    "s_mov_b32 s39, 16384\n"
    "v_mfma_f32_16x16x32_bf16 v[176:179], v[48:51], a[208:211], v[176:179]\n"
    "v_mfma_f32_16x16x32_bf16 v[156:159], v[52:55], a[16:19], v[156:159]\n"
    "ds_read_b64_tr_b16 v[44:45], v9 offset:53248\n"
    "s_mov_b32 s38, 16384\n"
    "v_mfma_f32_16x16x32_bf16 v[164:167], v[52:55], a[80:83], v[164:167]\n"
    "ds_read_b64_tr_b16 v[46:47], v9 offset:55296\n"
    "buffer_load_dwordx4 v7, s[44:47], s29 offen offset:3072 lds\n"
    "v_mfma_f32_16x16x32_bf16 v[172:175], v[52:55], a[144:147], v[172:175]\n"
    "s_mov_b32 s88, 81920\n"
    "s_add_u32 s36, s28, 32768\n"
    "v_mfma_f32_16x16x32_bf16 v[180:183], v[52:55], a[208:211], v[180:183]\n"
    "s_waitcnt vmcnt(27)\n"
    "s_waitcnt lgkmcnt(8)\n"
    "v_mfma_f32_16x16x32_bf16 v[56:59], v[24:27], a[20:23], v[56:59]\n"
    "ds_read_b64_tr_b16 v[48:49], v10 offset:53248\n"
    "s_add_u32 s37, s28, 98304\n"
    "v_mfma_f32_16x16x32_bf16 v[64:67], v[24:27], a[84:87], v[64:67]\n"
    "ds_read_b64_tr_b16 v[50:51], v10 offset:55296\n"
    "v_mfma_f32_16x16x32_bf16 v[72:75], v[24:27], a[148:151], v[72:75]\n"
    "buffer_load_dwordx4 a[40:43], v234, s[40:43], s88 offen offset:0 sc1\n"
    "s_mov_b32 s24, 0\n"
    "v_mfma_f32_16x16x32_bf16 v[80:83], v[24:27], a[212:215], v[80:83]\n"
    "v_mfma_f32_16x16x32_bf16 v[60:63], v[28:31], a[20:23], v[60:63]\n"
    "ds_read_b64_tr_b16 v[52:53], v11 offset:53248\n"
    "s_lshl_b32 s30, s20, 1\n"
    "v_mfma_f32_16x16x32_bf16 v[68:71], v[28:31], a[84:87], v[68:71]\n"
    "ds_read_b64_tr_b16 v[54:55], v11 offset:55296\n"
    "v_mfma_f32_16x16x32_bf16 v[76:79], v[28:31], a[148:151], v[76:79]\n"
    "buffer_load_dwordx4 a[104:107], v234, s[40:43], s88 offen offset:1024 sc1\n"
    "s_lshr_b32 s72, 129, s30\n"
    "v_mfma_f32_16x16x32_bf16 v[84:87], v[28:31], a[212:215], v[84:87]\n"
    "s_waitcnt lgkmcnt(8)\n"
    "v_mfma_f32_16x16x32_bf16 v[88:91], v[32:35], a[20:23], v[88:91]\n"
    "ds_read_b64_tr_b16 v[24:25], v8 offset:40960\n"
    "s_and_b32 s72, s72, 3\n"
    "v_mfma_f32_16x16x32_bf16 v[96:99], v[32:35], a[84:87], v[96:99]\n"
    "ds_read_b64_tr_b16 v[26:27], v8 offset:43008\n"
    "v_mfma_f32_16x16x32_bf16 v[104:107], v[32:35], a[148:151], v[104:107]\n"
    "s_mov_b32 s73, 0\n"
    "v_mfma_f32_16x16x32_bf16 v[112:115], v[32:35], a[212:215], v[112:115]\n"
    "buffer_load_dwordx4 a[168:171], v235, s[40:43], s88 offen offset:0 sc1\n"
    "v_mfma_f32_16x16x32_bf16 v[92:95], v[36:39], a[20:23], v[92:95]\n"
    "ds_read_b64_tr_b16 v[28:29], v9 offset:40960\n"
    "s_lshl_b64 s[72:73], s[72:73], 31\n"
    "v_mfma_f32_16x16x32_bf16 v[100:103], v[36:39], a[84:87], v[100:103]\n"
    "ds_read_b64_tr_b16 v[30:31], v9 offset:43008\n"
    "v_mfma_f32_16x16x32_bf16 v[108:111], v[36:39], a[148:151], v[108:111]\n"
    "s_lshr_b32 s74, 14, s30\n"
    "v_mfma_f32_16x16x32_bf16 v[116:119], v[36:39], a[212:215], v[116:119]\n"
    "buffer_load_dwordx4 a[232:235], v235, s[40:43], s88 offen offset:1024 sc1\n"
    "s_waitcnt lgkmcnt(8)\n"
    "v_mfma_f32_16x16x32_bf16 v[120:123], v[40:43], a[20:23], v[120:123]\n"
    "ds_read_b64_tr_b16 v[32:33], v10 offset:40960\n"
    "s_mov_b32 s88, 90112\n"
    "s_and_b32 s74, s74, 3\n"
    "v_mfma_f32_16x16x32_bf16 v[128:131], v[40:43], a[84:87], v[128:131]\n"
    "ds_read_b64_tr_b16 v[34:35], v10 offset:43008\n"
    "v_mfma_f32_16x16x32_bf16 v[136:139], v[40:43], a[148:151], v[136:139]\n"
    "s_mov_b32 s75, 0\n"
    "v_mfma_f32_16x16x32_bf16 v[144:147], v[40:43], a[212:215], v[144:147]\n"
    "v_mfma_f32_16x16x32_bf16 v[124:127], v[44:47], a[20:23], v[124:127]\n"
    "ds_read_b64_tr_b16 v[36:37], v11 offset:40960\n"
    "buffer_load_dwordx4 a[44:47], v234, s[40:43], s88 offen offset:0 sc1\n"
    "s_lshl_b64 s[74:75], s[74:75], 31\n"
    "v_mfma_f32_16x16x32_bf16 v[132:135], v[44:47], a[84:87], v[132:135]\n"
    "ds_read_b64_tr_b16 v[38:39], v11 offset:43008\n"
    "v_mfma_f32_16x16x32_bf16 v[140:143], v[44:47], a[148:151], v[140:143]\n"
    "s_lshr_b32 s76, 66, s30\n"
    "v_mfma_f32_16x16x32_bf16 v[148:151], v[44:47], a[212:215], v[148:151]\n"
    "s_waitcnt lgkmcnt(8)\n"
    "v_mfma_f32_16x16x32_bf16 v[152:155], v[48:51], a[20:23], v[152:155]\n"
    "ds_read_b64_tr_b16 v[40:41], v8 offset:57344\n"
    "buffer_load_dwordx4 a[108:111], v234, s[40:43], s88 offen offset:1024 sc1\n"
    "v_mfma_f32_16x16x32_bf16 v[160:163], v[48:51], a[84:87], v[160:163]\n"
    "ds_read_b64_tr_b16 v[42:43], v8 offset:59392\n"
    "s_and_b32 s76, s76, 3\n"
    "v_mfma_f32_16x16x32_bf16 v[168:171], v[48:51], a[148:151], v[168:171]\n"
    "v_mfma_f32_16x16x32_bf16 v[176:179], v[48:51], a[212:215], v[176:179]\n"
    "s_mov_b32 s77, 0\n"
    "v_mfma_f32_16x16x32_bf16 v[156:159], v[52:55], a[20:23], v[156:159]\n"
    "ds_read_b64_tr_b16 v[44:45], v9 offset:57344\n"
    "v_mfma_f32_16x16x32_bf16 v[164:167], v[52:55], a[84:87], v[164:167]\n"
    "ds_read_b64_tr_b16 v[46:47], v9 offset:59392\n"
    "buffer_load_dwordx4 a[172:175], v235, s[40:43], s88 offen offset:0 sc1\n"
    "s_lshl_b64 s[76:77], s[76:77], 31\n"
    "v_mfma_f32_16x16x32_bf16 v[172:175], v[52:55], a[148:151], v[172:175]\n"
    "v_mfma_f32_16x16x32_bf16 v[180:183], v[52:55], a[212:215], v[180:183]\n"
    "s_lshr_b32 s78, 140, s30\n"
    "s_waitcnt vmcnt(25)\n"
    "s_waitcnt lgkmcnt(8)\n"
    "v_mfma_f32_16x16x32_bf16 v[56:59], v[24:27], a[24:27], v[56:59]\n"
    "ds_read_b64_tr_b16 v[48:49], v10 offset:57344\n"
    "v_mfma_f32_16x16x32_bf16 v[64:67], v[24:27], a[88:91], v[64:67]\n"
    "ds_read_b64_tr_b16 v[50:51], v10 offset:59392\n"
    "buffer_load_dwordx4 a[236:239], v235, s[40:43], s88 offen offset:1024 sc1\n"
    "s_and_b32 s78, s78, 3\n"
    "v_mfma_f32_16x16x32_bf16 v[72:75], v[24:27], a[152:155], v[72:75]\n"
    "s_mov_b32 s88, 98304\n"
    "v_mfma_f32_16x16x32_bf16 v[80:83], v[24:27], a[216:219], v[80:83]\n"
    "s_mov_b32 s79, 0\n"
    "v_mfma_f32_16x16x32_bf16 v[60:63], v[28:31], a[24:27], v[60:63]\n"
    "ds_read_b64_tr_b16 v[52:53], v11 offset:57344\n"
    "v_mfma_f32_16x16x32_bf16 v[68:71], v[28:31], a[88:91], v[68:71]\n"
    "ds_read_b64_tr_b16 v[54:55], v11 offset:59392\n"
    "s_lshl_b64 s[78:79], s[78:79], 31\n"
    "v_mfma_f32_16x16x32_bf16 v[76:79], v[28:31], a[152:155], v[76:79]\n"
    "buffer_load_dwordx4 a[48:51], v234, s[40:43], s88 offen offset:0 sc1\n"
    "v_mfma_f32_16x16x32_bf16 v[84:87], v[28:31], a[216:219], v[84:87]\n"
    "s_lshl_b32 s30, s22, 2\n"
    "s_waitcnt lgkmcnt(8)\n"
    "v_mfma_f32_16x16x32_bf16 v[88:91], v[32:35], a[24:27], v[88:91]\n"
    "ds_read_b64_tr_b16 v[24:25], v8 offset:45056\n"
    "v_mfma_f32_16x16x32_bf16 v[96:99], v[32:35], a[88:91], v[96:99]\n"
    "ds_read_b64_tr_b16 v[26:27], v8 offset:47104\n"
    "s_add_u32 s31, s30, s20\n"
    "v_mfma_f32_16x16x32_bf16 v[104:107], v[32:35], a[152:155], v[104:107]\n"
    "buffer_load_dwordx4 a[112:115], v234, s[40:43], s88 offen offset:1024 sc1\n"
    "v_mfma_f32_16x16x32_bf16 v[112:115], v[32:35], a[216:219], v[112:115]\n"
    "s_lshl_b64 s[82:83], 1, s31\n"
    "v_mfma_f32_16x16x32_bf16 v[92:95], v[36:39], a[24:27], v[92:95]\n"
    "ds_read_b64_tr_b16 v[28:29], v9 offset:45056\n"
    "v_mfma_f32_16x16x32_bf16 v[100:103], v[36:39], a[88:91], v[100:103]\n"
    "ds_read_b64_tr_b16 v[30:31], v9 offset:47104\n"
    "s_lshr_b64 s[26:27], s[82:83], 1\n"
    "v_mfma_f32_16x16x32_bf16 v[108:111], v[36:39], a[152:155], v[108:111]\n"
    "v_mfma_f32_16x16x32_bf16 v[116:119], v[36:39], a[216:219], v[116:119]\n"
    "buffer_load_dwordx4 a[176:179], v235, s[40:43], s88 offen offset:0 sc1\n"
    "s_or_b64 s[76:77], s[76:77], s[26:27]\n"
    "s_waitcnt lgkmcnt(8)\n"
    "v_mfma_f32_16x16x32_bf16 v[120:123], v[40:43], a[24:27], v[120:123]\n"
    "ds_read_b64_tr_b16 v[32:33], v10 offset:45056\n"
    "v_mfma_f32_16x16x32_bf16 v[128:131], v[40:43], a[88:91], v[128:131]\n"
    "ds_read_b64_tr_b16 v[34:35], v10 offset:47104\n"
    "s_lshl_b64 s[26:27], 8, s30\n"
    "v_mfma_f32_16x16x32_bf16 v[136:139], v[40:43], a[152:155], v[136:139]\n"
    "v_mfma_f32_16x16x32_bf16 v[144:147], v[40:43], a[216:219], v[144:147]\n"
    "buffer_load_dwordx4 a[240:243], v235, s[40:43], s88 offen offset:1024 sc1\n"
    "s_sub_u32 s26, s26, s82\n\ts_subb_u32 s27, s27, s83\n"
    "v_mfma_f32_16x16x32_bf16 v[124:127], v[44:47], a[24:27], v[124:127]\n"
    "ds_read_b64_tr_b16 v[36:37], v11 offset:45056\n"
    "s_mov_b32 s88, 106496\n"
    "v_mfma_f32_16x16x32_bf16 v[132:135], v[44:47], a[88:91], v[132:135]\n"
    "ds_read_b64_tr_b16 v[38:39], v11 offset:47104\n"
    "s_or_b64 s[78:79], s[78:79], s[26:27]\n"
    "v_mfma_f32_16x16x32_bf16 v[140:143], v[44:47], a[152:155], v[140:143]\n"
    "v_mfma_f32_16x16x32_bf16 v[148:151], v[44:47], a[216:219], v[148:151]\n"
    "s_lshl_b64 s[26:27], 40, s30\n"
    "s_waitcnt lgkmcnt(8)\n"
    "v_mfma_f32_16x16x32_bf16 v[152:155], v[48:51], a[24:27], v[152:155]\n"
    "ds_read_b64_tr_b16 v[40:41], v8 offset:61440\n"
    "buffer_load_dwordx4 a[52:55], v234, s[40:43], s88 offen offset:0 sc1\n"
    "v_mfma_f32_16x16x32_bf16 v[160:163], v[48:51], a[88:91], v[160:163]\n"
    "ds_read_b64_tr_b16 v[42:43], v8 offset:63488\n"
    "s_lshl_b32 s80, s21, 14\n"
    "v_mfma_f32_16x16x32_bf16 v[168:171], v[48:51], a[152:155], v[168:171]\n"
    "v_mfma_f32_16x16x32_bf16 v[176:179], v[48:51], a[216:219], v[176:179]\n"
    "v_mfma_f32_16x16x32_bf16 v[156:159], v[52:55], a[24:27], v[156:159]\n"
    "ds_read_b64_tr_b16 v[44:45], v9 offset:61440\n"
    "buffer_load_dwordx4 a[116:119], v234, s[40:43], s88 offen offset:1024 sc1\n"
    "v_mfma_f32_16x16x32_bf16 v[164:167], v[52:55], a[88:91], v[164:167]\n"
    "ds_read_b64_tr_b16 v[46:47], v9 offset:63488\n"
    "v_mfma_f32_16x16x32_bf16 v[172:175], v[52:55], a[152:155], v[172:175]\n"
    "v_mfma_f32_16x16x32_bf16 v[180:183], v[52:55], a[216:219], v[180:183]\n"
    "s_waitcnt vmcnt(27)\n"
    "s_waitcnt lgkmcnt(8)\n"
    "v_mfma_f32_16x16x32_bf16 v[56:59], v[24:27], a[28:31], v[56:59]\n"
    "ds_read_b64_tr_b16 v[48:49], v10 offset:61440\n"
    "v_mfma_f32_16x16x32_bf16 v[64:67], v[24:27], a[92:95], v[64:67]\n"
    "ds_read_b64_tr_b16 v[50:51], v10 offset:63488\n"
    "buffer_load_dwordx4 a[180:183], v235, s[40:43], s88 offen offset:0 sc1\n"
    "v_mfma_f32_16x16x32_bf16 v[72:75], v[24:27], a[156:159], v[72:75]\n"
    "v_mfma_f32_16x16x32_bf16 v[80:83], v[24:27], a[220:223], v[80:83]\n"
    "v_mfma_f32_16x16x32_bf16 v[60:63], v[28:31], a[28:31], v[60:63]\n"
    "ds_read_b64_tr_b16 v[52:53], v11 offset:61440\n"
    "v_mfma_f32_16x16x32_bf16 v[68:71], v[28:31], a[92:95], v[68:71]\n"
    "ds_read_b64_tr_b16 v[54:55], v11 offset:63488\n"
    "buffer_load_dwordx4 a[244:247], v235, s[40:43], s88 offen offset:1024 sc1\n"
    "v_mfma_f32_16x16x32_bf16 v[76:79], v[28:31], a[156:159], v[76:79]\n"
    "s_mov_b32 s29, 3145856\n"
    "v_mfma_f32_16x16x32_bf16 v[84:87], v[28:31], a[220:223], v[84:87]\n"
    "s_add_u32 m0, s28, 114688\n"
    "s_waitcnt lgkmcnt(8)\n"
    "v_mfma_f32_16x16x32_bf16 v[88:91], v[32:35], a[28:31], v[88:91]\n"
    "v_mfma_f32_16x16x32_bf16 v[96:99], v[32:35], a[92:95], v[96:99]\n"
    "v_mfma_f32_16x16x32_bf16 v[104:107], v[32:35], a[156:159], v[104:107]\n"
    "buffer_load_dwordx4 v4, s[44:47], s29 offen offset:0 lds\n"
    "v_mfma_f32_16x16x32_bf16 v[112:115], v[32:35], a[220:223], v[112:115]\n"
    "v_mfma_f32_16x16x32_bf16 v[92:95], v[36:39], a[28:31], v[92:95]\n"
    "v_mfma_f32_16x16x32_bf16 v[100:103], v[36:39], a[92:95], v[100:103]\n"
    "v_mfma_f32_16x16x32_bf16 v[108:111], v[36:39], a[156:159], v[108:111]\n"
    "buffer_load_dwordx4 v5, s[44:47], s29 offen offset:1024 lds\n"
    "v_mfma_f32_16x16x32_bf16 v[116:119], v[36:39], a[220:223], v[116:119]\n"
    "s_waitcnt vmcnt(30) lgkmcnt(0)\n"
    "s_barrier\n"
    "s_cmp_lg_u32 s20, 0\n"
    "s_cbranch_scc1 L_skv_%=\n"
    "v_mov_b32 v18, s35\n"
    "v_mov_b32 v19, s94\n"
    "s_mov_b64 exec, 1\n"
    "global_atomic_swap v18, v19, s[60:61]\n"
    "s_mov_b64 exec, -1\n"
    "L_skv_%=:\n"
    "v_mfma_f32_16x16x32_bf16 v[120:123], v[40:43], a[28:31], v[120:123]\n"
    "ds_read_b64_tr_b16 v[24:25], v12 offset:0\n"
    "v_mfma_f32_16x16x32_bf16 v[128:131], v[40:43], a[92:95], v[128:131]\n"
    "ds_read_b64_tr_b16 v[26:27], v12 offset:2048\n"
    "v_mfma_f32_16x16x32_bf16 v[136:139], v[40:43], a[156:159], v[136:139]\n"
    "ds_read_b64_tr_b16 v[28:29], v13 offset:0\n"
    "v_mfma_f32_16x16x32_bf16 v[144:147], v[40:43], a[220:223], v[144:147]\n"
    "ds_read_b64_tr_b16 v[30:31], v13 offset:2048\n"
    "buffer_load_dwordx4 v6, s[44:47], s29 offen offset:2048 lds\n"
    "v_mfma_f32_16x16x32_bf16 v[124:127], v[44:47], a[28:31], v[124:127]\n"
    "ds_read_b64_tr_b16 v[32:33], v14 offset:0\n"
    "v_mfma_f32_16x16x32_bf16 v[132:135], v[44:47], a[92:95], v[132:135]\n"
    "ds_read_b64_tr_b16 v[34:35], v14 offset:2048\n"
    "v_mfma_f32_16x16x32_bf16 v[140:143], v[44:47], a[156:159], v[140:143]\n"
    "ds_read_b64_tr_b16 v[36:37], v15 offset:0\n"
    "v_mfma_f32_16x16x32_bf16 v[148:151], v[44:47], a[220:223], v[148:151]\n"
    "ds_read_b64_tr_b16 v[38:39], v15 offset:2048\n"
    "buffer_load_dwordx4 v7, s[44:47], s29 offen offset:3072 lds\n"
    "v_mfma_f32_16x16x32_bf16 v[152:155], v[48:51], a[28:31], v[152:155]\n"
    "ds_read_b64_tr_b16 v[40:41], v12 offset:16384\n"
    "s_mov_b32 s88, 114688\n"
    "v_mfma_f32_16x16x32_bf16 v[160:163], v[48:51], a[92:95], v[160:163]\n"
    "ds_read_b64_tr_b16 v[42:43], v12 offset:18432\n"
    "v_mfma_f32_16x16x32_bf16 v[168:171], v[48:51], a[156:159], v[168:171]\n"
    "v_mfma_f32_16x16x32_bf16 v[176:179], v[48:51], a[220:223], v[176:179]\n"
    "v_mfma_f32_16x16x32_bf16 v[156:159], v[52:55], a[28:31], v[156:159]\n"
    "ds_read_b64_tr_b16 v[44:45], v13 offset:16384\n"
    "buffer_load_dwordx4 a[56:59], v234, s[40:43], s88 offen offset:0 sc1\n"
    "v_mfma_f32_16x16x32_bf16 v[164:167], v[52:55], a[92:95], v[164:167]\n"
    "ds_read_b64_tr_b16 v[46:47], v13 offset:18432\n"
    "v_mfma_f32_16x16x32_bf16 v[172:175], v[52:55], a[156:159], v[172:175]\n"
    "v_mfma_f32_16x16x32_bf16 v[180:183], v[52:55], a[220:223], v[180:183]\n"
    "s_waitcnt vmcnt(29)\n"
    "s_waitcnt lgkmcnt(8)\n"
    "v_mfma_f32_16x16x32_bf16 v[56:59], v[24:27], a[32:35], v[56:59]\n"
    "ds_read_b64_tr_b16 v[48:49], v14 offset:16384\n"
    "buffer_load_dwordx4 a[120:123], v234, s[40:43], s88 offen offset:1024 sc1\n"
    "v_mov_b32 v184, 0\n"
    "v_mfma_f32_16x16x32_bf16 v[64:67], v[24:27], a[96:99], v[64:67]\n"
    "ds_read_b64_tr_b16 v[50:51], v14 offset:18432\n"
    "v_mov_b32 v185, 0\n"
    "v_mfma_f32_16x16x32_bf16 v[72:75], v[24:27], a[160:163], v[72:75]\n"
    "v_mov_b32 v186, 0\n"
    "v_mfma_f32_16x16x32_bf16 v[80:83], v[24:27], a[224:227], v[80:83]\n"
    "v_mov_b32 v187, 0\n"
    "v_mfma_f32_16x16x32_bf16 v[60:63], v[28:31], a[32:35], v[60:63]\n"
    "ds_read_b64_tr_b16 v[52:53], v15 offset:16384\n"
    "v_mov_b32 v188, 0\n"
    "v_mfma_f32_16x16x32_bf16 v[68:71], v[28:31], a[96:99], v[68:71]\n"
    "ds_read_b64_tr_b16 v[54:55], v15 offset:18432\n"
    "buffer_load_dwordx4 a[184:187], v235, s[40:43], s88 offen offset:0 sc1\n"
    "v_mov_b32 v189, 0\n"
    "v_mfma_f32_16x16x32_bf16 v[76:79], v[28:31], a[160:163], v[76:79]\n"
    "v_mov_b32 v190, 0\n"
    "v_mov_b32 v191, 0\n"
    "v_mfma_f32_16x16x32_bf16 v[84:87], v[28:31], a[224:227], v[84:87]\n"
    "v_mov_b32 v192, 0\n"
    "s_waitcnt lgkmcnt(8)\n"
    "v_mfma_f32_16x16x32_bf16 v[88:91], v[32:35], a[32:35], v[88:91]\n"
    "ds_read_b64_tr_b16 v[24:25], v12 offset:4096\n"
    "v_mov_b32 v193, 0\n"
    "v_mfma_f32_16x16x32_bf16 v[96:99], v[32:35], a[96:99], v[96:99]\n"
    "ds_read_b64_tr_b16 v[26:27], v12 offset:6144\n"
    "buffer_load_dwordx4 a[248:251], v235, s[40:43], s88 offen offset:1024 sc1\n"
    "v_mov_b32 v194, 0\n"
    "v_mfma_f32_16x16x32_bf16 v[104:107], v[32:35], a[160:163], v[104:107]\n"
    "s_mov_b32 s88, 122880\n"
    "v_mov_b32 v195, 0\n"
    "v_mfma_f32_16x16x32_bf16 v[112:115], v[32:35], a[224:227], v[112:115]\n"
    "v_mov_b32 v196, 0\n"
    "v_mfma_f32_16x16x32_bf16 v[92:95], v[36:39], a[32:35], v[92:95]\n"
    "ds_read_b64_tr_b16 v[28:29], v13 offset:4096\n"
    "v_mov_b32 v197, 0\n"
    "v_mfma_f32_16x16x32_bf16 v[100:103], v[36:39], a[96:99], v[100:103]\n"
    "ds_read_b64_tr_b16 v[30:31], v13 offset:6144\n"
    "v_mov_b32 v198, 0\n"
    "v_mov_b32 v199, 0\n"
    "v_mfma_f32_16x16x32_bf16 v[108:111], v[36:39], a[160:163], v[108:111]\n"
    "buffer_load_dwordx4 a[60:63], v234, s[40:43], s88 offen offset:0 sc1\n"
    "v_mov_b32 v200, 0\n"
    "v_mfma_f32_16x16x32_bf16 v[116:119], v[36:39], a[224:227], v[116:119]\n"
    "v_mov_b32 v201, 0\n"
    "s_waitcnt lgkmcnt(8)\n"
    "v_mfma_f32_16x16x32_bf16 v[120:123], v[40:43], a[32:35], v[120:123]\n"
    "ds_read_b64_tr_b16 v[32:33], v14 offset:4096\n"
    "v_mov_b32 v202, 0\n"
    "v_mfma_f32_16x16x32_bf16 v[128:131], v[40:43], a[96:99], v[128:131]\n"
    "ds_read_b64_tr_b16 v[34:35], v14 offset:6144\n"
    "v_mov_b32 v203, 0\n"
    "v_mfma_f32_16x16x32_bf16 v[136:139], v[40:43], a[160:163], v[136:139]\n"
    "buffer_load_dwordx4 a[124:127], v234, s[40:43], s88 offen offset:1024 sc1\n"
    "v_mov_b32 v204, 0\n"
    "v_mfma_f32_16x16x32_bf16 v[144:147], v[40:43], a[224:227], v[144:147]\n"
    "v_mov_b32 v205, 0\n"
    "v_mfma_f32_16x16x32_bf16 v[124:127], v[44:47], a[32:35], v[124:127]\n"
    "ds_read_b64_tr_b16 v[36:37], v15 offset:4096\n"
    "v_mov_b32 v206, 0\n"
    "v_mov_b32 v207, 0\n"
    "v_mfma_f32_16x16x32_bf16 v[132:135], v[44:47], a[96:99], v[132:135]\n"
    "ds_read_b64_tr_b16 v[38:39], v15 offset:6144\n"
    "v_mov_b32 v208, 0\n"
    "v_mfma_f32_16x16x32_bf16 v[140:143], v[44:47], a[160:163], v[140:143]\n"
    "v_mov_b32 v209, 0\n"
    "v_mfma_f32_16x16x32_bf16 v[148:151], v[44:47], a[224:227], v[148:151]\n"
    "buffer_load_dwordx4 a[188:191], v235, s[40:43], s88 offen offset:0 sc1\n"
    "v_mov_b32 v210, 0\n"
    "s_waitcnt lgkmcnt(8)\n"
    "v_mfma_f32_16x16x32_bf16 v[152:155], v[48:51], a[32:35], v[152:155]\n"
    "ds_read_b64_tr_b16 v[40:41], v12 offset:20480\n"
    "v_mov_b32 v211, 0\n"
    "v_mfma_f32_16x16x32_bf16 v[160:163], v[48:51], a[96:99], v[160:163]\n"
    "ds_read_b64_tr_b16 v[42:43], v12 offset:22528\n"
    "v_mov_b32 v212, 0\n"
    "v_mfma_f32_16x16x32_bf16 v[168:171], v[48:51], a[160:163], v[168:171]\n"
    "v_mov_b32 v213, 0\n"
    "v_mov_b32 v214, 0\n"
    "v_mfma_f32_16x16x32_bf16 v[176:179], v[48:51], a[224:227], v[176:179]\n"
    "buffer_load_dwordx4 a[252:255], v235, s[40:43], s88 offen offset:1024 sc1\n"
    "v_mov_b32 v215, 0\n"
    "v_mfma_f32_16x16x32_bf16 v[156:159], v[52:55], a[32:35], v[156:159]\n"
    "ds_read_b64_tr_b16 v[44:45], v13 offset:20480\n"
    "v_min_u32 v232, 15, v16\n"
    "v_and_b32 v216, 15, v16\n"
    "v_mfma_f32_16x16x32_bf16 v[164:167], v[52:55], a[96:99], v[164:167]\n"
    "ds_read_b64_tr_b16 v[46:47], v13 offset:22528\n"
    "v_lshlrev_b32 v232, 2, v232\n"
    "v_lshrrev_b32 v217, 4, v16\n"
    "v_mfma_f32_16x16x32_bf16 v[172:175], v[52:55], a[160:163], v[172:175]\n"
    "v_add_u32 v232, s85, v232\n"
    "v_lshlrev_b32 v217, 2, v217\n"
    "v_mfma_f32_16x16x32_bf16 v[180:183], v[52:55], a[224:227], v[180:183]\n"
    "s_nop 0\n"
    "v_sub_u32 v216, v216, v217\n"
    "s_waitcnt vmcnt(32)\n"
    "s_waitcnt lgkmcnt(8)\n"
    "v_mfma_f32_16x16x32_bf16 v[56:59], v[24:27], a[36:39], v[56:59]\n"
    "ds_read_b64_tr_b16 v[48:49], v14 offset:20480\n"
    "s_nop 0\n"
    "v_mov_b32 v217, 0xff800000\n"
    "v_mfma_f32_16x16x32_bf16 v[64:67], v[24:27], a[100:103], v[64:67]\n"
    "ds_read_b64_tr_b16 v[50:51], v14 offset:22528\n"
    "s_nop 0\n"
    "v_mov_b32 v218, 0x7f800000\n"
    "v_cmp_gt_i32 vcc, 0, v216\n"
    "v_mfma_f32_16x16x32_bf16 v[72:75], v[24:27], a[164:167], v[72:75]\n"
    "s_nop 0\n"
    "s_nop 1\n"
    "v_mfma_f32_16x16x32_bf16 v[80:83], v[24:27], a[228:231], v[80:83]\n"
    "s_nop 0\n"
    "v_cndmask_b32 v244, v218, v217, vcc\n"
    "v_mfma_f32_16x16x32_bf16 v[60:63], v[28:31], a[36:39], v[60:63]\n"
    "ds_read_b64_tr_b16 v[52:53], v15 offset:20480\n"
    "s_nop 0\n"
    "v_cmp_gt_i32 vcc, 1, v216\n"
    "v_mfma_f32_16x16x32_bf16 v[68:71], v[28:31], a[100:103], v[68:71]\n"
    "ds_read_b64_tr_b16 v[54:55], v15 offset:22528\n"
    "s_nop 0\n"
    "s_nop 1\n"
    "v_mfma_f32_16x16x32_bf16 v[76:79], v[28:31], a[164:167], v[76:79]\n"
    "s_nop 0\n"
    "v_cndmask_b32 v245, v218, v217, vcc\n"
    "v_mfma_f32_16x16x32_bf16 v[84:87], v[28:31], a[228:231], v[84:87]\n"
    "s_nop 0\n"
    "v_cmp_gt_i32 vcc, 2, v216\n"
    "s_waitcnt lgkmcnt(8)\n"
    "v_mfma_f32_16x16x32_bf16 v[88:91], v[32:35], a[36:39], v[88:91]\n"
    "ds_read_b64_tr_b16 v[24:25], v12 offset:8192\n"
    "s_nop 0\n"
    "s_nop 1\n"
    "v_cndmask_b32 v246, v218, v217, vcc\n"
    "v_mfma_f32_16x16x32_bf16 v[96:99], v[32:35], a[100:103], v[96:99]\n"
    "ds_read_b64_tr_b16 v[26:27], v12 offset:10240\n"
    "s_nop 0\n"
    "v_cmp_gt_i32 vcc, 3, v216\n"
    "v_mfma_f32_16x16x32_bf16 v[104:107], v[32:35], a[164:167], v[104:107]\n"
    "s_nop 0\n"
    "s_nop 1\n"
    "v_mfma_f32_16x16x32_bf16 v[112:115], v[32:35], a[228:231], v[112:115]\n"
    "s_nop 0\n"
    "v_cndmask_b32 v247, v218, v217, vcc\n"
    "v_mfma_f32_16x16x32_bf16 v[92:95], v[36:39], a[36:39], v[92:95]\n"
    "ds_read_b64_tr_b16 v[28:29], v13 offset:8192\n"
    "s_nop 0\n"
    "v_mov_b32 v216, 0x3f803f80\n"
    "v_mfma_f32_16x16x32_bf16 v[100:103], v[36:39], a[100:103], v[100:103]\n"
    "ds_read_b64_tr_b16 v[30:31], v13 offset:10240\n"
    "s_nop 0\n"
    "v_accvgpr_write_b32 a0, 0\n"
    "v_mfma_f32_16x16x32_bf16 v[108:111], v[36:39], a[164:167], v[108:111]\n"
    "s_nop 0\n"
    "v_accvgpr_write_b32 a1, 0\n"
    "v_accvgpr_write_b32 a2, 0\n"
    "v_mfma_f32_16x16x32_bf16 v[116:119], v[36:39], a[228:231], v[116:119]\n"
    "s_nop 0\n"
    "v_accvgpr_write_b32 a3, 0\n"
    "s_waitcnt lgkmcnt(8)\n"
    "v_mfma_f32_16x16x32_bf16 v[120:123], v[40:43], a[36:39], v[120:123]\n"
    "ds_read_b64_tr_b16 v[32:33], v14 offset:8192\n"
    "s_nop 0\n"
    "v_accvgpr_write_b32 a4, 0\n"
    "v_mfma_f32_16x16x32_bf16 v[128:131], v[40:43], a[100:103], v[128:131]\n"
    "ds_read_b64_tr_b16 v[34:35], v14 offset:10240\n"
    "s_nop 0\n"
    "v_accvgpr_write_b32 a5, 0\n"
    "v_mfma_f32_16x16x32_bf16 v[136:139], v[40:43], a[164:167], v[136:139]\n"
    "s_nop 0\n"
    "v_accvgpr_write_b32 a6, 0\n"
    "v_mfma_f32_16x16x32_bf16 v[144:147], v[40:43], a[228:231], v[144:147]\n"
    "s_nop 0\n"
    "v_accvgpr_write_b32 a7, 0\n"
    "v_mfma_f32_16x16x32_bf16 v[124:127], v[44:47], a[36:39], v[124:127]\n"
    "ds_read_b64_tr_b16 v[36:37], v15 offset:8192\n"
    "s_nop 0\n"
    "v_accvgpr_write_b32 a8, 0\n"
    "v_mfma_f32_16x16x32_bf16 v[132:135], v[44:47], a[100:103], v[132:135]\n"
    "ds_read_b64_tr_b16 v[38:39], v15 offset:10240\n"
    "s_nop 0\n"
    "v_accvgpr_write_b32 a9, 0\n"
    "v_accvgpr_write_b32 a10, 0\n"
    "v_mfma_f32_16x16x32_bf16 v[140:143], v[44:47], a[164:167], v[140:143]\n"
    "s_nop 0\n"
    "v_accvgpr_write_b32 a11, 0\n"
    "v_mfma_f32_16x16x32_bf16 v[148:151], v[44:47], a[228:231], v[148:151]\n"
    "s_nop 0\n"
    "v_accvgpr_write_b32 a12, 0\n"
    "s_waitcnt lgkmcnt(8)\n"
    "v_mfma_f32_16x16x32_bf16 v[152:155], v[48:51], a[36:39], v[152:155]\n"
    "ds_read_b64_tr_b16 v[40:41], v12 offset:24576\n"
    "s_nop 0\n"
    "v_accvgpr_write_b32 a13, 0\n"
    "v_mfma_f32_16x16x32_bf16 v[160:163], v[48:51], a[100:103], v[160:163]\n"
    "ds_read_b64_tr_b16 v[42:43], v12 offset:26624\n"
    "s_nop 0\n"
    "v_accvgpr_write_b32 a14, 0\n"
    "v_mfma_f32_16x16x32_bf16 v[168:171], v[48:51], a[164:167], v[168:171]\n"
    "s_nop 0\n"
    "v_accvgpr_write_b32 a15, 0\n"
    "v_mfma_f32_16x16x32_bf16 v[176:179], v[48:51], a[228:231], v[176:179]\n"
    "s_nop 0\n"
    "v_accvgpr_write_b32 a16, 0\n"
    "v_mfma_f32_16x16x32_bf16 v[156:159], v[52:55], a[36:39], v[156:159]\n"
    "ds_read_b64_tr_b16 v[44:45], v13 offset:24576\n"
    "s_nop 0\n"
    "v_accvgpr_write_b32 a17, 0\n"
    "v_accvgpr_write_b32 a18, 0\n"
    "v_mfma_f32_16x16x32_bf16 v[164:167], v[52:55], a[100:103], v[164:167]\n"
    "ds_read_b64_tr_b16 v[46:47], v13 offset:26624\n"
    "s_nop 0\n"
    "v_accvgpr_write_b32 a19, 0\n"
    "v_mfma_f32_16x16x32_bf16 v[172:175], v[52:55], a[164:167], v[172:175]\n"
    "s_nop 0\n"
    "v_accvgpr_write_b32 a20, 0\n"
    "v_mfma_f32_16x16x32_bf16 v[180:183], v[52:55], a[228:231], v[180:183]\n"
    "s_nop 0\n"
    "v_accvgpr_write_b32 a21, 0\n"
    "s_waitcnt vmcnt(24)\n"
    "s_waitcnt lgkmcnt(8)\n"
    "v_mfma_f32_16x16x32_bf16 v[56:59], v[24:27], a[40:43], v[56:59]\n"
    "ds_read_b64_tr_b16 v[48:49], v14 offset:24576\n"
    "s_nop 0\n"
    "v_accvgpr_write_b32 a22, 0\n"
    "v_mfma_f32_16x16x32_bf16 v[64:67], v[24:27], a[104:107], v[64:67]\n"
    "ds_read_b64_tr_b16 v[50:51], v14 offset:26624\n"
    "s_nop 0\n"
    "v_accvgpr_write_b32 a23, 0\n"
    "v_mfma_f32_16x16x32_bf16 v[72:75], v[24:27], a[168:171], v[72:75]\n"
    "s_nop 0\n"
    "v_accvgpr_write_b32 a24, 0\n"
    "v_accvgpr_write_b32 a25, 0\n"
    "v_mfma_f32_16x16x32_bf16 v[80:83], v[24:27], a[232:235], v[80:83]\n"
    "s_nop 0\n"
    "v_accvgpr_write_b32 a26, 0\n"
    "v_mfma_f32_16x16x32_bf16 v[60:63], v[28:31], a[40:43], v[60:63]\n"
    "ds_read_b64_tr_b16 v[52:53], v15 offset:24576\n"
    "s_nop 0\n"
    "v_accvgpr_write_b32 a27, 0\n"
    "v_mfma_f32_16x16x32_bf16 v[68:71], v[28:31], a[104:107], v[68:71]\n"
    "ds_read_b64_tr_b16 v[54:55], v15 offset:26624\n"
    "s_nop 0\n"
    "v_accvgpr_write_b32 a28, 0\n"
    "v_mfma_f32_16x16x32_bf16 v[76:79], v[28:31], a[168:171], v[76:79]\n"
    "s_nop 0\n"
    "v_accvgpr_write_b32 a29, 0\n"
    "v_mfma_f32_16x16x32_bf16 v[84:87], v[28:31], a[232:235], v[84:87]\n"
    "global_load_dword v233, v232, s[60:61] sc1\n"
    "v_accvgpr_write_b32 a30, 0\n"
    "s_waitcnt lgkmcnt(8)\n"
    "v_mfma_f32_16x16x32_bf16 v[88:91], v[32:35], a[40:43], v[88:91]\n"
    "ds_read_b64_tr_b16 v[24:25], v12 offset:12288\n"
    "v_accvgpr_write_b32 a31, 0\n"
    "v_mfma_f32_16x16x32_bf16 v[96:99], v[32:35], a[104:107], v[96:99]\n"
    "ds_read_b64_tr_b16 v[26:27], v12 offset:14336\n"
    "v_accvgpr_write_b32 a192, 0\n"
    "v_accvgpr_write_b32 a193, 0\n"
    "v_mfma_f32_16x16x32_bf16 v[104:107], v[32:35], a[168:171], v[104:107]\n"
    "v_accvgpr_write_b32 a194, 0\n"
    "v_mfma_f32_16x16x32_bf16 v[112:115], v[32:35], a[232:235], v[112:115]\n"
    "v_accvgpr_write_b32 a195, 0\n"
    "v_mfma_f32_16x16x32_bf16 v[92:95], v[36:39], a[40:43], v[92:95]\n"
    "ds_read_b64_tr_b16 v[28:29], v13 offset:12288\n"
    "v_accvgpr_write_b32 a196, 0\n"
    "v_mfma_f32_16x16x32_bf16 v[100:103], v[36:39], a[104:107], v[100:103]\n"
    "ds_read_b64_tr_b16 v[30:31], v13 offset:14336\n"
    "v_accvgpr_write_b32 a197, 0\n"
    "v_mfma_f32_16x16x32_bf16 v[108:111], v[36:39], a[168:171], v[108:111]\n"
    "v_accvgpr_write_b32 a198, 0\n"
    "v_mfma_f32_16x16x32_bf16 v[116:119], v[36:39], a[232:235], v[116:119]\n"
    "v_accvgpr_write_b32 a199, 0\n"
    "s_waitcnt lgkmcnt(8)\n"
    "v_mfma_f32_16x16x32_bf16 v[120:123], v[40:43], a[40:43], v[120:123]\n"
    "ds_read_b64_tr_b16 v[32:33], v14 offset:12288\n"
    "v_accvgpr_write_b32 a200, 0\n"
    "v_accvgpr_write_b32 a201, 0\n"
    "v_mfma_f32_16x16x32_bf16 v[128:131], v[40:43], a[104:107], v[128:131]\n"
    "ds_read_b64_tr_b16 v[34:35], v14 offset:14336\n"
    "v_accvgpr_write_b32 a202, 0\n"
    "v_mfma_f32_16x16x32_bf16 v[136:139], v[40:43], a[168:171], v[136:139]\n"
    "v_accvgpr_write_b32 a203, 0\n"
    "v_mfma_f32_16x16x32_bf16 v[144:147], v[40:43], a[232:235], v[144:147]\n"
    "v_accvgpr_write_b32 a204, 0\n"
    "v_mfma_f32_16x16x32_bf16 v[124:127], v[44:47], a[40:43], v[124:127]\n"
    "ds_read_b64_tr_b16 v[36:37], v15 offset:12288\n"
    "v_accvgpr_write_b32 a205, 0\n"
    "v_mfma_f32_16x16x32_bf16 v[132:135], v[44:47], a[104:107], v[132:135]\n"
    "ds_read_b64_tr_b16 v[38:39], v15 offset:14336\n"
    "v_accvgpr_write_b32 a206, 0\n"
    "v_mfma_f32_16x16x32_bf16 v[140:143], v[44:47], a[168:171], v[140:143]\n"
    "v_accvgpr_write_b32 a207, 0\n"
    "v_accvgpr_write_b32 a208, v216\n"
    "v_mfma_f32_16x16x32_bf16 v[148:151], v[44:47], a[232:235], v[148:151]\n"
    "v_accvgpr_write_b32 a209, v216\n"
    "s_waitcnt lgkmcnt(8)\n"
    "v_mfma_f32_16x16x32_bf16 v[152:155], v[48:51], a[40:43], v[152:155]\n"
    "ds_read_b64_tr_b16 v[40:41], v12 offset:28672\n"
    "v_accvgpr_write_b32 a210, v216\n"
    "v_mfma_f32_16x16x32_bf16 v[160:163], v[48:51], a[104:107], v[160:163]\n"
    "ds_read_b64_tr_b16 v[42:43], v12 offset:30720\n"
    "v_accvgpr_write_b32 a211, v216\n"
    "v_mfma_f32_16x16x32_bf16 v[168:171], v[48:51], a[168:171], v[168:171]\n"
    "v_mfma_f32_16x16x32_bf16 v[176:179], v[48:51], a[232:235], v[176:179]\n"
    "v_mfma_f32_16x16x32_bf16 v[156:159], v[52:55], a[40:43], v[156:159]\n"
    "ds_read_b64_tr_b16 v[44:45], v13 offset:28672\n"
    "v_mfma_f32_16x16x32_bf16 v[164:167], v[52:55], a[104:107], v[164:167]\n"
    "ds_read_b64_tr_b16 v[46:47], v13 offset:30720\n"
    "v_mfma_f32_16x16x32_bf16 v[172:175], v[52:55], a[168:171], v[172:175]\n"
    "v_mfma_f32_16x16x32_bf16 v[180:183], v[52:55], a[232:235], v[180:183]\n"
    "s_waitcnt vmcnt(21)\n"
    "s_waitcnt lgkmcnt(8)\n"
    "v_mfma_f32_16x16x32_bf16 v[56:59], v[24:27], a[44:47], v[56:59]\n"
    "ds_read_b64_tr_b16 v[48:49], v14 offset:28672\n"
    "v_mfma_f32_16x16x32_bf16 v[64:67], v[24:27], a[108:111], v[64:67]\n"
    "ds_read_b64_tr_b16 v[50:51], v14 offset:30720\n"
    "v_mfma_f32_16x16x32_bf16 v[72:75], v[24:27], a[172:175], v[72:75]\n"
    "v_mfma_f32_16x16x32_bf16 v[80:83], v[24:27], a[236:239], v[80:83]\n"
    "v_mfma_f32_16x16x32_bf16 v[60:63], v[28:31], a[44:47], v[60:63]\n"
    "ds_read_b64_tr_b16 v[52:53], v15 offset:28672\n"
    "v_mfma_f32_16x16x32_bf16 v[68:71], v[28:31], a[108:111], v[68:71]\n"
    "ds_read_b64_tr_b16 v[54:55], v15 offset:30720\n"
    "v_mfma_f32_16x16x32_bf16 v[76:79], v[28:31], a[172:175], v[76:79]\n"
    "v_mfma_f32_16x16x32_bf16 v[84:87], v[28:31], a[236:239], v[84:87]\n"
    "s_waitcnt lgkmcnt(8)\n"
    "v_mfma_f32_16x16x32_bf16 v[88:91], v[32:35], a[44:47], v[88:91]\n"
    "v_mfma_f32_16x16x32_bf16 v[96:99], v[32:35], a[108:111], v[96:99]\n"
    "v_mfma_f32_16x16x32_bf16 v[104:107], v[32:35], a[172:175], v[104:107]\n"
    "v_mfma_f32_16x16x32_bf16 v[112:115], v[32:35], a[236:239], v[112:115]\n"
    "v_mfma_f32_16x16x32_bf16 v[92:95], v[36:39], a[44:47], v[92:95]\n"
    "v_mfma_f32_16x16x32_bf16 v[100:103], v[36:39], a[108:111], v[100:103]\n"
    "v_mfma_f32_16x16x32_bf16 v[108:111], v[36:39], a[172:175], v[108:111]\n"
    "v_mfma_f32_16x16x32_bf16 v[116:119], v[36:39], a[236:239], v[116:119]\n"
    "s_waitcnt vmcnt(0)\n"
    "v_cmp_ne_u32 vcc, s94, v233\n"
    "s_mov_b32 s30, 0x400000\n"
    "s_nop 1\n"
    "s_cbranch_vccz L_pkv_%=_x\n"
    "L_pkv_%=:\n"
    "s_sleep 2\n"
    "global_load_dword v233, v232, s[60:61] sc1\n"
    "s_waitcnt vmcnt(0)\n"
    "v_cmp_ne_u32 vcc, s94, v233\n"
    "s_sub_u32 s30, s30, 1\n"
    "s_cbranch_scc1 L_pkv_%=_x\n"
    "s_nop 0\n"
    "s_cbranch_vccnz L_pkv_%=\n"
    "L_pkv_%=_x:\n"
    "s_waitcnt vmcnt(0) lgkmcnt(0)\n"
    "s_barrier\n"
    "v_mfma_f32_16x16x32_bf16 v[120:123], v[40:43], a[44:47], v[120:123]\n"
    "ds_read_b64_tr_b16 v[24:25], v12 offset:32768\n"
    "s_mov_b32 s99, 0\n"
    "v_mfma_f32_16x16x32_bf16 v[128:131], v[40:43], a[108:111], v[128:131]\n"
    "ds_read_b64_tr_b16 v[26:27], v12 offset:34816\n"
    "s_add_u32 m0, s28, 32768\n"
    "v_mfma_f32_16x16x32_bf16 v[136:139], v[40:43], a[172:175], v[136:139]\n"
    "ds_read_b64_tr_b16 v[28:29], v13 offset:32768\n"
    "buffer_load_dwordx4 v20, s[48:51], s99 offen offset:0 sc1 lds\n"
    "v_mfma_f32_16x16x32_bf16 v[144:147], v[40:43], a[236:239], v[144:147]\n"
    "ds_read_b64_tr_b16 v[30:31], v13 offset:34816\n"
    "v_mfma_f32_16x16x32_bf16 v[124:127], v[44:47], a[44:47], v[124:127]\n"
    "ds_read_b64_tr_b16 v[32:33], v14 offset:32768\n"
    "v_mfma_f32_16x16x32_bf16 v[132:135], v[44:47], a[108:111], v[132:135]\n"
    "ds_read_b64_tr_b16 v[34:35], v14 offset:34816\n"
    "v_mfma_f32_16x16x32_bf16 v[140:143], v[44:47], a[172:175], v[140:143]\n"
    "ds_read_b64_tr_b16 v[36:37], v15 offset:32768\n"
    "buffer_load_dwordx4 v20, s[48:51], s99 offen offset:1024 sc1 lds\n"
    "v_mfma_f32_16x16x32_bf16 v[148:151], v[44:47], a[236:239], v[148:151]\n"
    "ds_read_b64_tr_b16 v[38:39], v15 offset:34816\n"
    "v_mfma_f32_16x16x32_bf16 v[152:155], v[48:51], a[44:47], v[152:155]\n"
    "ds_read_b64_tr_b16 v[40:41], v12 offset:49152\n"
    "v_mfma_f32_16x16x32_bf16 v[160:163], v[48:51], a[108:111], v[160:163]\n"
    "ds_read_b64_tr_b16 v[42:43], v12 offset:51200\n"
    "v_mfma_f32_16x16x32_bf16 v[168:171], v[48:51], a[172:175], v[168:171]\n"
    "v_mfma_f32_16x16x32_bf16 v[176:179], v[48:51], a[236:239], v[176:179]\n"
    "buffer_load_dwordx4 v20, s[48:51], s99 offen offset:2048 sc1 lds\n"
    "v_mfma_f32_16x16x32_bf16 v[156:159], v[52:55], a[44:47], v[156:159]\n"
    "ds_read_b64_tr_b16 v[44:45], v13 offset:49152\n"
    "v_mfma_f32_16x16x32_bf16 v[164:167], v[52:55], a[108:111], v[164:167]\n"
    "ds_read_b64_tr_b16 v[46:47], v13 offset:51200\n"
    "v_mfma_f32_16x16x32_bf16 v[172:175], v[52:55], a[172:175], v[172:175]\n"
    "v_mfma_f32_16x16x32_bf16 v[180:183], v[52:55], a[236:239], v[180:183]\n"
    "buffer_load_dwordx4 v20, s[48:51], s99 offen offset:3072 sc1 lds\n"
    "s_waitcnt lgkmcnt(8)\n"
    "v_mfma_f32_16x16x32_bf16 v[56:59], v[24:27], a[48:51], v[56:59]\n"
    "ds_read_b64_tr_b16 v[48:49], v14 offset:49152\n"
    "s_mov_b32 s99, 16384\n"
    "v_mfma_f32_16x16x32_bf16 v[64:67], v[24:27], a[112:115], v[64:67]\n"
    "ds_read_b64_tr_b16 v[50:51], v14 offset:51200\n"
    "s_add_u32 m0, s28, 0\n"
    "v_mfma_f32_16x16x32_bf16 v[72:75], v[24:27], a[176:179], v[72:75]\n"
    "v_mfma_f32_16x16x32_bf16 v[80:83], v[24:27], a[240:243], v[80:83]\n"
    "v_mfma_f32_16x16x32_bf16 v[60:63], v[28:31], a[48:51], v[60:63]\n"
    "ds_read_b64_tr_b16 v[52:53], v15 offset:49152\n"
    "buffer_load_dwordx4 v20, s[48:51], s99 offen offset:0 sc1 lds\n"
    "v_mfma_f32_16x16x32_bf16 v[68:71], v[28:31], a[112:115], v[68:71]\n"
    "ds_read_b64_tr_b16 v[54:55], v15 offset:51200\n"
    "v_accvgpr_write_b32 a32, 0\n"
    "v_mfma_f32_16x16x32_bf16 v[76:79], v[28:31], a[176:179], v[76:79]\n"
    "v_mfma_f32_16x16x32_bf16 v[84:87], v[28:31], a[240:243], v[84:87]\n"
    "s_waitcnt lgkmcnt(8)\n"
    "v_mfma_f32_16x16x32_bf16 v[88:91], v[32:35], a[48:51], v[88:91]\n"
    "ds_read_b64_tr_b16 v[24:25], v12 offset:36864\n"
    "buffer_load_dwordx4 v20, s[48:51], s99 offen offset:1024 sc1 lds\n"
    "v_mfma_f32_16x16x32_bf16 v[96:99], v[32:35], a[112:115], v[96:99]\n"
    "ds_read_b64_tr_b16 v[26:27], v12 offset:38912\n"
    "v_mfma_f32_16x16x32_bf16 v[104:107], v[32:35], a[176:179], v[104:107]\n"
    "v_mfma_f32_16x16x32_bf16 v[112:115], v[32:35], a[240:243], v[112:115]\n"
    "v_accvgpr_write_b32 a33, 0\n"
    "v_mfma_f32_16x16x32_bf16 v[92:95], v[36:39], a[48:51], v[92:95]\n"
    "ds_read_b64_tr_b16 v[28:29], v13 offset:36864\n"
    "v_mfma_f32_16x16x32_bf16 v[100:103], v[36:39], a[112:115], v[100:103]\n"
    "ds_read_b64_tr_b16 v[30:31], v13 offset:38912\n"
    "buffer_load_dwordx4 v20, s[48:51], s99 offen offset:2048 sc1 lds\n"
    "v_mfma_f32_16x16x32_bf16 v[108:111], v[36:39], a[176:179], v[108:111]\n"
    "v_mfma_f32_16x16x32_bf16 v[116:119], v[36:39], a[240:243], v[116:119]\n"
    "s_waitcnt lgkmcnt(8)\n"
    "v_mfma_f32_16x16x32_bf16 v[120:123], v[40:43], a[48:51], v[120:123]\n"
    "ds_read_b64_tr_b16 v[32:33], v14 offset:36864\n"
    "v_accvgpr_write_b32 a34, 0\n"
    "v_mfma_f32_16x16x32_bf16 v[128:131], v[40:43], a[112:115], v[128:131]\n"
    "ds_read_b64_tr_b16 v[34:35], v14 offset:38912\n"
    "buffer_load_dwordx4 v20, s[48:51], s99 offen offset:3072 sc1 lds\n"
    "v_mfma_f32_16x16x32_bf16 v[136:139], v[40:43], a[176:179], v[136:139]\n"
    "s_mov_b32 s99, 0\n"
    "v_mfma_f32_16x16x32_bf16 v[144:147], v[40:43], a[240:243], v[144:147]\n"
    "s_add_u32 m0, s28, 65536\n"
    "v_mfma_f32_16x16x32_bf16 v[124:127], v[44:47], a[48:51], v[124:127]\n"
    "ds_read_b64_tr_b16 v[36:37], v15 offset:36864\n"
    "v_mfma_f32_16x16x32_bf16 v[132:135], v[44:47], a[112:115], v[132:135]\n"
    "ds_read_b64_tr_b16 v[38:39], v15 offset:38912\n"
    "v_mfma_f32_16x16x32_bf16 v[140:143], v[44:47], a[176:179], v[140:143]\n"
    "buffer_load_dwordx4 v20, s[52:55], s99 offen offset:0 sc1 lds\n"
    "v_accvgpr_write_b32 a35, 0\n"
    "v_mfma_f32_16x16x32_bf16 v[148:151], v[44:47], a[240:243], v[148:151]\n"
    "s_waitcnt lgkmcnt(8)\n"
    "v_mfma_f32_16x16x32_bf16 v[152:155], v[48:51], a[48:51], v[152:155]\n"
    "ds_read_b64_tr_b16 v[40:41], v12 offset:53248\n"
    "v_mfma_f32_16x16x32_bf16 v[160:163], v[48:51], a[112:115], v[160:163]\n"
    "ds_read_b64_tr_b16 v[42:43], v12 offset:55296\n"
    "v_mfma_f32_16x16x32_bf16 v[168:171], v[48:51], a[176:179], v[168:171]\n"
    "buffer_load_dwordx4 v20, s[52:55], s99 offen offset:1024 sc1 lds\n"
    "v_mfma_f32_16x16x32_bf16 v[176:179], v[48:51], a[240:243], v[176:179]\n"
    "v_accvgpr_write_b32 a36, 0\n"
    "v_mfma_f32_16x16x32_bf16 v[156:159], v[52:55], a[48:51], v[156:159]\n"
    "ds_read_b64_tr_b16 v[44:45], v13 offset:53248\n"
    "v_mfma_f32_16x16x32_bf16 v[164:167], v[52:55], a[112:115], v[164:167]\n"
    "ds_read_b64_tr_b16 v[46:47], v13 offset:55296\n"
    "v_mfma_f32_16x16x32_bf16 v[172:175], v[52:55], a[176:179], v[172:175]\n"
    "v_mfma_f32_16x16x32_bf16 v[180:183], v[52:55], a[240:243], v[180:183]\n"
    "buffer_load_dwordx4 v20, s[52:55], s99 offen offset:2048 sc1 lds\n"
    "s_waitcnt lgkmcnt(8)\n"
    "v_mfma_f32_16x16x32_bf16 v[56:59], v[24:27], a[52:55], v[56:59]\n"
    "ds_read_b64_tr_b16 v[48:49], v14 offset:53248\n"
    "v_mfma_f32_16x16x32_bf16 v[64:67], v[24:27], a[116:119], v[64:67]\n"
    "ds_read_b64_tr_b16 v[50:51], v14 offset:55296\n"
    "v_accvgpr_write_b32 a37, 0\n"
    "v_mfma_f32_16x16x32_bf16 v[72:75], v[24:27], a[180:183], v[72:75]\n"
    "v_mfma_f32_16x16x32_bf16 v[80:83], v[24:27], a[244:247], v[80:83]\n"
    "buffer_load_dwordx4 v20, s[52:55], s99 offen offset:3072 sc1 lds\n"
    "v_mfma_f32_16x16x32_bf16 v[60:63], v[28:31], a[52:55], v[60:63]\n"
    "ds_read_b64_tr_b16 v[52:53], v15 offset:53248\n"
    "v_mfma_f32_16x16x32_bf16 v[68:71], v[28:31], a[116:119], v[68:71]\n"
    "ds_read_b64_tr_b16 v[54:55], v15 offset:55296\n"
    "v_mfma_f32_16x16x32_bf16 v[76:79], v[28:31], a[180:183], v[76:79]\n"
    "v_mfma_f32_16x16x32_bf16 v[84:87], v[28:31], a[244:247], v[84:87]\n"
    "v_accvgpr_write_b32 a38, 0\n"
    "s_waitcnt lgkmcnt(8)\n"
    "v_mfma_f32_16x16x32_bf16 v[88:91], v[32:35], a[52:55], v[88:91]\n"
    "ds_read_b64_tr_b16 v[24:25], v12 offset:40960\n"
    "v_mfma_f32_16x16x32_bf16 v[96:99], v[32:35], a[116:119], v[96:99]\n"
    "ds_read_b64_tr_b16 v[26:27], v12 offset:43008\n"
    "v_mfma_f32_16x16x32_bf16 v[104:107], v[32:35], a[180:183], v[104:107]\n"
    "v_mfma_f32_16x16x32_bf16 v[112:115], v[32:35], a[244:247], v[112:115]\n"
    "v_mfma_f32_16x16x32_bf16 v[92:95], v[36:39], a[52:55], v[92:95]\n"
    "ds_read_b64_tr_b16 v[28:29], v13 offset:40960\n"
    "v_accvgpr_write_b32 a39, 0\n"
    "v_mfma_f32_16x16x32_bf16 v[100:103], v[36:39], a[116:119], v[100:103]\n"
    "ds_read_b64_tr_b16 v[30:31], v13 offset:43008\n"
    "v_mfma_f32_16x16x32_bf16 v[108:111], v[36:39], a[180:183], v[108:111]\n"
    "v_mfma_f32_16x16x32_bf16 v[116:119], v[36:39], a[244:247], v[116:119]\n"
    "s_waitcnt lgkmcnt(8)\n"
    "v_mfma_f32_16x16x32_bf16 v[120:123], v[40:43], a[52:55], v[120:123]\n"
    "ds_read_b64_tr_b16 v[32:33], v14 offset:40960\n"
    "v_mfma_f32_16x16x32_bf16 v[128:131], v[40:43], a[116:119], v[128:131]\n"
    "ds_read_b64_tr_b16 v[34:35], v14 offset:43008\n"
    "v_mfma_f32_16x16x32_bf16 v[136:139], v[40:43], a[180:183], v[136:139]\n"
    "v_accvgpr_write_b32 a40, 0\n"
    "v_mfma_f32_16x16x32_bf16 v[144:147], v[40:43], a[244:247], v[144:147]\n"
    "v_mfma_f32_16x16x32_bf16 v[124:127], v[44:47], a[52:55], v[124:127]\n"
    "ds_read_b64_tr_b16 v[36:37], v15 offset:40960\n"
    "v_mfma_f32_16x16x32_bf16 v[132:135], v[44:47], a[116:119], v[132:135]\n"
    "ds_read_b64_tr_b16 v[38:39], v15 offset:43008\n"
    "v_mfma_f32_16x16x32_bf16 v[140:143], v[44:47], a[180:183], v[140:143]\n"
    "v_mfma_f32_16x16x32_bf16 v[148:151], v[44:47], a[244:247], v[148:151]\n"
    "v_accvgpr_write_b32 a41, 0\n"
    "s_waitcnt lgkmcnt(8)\n"
    "v_mfma_f32_16x16x32_bf16 v[152:155], v[48:51], a[52:55], v[152:155]\n"
    "ds_read_b64_tr_b16 v[40:41], v12 offset:57344\n"
    "v_mfma_f32_16x16x32_bf16 v[160:163], v[48:51], a[116:119], v[160:163]\n"
    "ds_read_b64_tr_b16 v[42:43], v12 offset:59392\n"
    "v_mfma_f32_16x16x32_bf16 v[168:171], v[48:51], a[180:183], v[168:171]\n"
    "v_mfma_f32_16x16x32_bf16 v[176:179], v[48:51], a[244:247], v[176:179]\n"
    "v_mfma_f32_16x16x32_bf16 v[156:159], v[52:55], a[52:55], v[156:159]\n"
    "ds_read_b64_tr_b16 v[44:45], v13 offset:57344\n"
    "v_mfma_f32_16x16x32_bf16 v[164:167], v[52:55], a[116:119], v[164:167]\n"
    "ds_read_b64_tr_b16 v[46:47], v13 offset:59392\n"
    "v_accvgpr_write_b32 a42, 0\n"
    "v_mfma_f32_16x16x32_bf16 v[172:175], v[52:55], a[180:183], v[172:175]\n"
    "v_mfma_f32_16x16x32_bf16 v[180:183], v[52:55], a[244:247], v[180:183]\n"
    "s_waitcnt lgkmcnt(8)\n"
    "v_mfma_f32_16x16x32_bf16 v[56:59], v[24:27], a[56:59], v[56:59]\n"
    "ds_read_b64_tr_b16 v[48:49], v14 offset:57344\n"
    "v_mfma_f32_16x16x32_bf16 v[64:67], v[24:27], a[120:123], v[64:67]\n"
    "ds_read_b64_tr_b16 v[50:51], v14 offset:59392\n"
    "v_mfma_f32_16x16x32_bf16 v[72:75], v[24:27], a[184:187], v[72:75]\n"
    "v_mfma_f32_16x16x32_bf16 v[80:83], v[24:27], a[248:251], v[80:83]\n"
    "v_accvgpr_write_b32 a43, 0\n"
    "v_mfma_f32_16x16x32_bf16 v[60:63], v[28:31], a[56:59], v[60:63]\n"
    "ds_read_b64_tr_b16 v[52:53], v15 offset:57344\n"
    "v_mfma_f32_16x16x32_bf16 v[68:71], v[28:31], a[120:123], v[68:71]\n"
    "ds_read_b64_tr_b16 v[54:55], v15 offset:59392\n"
    "v_mfma_f32_16x16x32_bf16 v[76:79], v[28:31], a[184:187], v[76:79]\n"
    "v_mfma_f32_16x16x32_bf16 v[84:87], v[28:31], a[248:251], v[84:87]\n"
    "s_waitcnt lgkmcnt(8)\n"
    "v_mfma_f32_16x16x32_bf16 v[88:91], v[32:35], a[56:59], v[88:91]\n"
    "ds_read_b64_tr_b16 v[24:25], v12 offset:45056\n"
    "v_accvgpr_write_b32 a44, 0\n"
    "v_mfma_f32_16x16x32_bf16 v[96:99], v[32:35], a[120:123], v[96:99]\n"
    "ds_read_b64_tr_b16 v[26:27], v12 offset:47104\n"
    "v_mfma_f32_16x16x32_bf16 v[104:107], v[32:35], a[184:187], v[104:107]\n"
    "v_mfma_f32_16x16x32_bf16 v[112:115], v[32:35], a[248:251], v[112:115]\n"
    "v_mfma_f32_16x16x32_bf16 v[92:95], v[36:39], a[56:59], v[92:95]\n"
    "ds_read_b64_tr_b16 v[28:29], v13 offset:45056\n"
    "v_mfma_f32_16x16x32_bf16 v[100:103], v[36:39], a[120:123], v[100:103]\n"
    "ds_read_b64_tr_b16 v[30:31], v13 offset:47104\n"
    "v_mfma_f32_16x16x32_bf16 v[108:111], v[36:39], a[184:187], v[108:111]\n"
    "v_accvgpr_write_b32 a45, 0\n"
    "v_mfma_f32_16x16x32_bf16 v[116:119], v[36:39], a[248:251], v[116:119]\n"
    "s_waitcnt lgkmcnt(8)\n"
    "v_mfma_f32_16x16x32_bf16 v[120:123], v[40:43], a[56:59], v[120:123]\n"
    "ds_read_b64_tr_b16 v[32:33], v14 offset:45056\n"
    "v_mfma_f32_16x16x32_bf16 v[128:131], v[40:43], a[120:123], v[128:131]\n"
    "ds_read_b64_tr_b16 v[34:35], v14 offset:47104\n"
    "v_mfma_f32_16x16x32_bf16 v[136:139], v[40:43], a[184:187], v[136:139]\n"
    "v_mfma_f32_16x16x32_bf16 v[144:147], v[40:43], a[248:251], v[144:147]\n"
    "v_accvgpr_write_b32 a46, 0\n"
    "v_mfma_f32_16x16x32_bf16 v[124:127], v[44:47], a[56:59], v[124:127]\n"
    "ds_read_b64_tr_b16 v[36:37], v15 offset:45056\n"
    "v_mfma_f32_16x16x32_bf16 v[132:135], v[44:47], a[120:123], v[132:135]\n"
    "ds_read_b64_tr_b16 v[38:39], v15 offset:47104\n"
    "v_mfma_f32_16x16x32_bf16 v[140:143], v[44:47], a[184:187], v[140:143]\n"
    "v_mfma_f32_16x16x32_bf16 v[148:151], v[44:47], a[248:251], v[148:151]\n"
    "s_waitcnt lgkmcnt(8)\n"
    "v_mfma_f32_16x16x32_bf16 v[152:155], v[48:51], a[56:59], v[152:155]\n"
    "ds_read_b64_tr_b16 v[40:41], v12 offset:61440\n"
    "v_mfma_f32_16x16x32_bf16 v[160:163], v[48:51], a[120:123], v[160:163]\n"
    "ds_read_b64_tr_b16 v[42:43], v12 offset:63488\n"
    "v_accvgpr_write_b32 a47, 0\n"
    "v_mfma_f32_16x16x32_bf16 v[168:171], v[48:51], a[184:187], v[168:171]\n"
    "v_mfma_f32_16x16x32_bf16 v[176:179], v[48:51], a[248:251], v[176:179]\n"
    "v_mfma_f32_16x16x32_bf16 v[156:159], v[52:55], a[56:59], v[156:159]\n"
    "ds_read_b64_tr_b16 v[44:45], v13 offset:61440\n"
    "v_mfma_f32_16x16x32_bf16 v[164:167], v[52:55], a[120:123], v[164:167]\n"
    "ds_read_b64_tr_b16 v[46:47], v13 offset:63488\n"
    "v_mfma_f32_16x16x32_bf16 v[172:175], v[52:55], a[184:187], v[172:175]\n"
    "v_mfma_f32_16x16x32_bf16 v[180:183], v[52:55], a[248:251], v[180:183]\n"
    "s_waitcnt lgkmcnt(8)\n"
    "v_mfma_f32_16x16x32_bf16 v[56:59], v[24:27], a[60:63], v[56:59]\n"
    "ds_read_b64_tr_b16 v[48:49], v14 offset:61440\n"
    "v_mfma_f32_16x16x32_bf16 v[64:67], v[24:27], a[124:127], v[64:67]\n"
    "ds_read_b64_tr_b16 v[50:51], v14 offset:63488\n"
    "v_mfma_f32_16x16x32_bf16 v[72:75], v[24:27], a[188:191], v[72:75]\n"
    "v_mfma_f32_16x16x32_bf16 v[80:83], v[24:27], a[252:255], v[80:83]\n"
    "v_mfma_f32_16x16x32_bf16 v[60:63], v[28:31], a[60:63], v[60:63]\n"
    "ds_read_b64_tr_b16 v[52:53], v15 offset:61440\n"
    "v_mfma_f32_16x16x32_bf16 v[68:71], v[28:31], a[124:127], v[68:71]\n"
    "ds_read_b64_tr_b16 v[54:55], v15 offset:63488\n"
    "v_mfma_f32_16x16x32_bf16 v[76:79], v[28:31], a[188:191], v[76:79]\n"
    "v_mfma_f32_16x16x32_bf16 v[84:87], v[28:31], a[252:255], v[84:87]\n"
    "s_waitcnt lgkmcnt(8)\n"
    "v_mfma_f32_16x16x32_bf16 v[88:91], v[32:35], a[60:63], v[88:91]\n"
    "v_mfma_f32_16x16x32_bf16 v[96:99], v[32:35], a[124:127], v[96:99]\n"
    "v_mfma_f32_16x16x32_bf16 v[104:107], v[32:35], a[188:191], v[104:107]\n"
    "v_mfma_f32_16x16x32_bf16 v[112:115], v[32:35], a[252:255], v[112:115]\n"
    "v_mfma_f32_16x16x32_bf16 v[92:95], v[36:39], a[60:63], v[92:95]\n"
    "v_mfma_f32_16x16x32_bf16 v[100:103], v[36:39], a[124:127], v[100:103]\n"
    "v_mfma_f32_16x16x32_bf16 v[108:111], v[36:39], a[188:191], v[108:111]\n"
    "v_mfma_f32_16x16x32_bf16 v[116:119], v[36:39], a[252:255], v[116:119]\n"
    "s_waitcnt lgkmcnt(4)\n"
    "v_mfma_f32_16x16x32_bf16 v[120:123], v[40:43], a[60:63], v[120:123]\n"
    "v_mfma_f32_16x16x32_bf16 v[128:131], v[40:43], a[124:127], v[128:131]\n"
    "v_mfma_f32_16x16x32_bf16 v[136:139], v[40:43], a[188:191], v[136:139]\n"
    "v_mfma_f32_16x16x32_bf16 v[144:147], v[40:43], a[252:255], v[144:147]\n"
    "v_mfma_f32_16x16x32_bf16 v[124:127], v[44:47], a[60:63], v[124:127]\n"
    "v_mfma_f32_16x16x32_bf16 v[132:135], v[44:47], a[124:127], v[132:135]\n"
    "v_mfma_f32_16x16x32_bf16 v[140:143], v[44:47], a[188:191], v[140:143]\n"
    "v_mfma_f32_16x16x32_bf16 v[148:151], v[44:47], a[252:255], v[148:151]\n"
    "s_waitcnt lgkmcnt(0)\n"
    "v_mfma_f32_16x16x32_bf16 v[152:155], v[48:51], a[60:63], v[152:155]\n"
    "v_mfma_f32_16x16x32_bf16 v[160:163], v[48:51], a[124:127], v[160:163]\n"
    "v_mfma_f32_16x16x32_bf16 v[168:171], v[48:51], a[188:191], v[168:171]\n"
    "v_mfma_f32_16x16x32_bf16 v[176:179], v[48:51], a[252:255], v[176:179]\n"
    "v_mfma_f32_16x16x32_bf16 v[156:159], v[52:55], a[60:63], v[156:159]\n"
    "v_mfma_f32_16x16x32_bf16 v[164:167], v[52:55], a[124:127], v[164:167]\n"
    "v_mfma_f32_16x16x32_bf16 v[172:175], v[52:55], a[188:191], v[172:175]\n"
    "v_mfma_f32_16x16x32_bf16 v[180:183], v[52:55], a[252:255], v[180:183]\n"
    "L_k1_end_%=:\n"
    "v_xor_b32 v18, 0x2000, v17\n"
    "v_mov_b32 v3, v2\n"
    "v_mov_b32 v2, v20\n"
    "v_add_u32 v0, 32768, v3\n"
    "v_add_u32 v1, 65536, v3\n"
    "v_add_u32 v15, 16384, v2\n"
    "s_mov_b32 s46, 4267704320\n"
    "v_accvgpr_write_b32 a48, 0\n"
    "v_accvgpr_write_b32 a49, 0\n"
    "v_accvgpr_write_b32 a50, 0\n"
    "v_accvgpr_write_b32 a51, 0\n"
    "v_accvgpr_write_b32 a52, 0\n"
    "v_accvgpr_write_b32 a53, 0\n"
    "v_accvgpr_write_b32 a54, 0\n"
    "v_accvgpr_write_b32 a55, 0\n"
    "v_accvgpr_write_b32 a56, 0\n"
    "v_accvgpr_write_b32 a57, 0\n"
    "v_accvgpr_write_b32 a58, 0\n"
    "v_accvgpr_write_b32 a59, 0\n"
    "v_accvgpr_write_b32 a60, 0\n"
    "v_accvgpr_write_b32 a61, 0\n"
    "v_accvgpr_write_b32 a62, 0\n"
    "v_accvgpr_write_b32 a63, 0\n"
    "s_mov_b32 s88, 0x3e0293ee\n"
    "s_mov_b32 s89, 0x3e0293ee\n"
    "v_pk_mul_f32 v[56:57], v[56:57], s[88:89]\n"
    "v_pk_mul_f32 v[58:59], v[58:59], s[88:89]\n"
    "v_pk_mul_f32 v[60:61], v[60:61], s[88:89]\n"
    "v_pk_mul_f32 v[62:63], v[62:63], s[88:89]\n"
    "v_cvt_pk_bf16_f32 v216, v56, v57\n"
    "v_cvt_pk_bf16_f32 v217, v58, v59\n"
    "v_cvt_pk_bf16_f32 v218, v60, v61\n"
    "v_cvt_pk_bf16_f32 v219, v62, v63\n"
    "v_accvgpr_write_b32 a128, v216\n"
    "v_accvgpr_write_b32 a129, v217\n"
    "v_accvgpr_write_b32 a130, v218\n"
    "v_accvgpr_write_b32 a131, v219\n"
    "ds_write_b128 v17, v[216:219] offset:0\n"
    "v_pk_mul_f32 v[88:89], v[88:89], s[88:89]\n"
    "v_pk_mul_f32 v[90:91], v[90:91], s[88:89]\n"
    "v_pk_mul_f32 v[92:93], v[92:93], s[88:89]\n"
    "v_pk_mul_f32 v[94:95], v[94:95], s[88:89]\n"
    "v_cvt_pk_bf16_f32 v220, v88, v89\n"
    "v_cvt_pk_bf16_f32 v221, v90, v91\n"
    "v_cvt_pk_bf16_f32 v222, v92, v93\n"
    "v_cvt_pk_bf16_f32 v223, v94, v95\n"
    "v_accvgpr_write_b32 a132, v220\n"
    "v_accvgpr_write_b32 a133, v221\n"
    "v_accvgpr_write_b32 a134, v222\n"
    "v_accvgpr_write_b32 a135, v223\n"
    "ds_write_b128 v17, v[220:223] offset:1024\n"
    "v_pk_mul_f32 v[120:121], v[120:121], s[88:89]\n"
    "v_pk_mul_f32 v[122:123], v[122:123], s[88:89]\n"
    "v_pk_mul_f32 v[124:125], v[124:125], s[88:89]\n"
    "v_pk_mul_f32 v[126:127], v[126:127], s[88:89]\n"
    "v_cvt_pk_bf16_f32 v224, v120, v121\n"
    "v_cvt_pk_bf16_f32 v225, v122, v123\n"
    "v_cvt_pk_bf16_f32 v226, v124, v125\n"
    "v_cvt_pk_bf16_f32 v227, v126, v127\n"
    "v_accvgpr_write_b32 a136, v224\n"
    "v_accvgpr_write_b32 a137, v225\n"
    "v_accvgpr_write_b32 a138, v226\n"
    "v_accvgpr_write_b32 a139, v227\n"
    "ds_write_b128 v17, v[224:227] offset:2048\n"
    "v_pk_mul_f32 v[152:153], v[152:153], s[88:89]\n"
    "v_pk_mul_f32 v[154:155], v[154:155], s[88:89]\n"
    "v_pk_mul_f32 v[156:157], v[156:157], s[88:89]\n"
    "v_pk_mul_f32 v[158:159], v[158:159], s[88:89]\n"
    "v_cvt_pk_bf16_f32 v228, v152, v153\n"
    "v_cvt_pk_bf16_f32 v229, v154, v155\n"
    "v_cvt_pk_bf16_f32 v230, v156, v157\n"
    "v_cvt_pk_bf16_f32 v231, v158, v159\n"
    "v_accvgpr_write_b32 a140, v228\n"
    "v_accvgpr_write_b32 a141, v229\n"
    "v_accvgpr_write_b32 a142, v230\n"
    "v_accvgpr_write_b32 a143, v231\n"
    "ds_write_b128 v17, v[228:231] offset:3072\n"
    "v_pk_mul_f32 v[64:65], v[64:65], s[88:89]\n"
    "v_pk_mul_f32 v[66:67], v[66:67], s[88:89]\n"
    "v_pk_mul_f32 v[68:69], v[68:69], s[88:89]\n"
    "v_pk_mul_f32 v[70:71], v[70:71], s[88:89]\n"
    "v_cvt_pk_bf16_f32 v216, v64, v65\n"
    "v_cvt_pk_bf16_f32 v217, v66, v67\n"
    "v_cvt_pk_bf16_f32 v218, v68, v69\n"
    "v_cvt_pk_bf16_f32 v219, v70, v71\n"
    "v_accvgpr_write_b32 a144, v216\n"
    "v_accvgpr_write_b32 a145, v217\n"
    "v_accvgpr_write_b32 a146, v218\n"
    "v_accvgpr_write_b32 a147, v219\n"
    "ds_write_b128 v17, v[216:219] offset:4096\n"
    "v_pk_mul_f32 v[96:97], v[96:97], s[88:89]\n"
    "v_pk_mul_f32 v[98:99], v[98:99], s[88:89]\n"
    "v_pk_mul_f32 v[100:101], v[100:101], s[88:89]\n"
    "v_pk_mul_f32 v[102:103], v[102:103], s[88:89]\n"
    "v_cvt_pk_bf16_f32 v220, v96, v97\n"
    "v_cvt_pk_bf16_f32 v221, v98, v99\n"
    "v_cvt_pk_bf16_f32 v222, v100, v101\n"
    "v_cvt_pk_bf16_f32 v223, v102, v103\n"
    "v_accvgpr_write_b32 a148, v220\n"
    "v_accvgpr_write_b32 a149, v221\n"
    "v_accvgpr_write_b32 a150, v222\n"
    "v_accvgpr_write_b32 a151, v223\n"
    "ds_write_b128 v17, v[220:223] offset:5120\n"
    "v_pk_mul_f32 v[128:129], v[128:129], s[88:89]\n"
    "v_pk_mul_f32 v[130:131], v[130:131], s[88:89]\n"
    "v_pk_mul_f32 v[132:133], v[132:133], s[88:89]\n"
    "v_pk_mul_f32 v[134:135], v[134:135], s[88:89]\n"
    "v_cvt_pk_bf16_f32 v224, v128, v129\n"
    "v_cvt_pk_bf16_f32 v225, v130, v131\n"
    "v_cvt_pk_bf16_f32 v226, v132, v133\n"
    "v_cvt_pk_bf16_f32 v227, v134, v135\n"
    "v_accvgpr_write_b32 a152, v224\n"
    "v_accvgpr_write_b32 a153, v225\n"
    "v_accvgpr_write_b32 a154, v226\n"
    "v_accvgpr_write_b32 a155, v227\n"
    "ds_write_b128 v17, v[224:227] offset:6144\n"
    "v_pk_mul_f32 v[160:161], v[160:161], s[88:89]\n"
    "v_pk_mul_f32 v[162:163], v[162:163], s[88:89]\n"
    "v_pk_mul_f32 v[164:165], v[164:165], s[88:89]\n"
    "v_pk_mul_f32 v[166:167], v[166:167], s[88:89]\n"
    "v_cvt_pk_bf16_f32 v228, v160, v161\n"
    "v_cvt_pk_bf16_f32 v229, v162, v163\n"
    "v_cvt_pk_bf16_f32 v230, v164, v165\n"
    "v_cvt_pk_bf16_f32 v231, v166, v167\n"
    "v_accvgpr_write_b32 a156, v228\n"
    "v_accvgpr_write_b32 a157, v229\n"
    "v_accvgpr_write_b32 a158, v230\n"
    "v_accvgpr_write_b32 a159, v231\n"
    "ds_write_b128 v17, v[228:231] offset:7168\n"
    "s_waitcnt vmcnt(0) lgkmcnt(0)\n"
    "s_barrier\n"
    "ds_read_b128 a[64:67], v0 offset:0\n"
    "ds_read_b128 a[68:71], v0 offset:1024\n"
    "ds_read_b128 a[72:75], v0 offset:2048\n"
    "ds_read_b128 a[76:79], v0 offset:3072\n"
    "ds_read_b128 a[80:83], v0 offset:4096\n"
    "ds_read_b128 a[84:87], v0 offset:5120\n"
    "ds_read_b128 a[88:91], v0 offset:6144\n"
    "ds_read_b128 a[92:95], v0 offset:7168\n"
    "ds_read_b128 a[212:215], v18 offset:0\n"
    "ds_read_b128 a[216:219], v18 offset:1024\n"
    "ds_read_b128 a[220:223], v18 offset:2048\n"
    "ds_read_b128 a[224:227], v18 offset:3072\n"
    "ds_read_b128 a[228:231], v18 offset:4096\n"
    "ds_read_b128 a[232:235], v18 offset:5120\n"
    "ds_read_b128 a[236:239], v18 offset:6144\n"
    "ds_read_b128 a[240:243], v18 offset:7168\n"
    "v_pk_mul_f32 v[72:73], v[72:73], s[88:89]\n"
    "v_pk_mul_f32 v[74:75], v[74:75], s[88:89]\n"
    "v_pk_mul_f32 v[76:77], v[76:77], s[88:89]\n"
    "v_pk_mul_f32 v[78:79], v[78:79], s[88:89]\n"
    "v_cvt_pk_bf16_f32 v216, v72, v73\n"
    "v_cvt_pk_bf16_f32 v217, v74, v75\n"
    "v_cvt_pk_bf16_f32 v218, v76, v77\n"
    "v_cvt_pk_bf16_f32 v219, v78, v79\n"
    "v_accvgpr_write_b32 a160, v216\n"
    "v_accvgpr_write_b32 a161, v217\n"
    "v_accvgpr_write_b32 a162, v218\n"
    "v_accvgpr_write_b32 a163, v219\n"
    "v_pk_mul_f32 v[104:105], v[104:105], s[88:89]\n"
    "v_pk_mul_f32 v[106:107], v[106:107], s[88:89]\n"
    "v_pk_mul_f32 v[108:109], v[108:109], s[88:89]\n"
    "v_pk_mul_f32 v[110:111], v[110:111], s[88:89]\n"
    "v_cvt_pk_bf16_f32 v220, v104, v105\n"
    "v_cvt_pk_bf16_f32 v221, v106, v107\n"
    "v_cvt_pk_bf16_f32 v222, v108, v109\n"
    "v_cvt_pk_bf16_f32 v223, v110, v111\n"
    "v_accvgpr_write_b32 a164, v220\n"
    "v_accvgpr_write_b32 a165, v221\n"
    "v_accvgpr_write_b32 a166, v222\n"
    "v_accvgpr_write_b32 a167, v223\n"
    "v_pk_mul_f32 v[136:137], v[136:137], s[88:89]\n"
    "v_pk_mul_f32 v[138:139], v[138:139], s[88:89]\n"
    "v_pk_mul_f32 v[140:141], v[140:141], s[88:89]\n"
    "v_pk_mul_f32 v[142:143], v[142:143], s[88:89]\n"
    "v_cvt_pk_bf16_f32 v224, v136, v137\n"
    "v_cvt_pk_bf16_f32 v225, v138, v139\n"
    "v_cvt_pk_bf16_f32 v226, v140, v141\n"
    "v_cvt_pk_bf16_f32 v227, v142, v143\n"
    "v_accvgpr_write_b32 a168, v224\n"
    "v_accvgpr_write_b32 a169, v225\n"
    "v_accvgpr_write_b32 a170, v226\n"
    "v_accvgpr_write_b32 a171, v227\n"
    "v_pk_mul_f32 v[168:169], v[168:169], s[88:89]\n"
    "v_pk_mul_f32 v[170:171], v[170:171], s[88:89]\n"
    "v_pk_mul_f32 v[172:173], v[172:173], s[88:89]\n"
    "v_pk_mul_f32 v[174:175], v[174:175], s[88:89]\n"
    "v_cvt_pk_bf16_f32 v228, v168, v169\n"
    "v_cvt_pk_bf16_f32 v229, v170, v171\n"
    "v_cvt_pk_bf16_f32 v230, v172, v173\n"
    "v_cvt_pk_bf16_f32 v231, v174, v175\n"
    "v_accvgpr_write_b32 a172, v228\n"
    "v_accvgpr_write_b32 a173, v229\n"
    "v_accvgpr_write_b32 a174, v230\n"
    "v_accvgpr_write_b32 a175, v231\n"
    "v_pk_mul_f32 v[80:81], v[80:81], s[88:89]\n"
    "v_pk_mul_f32 v[82:83], v[82:83], s[88:89]\n"
    "v_pk_mul_f32 v[84:85], v[84:85], s[88:89]\n"
    "v_pk_mul_f32 v[86:87], v[86:87], s[88:89]\n"
    "v_cvt_pk_bf16_f32 v216, v80, v81\n"
    "v_cvt_pk_bf16_f32 v217, v82, v83\n"
    "v_cvt_pk_bf16_f32 v218, v84, v85\n"
    "v_cvt_pk_bf16_f32 v219, v86, v87\n"
    "v_accvgpr_write_b32 a176, v216\n"
    "v_accvgpr_write_b32 a177, v217\n"
    "v_accvgpr_write_b32 a178, v218\n"
    "v_accvgpr_write_b32 a179, v219\n"
    "v_pk_mul_f32 v[112:113], v[112:113], s[88:89]\n"
    "v_pk_mul_f32 v[114:115], v[114:115], s[88:89]\n"
    "v_pk_mul_f32 v[116:117], v[116:117], s[88:89]\n"
    "v_pk_mul_f32 v[118:119], v[118:119], s[88:89]\n"
    "v_cvt_pk_bf16_f32 v220, v112, v113\n"
    "v_cvt_pk_bf16_f32 v221, v114, v115\n"
    "v_cvt_pk_bf16_f32 v222, v116, v117\n"
    "v_cvt_pk_bf16_f32 v223, v118, v119\n"
    "v_accvgpr_write_b32 a180, v220\n"
    "v_accvgpr_write_b32 a181, v221\n"
    "v_accvgpr_write_b32 a182, v222\n"
    "v_accvgpr_write_b32 a183, v223\n"
    "v_pk_mul_f32 v[144:145], v[144:145], s[88:89]\n"
    "v_pk_mul_f32 v[146:147], v[146:147], s[88:89]\n"
    "v_pk_mul_f32 v[148:149], v[148:149], s[88:89]\n"
    "v_pk_mul_f32 v[150:151], v[150:151], s[88:89]\n"
    "v_cvt_pk_bf16_f32 v224, v144, v145\n"
    "v_cvt_pk_bf16_f32 v225, v146, v147\n"
    "v_cvt_pk_bf16_f32 v226, v148, v149\n"
    "v_cvt_pk_bf16_f32 v227, v150, v151\n"
    "v_accvgpr_write_b32 a184, v224\n"
    "v_accvgpr_write_b32 a185, v225\n"
    "v_accvgpr_write_b32 a186, v226\n"
    "v_accvgpr_write_b32 a187, v227\n"
    "v_pk_mul_f32 v[176:177], v[176:177], s[88:89]\n"
    "v_pk_mul_f32 v[178:179], v[178:179], s[88:89]\n"
    "v_pk_mul_f32 v[180:181], v[180:181], s[88:89]\n"
    "v_pk_mul_f32 v[182:183], v[182:183], s[88:89]\n"
    "v_cvt_pk_bf16_f32 v228, v176, v177\n"
    "v_cvt_pk_bf16_f32 v229, v178, v179\n"
    "v_cvt_pk_bf16_f32 v230, v180, v181\n"
    "v_cvt_pk_bf16_f32 v231, v182, v183\n"
    "v_accvgpr_write_b32 a188, v228\n"
    "v_accvgpr_write_b32 a189, v229\n"
    "v_accvgpr_write_b32 a190, v230\n"
    "v_accvgpr_write_b32 a191, v231\n"
    "s_or_b32 s30, s22, s20\n"
    "s_cmp_lg_u32 s30, 0\n"
    "s_cbranch_scc1 L_nd_%=\n"
    "v_min_f32 v200, 0, v244\n"
    "v_mov_b32 v204, 0xff800000\n"
    "v_min_f32 v212, 0, v244\n"
    "v_min_f32 v201, 0, v245\n"
    "v_mov_b32 v205, 0xff800000\n"
    "v_min_f32 v213, 0, v245\n"
    "v_min_f32 v202, 0, v246\n"
    "v_mov_b32 v206, 0xff800000\n"
    "v_min_f32 v214, 0, v246\n"
    "v_min_f32 v203, 0, v247\n"
    "v_mov_b32 v207, 0xff800000\n"
    "v_min_f32 v215, 0, v247\n"
    "L_nd_%=:\n"
    "s_waitcnt lgkmcnt(8)\n"
    "v_mfma_f32_16x16x32_bf16 v[24:27], a[64:67], a[128:131], v[184:187]\n"
    "v_mov_b32 v120, 0\n"
    "v_mov_b32 v121, 0\n"
    "v_mfma_f32_16x16x32_bf16 v[32:35], a[64:67], a[144:147], v[192:195]\n"
    "v_mov_b32 v122, 0\n"
    "v_mov_b32 v123, 0\n"
    "v_mfma_f32_16x16x32_bf16 v[40:43], a[64:67], a[160:163], v[200:203]\n"
    "v_mov_b32 v124, 0\n"
    "v_mov_b32 v125, 0\n"
    "v_mfma_f32_16x16x32_bf16 v[48:51], a[64:67], a[176:179], v[208:211]\n"
    "ds_read_b128 a[64:67], v0 offset:8192\n"
    "v_mov_b32 v126, 0\n"
    "v_mov_b32 v127, 0\n"
    "v_mfma_f32_16x16x32_bf16 v[28:31], a[68:71], a[128:131], v[188:191]\n"
    "v_mov_b32 v128, 0\n"
    "v_mov_b32 v129, 0\n"
    "v_mfma_f32_16x16x32_bf16 v[36:39], a[68:71], a[144:147], v[196:199]\n"
    "v_mov_b32 v130, 0\n"
    "v_mov_b32 v131, 0\n"
    "v_mfma_f32_16x16x32_bf16 v[44:47], a[68:71], a[160:163], v[204:207]\n"
    "v_mov_b32 v132, 0\n"
    "v_mov_b32 v133, 0\n"
    "v_mfma_f32_16x16x32_bf16 v[52:55], a[68:71], a[176:179], v[212:215]\n"
    "ds_read_b128 a[68:71], v0 offset:9216\n"
    "v_mov_b32 v134, 0\n"
    "v_mov_b32 v135, 0\n"
    "v_mfma_f32_16x16x32_bf16 v[24:27], a[72:75], a[132:135], v[24:27]\n"
    "v_mov_b32 v136, 0\n"
    "v_mov_b32 v137, 0\n"
    "v_mfma_f32_16x16x32_bf16 v[32:35], a[72:75], a[148:151], v[32:35]\n"
    "v_mov_b32 v138, 0\n"
    "v_mov_b32 v139, 0\n"
    "v_mfma_f32_16x16x32_bf16 v[40:43], a[72:75], a[164:167], v[40:43]\n"
    "v_mov_b32 v140, 0\n"
    "v_mov_b32 v141, 0\n"
    "v_mfma_f32_16x16x32_bf16 v[48:51], a[72:75], a[180:183], v[48:51]\n"
    "ds_read_b128 a[72:75], v0 offset:10240\n"
    "v_mov_b32 v142, 0\n"
    "v_mov_b32 v143, 0\n"
    "v_mfma_f32_16x16x32_bf16 v[28:31], a[76:79], a[132:135], v[28:31]\n"
    "v_mov_b32 v144, 0\n"
    "v_mov_b32 v145, 0\n"
    "v_mfma_f32_16x16x32_bf16 v[36:39], a[76:79], a[148:151], v[36:39]\n"
    "v_mov_b32 v146, 0\n"
    "v_mov_b32 v147, 0\n"
    "v_mfma_f32_16x16x32_bf16 v[44:47], a[76:79], a[164:167], v[44:47]\n"
    "v_mov_b32 v148, 0\n"
    "v_mov_b32 v149, 0\n"
    "v_mfma_f32_16x16x32_bf16 v[52:55], a[76:79], a[180:183], v[52:55]\n"
    "ds_read_b128 a[76:79], v0 offset:11264\n"
    "v_mov_b32 v150, 0\n"
    "v_mov_b32 v151, 0\n"
    "v_mfma_f32_16x16x32_bf16 v[24:27], a[80:83], a[136:139], v[24:27]\n"
    "v_mov_b32 v152, 0\n"
    "v_mov_b32 v153, 0\n"
    "v_mfma_f32_16x16x32_bf16 v[32:35], a[80:83], a[152:155], v[32:35]\n"
    "v_mov_b32 v154, 0\n"
    "v_mov_b32 v155, 0\n"
    "v_mfma_f32_16x16x32_bf16 v[40:43], a[80:83], a[168:171], v[40:43]\n"
    "v_mov_b32 v156, 0\n"
    "v_mov_b32 v157, 0\n"
    "v_mfma_f32_16x16x32_bf16 v[48:51], a[80:83], a[184:187], v[48:51]\n"
    "ds_read_b128 a[80:83], v0 offset:12288\n"
    "v_mov_b32 v158, 0\n"
    "v_mov_b32 v159, 0\n"
    "v_mfma_f32_16x16x32_bf16 v[28:31], a[84:87], a[136:139], v[28:31]\n"
    "v_mov_b32 v160, 0\n"
    "v_mov_b32 v161, 0\n"
    "v_mfma_f32_16x16x32_bf16 v[36:39], a[84:87], a[152:155], v[36:39]\n"
    "v_mov_b32 v162, 0\n"
    "v_mov_b32 v163, 0\n"
    "v_mfma_f32_16x16x32_bf16 v[44:47], a[84:87], a[168:171], v[44:47]\n"
    "v_mov_b32 v164, 0\n"
    "v_mov_b32 v165, 0\n"
    "v_mfma_f32_16x16x32_bf16 v[52:55], a[84:87], a[184:187], v[52:55]\n"
    "ds_read_b128 a[84:87], v0 offset:13312\n"
    "v_mov_b32 v166, 0\n"
    "v_mov_b32 v167, 0\n"
    "v_mfma_f32_16x16x32_bf16 v[24:27], a[88:91], a[140:143], v[24:27]\n"
    "v_mov_b32 v168, 0\n"
    "v_mov_b32 v169, 0\n"
    "v_mfma_f32_16x16x32_bf16 v[32:35], a[88:91], a[156:159], v[32:35]\n"
    "v_mov_b32 v170, 0\n"
    "v_mov_b32 v171, 0\n"
    "v_mfma_f32_16x16x32_bf16 v[40:43], a[88:91], a[172:175], v[40:43]\n"
    "v_mov_b32 v172, 0\n"
    "v_mov_b32 v173, 0\n"
    "v_mfma_f32_16x16x32_bf16 v[48:51], a[88:91], a[188:191], v[48:51]\n"
    "ds_read_b128 a[88:91], v0 offset:14336\n"
    "v_mov_b32 v174, 0\n"
    "v_mov_b32 v175, 0\n"
    "v_mfma_f32_16x16x32_bf16 v[28:31], a[92:95], a[140:143], v[28:31]\n"
    "v_mov_b32 v176, 0\n"
    "v_mov_b32 v177, 0\n"
    "v_mfma_f32_16x16x32_bf16 v[36:39], a[92:95], a[156:159], v[36:39]\n"
    "v_mov_b32 v178, 0\n"
    "v_mov_b32 v179, 0\n"
    "v_mfma_f32_16x16x32_bf16 v[44:47], a[92:95], a[172:175], v[44:47]\n"
    "v_mov_b32 v180, 0\n"
    "v_mov_b32 v181, 0\n"
    "v_mfma_f32_16x16x32_bf16 v[52:55], a[92:95], a[188:191], v[52:55]\n"
    "ds_read_b128 a[92:95], v0 offset:15360\n"
    "v_mov_b32 v182, 0\n"
    "v_mov_b32 v183, 0\n"
    "v_mov_b32 v0, v3\n"
    "s_mov_b32 s25, 0\n"
    "v_mov_b32 v104, 0\n"
    "v_mov_b32 v105, 0\n"
    "v_mov_b32 v106, 0\n"
    "v_mov_b32 v107, 0\n"
    "v_mov_b32 v108, 0\n"
    "v_mov_b32 v109, 0\n"
    "v_mov_b32 v110, 0\n"
    "v_mov_b32 v111, 0\n"
    "v_mov_b32 v112, 0\n"
    "v_mov_b32 v113, 0\n"
    "v_mov_b32 v114, 0\n"
    "v_mov_b32 v115, 0\n"
    "v_mov_b32 v116, 0\n"
    "v_mov_b32 v117, 0\n"
    "v_mov_b32 v118, 0\n"
    "v_mov_b32 v119, 0\n"
    "v_max3_f32 v216, v24, v25, v26\n"
    "v_max3_f32 v217, v27, v28, v29\n"
    "v_max3_f32 v216, v216, v30, v31\n"
    "v_max_f32 v216, v216, v217\n"
    "v_max3_f32 v218, v32, v33, v34\n"
    "v_max3_f32 v219, v35, v36, v37\n"
    "v_max3_f32 v218, v218, v38, v39\n"
    "v_max_f32 v218, v218, v219\n"
    "v_max3_f32 v220, v40, v41, v42\n"
    "v_max3_f32 v221, v43, v44, v45\n"
    "v_max3_f32 v220, v220, v46, v47\n"
    "v_max_f32 v220, v220, v221\n"
    "v_max3_f32 v222, v48, v49, v50\n"
    "v_max3_f32 v223, v51, v52, v53\n"
    "v_max3_f32 v222, v222, v54, v55\n"
    "v_max_f32 v222, v222, v223\n"
    "v_mov_b32 v217, v216\n"
    "v_mov_b32 v219, v218\n"
    "v_mov_b32 v221, v220\n"
    "v_mov_b32 v223, v222\n"
    "s_nop 1\n"
    "v_permlane16_swap_b32 v216, v217\n"
    "v_permlane16_swap_b32 v218, v219\n"
    "v_permlane16_swap_b32 v220, v221\n"
    "v_permlane16_swap_b32 v222, v223\n"
    "s_nop 1\n"
    "v_max_f32 v216, v216, v217\n"
    "v_max_f32 v218, v218, v219\n"
    "v_max_f32 v220, v220, v221\n"
    "v_max_f32 v222, v222, v223\n"
    "v_mov_b32 v217, v216\n"
    "v_mov_b32 v219, v218\n"
    "v_mov_b32 v221, v220\n"
    "v_mov_b32 v223, v222\n"
    "s_nop 1\n"
    "v_permlane32_swap_b32 v216, v217\n"
    "v_permlane32_swap_b32 v218, v219\n"
    "v_permlane32_swap_b32 v220, v221\n"
    "v_permlane32_swap_b32 v222, v223\n"
    "s_nop 1\n"
    "v_max_f32 v216, v216, v217\n"
    "v_max_f32 v218, v218, v219\n"
    "v_max_f32 v220, v220, v221\n"
    "v_max_f32 v222, v222, v223\n"
    "v_mul_f32 v8, -1.0, v216\n"
    "v_mul_f32 v9, -1.0, v218\n"
    "v_mul_f32 v10, -1.0, v220\n"
    "v_mul_f32 v11, -1.0, v222\n"
    "v_pk_add_f32 v[24:25], v[24:25], v[8:9] op_sel:[0,0] op_sel_hi:[1,0]\n"
    "v_pk_add_f32 v[26:27], v[26:27], v[8:9] op_sel:[0,0] op_sel_hi:[1,0]\n"
    "v_pk_add_f32 v[28:29], v[28:29], v[8:9] op_sel:[0,0] op_sel_hi:[1,0]\n"
    "v_pk_add_f32 v[30:31], v[30:31], v[8:9] op_sel:[0,0] op_sel_hi:[1,0]\n"
    "v_pk_add_f32 v[32:33], v[32:33], v[8:9] op_sel:[0,1] op_sel_hi:[1,1]\n"
    "v_pk_add_f32 v[34:35], v[34:35], v[8:9] op_sel:[0,1] op_sel_hi:[1,1]\n"
    "v_pk_add_f32 v[36:37], v[36:37], v[8:9] op_sel:[0,1] op_sel_hi:[1,1]\n"
    "v_pk_add_f32 v[38:39], v[38:39], v[8:9] op_sel:[0,1] op_sel_hi:[1,1]\n"
    "v_pk_add_f32 v[40:41], v[40:41], v[10:11] op_sel:[0,0] op_sel_hi:[1,0]\n"
    "v_pk_add_f32 v[42:43], v[42:43], v[10:11] op_sel:[0,0] op_sel_hi:[1,0]\n"
    "v_pk_add_f32 v[44:45], v[44:45], v[10:11] op_sel:[0,0] op_sel_hi:[1,0]\n"
    "v_pk_add_f32 v[46:47], v[46:47], v[10:11] op_sel:[0,0] op_sel_hi:[1,0]\n"
    "v_pk_add_f32 v[48:49], v[48:49], v[10:11] op_sel:[0,1] op_sel_hi:[1,1]\n"
    "v_pk_add_f32 v[50:51], v[50:51], v[10:11] op_sel:[0,1] op_sel_hi:[1,1]\n"
    "v_pk_add_f32 v[52:53], v[52:53], v[10:11] op_sel:[0,1] op_sel_hi:[1,1]\n"
    "v_pk_add_f32 v[54:55], v[54:55], v[10:11] op_sel:[0,1] op_sel_hi:[1,1]\n"
    "v_mov_b32 v184, v8\n"
    "v_mov_b32 v185, v8\n"
    "v_mov_b32 v186, v8\n"
    "v_mov_b32 v187, v8\n"
    "v_mov_b32 v188, v8\n"
    "v_mov_b32 v189, v8\n"
    "v_mov_b32 v190, v8\n"
    "v_mov_b32 v191, v8\n"
    "v_mov_b32 v192, v9\n"
    "v_mov_b32 v193, v9\n"
    "v_mov_b32 v194, v9\n"
    "v_mov_b32 v195, v9\n"
    "v_mov_b32 v196, v9\n"
    "v_mov_b32 v197, v9\n"
    "v_mov_b32 v198, v9\n"
    "v_mov_b32 v199, v9\n"
    "s_bitcmp1_b64 s[78:79], s25\n"
    "s_cselect_b32 s31, 0xff800000, s97\n"
    "s_bitcmp1_b64 s[76:77], s25\n"
    "s_cselect_b32 s30, 0xff800000, s97\n"
    "v_min_f32 v218, s31, v10\n"
    "v_min_f32 v219, s31, v11\n"
    "v_min_f32 v204, s30, v218\n"
    "v_mov_b32 v205, v204\n"
    "v_mov_b32 v206, v204\n"
    "v_mov_b32 v207, v204\n"
    "v_mov_b32 v208, v219\n"
    "v_mov_b32 v209, v219\n"
    "v_mov_b32 v210, v219\n"
    "v_mov_b32 v211, v219\n"
    "v_max_f32 v220, s30, v244\n"
    "v_min_f32 v200, v220, v218\n"
    "v_min_f32 v212, v220, v219\n"
    "v_max_f32 v221, s30, v245\n"
    "v_min_f32 v201, v221, v218\n"
    "v_min_f32 v213, v221, v219\n"
    "v_max_f32 v220, s30, v246\n"
    "v_min_f32 v202, v220, v218\n"
    "v_min_f32 v214, v220, v219\n"
    "v_max_f32 v221, s30, v247\n"
    "v_min_f32 v203, v221, v218\n"
    "v_min_f32 v215, v221, v219\n"
    "s_waitcnt lgkmcnt(0)\n"
    "s_sub_u32 s47, s23, 3\n"
    "s_cmp_eq_u32 s22, 0\n"
    "s_cbranch_scc1 L_H1_%=\n"
    ".p2align 6\n"
    "L_A_%=:\n"
    "s_waitcnt lgkmcnt(8)\n"
    "v_mfma_f32_16x16x32_bf16 v[56:59], a[64:67], a[128:131], v[184:187]\n"
    "v_exp_f32_e32 v24, v24\n"
    "v_mfma_f32_16x16x32_bf16 v[64:67], a[64:67], a[144:147], v[192:195]\n"
    "v_exp_f32_e32 v25, v25\n"
    "v_mfma_f32_16x16x32_bf16 v[72:75], a[64:67], a[160:163], v[200:203]\n"
    "v_exp_f32_e32 v26, v26\n"
    "v_mfma_f32_16x16x32_bf16 v[80:83], a[64:67], a[176:179], v[208:211]\n"
    "v_cvt_pk_bf16_f32 v88, v24, v25\n"
    "v_mfma_f32_16x16x32_bf16 v[60:63], a[68:71], a[128:131], v[188:191]\n"
    "v_exp_f32_e32 v27, v27\n"
    "v_mfma_f32_16x16x32_bf16 v[68:71], a[68:71], a[144:147], v[196:199]\n"
    "v_mfma_f32_16x16x32_bf16 v[76:79], a[68:71], a[160:163], v[204:207]\n"
    "v_exp_f32_e32 v28, v28\n"
    "v_mfma_f32_16x16x32_bf16 v[84:87], a[68:71], a[176:179], v[212:215]\n"
    "v_cvt_pk_bf16_f32 v89, v26, v27\n"
    "v_mfma_f32_16x16x32_bf16 v[56:59], a[72:75], a[132:135], v[56:59]\n"
    "v_mfma_f32_16x16x32_bf16 v[64:67], a[72:75], a[148:151], v[64:67]\n"
    "v_exp_f32_e32 v29, v29\n"
    "v_mfma_f32_16x16x32_bf16 v[72:75], a[72:75], a[164:167], v[72:75]\n"
    "v_exp_f32_e32 v30, v30\n"
    "v_mfma_f32_16x16x32_bf16 v[80:83], a[72:75], a[180:183], v[80:83]\n"
    "s_waitcnt vmcnt(2) lgkmcnt(0)\n"
    "s_barrier\n"
    "v_mfma_f32_16x16x32_bf16 v[60:63], a[76:79], a[132:135], v[60:63]\n"
    "ds_read_b128 a[64:67], v0 offset:0\n"
    "ds_read_b128 a[68:71], v0 offset:1024\n"
    "v_mfma_f32_16x16x32_bf16 v[68:71], a[76:79], a[148:151], v[68:71]\n"
    "ds_read_b128 a[72:75], v0 offset:2048\n"
    "s_mov_b32 m0, s36\n"
    "v_mfma_f32_16x16x32_bf16 v[76:79], a[76:79], a[164:167], v[76:79]\n"
    "buffer_load_dwordx4 v2, s[48:51], s86 offen offset:0 sc1 lds\n"
    "v_cvt_pk_bf16_f32 v90, v28, v29\n"
    "v_mfma_f32_16x16x32_bf16 v[84:87], a[76:79], a[180:183], v[84:87]\n"
    "v_exp_f32_e32 v31, v31\n"
    "v_mfma_f32_16x16x32_bf16 v[56:59], a[80:83], a[136:139], v[56:59]\n"
    "ds_read_b128 a[76:79], v0 offset:3072\n"
    "v_mfma_f32_16x16x32_bf16 v[64:67], a[80:83], a[152:155], v[64:67]\n"
    "v_exp_f32_e32 v32, v32\n"
    "v_mfma_f32_16x16x32_bf16 v[72:75], a[80:83], a[168:171], v[72:75]\n"
    "buffer_load_dwordx4 v2, s[48:51], s86 offen offset:1024 sc1 lds\n"
    "v_cvt_pk_bf16_f32 v91, v30, v31\n"
    "v_mfma_f32_16x16x32_bf16 v[80:83], a[80:83], a[184:187], v[80:83]\n"
    "v_exp_f32_e32 v33, v33\n"
    "v_mfma_f32_16x16x32_bf16 v[60:63], a[84:87], a[136:139], v[60:63]\n"
    "ds_read_b128 a[80:83], v0 offset:4096\n"
    "v_mfma_f32_16x16x32_bf16 v[68:71], a[84:87], a[152:155], v[68:71]\n"
    "v_exp_f32_e32 v34, v34\n"
    "v_mfma_f32_16x16x32_bf16 v[76:79], a[84:87], a[168:171], v[76:79]\n"
    "buffer_load_dwordx4 v2, s[48:51], s86 offen offset:2048 sc1 lds\n"
    "v_cvt_pk_bf16_f32 v92, v32, v33\n"
    "v_mfma_f32_16x16x32_bf16 v[84:87], a[84:87], a[184:187], v[84:87]\n"
    "v_exp_f32_e32 v35, v35\n"
    "v_mfma_f32_16x16x32_bf16 v[56:59], a[88:91], a[140:143], v[56:59]\n"
    "ds_read_b128 a[84:87], v0 offset:5120\n"
    "v_mfma_f32_16x16x32_bf16 v[64:67], a[88:91], a[156:159], v[64:67]\n"
    "v_exp_f32_e32 v36, v36\n"
    "v_mfma_f32_16x16x32_bf16 v[72:75], a[88:91], a[172:175], v[72:75]\n"
    "buffer_load_dwordx4 v2, s[48:51], s86 offen offset:3072 sc1 lds\n"
    "v_cvt_pk_bf16_f32 v93, v34, v35\n"
    "v_mfma_f32_16x16x32_bf16 v[80:83], a[88:91], a[188:191], v[80:83]\n"
    "v_exp_f32_e32 v37, v37\n"
    "v_mfma_f32_16x16x32_bf16 v[60:63], a[92:95], a[140:143], v[60:63]\n"
    "ds_read_b128 a[88:91], v0 offset:6144\n"
    "v_mfma_f32_16x16x32_bf16 v[68:71], a[92:95], a[156:159], v[68:71]\n"
    "v_exp_f32_e32 v38, v38\n"
    "v_mfma_f32_16x16x32_bf16 v[76:79], a[92:95], a[172:175], v[76:79]\n"
    "s_mov_b32 m0, s37\n"
    "buffer_load_dwordx4 v2, s[52:55], s34 offen offset:0 sc1 lds\n"
    "v_mfma_f32_16x16x32_bf16 v[84:87], a[92:95], a[188:191], v[84:87]\n"
    "ds_read_b128 a[92:95], v0 offset:7168\n"
    "v_cvt_pk_bf16_f32 v94, v36, v37\n"
    "v_mfma_f32_16x16x32_bf16 a[0:3], a[96:99], v[104:107], a[0:3]\n"
    "v_exp_f32_e32 v39, v39\n"
    "v_mfma_f32_16x16x32_bf16 a[32:35], a[96:99], v[108:111], a[32:35]\n"
    "v_exp_f32_e32 v40, v40\n"
    "v_mfma_f32_16x16x32_bf16 v[120:123], a[96:99], v[112:115], v[120:123]\n"
    "buffer_load_dwordx4 v2, s[52:55], s34 offen offset:1024 sc1 lds\n"
    "v_cvt_pk_bf16_f32 v95, v38, v39\n"
    "v_mfma_f32_16x16x32_bf16 v[152:155], a[96:99], v[116:119], v[152:155]\n"
    "v_exp_f32_e32 v41, v41\n"
    "v_mfma_f32_16x16x32_bf16 a[192:195], a[208:211], v[104:107], a[192:195]\n"
    "ds_read_b128 a[96:99], v1 offset:0\n"
    "v_mfma_f32_16x16x32_bf16 a[4:7], a[100:103], v[104:107], a[4:7]\n"
    "v_exp_f32_e32 v42, v42\n"
    "v_mfma_f32_16x16x32_bf16 a[36:39], a[100:103], v[108:111], a[36:39]\n"
    "buffer_load_dwordx4 v2, s[52:55], s34 offen offset:2048 sc1 lds\n"
    "v_cvt_pk_bf16_f32 v96, v40, v41\n"
    "v_mfma_f32_16x16x32_bf16 v[124:127], a[100:103], v[112:115], v[124:127]\n"
    "v_exp_f32_e32 v43, v43\n"
    "v_mfma_f32_16x16x32_bf16 v[156:159], a[100:103], v[116:119], v[156:159]\n"
    "ds_read_b128 a[100:103], v1 offset:1024\n"
    "v_mfma_f32_16x16x32_bf16 a[8:11], a[104:107], v[104:107], a[8:11]\n"
    "v_exp_f32_e32 v44, v44\n"
    "v_mfma_f32_16x16x32_bf16 a[40:43], a[104:107], v[108:111], a[40:43]\n"
    "buffer_load_dwordx4 v2, s[52:55], s34 offen offset:3072 sc1 lds\n"
    "v_cvt_pk_bf16_f32 v97, v42, v43\n"
    "v_mfma_f32_16x16x32_bf16 v[128:131], a[104:107], v[112:115], v[128:131]\n"
    "v_exp_f32_e32 v45, v45\n"
    "v_mfma_f32_16x16x32_bf16 v[160:163], a[104:107], v[116:119], v[160:163]\n"
    "ds_read_b128 a[104:107], v1 offset:2048\n"
    "v_mfma_f32_16x16x32_bf16 a[196:199], a[208:211], v[108:111], a[196:199]\n"
    "v_exp_f32_e32 v46, v46\n"
    "v_mfma_f32_16x16x32_bf16 a[12:15], a[108:111], v[104:107], a[12:15]\n"
    "v_cvt_pk_bf16_f32 v98, v44, v45\n"
    "v_mfma_f32_16x16x32_bf16 a[44:47], a[108:111], v[108:111], a[44:47]\n"
    "v_exp_f32_e32 v47, v47\n"
    "v_mfma_f32_16x16x32_bf16 v[132:135], a[108:111], v[112:115], v[132:135]\n"
    "v_exp_f32_e32 v48, v48\n"
    "v_mfma_f32_16x16x32_bf16 v[164:167], a[108:111], v[116:119], v[164:167]\n"
    "ds_read_b128 a[108:111], v1 offset:3072\n"
    "v_cvt_pk_bf16_f32 v99, v46, v47\n"
    "v_mfma_f32_16x16x32_bf16 a[16:19], a[112:115], v[104:107], a[16:19]\n"
    "v_exp_f32_e32 v49, v49\n"
    "v_mfma_f32_16x16x32_bf16 a[48:51], a[112:115], v[108:111], a[48:51]\n"
    "v_exp_f32_e32 v50, v50\n"
    "v_mfma_f32_16x16x32_bf16 v[136:139], a[112:115], v[112:115], v[136:139]\n"
    "v_cvt_pk_bf16_f32 v100, v48, v49\n"
    "v_mfma_f32_16x16x32_bf16 v[168:171], a[112:115], v[116:119], v[168:171]\n"
    "ds_read_b128 a[112:115], v1 offset:4096\n"
    "v_mfma_f32_16x16x32_bf16 a[200:203], a[208:211], v[112:115], a[200:203]\n"
    "v_exp_f32_e32 v51, v51\n"
    "v_mfma_f32_16x16x32_bf16 a[20:23], a[116:119], v[104:107], a[20:23]\n"
    "v_mfma_f32_16x16x32_bf16 a[52:55], a[116:119], v[108:111], a[52:55]\n"
    "v_exp_f32_e32 v52, v52\n"
    "v_mfma_f32_16x16x32_bf16 v[140:143], a[116:119], v[112:115], v[140:143]\n"
    "v_cvt_pk_bf16_f32 v101, v50, v51\n"
    "v_mfma_f32_16x16x32_bf16 v[172:175], a[116:119], v[116:119], v[172:175]\n"
    "ds_read_b128 a[116:119], v1 offset:5120\n"
    "v_mfma_f32_16x16x32_bf16 a[24:27], a[120:123], v[104:107], a[24:27]\n"
    "v_exp_f32_e32 v53, v53\n"
    "v_mfma_f32_16x16x32_bf16 a[56:59], a[120:123], v[108:111], a[56:59]\n"
    "v_exp_f32_e32 v54, v54\n"
    "v_mfma_f32_16x16x32_bf16 v[144:147], a[120:123], v[112:115], v[144:147]\n"
    "v_cvt_pk_bf16_f32 v102, v52, v53\n"
    "v_mfma_f32_16x16x32_bf16 v[176:179], a[120:123], v[116:119], v[176:179]\n"
    "ds_read_b128 a[120:123], v1 offset:6144\n"
    "v_mfma_f32_16x16x32_bf16 a[204:207], a[208:211], v[116:119], a[204:207]\n"
    "v_exp_f32_e32 v55, v55\n"
    "v_mfma_f32_16x16x32_bf16 a[28:31], a[124:127], v[104:107], a[28:31]\n"
    "v_mfma_f32_16x16x32_bf16 a[60:63], a[124:127], v[108:111], a[60:63]\n"
    "v_mfma_f32_16x16x32_bf16 v[148:151], a[124:127], v[112:115], v[148:151]\n"
    "v_mfma_f32_16x16x32_bf16 v[180:183], a[124:127], v[116:119], v[180:183]\n"
    "ds_read_b128 a[124:127], v1 offset:7168\n"
    "v_cvt_pk_bf16_f32 v103, v54, v55\n"
    "s_waitcnt lgkmcnt(8)\n"
    "v_mfma_f32_16x16x32_bf16 v[24:27], a[64:67], a[128:131], v[184:187]\n"
    "v_exp_f32_e32 v56, v56\n"
    "v_mfma_f32_16x16x32_bf16 v[32:35], a[64:67], a[144:147], v[192:195]\n"
    "v_exp_f32_e32 v57, v57\n"
    "v_mfma_f32_16x16x32_bf16 v[40:43], a[64:67], a[160:163], v[200:203]\n"
    "s_min_u32 s33, s24, 4\n"
    "v_mad_u64_u32 v[252:253], vcc, v251, s33, v[254:255]\n"
    "v_mfma_f32_16x16x32_bf16 v[48:51], a[64:67], a[176:179], v[208:211]\n"
    "v_exp_f32_e32 v58, v58\n"
    "v_mfma_f32_16x16x32_bf16 v[28:31], a[68:71], a[128:131], v[188:191]\n"
    "ds_read_b128 a[64:67], v0 offset:8192\n"
    "buffer_load_dword v249, v248, s[16:19], s46 offen\n"
    "v_mfma_f32_16x16x32_bf16 v[36:39], a[68:71], a[144:147], v[196:199]\n"
    "v_cvt_pk_bf16_f32 v104, v56, v57\n"
    "v_mfma_f32_16x16x32_bf16 v[44:47], a[68:71], a[160:163], v[204:207]\n"
    "v_exp_f32_e32 v59, v59\n"
    "v_mfma_f32_16x16x32_bf16 v[52:55], a[68:71], a[176:179], v[212:215]\n"
    "v_exp_f32_e32 v60, v60\n"
    "v_mfma_f32_16x16x32_bf16 v[24:27], a[72:75], a[132:135], v[24:27]\n"
    "ds_read_b128 a[68:71], v0 offset:9216\n"
    "global_load_dword v249, v[252:253], off\n"
    "v_mfma_f32_16x16x32_bf16 v[32:35], a[72:75], a[148:151], v[32:35]\n"
    "v_cvt_pk_bf16_f32 v105, v58, v59\n"
    "v_mfma_f32_16x16x32_bf16 v[40:43], a[72:75], a[164:167], v[40:43]\n"
    "v_exp_f32_e32 v61, v61\n"
    "v_mfma_f32_16x16x32_bf16 v[48:51], a[72:75], a[180:183], v[48:51]\n"
    "v_exp_f32_e32 v62, v62\n"
    "v_mfma_f32_16x16x32_bf16 v[28:31], a[76:79], a[132:135], v[28:31]\n"
    "ds_read_b128 a[72:75], v0 offset:10240\n"
    "s_add_u32 s46, s46, 0x200000\n"
    "v_mfma_f32_16x16x32_bf16 v[36:39], a[76:79], a[148:151], v[36:39]\n"
    "s_add_u32 s86, s86, 16384\n"
    "s_add_u32 s34, s34, 16384\n"
    "v_mfma_f32_16x16x32_bf16 v[44:47], a[76:79], a[164:167], v[44:47]\n"
    "s_xor_b32 s36, s36, 0x8000\n"
    "s_xor_b32 s37, s37, 0x8000\n"
    "v_mfma_f32_16x16x32_bf16 v[52:55], a[76:79], a[180:183], v[52:55]\n"
    "ds_read_b128 a[76:79], v0 offset:11264\n"
    "s_add_u32 s24, s24, 1\n"
    "v_mfma_f32_16x16x32_bf16 v[24:27], a[80:83], a[136:139], v[24:27]\n"
    "v_cvt_pk_bf16_f32 v106, v60, v61\n"
    "v_mfma_f32_16x16x32_bf16 v[32:35], a[80:83], a[152:155], v[32:35]\n"
    "v_exp_f32_e32 v63, v63\n"
    "v_mfma_f32_16x16x32_bf16 v[40:43], a[80:83], a[168:171], v[40:43]\n"
    "v_exp_f32_e32 v64, v64\n"
    "v_mfma_f32_16x16x32_bf16 v[48:51], a[80:83], a[184:187], v[48:51]\n"
    "ds_read_b128 a[80:83], v0 offset:12288\n"
    "v_cvt_pk_bf16_f32 v107, v62, v63\n"
    "v_mfma_f32_16x16x32_bf16 v[28:31], a[84:87], a[136:139], v[28:31]\n"
    "v_exp_f32_e32 v65, v65\n"
    "v_mfma_f32_16x16x32_bf16 v[36:39], a[84:87], a[152:155], v[36:39]\n"
    "v_exp_f32_e32 v66, v66\n"
    "v_mfma_f32_16x16x32_bf16 v[44:47], a[84:87], a[168:171], v[44:47]\n"
    "v_cvt_pk_bf16_f32 v108, v64, v65\n"
    "v_mfma_f32_16x16x32_bf16 v[52:55], a[84:87], a[184:187], v[52:55]\n"
    "v_exp_f32_e32 v67, v67\n"
    "v_mfma_f32_16x16x32_bf16 v[24:27], a[88:91], a[140:143], v[24:27]\n"
    "ds_read_b128 a[84:87], v0 offset:13312\n"
    "v_mfma_f32_16x16x32_bf16 v[32:35], a[88:91], a[156:159], v[32:35]\n"
    "v_exp_f32_e32 v68, v68\n"
    "v_mfma_f32_16x16x32_bf16 v[40:43], a[88:91], a[172:175], v[40:43]\n"
    "v_cvt_pk_bf16_f32 v109, v66, v67\n"
    "v_mfma_f32_16x16x32_bf16 v[48:51], a[88:91], a[188:191], v[48:51]\n"
    "v_exp_f32_e32 v69, v69\n"
    "v_mfma_f32_16x16x32_bf16 v[28:31], a[92:95], a[140:143], v[28:31]\n"
    "ds_read_b128 a[88:91], v0 offset:14336\n"
    "v_mfma_f32_16x16x32_bf16 v[36:39], a[92:95], a[156:159], v[36:39]\n"
    "v_exp_f32_e32 v70, v70\n"
    "v_mfma_f32_16x16x32_bf16 v[44:47], a[92:95], a[172:175], v[44:47]\n"
    "v_cvt_pk_bf16_f32 v110, v68, v69\n"
    "v_mfma_f32_16x16x32_bf16 v[52:55], a[92:95], a[188:191], v[52:55]\n"
    "ds_read_b128 a[92:95], v0 offset:15360\n"
    "s_waitcnt lgkmcnt(8)\n"
    "v_mfma_f32_16x16x32_bf16 a[0:3], a[96:99], v[88:91], a[0:3]\n"
    "v_exp_f32_e32 v71, v71\n"
    "v_mfma_f32_16x16x32_bf16 a[32:35], a[96:99], v[92:95], a[32:35]\n"
    "v_exp_f32_e32 v72, v72\n"
    "v_mfma_f32_16x16x32_bf16 v[120:123], a[96:99], v[96:99], v[120:123]\n"
    "v_cvt_pk_bf16_f32 v111, v70, v71\n"
    "v_mfma_f32_16x16x32_bf16 v[152:155], a[96:99], v[100:103], v[152:155]\n"
    "v_exp_f32_e32 v73, v73\n"
    "v_mfma_f32_16x16x32_bf16 a[192:195], a[208:211], v[88:91], a[192:195]\n"
    "ds_read_b128 a[96:99], v1 offset:8192\n"
    "v_mfma_f32_16x16x32_bf16 a[4:7], a[100:103], v[88:91], a[4:7]\n"
    "v_exp_f32_e32 v74, v74\n"
    "v_mfma_f32_16x16x32_bf16 a[36:39], a[100:103], v[92:95], a[36:39]\n"
    "v_cvt_pk_bf16_f32 v112, v72, v73\n"
    "v_mfma_f32_16x16x32_bf16 v[124:127], a[100:103], v[96:99], v[124:127]\n"
    "v_exp_f32_e32 v75, v75\n"
    "v_mfma_f32_16x16x32_bf16 v[156:159], a[100:103], v[100:103], v[156:159]\n"
    "ds_read_b128 a[100:103], v1 offset:9216\n"
    "v_mfma_f32_16x16x32_bf16 a[8:11], a[104:107], v[88:91], a[8:11]\n"
    "v_exp_f32_e32 v76, v76\n"
    "v_mfma_f32_16x16x32_bf16 a[40:43], a[104:107], v[92:95], a[40:43]\n"
    "v_cvt_pk_bf16_f32 v113, v74, v75\n"
    "v_mfma_f32_16x16x32_bf16 v[128:131], a[104:107], v[96:99], v[128:131]\n"
    "v_exp_f32_e32 v77, v77\n"
    "v_mfma_f32_16x16x32_bf16 v[160:163], a[104:107], v[100:103], v[160:163]\n"
    "ds_read_b128 a[104:107], v1 offset:10240\n"
    "v_mfma_f32_16x16x32_bf16 a[196:199], a[208:211], v[92:95], a[196:199]\n"
    "v_exp_f32_e32 v78, v78\n"
    "v_mfma_f32_16x16x32_bf16 a[12:15], a[108:111], v[88:91], a[12:15]\n"
    "v_cvt_pk_bf16_f32 v114, v76, v77\n"
    "v_mfma_f32_16x16x32_bf16 a[44:47], a[108:111], v[92:95], a[44:47]\n"
    "v_exp_f32_e32 v79, v79\n"
    "v_mfma_f32_16x16x32_bf16 v[132:135], a[108:111], v[96:99], v[132:135]\n"
    "v_exp_f32_e32 v80, v80\n"
    "v_mfma_f32_16x16x32_bf16 v[164:167], a[108:111], v[100:103], v[164:167]\n"
    "ds_read_b128 a[108:111], v1 offset:11264\n"
    "v_cvt_pk_bf16_f32 v115, v78, v79\n"
    "v_mfma_f32_16x16x32_bf16 a[16:19], a[112:115], v[88:91], a[16:19]\n"
    "v_exp_f32_e32 v81, v81\n"
    "v_mfma_f32_16x16x32_bf16 a[48:51], a[112:115], v[92:95], a[48:51]\n"
    "v_exp_f32_e32 v82, v82\n"
    "v_mfma_f32_16x16x32_bf16 v[136:139], a[112:115], v[96:99], v[136:139]\n"
    "v_cvt_pk_bf16_f32 v116, v80, v81\n"
    "v_mfma_f32_16x16x32_bf16 v[168:171], a[112:115], v[100:103], v[168:171]\n"
    "ds_read_b128 a[112:115], v1 offset:12288\n"
    "v_mfma_f32_16x16x32_bf16 a[200:203], a[208:211], v[96:99], a[200:203]\n"
    "v_exp_f32_e32 v83, v83\n"
    "v_mfma_f32_16x16x32_bf16 a[20:23], a[116:119], v[88:91], a[20:23]\n"
    "v_mfma_f32_16x16x32_bf16 a[52:55], a[116:119], v[92:95], a[52:55]\n"
    "v_exp_f32_e32 v84, v84\n"
    "v_mfma_f32_16x16x32_bf16 v[140:143], a[116:119], v[96:99], v[140:143]\n"
    "v_cvt_pk_bf16_f32 v117, v82, v83\n"
    "v_mfma_f32_16x16x32_bf16 v[172:175], a[116:119], v[100:103], v[172:175]\n"
    "ds_read_b128 a[116:119], v1 offset:13312\n"
    "v_mfma_f32_16x16x32_bf16 a[24:27], a[120:123], v[88:91], a[24:27]\n"
    "v_exp_f32_e32 v85, v85\n"
    "v_mfma_f32_16x16x32_bf16 a[56:59], a[120:123], v[92:95], a[56:59]\n"
    "v_exp_f32_e32 v86, v86\n"
    "v_mfma_f32_16x16x32_bf16 v[144:147], a[120:123], v[96:99], v[144:147]\n"
    "v_cvt_pk_bf16_f32 v118, v84, v85\n"
    "v_mfma_f32_16x16x32_bf16 v[176:179], a[120:123], v[100:103], v[176:179]\n"
    "ds_read_b128 a[120:123], v1 offset:14336\n"
    "v_mfma_f32_16x16x32_bf16 a[204:207], a[208:211], v[100:103], a[204:207]\n"
    "v_exp_f32_e32 v87, v87\n"
    "v_mfma_f32_16x16x32_bf16 a[28:31], a[124:127], v[88:91], a[28:31]\n"
    "v_mfma_f32_16x16x32_bf16 a[60:63], a[124:127], v[92:95], a[60:63]\n"
    "v_mfma_f32_16x16x32_bf16 v[148:151], a[124:127], v[96:99], v[148:151]\n"
    "v_mfma_f32_16x16x32_bf16 v[180:183], a[124:127], v[100:103], v[180:183]\n"
    "ds_read_b128 a[124:127], v1 offset:15360\n"
    "v_cvt_pk_bf16_f32 v119, v86, v87\n"
    "v_xor_b32 v0, 0x8000, v0\n"
    "v_xor_b32 v1, 0x8000, v1\n"
    "s_cmp_lt_u32 s24, s47\n"
    "s_cbranch_scc1 L_A_%=\n"
    "s_lshl_b32 s25, s24, 1\n"
    "s_add_u32 s47, s23, 1\n"
    "s_min_u32 s47, s47, 15\n"
    ".p2align 6\n"
    "L_H1_%=:\n"
    "s_waitcnt lgkmcnt(8)\n"
    "v_mfma_f32_16x16x32_bf16 v[56:59], a[64:67], a[128:131], v[184:187]\n"
    "s_add_u32 s30, s24, 2\n"
    "s_cmp_ge_u32 s30, s23\n"
    "s_cselect_b32 s38, 0x8000, s38\n"
    "v_mfma_f32_16x16x32_bf16 v[64:67], a[64:67], a[144:147], v[192:195]\n"
    "v_exp_f32_e32 v24, v24\n"
    "v_mfma_f32_16x16x32_bf16 v[72:75], a[64:67], a[160:163], v[200:203]\n"
    "s_add_u32 s30, s24, 1\n"
    "s_cmp_ge_u32 s30, s23\n"
    "s_cselect_b32 s39, 0x8000, s39\n"
    "v_mfma_f32_16x16x32_bf16 v[80:83], a[64:67], a[176:179], v[208:211]\n"
    "v_exp_f32_e32 v25, v25\n"
    "v_mfma_f32_16x16x32_bf16 v[60:63], a[68:71], a[128:131], v[188:191]\n"
    "s_cmp_eq_u32 s30, s23\n"
    "s_cselect_b32 s30, s80, 0\n"
    "s_cselect_b64 vcc, -1, 0\n"
    "v_mfma_f32_16x16x32_bf16 v[68:71], a[68:71], a[144:147], v[196:199]\n"
    "v_exp_f32_e32 v26, v26\n"
    "v_mfma_f32_16x16x32_bf16 v[76:79], a[68:71], a[160:163], v[204:207]\n"
    "v_add_u32 v0, s30, v0\n"
    "s_cmp_eq_u32 s24, s23\n"
    "s_cselect_b32 s30, s80, 0\n"
    "v_mfma_f32_16x16x32_bf16 v[84:87], a[68:71], a[176:179], v[212:215]\n"
    "v_add_u32 v1, s30, v1\n"
    "v_cvt_pk_bf16_f32 v88, v24, v25\n"
    "v_mfma_f32_16x16x32_bf16 v[56:59], a[72:75], a[132:135], v[56:59]\n"
    "v_exp_f32_e32 v27, v27\n"
    "v_mfma_f32_16x16x32_bf16 v[64:67], a[72:75], a[148:151], v[64:67]\n"
    "v_exp_f32_e32 v28, v28\n"
    "v_mfma_f32_16x16x32_bf16 v[72:75], a[72:75], a[164:167], v[72:75]\n"
    "v_cvt_pk_bf16_f32 v89, v26, v27\n"
    "v_mfma_f32_16x16x32_bf16 v[80:83], a[72:75], a[180:183], v[80:83]\n"
    "s_waitcnt vmcnt(2) lgkmcnt(0)\n"
    "s_barrier\n"
    "ds_read_b64 v[216:217], v18\n"
    "v_mfma_f32_16x16x32_bf16 v[60:63], a[76:79], a[132:135], v[60:63]\n"
    "ds_read_b128 a[64:67], v0 offset:0\n"
    "ds_read_b128 a[68:71], v0 offset:1024\n"
    "v_mfma_f32_16x16x32_bf16 v[68:71], a[76:79], a[148:151], v[68:71]\n"
    "v_exp_f32_e32 v29, v29\n"
    "v_mfma_f32_16x16x32_bf16 v[76:79], a[76:79], a[164:167], v[76:79]\n"
    "ds_read_b128 a[72:75], v0 offset:2048\n"
    "s_mov_b32 m0, s36\n"
    "v_mfma_f32_16x16x32_bf16 v[84:87], a[76:79], a[180:183], v[84:87]\n"
    "v_exp_f32_e32 v30, v30\n"
    "v_mfma_f32_16x16x32_bf16 v[56:59], a[80:83], a[136:139], v[56:59]\n"
    "ds_read_b128 a[76:79], v0 offset:3072\n"
    "buffer_load_dwordx4 v2, s[48:51], s86 offen offset:0 sc1 lds\n"
    "v_cvt_pk_bf16_f32 v90, v28, v29\n"
    "v_mfma_f32_16x16x32_bf16 v[64:67], a[80:83], a[152:155], v[64:67]\n"
    "v_exp_f32_e32 v31, v31\n"
    "v_mfma_f32_16x16x32_bf16 v[72:75], a[80:83], a[168:171], v[72:75]\n"
    "ds_write_b64 v17, v[8:9]\n"
    "s_nop 0\n"
    "v_mfma_f32_16x16x32_bf16 v[80:83], a[80:83], a[184:187], v[80:83]\n"
    "v_exp_f32_e32 v32, v32\n"
    "v_mfma_f32_16x16x32_bf16 v[60:63], a[84:87], a[136:139], v[60:63]\n"
    "ds_read_b128 a[80:83], v0 offset:4096\n"
    "buffer_load_dwordx4 v2, s[48:51], s86 offen offset:1024 sc1 lds\n"
    "v_cvt_pk_bf16_f32 v91, v30, v31\n"
    "v_mfma_f32_16x16x32_bf16 v[68:71], a[84:87], a[152:155], v[68:71]\n"
    "v_exp_f32_e32 v33, v33\n"
    "v_mfma_f32_16x16x32_bf16 v[76:79], a[84:87], a[168:171], v[76:79]\n"
    "s_waitcnt lgkmcnt(6)\n"
    "v_cndmask_b32 v10, v10, v216, vcc\n"
    "v_mfma_f32_16x16x32_bf16 v[84:87], a[84:87], a[184:187], v[84:87]\n"
    "v_exp_f32_e32 v34, v34\n"
    "v_mfma_f32_16x16x32_bf16 v[56:59], a[88:91], a[140:143], v[56:59]\n"
    "ds_read_b128 a[84:87], v0 offset:5120\n"
    "buffer_load_dwordx4 v2, s[48:51], s86 offen offset:2048 sc1 lds\n"
    "v_cvt_pk_bf16_f32 v92, v32, v33\n"
    "v_mfma_f32_16x16x32_bf16 v[64:67], a[88:91], a[156:159], v[64:67]\n"
    "v_exp_f32_e32 v35, v35\n"
    "v_mfma_f32_16x16x32_bf16 v[72:75], a[88:91], a[172:175], v[72:75]\n"
    "v_cndmask_b32 v11, v11, v217, vcc\n"
    "s_add_u32 s25, s25, 1\n"
    "v_mfma_f32_16x16x32_bf16 v[80:83], a[88:91], a[188:191], v[80:83]\n"
    "v_exp_f32_e32 v36, v36\n"
    "v_mfma_f32_16x16x32_bf16 v[60:63], a[92:95], a[140:143], v[60:63]\n"
    "ds_read_b128 a[88:91], v0 offset:6144\n"
    "buffer_load_dwordx4 v2, s[48:51], s86 offen offset:3072 sc1 lds\n"
    "v_cvt_pk_bf16_f32 v93, v34, v35\n"
    "v_mfma_f32_16x16x32_bf16 v[68:71], a[92:95], a[156:159], v[68:71]\n"
    "v_exp_f32_e32 v37, v37\n"
    "v_mfma_f32_16x16x32_bf16 v[76:79], a[92:95], a[172:175], v[76:79]\n"
    "s_bitcmp1_b64 s[78:79], s25\n"
    "s_cselect_b32 s31, 0xff800000, s97\n"
    "v_mfma_f32_16x16x32_bf16 v[84:87], a[92:95], a[188:191], v[84:87]\n"
    "ds_read_b128 a[92:95], v0 offset:7168\n"
    "s_bitcmp1_b64 s[76:77], s25\n"
    "s_cselect_b32 s30, 0xff800000, s97\n"
    "v_mfma_f32_16x16x32_bf16 a[0:3], a[96:99], v[104:107], a[0:3]\n"
    "v_exp_f32_e32 v38, v38\n"
    "v_cvt_pk_bf16_f32 v94, v36, v37\n"
    "v_mfma_f32_16x16x32_bf16 a[32:35], a[96:99], v[108:111], a[32:35]\n"
    "v_exp_f32_e32 v39, v39\n"
    "v_mfma_f32_16x16x32_bf16 v[120:123], a[96:99], v[112:115], v[120:123]\n"
    "s_add_u32 m0, s36, 16384\n"
    "buffer_load_dwordx4 v15, s[48:51], s86 offen offset:0 sc1 lds\n"
    "v_mfma_f32_16x16x32_bf16 v[152:155], a[96:99], v[116:119], v[152:155]\n"
    "v_exp_f32_e32 v40, v40\n"
    "v_mfma_f32_16x16x32_bf16 a[192:195], a[208:211], v[104:107], a[192:195]\n"
    "ds_read_b128 a[96:99], v1 offset:0\n"
    "v_cvt_pk_bf16_f32 v95, v38, v39\n"
    "v_mfma_f32_16x16x32_bf16 a[4:7], a[100:103], v[104:107], a[4:7]\n"
    "v_exp_f32_e32 v41, v41\n"
    "v_mfma_f32_16x16x32_bf16 a[36:39], a[100:103], v[108:111], a[36:39]\n"
    "buffer_load_dwordx4 v15, s[48:51], s86 offen offset:1024 sc1 lds\n"
    "v_min_f32 v218, s31, v10\n"
    "v_mfma_f32_16x16x32_bf16 v[124:127], a[100:103], v[112:115], v[124:127]\n"
    "v_exp_f32_e32 v42, v42\n"
    "v_mfma_f32_16x16x32_bf16 v[156:159], a[100:103], v[116:119], v[156:159]\n"
    "ds_read_b128 a[100:103], v1 offset:1024\n"
    "v_cvt_pk_bf16_f32 v96, v40, v41\n"
    "v_mfma_f32_16x16x32_bf16 a[8:11], a[104:107], v[104:107], a[8:11]\n"
    "v_exp_f32_e32 v43, v43\n"
    "v_mfma_f32_16x16x32_bf16 a[40:43], a[104:107], v[108:111], a[40:43]\n"
    "buffer_load_dwordx4 v15, s[48:51], s86 offen offset:2048 sc1 lds\n"
    "v_min_f32 v219, s31, v11\n"
    "v_mfma_f32_16x16x32_bf16 v[128:131], a[104:107], v[112:115], v[128:131]\n"
    "v_exp_f32_e32 v44, v44\n"
    "v_mfma_f32_16x16x32_bf16 v[160:163], a[104:107], v[116:119], v[160:163]\n"
    "ds_read_b128 a[104:107], v1 offset:2048\n"
    "v_cvt_pk_bf16_f32 v97, v42, v43\n"
    "v_mfma_f32_16x16x32_bf16 a[196:199], a[208:211], v[108:111], a[196:199]\n"
    "v_exp_f32_e32 v45, v45\n"
    "v_mfma_f32_16x16x32_bf16 a[12:15], a[108:111], v[104:107], a[12:15]\n"
    "buffer_load_dwordx4 v15, s[48:51], s86 offen offset:3072 sc1 lds\n"
    "v_min_f32 v204, s30, v218\n"
    "v_mfma_f32_16x16x32_bf16 a[44:47], a[108:111], v[108:111], a[44:47]\n"
    "v_exp_f32_e32 v46, v46\n"
    "v_mfma_f32_16x16x32_bf16 v[132:135], a[108:111], v[112:115], v[132:135]\n"
    "v_cvt_pk_bf16_f32 v98, v44, v45\n"
    "v_mov_b32 v205, v204\n"
    "v_mfma_f32_16x16x32_bf16 v[164:167], a[108:111], v[116:119], v[164:167]\n"
    "v_exp_f32_e32 v47, v47\n"
    "v_mfma_f32_16x16x32_bf16 a[16:19], a[112:115], v[104:107], a[16:19]\n"
    "ds_read_b128 a[108:111], v1 offset:3072\n"
    "s_mov_b32 m0, s37\n"
    "v_mfma_f32_16x16x32_bf16 a[48:51], a[112:115], v[108:111], a[48:51]\n"
    "v_exp_f32_e32 v48, v48\n"
    "v_mfma_f32_16x16x32_bf16 v[136:139], a[112:115], v[112:115], v[136:139]\n"
    "buffer_load_dwordx4 v2, s[52:55], s34 offen offset:0 sc1 lds\n"
    "v_cvt_pk_bf16_f32 v99, v46, v47\n"
    "v_mfma_f32_16x16x32_bf16 v[168:171], a[112:115], v[116:119], v[168:171]\n"
    "v_exp_f32_e32 v49, v49\n"
    "v_mfma_f32_16x16x32_bf16 a[200:203], a[208:211], v[112:115], a[200:203]\n"
    "ds_read_b128 a[112:115], v1 offset:4096\n"
    "v_mov_b32 v206, v204\n"
    "v_mfma_f32_16x16x32_bf16 a[20:23], a[116:119], v[104:107], a[20:23]\n"
    "v_exp_f32_e32 v50, v50\n"
    "v_mfma_f32_16x16x32_bf16 a[52:55], a[116:119], v[108:111], a[52:55]\n"
    "buffer_load_dwordx4 v2, s[52:55], s34 offen offset:1024 sc1 lds\n"
    "v_cvt_pk_bf16_f32 v100, v48, v49\n"
    "v_mfma_f32_16x16x32_bf16 v[140:143], a[116:119], v[112:115], v[140:143]\n"
    "v_exp_f32_e32 v51, v51\n"
    "v_mfma_f32_16x16x32_bf16 v[172:175], a[116:119], v[116:119], v[172:175]\n"
    "ds_read_b128 a[116:119], v1 offset:5120\n"
    "v_mov_b32 v207, v204\n"
    "v_mfma_f32_16x16x32_bf16 a[24:27], a[120:123], v[104:107], a[24:27]\n"
    "v_exp_f32_e32 v52, v52\n"
    "v_mfma_f32_16x16x32_bf16 a[56:59], a[120:123], v[108:111], a[56:59]\n"
    "v_cvt_pk_bf16_f32 v101, v50, v51\n"
    "v_mov_b32 v208, v219\n"
    "v_mov_b32 v209, v219\n"
    "v_mfma_f32_16x16x32_bf16 v[144:147], a[120:123], v[112:115], v[144:147]\n"
    "v_exp_f32_e32 v53, v53\n"
    "v_mov_b32 v210, v219\n"
    "v_mov_b32 v211, v219\n"
    "v_mfma_f32_16x16x32_bf16 v[176:179], a[120:123], v[116:119], v[176:179]\n"
    "ds_read_b128 a[120:123], v1 offset:6144\n"
    "v_max_f32 v220, s30, v244\n"
    "v_min_f32 v200, v220, v218\n"
    "v_mfma_f32_16x16x32_bf16 a[204:207], a[208:211], v[116:119], a[204:207]\n"
    "v_exp_f32_e32 v54, v54\n"
    "v_min_f32 v212, v220, v219\n"
    "v_max_f32 v221, s30, v245\n"
    "v_mfma_f32_16x16x32_bf16 a[28:31], a[124:127], v[104:107], a[28:31]\n"
    "v_cvt_pk_bf16_f32 v102, v52, v53\n"
    "v_min_f32 v201, v221, v218\n"
    "v_min_f32 v213, v221, v219\n"
    "v_mfma_f32_16x16x32_bf16 a[60:63], a[124:127], v[108:111], a[60:63]\n"
    "v_exp_f32_e32 v55, v55\n"
    "v_max_f32 v220, s30, v246\n"
    "v_min_f32 v202, v220, v218\n"
    "v_mfma_f32_16x16x32_bf16 v[148:151], a[124:127], v[112:115], v[148:151]\n"
    "v_min_f32 v214, v220, v219\n"
    "v_max_f32 v221, s30, v247\n"
    "v_mfma_f32_16x16x32_bf16 v[180:183], a[124:127], v[116:119], v[180:183]\n"
    "ds_read_b128 a[124:127], v1 offset:7168\n"
    "v_min_f32 v203, v221, v218\n"
    "v_min_f32 v215, v221, v219\n"
    "v_cvt_pk_bf16_f32 v103, v54, v55\n"
    "s_bitcmp1_b64 s[26:27], s25\n"
    "s_cbranch_scc1 L_hkH1_%=\n"
    "L_rtH1_%=:\n"
    "s_waitcnt lgkmcnt(8)\n"
    "v_mfma_f32_16x16x32_bf16 v[24:27], a[64:67], a[128:131], v[184:187]\n"
    "v_exp_f32_e32 v56, v56\n"
    "v_mfma_f32_16x16x32_bf16 v[32:35], a[64:67], a[144:147], v[192:195]\n"
    "v_exp_f32_e32 v57, v57\n"
    "v_mfma_f32_16x16x32_bf16 v[40:43], a[64:67], a[160:163], v[200:203]\n"
    "buffer_load_dwordx4 v2, s[52:55], s34 offen offset:2048 sc1 lds\n"
    "v_mfma_f32_16x16x32_bf16 v[48:51], a[64:67], a[176:179], v[208:211]\n"
    "v_exp_f32_e32 v58, v58\n"
    "v_mfma_f32_16x16x32_bf16 v[28:31], a[68:71], a[128:131], v[188:191]\n"
    "ds_read_b128 a[64:67], v0 offset:8192\n"
    "v_cvt_pk_bf16_f32 v104, v56, v57\n"
    "v_mfma_f32_16x16x32_bf16 v[36:39], a[68:71], a[144:147], v[196:199]\n"
    "v_exp_f32_e32 v59, v59\n"
    "v_mfma_f32_16x16x32_bf16 v[44:47], a[68:71], a[160:163], v[204:207]\n"
    "buffer_load_dwordx4 v2, s[52:55], s34 offen offset:3072 sc1 lds\n"
    "v_mfma_f32_16x16x32_bf16 v[52:55], a[68:71], a[176:179], v[212:215]\n"
    "v_exp_f32_e32 v60, v60\n"
    "v_mfma_f32_16x16x32_bf16 v[24:27], a[72:75], a[132:135], v[24:27]\n"
    "ds_read_b128 a[68:71], v0 offset:9216\n"
    "v_cvt_pk_bf16_f32 v105, v58, v59\n"
    "v_mfma_f32_16x16x32_bf16 v[32:35], a[72:75], a[148:151], v[32:35]\n"
    "v_exp_f32_e32 v61, v61\n"
    "v_mfma_f32_16x16x32_bf16 v[40:43], a[72:75], a[164:167], v[40:43]\n"
    "s_add_u32 m0, s37, 16384\n"
    "buffer_load_dwordx4 v15, s[52:55], s34 offen offset:0 sc1 lds\n"
    "v_mfma_f32_16x16x32_bf16 v[48:51], a[72:75], a[180:183], v[48:51]\n"
    "v_exp_f32_e32 v62, v62\n"
    "v_mfma_f32_16x16x32_bf16 v[28:31], a[76:79], a[132:135], v[28:31]\n"
    "ds_read_b128 a[72:75], v0 offset:10240\n"
    "v_cvt_pk_bf16_f32 v106, v60, v61\n"
    "v_mfma_f32_16x16x32_bf16 v[36:39], a[76:79], a[148:151], v[36:39]\n"
    "v_exp_f32_e32 v63, v63\n"
    "v_mfma_f32_16x16x32_bf16 v[44:47], a[76:79], a[164:167], v[44:47]\n"
    "buffer_load_dwordx4 v15, s[52:55], s34 offen offset:1024 sc1 lds\n"
    "s_add_u32 s25, s25, 1\n"
    "v_mfma_f32_16x16x32_bf16 v[52:55], a[76:79], a[180:183], v[52:55]\n"
    "v_exp_f32_e32 v64, v64\n"
    "v_mfma_f32_16x16x32_bf16 v[24:27], a[80:83], a[136:139], v[24:27]\n"
    "ds_read_b128 a[76:79], v0 offset:11264\n"
    "v_cvt_pk_bf16_f32 v107, v62, v63\n"
    "v_mfma_f32_16x16x32_bf16 v[32:35], a[80:83], a[152:155], v[32:35]\n"
    "v_exp_f32_e32 v65, v65\n"
    "v_mfma_f32_16x16x32_bf16 v[40:43], a[80:83], a[168:171], v[40:43]\n"
    "buffer_load_dwordx4 v15, s[52:55], s34 offen offset:2048 sc1 lds\n"
    "s_bitcmp1_b64 s[78:79], s25\n"
    "s_cselect_b32 s31, 0xff800000, s97\n"
    "v_mfma_f32_16x16x32_bf16 v[48:51], a[80:83], a[184:187], v[48:51]\n"
    "v_exp_f32_e32 v66, v66\n"
    "v_mfma_f32_16x16x32_bf16 v[28:31], a[84:87], a[136:139], v[28:31]\n"
    "ds_read_b128 a[80:83], v0 offset:12288\n"
    "v_cvt_pk_bf16_f32 v108, v64, v65\n"
    "v_mfma_f32_16x16x32_bf16 v[36:39], a[84:87], a[152:155], v[36:39]\n"
    "v_exp_f32_e32 v67, v67\n"
    "v_mfma_f32_16x16x32_bf16 v[44:47], a[84:87], a[168:171], v[44:47]\n"
    "buffer_load_dwordx4 v15, s[52:55], s34 offen offset:3072 sc1 lds\n"
    "s_bitcmp1_b64 s[76:77], s25\n"
    "s_cselect_b32 s30, 0xff800000, s97\n"
    "v_mfma_f32_16x16x32_bf16 v[52:55], a[84:87], a[184:187], v[52:55]\n"
    "v_exp_f32_e32 v68, v68\n"
    "v_mfma_f32_16x16x32_bf16 v[24:27], a[88:91], a[140:143], v[24:27]\n"
    "ds_read_b128 a[84:87], v0 offset:13312\n"
    "v_cvt_pk_bf16_f32 v109, v66, v67\n"
    "v_mfma_f32_16x16x32_bf16 v[32:35], a[88:91], a[156:159], v[32:35]\n"
    "v_exp_f32_e32 v69, v69\n"
    "v_mfma_f32_16x16x32_bf16 v[40:43], a[88:91], a[172:175], v[40:43]\n"
    "s_min_u32 s33, s24, 4\n"
    "v_mad_u64_u32 v[252:253], vcc, v251, s33, v[254:255]\n"
    "v_mfma_f32_16x16x32_bf16 v[48:51], a[88:91], a[188:191], v[48:51]\n"
    "v_exp_f32_e32 v70, v70\n"
    "v_mfma_f32_16x16x32_bf16 v[28:31], a[92:95], a[140:143], v[28:31]\n"
    "ds_read_b128 a[88:91], v0 offset:14336\n"
    "buffer_load_dword v249, v248, s[16:19], s46 offen\n"
    "v_mfma_f32_16x16x32_bf16 v[36:39], a[92:95], a[156:159], v[36:39]\n"
    "v_cvt_pk_bf16_f32 v110, v68, v69\n"
    "v_min_f32 v218, s31, v10\n"
    "v_mfma_f32_16x16x32_bf16 v[44:47], a[92:95], a[172:175], v[44:47]\n"
    "v_min_f32 v219, s31, v11\n"
    "v_min_f32 v204, s30, v218\n"
    "v_mfma_f32_16x16x32_bf16 v[52:55], a[92:95], a[188:191], v[52:55]\n"
    "ds_read_b128 a[92:95], v0 offset:15360\n"
    "s_waitcnt lgkmcnt(8)\n"
    "v_mfma_f32_16x16x32_bf16 a[0:3], a[96:99], v[88:91], a[0:3]\n"
    "global_load_dword v249, v[252:253], off\n"
    "v_mov_b32 v205, v204\n"
    "v_mfma_f32_16x16x32_bf16 a[32:35], a[96:99], v[92:95], a[32:35]\n"
    "v_exp_f32_e32 v71, v71\n"
    "v_mfma_f32_16x16x32_bf16 v[120:123], a[96:99], v[96:99], v[120:123]\n"
    "v_mov_b32 v206, v204\n"
    "v_mov_b32 v207, v204\n"
    "v_mfma_f32_16x16x32_bf16 v[152:155], a[96:99], v[100:103], v[152:155]\n"
    "v_exp_f32_e32 v72, v72\n"
    "v_mfma_f32_16x16x32_bf16 a[192:195], a[208:211], v[88:91], a[192:195]\n"
    "ds_read_b128 a[96:99], v1 offset:8192\n"
    "s_add_u32 s46, s46, 0x200000\n"
    "v_mfma_f32_16x16x32_bf16 a[4:7], a[100:103], v[88:91], a[4:7]\n"
    "s_add_u32 s86, s86, s38\n"
    "s_add_u32 s34, s34, s39\n"
    "v_cvt_pk_bf16_f32 v111, v70, v71\n"
    "v_mfma_f32_16x16x32_bf16 a[36:39], a[100:103], v[92:95], a[36:39]\n"
    "v_exp_f32_e32 v73, v73\n"
    "v_mfma_f32_16x16x32_bf16 v[124:127], a[100:103], v[96:99], v[124:127]\n"
    "v_exp_f32_e32 v74, v74\n"
    "v_mfma_f32_16x16x32_bf16 v[156:159], a[100:103], v[100:103], v[156:159]\n"
    "ds_read_b128 a[100:103], v1 offset:9216\n"
    "s_xor_b32 s36, s36, 0x8000\n"
    "v_mfma_f32_16x16x32_bf16 a[8:11], a[104:107], v[88:91], a[8:11]\n"
    "s_xor_b32 s37, s37, 0x8000\n"
    "s_add_u32 s24, s24, 1\n"
    "v_cvt_pk_bf16_f32 v112, v72, v73\n"
    "v_mfma_f32_16x16x32_bf16 a[40:43], a[104:107], v[92:95], a[40:43]\n"
    "v_exp_f32_e32 v75, v75\n"
    "v_mfma_f32_16x16x32_bf16 v[128:131], a[104:107], v[96:99], v[128:131]\n"
    "v_exp_f32_e32 v76, v76\n"
    "v_mfma_f32_16x16x32_bf16 v[160:163], a[104:107], v[100:103], v[160:163]\n"
    "ds_read_b128 a[104:107], v1 offset:10240\n"
    "v_cvt_pk_bf16_f32 v113, v74, v75\n"
    "v_mfma_f32_16x16x32_bf16 a[196:199], a[208:211], v[92:95], a[196:199]\n"
    "v_exp_f32_e32 v77, v77\n"
    "v_mfma_f32_16x16x32_bf16 a[12:15], a[108:111], v[88:91], a[12:15]\n"
    "v_mov_b32 v208, v219\n"
    "v_mov_b32 v209, v219\n"
    "v_mfma_f32_16x16x32_bf16 a[44:47], a[108:111], v[92:95], a[44:47]\n"
    "v_exp_f32_e32 v78, v78\n"
    "v_mfma_f32_16x16x32_bf16 v[132:135], a[108:111], v[96:99], v[132:135]\n"
    "v_cvt_pk_bf16_f32 v114, v76, v77\n"
    "v_mov_b32 v210, v219\n"
    "v_mfma_f32_16x16x32_bf16 v[164:167], a[108:111], v[100:103], v[164:167]\n"
    "v_exp_f32_e32 v79, v79\n"
    "v_mfma_f32_16x16x32_bf16 a[16:19], a[112:115], v[88:91], a[16:19]\n"
    "ds_read_b128 a[108:111], v1 offset:11264\n"
    "v_mov_b32 v211, v219\n"
    "v_mfma_f32_16x16x32_bf16 a[48:51], a[112:115], v[92:95], a[48:51]\n"
    "v_exp_f32_e32 v80, v80\n"
    "v_mfma_f32_16x16x32_bf16 v[136:139], a[112:115], v[96:99], v[136:139]\n"
    "v_cvt_pk_bf16_f32 v115, v78, v79\n"
    "v_max_f32 v220, s30, v244\n"
    "v_mfma_f32_16x16x32_bf16 v[168:171], a[112:115], v[100:103], v[168:171]\n"
    "v_exp_f32_e32 v81, v81\n"
    "v_mfma_f32_16x16x32_bf16 a[200:203], a[208:211], v[96:99], a[200:203]\n"
    "ds_read_b128 a[112:115], v1 offset:12288\n"
    "v_min_f32 v200, v220, v218\n"
    "v_mfma_f32_16x16x32_bf16 a[20:23], a[116:119], v[88:91], a[20:23]\n"
    "v_exp_f32_e32 v82, v82\n"
    "v_mfma_f32_16x16x32_bf16 a[52:55], a[116:119], v[92:95], a[52:55]\n"
    "v_cvt_pk_bf16_f32 v116, v80, v81\n"
    "v_min_f32 v212, v220, v219\n"
    "v_mfma_f32_16x16x32_bf16 v[140:143], a[116:119], v[96:99], v[140:143]\n"
    "v_exp_f32_e32 v83, v83\n"
    "v_mfma_f32_16x16x32_bf16 v[172:175], a[116:119], v[100:103], v[172:175]\n"
    "ds_read_b128 a[116:119], v1 offset:13312\n"
    "v_max_f32 v221, s30, v245\n"
    "v_mfma_f32_16x16x32_bf16 a[24:27], a[120:123], v[88:91], a[24:27]\n"
    "v_exp_f32_e32 v84, v84\n"
    "v_mfma_f32_16x16x32_bf16 a[56:59], a[120:123], v[92:95], a[56:59]\n"
    "v_cvt_pk_bf16_f32 v117, v82, v83\n"
    "v_min_f32 v201, v221, v218\n"
    "v_mfma_f32_16x16x32_bf16 v[144:147], a[120:123], v[96:99], v[144:147]\n"
    "v_exp_f32_e32 v85, v85\n"
    "v_mfma_f32_16x16x32_bf16 v[176:179], a[120:123], v[100:103], v[176:179]\n"
    "ds_read_b128 a[120:123], v1 offset:14336\n"
    "v_min_f32 v213, v221, v219\n"
    "v_mfma_f32_16x16x32_bf16 a[204:207], a[208:211], v[100:103], a[204:207]\n"
    "v_exp_f32_e32 v86, v86\n"
    "v_mfma_f32_16x16x32_bf16 a[28:31], a[124:127], v[88:91], a[28:31]\n"
    "v_cvt_pk_bf16_f32 v118, v84, v85\n"
    "v_max_f32 v220, s30, v246\n"
    "v_mfma_f32_16x16x32_bf16 a[60:63], a[124:127], v[92:95], a[60:63]\n"
    "v_exp_f32_e32 v87, v87\n"
    "v_min_f32 v202, v220, v218\n"
    "v_mfma_f32_16x16x32_bf16 v[148:151], a[124:127], v[96:99], v[148:151]\n"
    "v_min_f32 v214, v220, v219\n"
    "v_max_f32 v221, s30, v247\n"
    "v_mfma_f32_16x16x32_bf16 v[180:183], a[124:127], v[100:103], v[180:183]\n"
    "ds_read_b128 a[124:127], v1 offset:15360\n"
    "v_min_f32 v203, v221, v218\n"
    "v_min_f32 v215, v221, v219\n"
    "v_cvt_pk_bf16_f32 v119, v86, v87\n"
    "v_xor_b32 v0, 0x8000, v0\n"
    "v_xor_b32 v1, 0x8000, v1\n"
    "s_add_u32 s47, s23, 1\n"
    "s_min_u32 s47, s47, 15\n"
    "s_cmp_lt_u32 s24, s47\n"
    "s_cbranch_scc1 L_H1_%=\n"
    "s_cmp_eq_u32 s24, 15\n"
    "s_cbranch_scc1 L_H2_%=\n"
    ".p2align 6\n"
    "L_B_%=:\n"
    "s_waitcnt lgkmcnt(8)\n"
    "v_mfma_f32_16x16x32_bf16 v[56:59], a[64:67], a[128:131], v[184:187]\n"
    "v_exp_f32_e32 v24, v24\n"
    "v_mfma_f32_16x16x32_bf16 v[64:67], a[64:67], a[144:147], v[192:195]\n"
    "v_exp_f32_e32 v25, v25\n"
    "v_mfma_f32_16x16x32_bf16 v[72:75], a[64:67], a[160:163], v[200:203]\n"
    "v_exp_f32_e32 v26, v26\n"
    "v_mfma_f32_16x16x32_bf16 v[80:83], a[64:67], a[176:179], v[208:211]\n"
    "v_cvt_pk_bf16_f32 v88, v24, v25\n"
    "v_mfma_f32_16x16x32_bf16 v[60:63], a[68:71], a[128:131], v[188:191]\n"
    "v_exp_f32_e32 v27, v27\n"
    "v_mfma_f32_16x16x32_bf16 v[68:71], a[68:71], a[144:147], v[196:199]\n"
    "v_mfma_f32_16x16x32_bf16 v[76:79], a[68:71], a[160:163], v[204:207]\n"
    "v_exp_f32_e32 v28, v28\n"
    "v_mfma_f32_16x16x32_bf16 v[84:87], a[68:71], a[176:179], v[212:215]\n"
    "v_cvt_pk_bf16_f32 v89, v26, v27\n"
    "v_mfma_f32_16x16x32_bf16 v[56:59], a[72:75], a[132:135], v[56:59]\n"
    "v_mfma_f32_16x16x32_bf16 v[64:67], a[72:75], a[148:151], v[64:67]\n"
    "v_exp_f32_e32 v29, v29\n"
    "v_mfma_f32_16x16x32_bf16 v[72:75], a[72:75], a[164:167], v[72:75]\n"
    "v_exp_f32_e32 v30, v30\n"
    "v_mfma_f32_16x16x32_bf16 v[80:83], a[72:75], a[180:183], v[80:83]\n"
    "s_waitcnt vmcnt(2) lgkmcnt(0)\n"
    "s_barrier\n"
    "v_mfma_f32_16x16x32_bf16 v[60:63], a[76:79], a[132:135], v[60:63]\n"
    "ds_read_b128 a[64:67], v0 offset:0\n"
    "ds_read_b128 a[68:71], v0 offset:1024\n"
    "v_mfma_f32_16x16x32_bf16 v[68:71], a[76:79], a[148:151], v[68:71]\n"
    "ds_read_b128 a[72:75], v0 offset:2048\n"
    "s_mov_b32 m0, s36\n"
    "v_mfma_f32_16x16x32_bf16 v[76:79], a[76:79], a[164:167], v[76:79]\n"
    "buffer_load_dwordx4 v2, s[48:51], s86 offen offset:0 sc1 lds\n"
    "v_cvt_pk_bf16_f32 v90, v28, v29\n"
    "v_mfma_f32_16x16x32_bf16 v[84:87], a[76:79], a[180:183], v[84:87]\n"
    "v_exp_f32_e32 v31, v31\n"
    "v_mfma_f32_16x16x32_bf16 v[56:59], a[80:83], a[136:139], v[56:59]\n"
    "ds_read_b128 a[76:79], v0 offset:3072\n"
    "v_mfma_f32_16x16x32_bf16 v[64:67], a[80:83], a[152:155], v[64:67]\n"
    "v_exp_f32_e32 v32, v32\n"
    "v_mfma_f32_16x16x32_bf16 v[72:75], a[80:83], a[168:171], v[72:75]\n"
    "buffer_load_dwordx4 v2, s[48:51], s86 offen offset:1024 sc1 lds\n"
    "v_cvt_pk_bf16_f32 v91, v30, v31\n"
    "v_mfma_f32_16x16x32_bf16 v[80:83], a[80:83], a[184:187], v[80:83]\n"
    "v_exp_f32_e32 v33, v33\n"
    "v_mfma_f32_16x16x32_bf16 v[60:63], a[84:87], a[136:139], v[60:63]\n"
    "ds_read_b128 a[80:83], v0 offset:4096\n"
    "v_mfma_f32_16x16x32_bf16 v[68:71], a[84:87], a[152:155], v[68:71]\n"
    "v_exp_f32_e32 v34, v34\n"
    "v_mfma_f32_16x16x32_bf16 v[76:79], a[84:87], a[168:171], v[76:79]\n"
    "buffer_load_dwordx4 v2, s[48:51], s86 offen offset:2048 sc1 lds\n"
    "v_cvt_pk_bf16_f32 v92, v32, v33\n"
    "v_mfma_f32_16x16x32_bf16 v[84:87], a[84:87], a[184:187], v[84:87]\n"
    "v_exp_f32_e32 v35, v35\n"
    "v_mfma_f32_16x16x32_bf16 v[56:59], a[88:91], a[140:143], v[56:59]\n"
    "ds_read_b128 a[84:87], v0 offset:5120\n"
    "v_mfma_f32_16x16x32_bf16 v[64:67], a[88:91], a[156:159], v[64:67]\n"
    "v_exp_f32_e32 v36, v36\n"
    "v_mfma_f32_16x16x32_bf16 v[72:75], a[88:91], a[172:175], v[72:75]\n"
    "buffer_load_dwordx4 v2, s[48:51], s86 offen offset:3072 sc1 lds\n"
    "v_cvt_pk_bf16_f32 v93, v34, v35\n"
    "v_mfma_f32_16x16x32_bf16 v[80:83], a[88:91], a[188:191], v[80:83]\n"
    "v_exp_f32_e32 v37, v37\n"
    "v_mfma_f32_16x16x32_bf16 v[60:63], a[92:95], a[140:143], v[60:63]\n"
    "ds_read_b128 a[88:91], v0 offset:6144\n"
    "v_mfma_f32_16x16x32_bf16 v[68:71], a[92:95], a[156:159], v[68:71]\n"
    "v_exp_f32_e32 v38, v38\n"
    "v_mfma_f32_16x16x32_bf16 v[76:79], a[92:95], a[172:175], v[76:79]\n"
    "s_add_u32 m0, s36, 16384\n"
    "buffer_load_dwordx4 v15, s[48:51], s86 offen offset:0 sc1 lds\n"
    "v_mfma_f32_16x16x32_bf16 v[84:87], a[92:95], a[188:191], v[84:87]\n"
    "ds_read_b128 a[92:95], v0 offset:7168\n"
    "v_cvt_pk_bf16_f32 v94, v36, v37\n"
    "v_mfma_f32_16x16x32_bf16 a[0:3], a[96:99], v[104:107], a[0:3]\n"
    "v_exp_f32_e32 v39, v39\n"
    "v_mfma_f32_16x16x32_bf16 a[32:35], a[96:99], v[108:111], a[32:35]\n"
    "v_exp_f32_e32 v40, v40\n"
    "v_mfma_f32_16x16x32_bf16 v[120:123], a[96:99], v[112:115], v[120:123]\n"
    "buffer_load_dwordx4 v15, s[48:51], s86 offen offset:1024 sc1 lds\n"
    "v_cvt_pk_bf16_f32 v95, v38, v39\n"
    "v_mfma_f32_16x16x32_bf16 v[152:155], a[96:99], v[116:119], v[152:155]\n"
    "v_exp_f32_e32 v41, v41\n"
    "v_mfma_f32_16x16x32_bf16 a[192:195], a[208:211], v[104:107], a[192:195]\n"
    "ds_read_b128 a[96:99], v1 offset:0\n"
    "v_mfma_f32_16x16x32_bf16 a[4:7], a[100:103], v[104:107], a[4:7]\n"
    "v_exp_f32_e32 v42, v42\n"
    "v_mfma_f32_16x16x32_bf16 a[36:39], a[100:103], v[108:111], a[36:39]\n"
    "buffer_load_dwordx4 v15, s[48:51], s86 offen offset:2048 sc1 lds\n"
    "v_cvt_pk_bf16_f32 v96, v40, v41\n"
    "v_mfma_f32_16x16x32_bf16 v[124:127], a[100:103], v[112:115], v[124:127]\n"
    "v_exp_f32_e32 v43, v43\n"
    "v_mfma_f32_16x16x32_bf16 v[156:159], a[100:103], v[116:119], v[156:159]\n"
    "ds_read_b128 a[100:103], v1 offset:1024\n"
    "v_mfma_f32_16x16x32_bf16 a[8:11], a[104:107], v[104:107], a[8:11]\n"
    "v_exp_f32_e32 v44, v44\n"
    "v_mfma_f32_16x16x32_bf16 a[40:43], a[104:107], v[108:111], a[40:43]\n"
    "buffer_load_dwordx4 v15, s[48:51], s86 offen offset:3072 sc1 lds\n"
    "v_cvt_pk_bf16_f32 v97, v42, v43\n"
    "v_mfma_f32_16x16x32_bf16 v[128:131], a[104:107], v[112:115], v[128:131]\n"
    "v_exp_f32_e32 v45, v45\n"
    "v_mfma_f32_16x16x32_bf16 v[160:163], a[104:107], v[116:119], v[160:163]\n"
    "ds_read_b128 a[104:107], v1 offset:2048\n"
    "v_mfma_f32_16x16x32_bf16 a[196:199], a[208:211], v[108:111], a[196:199]\n"
    "v_exp_f32_e32 v46, v46\n"
    "v_mfma_f32_16x16x32_bf16 a[12:15], a[108:111], v[104:107], a[12:15]\n"
    "s_mov_b32 m0, s37\n"
    "buffer_load_dwordx4 v2, s[52:55], s34 offen offset:0 sc1 lds\n"
    "v_mfma_f32_16x16x32_bf16 a[44:47], a[108:111], v[108:111], a[44:47]\n"
    "v_cvt_pk_bf16_f32 v98, v44, v45\n"
    "v_mfma_f32_16x16x32_bf16 v[132:135], a[108:111], v[112:115], v[132:135]\n"
    "v_exp_f32_e32 v47, v47\n"
    "v_mfma_f32_16x16x32_bf16 v[164:167], a[108:111], v[116:119], v[164:167]\n"
    "v_exp_f32_e32 v48, v48\n"
    "v_mfma_f32_16x16x32_bf16 a[16:19], a[112:115], v[104:107], a[16:19]\n"
    "ds_read_b128 a[108:111], v1 offset:3072\n"
    "buffer_load_dwordx4 v2, s[52:55], s34 offen offset:1024 sc1 lds\n"
    "v_mfma_f32_16x16x32_bf16 a[48:51], a[112:115], v[108:111], a[48:51]\n"
    "v_cvt_pk_bf16_f32 v99, v46, v47\n"
    "v_mfma_f32_16x16x32_bf16 v[136:139], a[112:115], v[112:115], v[136:139]\n"
    "v_exp_f32_e32 v49, v49\n"
    "v_mfma_f32_16x16x32_bf16 v[168:171], a[112:115], v[116:119], v[168:171]\n"
    "v_exp_f32_e32 v50, v50\n"
    "v_mfma_f32_16x16x32_bf16 a[200:203], a[208:211], v[112:115], a[200:203]\n"
    "ds_read_b128 a[112:115], v1 offset:4096\n"
    "v_cvt_pk_bf16_f32 v100, v48, v49\n"
    "v_mfma_f32_16x16x32_bf16 a[20:23], a[116:119], v[104:107], a[20:23]\n"
    "v_exp_f32_e32 v51, v51\n"
    "v_mfma_f32_16x16x32_bf16 a[52:55], a[116:119], v[108:111], a[52:55]\n"
    "v_exp_f32_e32 v52, v52\n"
    "v_mfma_f32_16x16x32_bf16 v[140:143], a[116:119], v[112:115], v[140:143]\n"
    "v_cvt_pk_bf16_f32 v101, v50, v51\n"
    "v_mfma_f32_16x16x32_bf16 v[172:175], a[116:119], v[116:119], v[172:175]\n"
    "ds_read_b128 a[116:119], v1 offset:5120\n"
    "v_mfma_f32_16x16x32_bf16 a[24:27], a[120:123], v[104:107], a[24:27]\n"
    "v_exp_f32_e32 v53, v53\n"
    "v_mfma_f32_16x16x32_bf16 a[56:59], a[120:123], v[108:111], a[56:59]\n"
    "v_exp_f32_e32 v54, v54\n"
    "v_mfma_f32_16x16x32_bf16 v[144:147], a[120:123], v[112:115], v[144:147]\n"
    "v_cvt_pk_bf16_f32 v102, v52, v53\n"
    "v_mfma_f32_16x16x32_bf16 v[176:179], a[120:123], v[116:119], v[176:179]\n"
    "ds_read_b128 a[120:123], v1 offset:6144\n"
    "v_mfma_f32_16x16x32_bf16 a[204:207], a[208:211], v[116:119], a[204:207]\n"
    "v_exp_f32_e32 v55, v55\n"
    "v_mfma_f32_16x16x32_bf16 a[28:31], a[124:127], v[104:107], a[28:31]\n"
    "v_mfma_f32_16x16x32_bf16 a[60:63], a[124:127], v[108:111], a[60:63]\n"
    "v_mfma_f32_16x16x32_bf16 v[148:151], a[124:127], v[112:115], v[148:151]\n"
    "v_mfma_f32_16x16x32_bf16 v[180:183], a[124:127], v[116:119], v[180:183]\n"
    "ds_read_b128 a[124:127], v1 offset:7168\n"
    "v_cvt_pk_bf16_f32 v103, v54, v55\n"
    "s_waitcnt lgkmcnt(8)\n"
    "v_mfma_f32_16x16x32_bf16 v[24:27], a[64:67], a[128:131], v[184:187]\n"
    "v_exp_f32_e32 v56, v56\n"
    "v_mfma_f32_16x16x32_bf16 v[32:35], a[64:67], a[144:147], v[192:195]\n"
    "v_exp_f32_e32 v57, v57\n"
    "v_mfma_f32_16x16x32_bf16 v[40:43], a[64:67], a[160:163], v[200:203]\n"
    "buffer_load_dwordx4 v2, s[52:55], s34 offen offset:2048 sc1 lds\n"
    "v_mfma_f32_16x16x32_bf16 v[48:51], a[64:67], a[176:179], v[208:211]\n"
    "v_exp_f32_e32 v58, v58\n"
    "v_mfma_f32_16x16x32_bf16 v[28:31], a[68:71], a[128:131], v[188:191]\n"
    "ds_read_b128 a[64:67], v0 offset:8192\n"
    "v_cvt_pk_bf16_f32 v104, v56, v57\n"
    "v_mfma_f32_16x16x32_bf16 v[36:39], a[68:71], a[144:147], v[196:199]\n"
    "v_exp_f32_e32 v59, v59\n"
    "v_mfma_f32_16x16x32_bf16 v[44:47], a[68:71], a[160:163], v[204:207]\n"
    "buffer_load_dwordx4 v2, s[52:55], s34 offen offset:3072 sc1 lds\n"
    "v_mfma_f32_16x16x32_bf16 v[52:55], a[68:71], a[176:179], v[212:215]\n"
    "v_exp_f32_e32 v60, v60\n"
    "v_mfma_f32_16x16x32_bf16 v[24:27], a[72:75], a[132:135], v[24:27]\n"
    "ds_read_b128 a[68:71], v0 offset:9216\n"
    "v_cvt_pk_bf16_f32 v105, v58, v59\n"
    "v_mfma_f32_16x16x32_bf16 v[32:35], a[72:75], a[148:151], v[32:35]\n"
    "v_exp_f32_e32 v61, v61\n"
    "v_mfma_f32_16x16x32_bf16 v[40:43], a[72:75], a[164:167], v[40:43]\n"
    "s_add_u32 m0, s37, 16384\n"
    "buffer_load_dwordx4 v15, s[52:55], s34 offen offset:0 sc1 lds\n"
    "v_mfma_f32_16x16x32_bf16 v[48:51], a[72:75], a[180:183], v[48:51]\n"
    "v_exp_f32_e32 v62, v62\n"
    "v_mfma_f32_16x16x32_bf16 v[28:31], a[76:79], a[132:135], v[28:31]\n"
    "ds_read_b128 a[72:75], v0 offset:10240\n"
    "v_cvt_pk_bf16_f32 v106, v60, v61\n"
    "v_mfma_f32_16x16x32_bf16 v[36:39], a[76:79], a[148:151], v[36:39]\n"
    "v_exp_f32_e32 v63, v63\n"
    "v_mfma_f32_16x16x32_bf16 v[44:47], a[76:79], a[164:167], v[44:47]\n"
    "buffer_load_dwordx4 v15, s[52:55], s34 offen offset:1024 sc1 lds\n"
    "v_mfma_f32_16x16x32_bf16 v[52:55], a[76:79], a[180:183], v[52:55]\n"
    "v_exp_f32_e32 v64, v64\n"
    "v_mfma_f32_16x16x32_bf16 v[24:27], a[80:83], a[136:139], v[24:27]\n"
    "ds_read_b128 a[76:79], v0 offset:11264\n"
    "v_cvt_pk_bf16_f32 v107, v62, v63\n"
    "v_mfma_f32_16x16x32_bf16 v[32:35], a[80:83], a[152:155], v[32:35]\n"
    "v_exp_f32_e32 v65, v65\n"
    "v_mfma_f32_16x16x32_bf16 v[40:43], a[80:83], a[168:171], v[40:43]\n"
    "buffer_load_dwordx4 v15, s[52:55], s34 offen offset:2048 sc1 lds\n"
    "v_mfma_f32_16x16x32_bf16 v[48:51], a[80:83], a[184:187], v[48:51]\n"
    "v_exp_f32_e32 v66, v66\n"
    "v_mfma_f32_16x16x32_bf16 v[28:31], a[84:87], a[136:139], v[28:31]\n"
    "ds_read_b128 a[80:83], v0 offset:12288\n"
    "v_cvt_pk_bf16_f32 v108, v64, v65\n"
    "v_mfma_f32_16x16x32_bf16 v[36:39], a[84:87], a[152:155], v[36:39]\n"
    "v_exp_f32_e32 v67, v67\n"
    "v_mfma_f32_16x16x32_bf16 v[44:47], a[84:87], a[168:171], v[44:47]\n"
    "buffer_load_dwordx4 v15, s[52:55], s34 offen offset:3072 sc1 lds\n"
    "v_mfma_f32_16x16x32_bf16 v[52:55], a[84:87], a[184:187], v[52:55]\n"
    "v_exp_f32_e32 v68, v68\n"
    "v_mfma_f32_16x16x32_bf16 v[24:27], a[88:91], a[140:143], v[24:27]\n"
    "ds_read_b128 a[84:87], v0 offset:13312\n"
    "v_cvt_pk_bf16_f32 v109, v66, v67\n"
    "v_mfma_f32_16x16x32_bf16 v[32:35], a[88:91], a[156:159], v[32:35]\n"
    "v_exp_f32_e32 v69, v69\n"
    "v_mfma_f32_16x16x32_bf16 v[40:43], a[88:91], a[172:175], v[40:43]\n"
    "s_min_u32 s33, s24, 4\n"
    "v_mad_u64_u32 v[252:253], vcc, v251, s33, v[254:255]\n"
    "v_mfma_f32_16x16x32_bf16 v[48:51], a[88:91], a[188:191], v[48:51]\n"
    "v_exp_f32_e32 v70, v70\n"
    "v_mfma_f32_16x16x32_bf16 v[28:31], a[92:95], a[140:143], v[28:31]\n"
    "ds_read_b128 a[88:91], v0 offset:14336\n"
    "buffer_load_dword v249, v248, s[16:19], s46 offen\n"
    "v_mfma_f32_16x16x32_bf16 v[36:39], a[92:95], a[156:159], v[36:39]\n"
    "v_cvt_pk_bf16_f32 v110, v68, v69\n"
    "v_mfma_f32_16x16x32_bf16 v[44:47], a[92:95], a[172:175], v[44:47]\n"
    "v_exp_f32_e32 v71, v71\n"
    "v_mfma_f32_16x16x32_bf16 v[52:55], a[92:95], a[188:191], v[52:55]\n"
    "ds_read_b128 a[92:95], v0 offset:15360\n"
    "s_waitcnt lgkmcnt(8)\n"
    "v_mfma_f32_16x16x32_bf16 a[0:3], a[96:99], v[88:91], a[0:3]\n"
    "global_load_dword v249, v[252:253], off\n"
    "v_mfma_f32_16x16x32_bf16 a[32:35], a[96:99], v[92:95], a[32:35]\n"
    "v_exp_f32_e32 v72, v72\n"
    "v_mfma_f32_16x16x32_bf16 v[120:123], a[96:99], v[96:99], v[120:123]\n"
    "v_cvt_pk_bf16_f32 v111, v70, v71\n"
    "v_mfma_f32_16x16x32_bf16 v[152:155], a[96:99], v[100:103], v[152:155]\n"
    "v_exp_f32_e32 v73, v73\n"
    "v_mfma_f32_16x16x32_bf16 a[192:195], a[208:211], v[88:91], a[192:195]\n"
    "ds_read_b128 a[96:99], v1 offset:8192\n"
    "s_add_u32 s46, s46, 0x200000\n"
    "v_mfma_f32_16x16x32_bf16 a[4:7], a[100:103], v[88:91], a[4:7]\n"
    "v_exp_f32_e32 v74, v74\n"
    "v_mfma_f32_16x16x32_bf16 a[36:39], a[100:103], v[92:95], a[36:39]\n"
    "s_add_u32 s86, s86, 32768\n"
    "s_add_u32 s34, s34, 32768\n"
    "v_mfma_f32_16x16x32_bf16 v[124:127], a[100:103], v[96:99], v[124:127]\n"
    "s_xor_b32 s36, s36, 0x8000\n"
    "s_xor_b32 s37, s37, 0x8000\n"
    "v_mfma_f32_16x16x32_bf16 v[156:159], a[100:103], v[100:103], v[156:159]\n"
    "ds_read_b128 a[100:103], v1 offset:9216\n"
    "s_add_u32 s24, s24, 1\n"
    "v_mfma_f32_16x16x32_bf16 a[8:11], a[104:107], v[88:91], a[8:11]\n"
    "v_cvt_pk_bf16_f32 v112, v72, v73\n"
    "v_mfma_f32_16x16x32_bf16 a[40:43], a[104:107], v[92:95], a[40:43]\n"
    "v_exp_f32_e32 v75, v75\n"
    "v_mfma_f32_16x16x32_bf16 v[128:131], a[104:107], v[96:99], v[128:131]\n"
    "v_exp_f32_e32 v76, v76\n"
    "v_mfma_f32_16x16x32_bf16 v[160:163], a[104:107], v[100:103], v[160:163]\n"
    "ds_read_b128 a[104:107], v1 offset:10240\n"
    "v_cvt_pk_bf16_f32 v113, v74, v75\n"
    "v_mfma_f32_16x16x32_bf16 a[196:199], a[208:211], v[92:95], a[196:199]\n"
    "v_exp_f32_e32 v77, v77\n"
    "v_mfma_f32_16x16x32_bf16 a[12:15], a[108:111], v[88:91], a[12:15]\n"
    "v_exp_f32_e32 v78, v78\n"
    "v_mfma_f32_16x16x32_bf16 a[44:47], a[108:111], v[92:95], a[44:47]\n"
    "v_cvt_pk_bf16_f32 v114, v76, v77\n"
    "v_mfma_f32_16x16x32_bf16 v[132:135], a[108:111], v[96:99], v[132:135]\n"
    "v_exp_f32_e32 v79, v79\n"
    "v_mfma_f32_16x16x32_bf16 v[164:167], a[108:111], v[100:103], v[164:167]\n"
    "v_exp_f32_e32 v80, v80\n"
    "v_mfma_f32_16x16x32_bf16 a[16:19], a[112:115], v[88:91], a[16:19]\n"
    "ds_read_b128 a[108:111], v1 offset:11264\n"
    "v_cvt_pk_bf16_f32 v115, v78, v79\n"
    "v_mfma_f32_16x16x32_bf16 a[48:51], a[112:115], v[92:95], a[48:51]\n"
    "v_exp_f32_e32 v81, v81\n"
    "v_mfma_f32_16x16x32_bf16 v[136:139], a[112:115], v[96:99], v[136:139]\n"
    "v_exp_f32_e32 v82, v82\n"
    "v_mfma_f32_16x16x32_bf16 v[168:171], a[112:115], v[100:103], v[168:171]\n"
    "ds_read_b128 a[112:115], v1 offset:12288\n"
    "v_cvt_pk_bf16_f32 v116, v80, v81\n"
    "v_mfma_f32_16x16x32_bf16 a[200:203], a[208:211], v[96:99], a[200:203]\n"
    "v_exp_f32_e32 v83, v83\n"
    "v_mfma_f32_16x16x32_bf16 a[20:23], a[116:119], v[88:91], a[20:23]\n"
    "v_mfma_f32_16x16x32_bf16 a[52:55], a[116:119], v[92:95], a[52:55]\n"
    "v_exp_f32_e32 v84, v84\n"
    "v_mfma_f32_16x16x32_bf16 v[140:143], a[116:119], v[96:99], v[140:143]\n"
    "v_cvt_pk_bf16_f32 v117, v82, v83\n"
    "v_mfma_f32_16x16x32_bf16 v[172:175], a[116:119], v[100:103], v[172:175]\n"
    "ds_read_b128 a[116:119], v1 offset:13312\n"
    "v_mfma_f32_16x16x32_bf16 a[24:27], a[120:123], v[88:91], a[24:27]\n"
    "v_exp_f32_e32 v85, v85\n"
    "v_mfma_f32_16x16x32_bf16 a[56:59], a[120:123], v[92:95], a[56:59]\n"
    "v_exp_f32_e32 v86, v86\n"
    "v_mfma_f32_16x16x32_bf16 v[144:147], a[120:123], v[96:99], v[144:147]\n"
    "v_cvt_pk_bf16_f32 v118, v84, v85\n"
    "v_mfma_f32_16x16x32_bf16 v[176:179], a[120:123], v[100:103], v[176:179]\n"
    "ds_read_b128 a[120:123], v1 offset:14336\n"
    "v_mfma_f32_16x16x32_bf16 a[204:207], a[208:211], v[100:103], a[204:207]\n"
    "v_exp_f32_e32 v87, v87\n"
    "v_mfma_f32_16x16x32_bf16 a[28:31], a[124:127], v[88:91], a[28:31]\n"
    "v_mfma_f32_16x16x32_bf16 a[60:63], a[124:127], v[92:95], a[60:63]\n"
    "v_mfma_f32_16x16x32_bf16 v[148:151], a[124:127], v[96:99], v[148:151]\n"
    "v_mfma_f32_16x16x32_bf16 v[180:183], a[124:127], v[100:103], v[180:183]\n"
    "ds_read_b128 a[124:127], v1 offset:15360\n"
    "v_cvt_pk_bf16_f32 v119, v86, v87\n"
    "v_xor_b32 v0, 0x8000, v0\n"
    "v_xor_b32 v1, 0x8000, v1\n"
    "s_cmp_lt_u32 s24, 15\n"
    "s_cbranch_scc1 L_B_%=\n"
    "s_mov_b32 s25, 30\n"
    ".p2align 6\n"
    "L_H2_%=:\n"
    "s_waitcnt lgkmcnt(8)\n"
    "v_mfma_f32_16x16x32_bf16 v[56:59], a[64:67], a[128:131], v[184:187]\n"
    "s_add_u32 s30, s24, 2\n"
    "s_cmp_ge_u32 s30, s23\n"
    "s_cselect_b32 s38, 0x8000, s38\n"
    "v_mfma_f32_16x16x32_bf16 v[64:67], a[64:67], a[144:147], v[192:195]\n"
    "v_exp_f32_e32 v24, v24\n"
    "v_mfma_f32_16x16x32_bf16 v[72:75], a[64:67], a[160:163], v[200:203]\n"
    "s_add_u32 s30, s24, 1\n"
    "s_cmp_ge_u32 s30, s23\n"
    "s_cselect_b32 s39, 0x8000, s39\n"
    "v_mfma_f32_16x16x32_bf16 v[80:83], a[64:67], a[176:179], v[208:211]\n"
    "v_exp_f32_e32 v25, v25\n"
    "v_mfma_f32_16x16x32_bf16 v[60:63], a[68:71], a[128:131], v[188:191]\n"
    "s_cmp_eq_u32 s30, s23\n"
    "s_cselect_b32 s30, s80, 0\n"
    "s_cselect_b64 vcc, -1, 0\n"
    "v_mfma_f32_16x16x32_bf16 v[68:71], a[68:71], a[144:147], v[196:199]\n"
    "v_exp_f32_e32 v26, v26\n"
    "v_mfma_f32_16x16x32_bf16 v[76:79], a[68:71], a[160:163], v[204:207]\n"
    "v_add_u32 v0, s30, v0\n"
    "s_cmp_eq_u32 s24, s23\n"
    "s_cselect_b32 s30, s80, 0\n"
    "v_mfma_f32_16x16x32_bf16 v[84:87], a[68:71], a[176:179], v[212:215]\n"
    "v_add_u32 v1, s30, v1\n"
    "v_cvt_pk_bf16_f32 v88, v24, v25\n"
    "v_mfma_f32_16x16x32_bf16 v[56:59], a[72:75], a[132:135], v[56:59]\n"
    "v_exp_f32_e32 v27, v27\n"
    "v_mfma_f32_16x16x32_bf16 v[64:67], a[72:75], a[148:151], v[64:67]\n"
    "v_exp_f32_e32 v28, v28\n"
    "v_mfma_f32_16x16x32_bf16 v[72:75], a[72:75], a[164:167], v[72:75]\n"
    "v_cvt_pk_bf16_f32 v89, v26, v27\n"
    "v_mfma_f32_16x16x32_bf16 v[80:83], a[72:75], a[180:183], v[80:83]\n"
    "s_waitcnt vmcnt(2) lgkmcnt(0)\n"
    "s_barrier\n"
    "ds_read_b64 v[216:217], v18\n"
    "v_mfma_f32_16x16x32_bf16 v[60:63], a[76:79], a[132:135], v[60:63]\n"
    "ds_read_b128 a[64:67], v0 offset:0\n"
    "ds_read_b128 a[68:71], v0 offset:1024\n"
    "v_mfma_f32_16x16x32_bf16 v[68:71], a[76:79], a[148:151], v[68:71]\n"
    "v_exp_f32_e32 v29, v29\n"
    "v_mfma_f32_16x16x32_bf16 v[76:79], a[76:79], a[164:167], v[76:79]\n"
    "ds_read_b128 a[72:75], v0 offset:2048\n"
    "s_mov_b32 m0, s36\n"
    "v_mfma_f32_16x16x32_bf16 v[84:87], a[76:79], a[180:183], v[84:87]\n"
    "v_exp_f32_e32 v30, v30\n"
    "v_mfma_f32_16x16x32_bf16 v[56:59], a[80:83], a[136:139], v[56:59]\n"
    "ds_read_b128 a[76:79], v0 offset:3072\n"
    "buffer_load_dwordx4 v2, s[48:51], s86 offen offset:0 sc1 lds\n"
    "v_cvt_pk_bf16_f32 v90, v28, v29\n"
    "v_mfma_f32_16x16x32_bf16 v[64:67], a[80:83], a[152:155], v[64:67]\n"
    "v_exp_f32_e32 v31, v31\n"
    "v_mfma_f32_16x16x32_bf16 v[72:75], a[80:83], a[168:171], v[72:75]\n"
    "ds_write_b64 v17, v[8:9]\n"
    "s_nop 0\n"
    "v_mfma_f32_16x16x32_bf16 v[80:83], a[80:83], a[184:187], v[80:83]\n"
    "v_exp_f32_e32 v32, v32\n"
    "v_mfma_f32_16x16x32_bf16 v[60:63], a[84:87], a[136:139], v[60:63]\n"
    "ds_read_b128 a[80:83], v0 offset:4096\n"
    "buffer_load_dwordx4 v2, s[48:51], s86 offen offset:1024 sc1 lds\n"
    "v_cvt_pk_bf16_f32 v91, v30, v31\n"
    "v_mfma_f32_16x16x32_bf16 v[68:71], a[84:87], a[152:155], v[68:71]\n"
    "v_exp_f32_e32 v33, v33\n"
    "v_mfma_f32_16x16x32_bf16 v[76:79], a[84:87], a[168:171], v[76:79]\n"
    "s_waitcnt lgkmcnt(6)\n"
    "v_cndmask_b32 v10, v10, v216, vcc\n"
    "v_mfma_f32_16x16x32_bf16 v[84:87], a[84:87], a[184:187], v[84:87]\n"
    "v_exp_f32_e32 v34, v34\n"
    "v_mfma_f32_16x16x32_bf16 v[56:59], a[88:91], a[140:143], v[56:59]\n"
    "ds_read_b128 a[84:87], v0 offset:5120\n"
    "buffer_load_dwordx4 v2, s[48:51], s86 offen offset:2048 sc1 lds\n"
    "v_cvt_pk_bf16_f32 v92, v32, v33\n"
    "v_mfma_f32_16x16x32_bf16 v[64:67], a[88:91], a[156:159], v[64:67]\n"
    "v_exp_f32_e32 v35, v35\n"
    "v_mfma_f32_16x16x32_bf16 v[72:75], a[88:91], a[172:175], v[72:75]\n"
    "v_cndmask_b32 v11, v11, v217, vcc\n"
    "s_add_u32 s25, s25, 1\n"
    "v_mfma_f32_16x16x32_bf16 v[80:83], a[88:91], a[188:191], v[80:83]\n"
    "v_exp_f32_e32 v36, v36\n"
    "v_mfma_f32_16x16x32_bf16 v[60:63], a[92:95], a[140:143], v[60:63]\n"
    "ds_read_b128 a[88:91], v0 offset:6144\n"
    "buffer_load_dwordx4 v2, s[48:51], s86 offen offset:3072 sc1 lds\n"
    "v_cvt_pk_bf16_f32 v93, v34, v35\n"
    "v_mfma_f32_16x16x32_bf16 v[68:71], a[92:95], a[156:159], v[68:71]\n"
    "v_exp_f32_e32 v37, v37\n"
    "v_mfma_f32_16x16x32_bf16 v[76:79], a[92:95], a[172:175], v[76:79]\n"
    "s_bitcmp1_b64 s[78:79], s25\n"
    "s_cselect_b32 s31, 0xff800000, s97\n"
    "v_mfma_f32_16x16x32_bf16 v[84:87], a[92:95], a[188:191], v[84:87]\n"
    "ds_read_b128 a[92:95], v0 offset:7168\n"
    "s_bitcmp1_b64 s[76:77], s25\n"
    "s_cselect_b32 s30, 0xff800000, s97\n"
    "v_mfma_f32_16x16x32_bf16 a[0:3], a[96:99], v[104:107], a[0:3]\n"
    "v_exp_f32_e32 v38, v38\n"
    "v_cvt_pk_bf16_f32 v94, v36, v37\n"
    "v_mfma_f32_16x16x32_bf16 a[32:35], a[96:99], v[108:111], a[32:35]\n"
    "v_exp_f32_e32 v39, v39\n"
    "v_mfma_f32_16x16x32_bf16 v[120:123], a[96:99], v[112:115], v[120:123]\n"
    "s_add_u32 m0, s36, 16384\n"
    "buffer_load_dwordx4 v15, s[48:51], s86 offen offset:0 sc1 lds\n"
    "v_mfma_f32_16x16x32_bf16 v[152:155], a[96:99], v[116:119], v[152:155]\n"
    "v_exp_f32_e32 v40, v40\n"
    "v_mfma_f32_16x16x32_bf16 a[192:195], a[208:211], v[104:107], a[192:195]\n"
    "ds_read_b128 a[96:99], v1 offset:0\n"
    "v_cvt_pk_bf16_f32 v95, v38, v39\n"
    "v_mfma_f32_16x16x32_bf16 a[4:7], a[100:103], v[104:107], a[4:7]\n"
    "v_exp_f32_e32 v41, v41\n"
    "v_mfma_f32_16x16x32_bf16 a[36:39], a[100:103], v[108:111], a[36:39]\n"
    "buffer_load_dwordx4 v15, s[48:51], s86 offen offset:1024 sc1 lds\n"
    "v_min_f32 v218, s31, v10\n"
    "v_mfma_f32_16x16x32_bf16 v[124:127], a[100:103], v[112:115], v[124:127]\n"
    "v_exp_f32_e32 v42, v42\n"
    "v_mfma_f32_16x16x32_bf16 v[156:159], a[100:103], v[116:119], v[156:159]\n"
    "ds_read_b128 a[100:103], v1 offset:1024\n"
    "v_cvt_pk_bf16_f32 v96, v40, v41\n"
    "v_mfma_f32_16x16x32_bf16 a[8:11], a[104:107], v[104:107], a[8:11]\n"
    "v_exp_f32_e32 v43, v43\n"
    "v_mfma_f32_16x16x32_bf16 a[40:43], a[104:107], v[108:111], a[40:43]\n"
    "buffer_load_dwordx4 v15, s[48:51], s86 offen offset:2048 sc1 lds\n"
    "v_min_f32 v219, s31, v11\n"
    "v_mfma_f32_16x16x32_bf16 v[128:131], a[104:107], v[112:115], v[128:131]\n"
    "v_exp_f32_e32 v44, v44\n"
    "v_mfma_f32_16x16x32_bf16 v[160:163], a[104:107], v[116:119], v[160:163]\n"
    "ds_read_b128 a[104:107], v1 offset:2048\n"
    "v_cvt_pk_bf16_f32 v97, v42, v43\n"
    "v_mfma_f32_16x16x32_bf16 a[196:199], a[208:211], v[108:111], a[196:199]\n"
    "v_exp_f32_e32 v45, v45\n"
    "v_mfma_f32_16x16x32_bf16 a[12:15], a[108:111], v[104:107], a[12:15]\n"
    "buffer_load_dwordx4 v15, s[48:51], s86 offen offset:3072 sc1 lds\n"
    "v_min_f32 v204, s30, v218\n"
    "v_mov_b32 v205, v204\n"
    "v_mfma_f32_16x16x32_bf16 a[44:47], a[108:111], v[108:111], a[44:47]\n"
    "v_exp_f32_e32 v46, v46\n"
    "v_mov_b32 v206, v204\n"
    "v_mov_b32 v207, v204\n"
    "v_mfma_f32_16x16x32_bf16 v[132:135], a[108:111], v[112:115], v[132:135]\n"
    "v_cvt_pk_bf16_f32 v98, v44, v45\n"
    "v_mov_b32 v208, v219\n"
    "v_mov_b32 v209, v219\n"
    "v_mfma_f32_16x16x32_bf16 v[164:167], a[108:111], v[116:119], v[164:167]\n"
    "v_exp_f32_e32 v47, v47\n"
    "v_mov_b32 v210, v219\n"
    "v_mov_b32 v211, v219\n"
    "v_mfma_f32_16x16x32_bf16 a[16:19], a[112:115], v[104:107], a[16:19]\n"
    "ds_read_b128 a[108:111], v1 offset:3072\n"
    "s_mov_b32 m0, s37\n"
    "v_max_f32 v220, s30, v244\n"
    "v_min_f32 v200, v220, v218\n"
    "v_mfma_f32_16x16x32_bf16 a[48:51], a[112:115], v[108:111], a[48:51]\n"
    "v_exp_f32_e32 v48, v48\n"
    "v_min_f32 v212, v220, v219\n"
    "v_max_f32 v221, s30, v245\n"
    "v_mfma_f32_16x16x32_bf16 v[136:139], a[112:115], v[112:115], v[136:139]\n"
    "buffer_load_dwordx4 v2, s[52:55], s34 offen offset:0 sc1 lds\n"
    "v_cvt_pk_bf16_f32 v99, v46, v47\n"
    "v_min_f32 v201, v221, v218\n"
    "v_min_f32 v213, v221, v219\n"
    "v_mfma_f32_16x16x32_bf16 v[168:171], a[112:115], v[116:119], v[168:171]\n"
    "v_exp_f32_e32 v49, v49\n"
    "v_max_f32 v220, s30, v246\n"
    "v_min_f32 v202, v220, v218\n"
    "v_mfma_f32_16x16x32_bf16 a[200:203], a[208:211], v[112:115], a[200:203]\n"
    "ds_read_b128 a[112:115], v1 offset:4096\n"
    "v_min_f32 v214, v220, v219\n"
    "v_max_f32 v221, s30, v247\n"
    "v_mfma_f32_16x16x32_bf16 a[20:23], a[116:119], v[104:107], a[20:23]\n"
    "v_exp_f32_e32 v50, v50\n"
    "v_min_f32 v203, v221, v218\n"
    "v_min_f32 v215, v221, v219\n"
    "v_mfma_f32_16x16x32_bf16 a[52:55], a[116:119], v[108:111], a[52:55]\n"
    "buffer_load_dwordx4 v2, s[52:55], s34 offen offset:1024 sc1 lds\n"
    "v_cvt_pk_bf16_f32 v100, v48, v49\n"
    "s_bitcmp1_b64 s[74:75], s25\n"
    "s_cselect_b32 s31, 0xff800000, s97\n"
    "s_bitcmp1_b64 s[72:73], s25\n"
    "s_cselect_b32 s30, 0xff800000, s97\n"
    "v_mfma_f32_16x16x32_bf16 v[140:143], a[116:119], v[112:115], v[140:143]\n"
    "v_exp_f32_e32 v51, v51\n"
    "v_min_f32 v218, s31, v8\n"
    "v_min_f32 v219, s31, v9\n"
    "v_mfma_f32_16x16x32_bf16 v[172:175], a[116:119], v[116:119], v[172:175]\n"
    "ds_read_b128 a[116:119], v1 offset:5120\n"
    "v_min_f32 v188, s30, v218\n"
    "v_mov_b32 v189, v188\n"
    "v_mfma_f32_16x16x32_bf16 a[24:27], a[120:123], v[104:107], a[24:27]\n"
    "v_exp_f32_e32 v52, v52\n"
    "v_mov_b32 v190, v188\n"
    "v_mov_b32 v191, v188\n"
    "v_mfma_f32_16x16x32_bf16 a[56:59], a[120:123], v[108:111], a[56:59]\n"
    "v_cvt_pk_bf16_f32 v101, v50, v51\n"
    "v_mov_b32 v192, v219\n"
    "v_mov_b32 v193, v219\n"
    "v_mfma_f32_16x16x32_bf16 v[144:147], a[120:123], v[112:115], v[144:147]\n"
    "v_exp_f32_e32 v53, v53\n"
    "v_mov_b32 v194, v219\n"
    "v_mov_b32 v195, v219\n"
    "v_mfma_f32_16x16x32_bf16 v[176:179], a[120:123], v[116:119], v[176:179]\n"
    "ds_read_b128 a[120:123], v1 offset:6144\n"
    "v_max_f32 v220, s30, v244\n"
    "v_min_f32 v184, v220, v218\n"
    "v_mfma_f32_16x16x32_bf16 a[204:207], a[208:211], v[116:119], a[204:207]\n"
    "v_exp_f32_e32 v54, v54\n"
    "v_min_f32 v196, v220, v219\n"
    "v_max_f32 v221, s30, v245\n"
    "v_mfma_f32_16x16x32_bf16 a[28:31], a[124:127], v[104:107], a[28:31]\n"
    "v_cvt_pk_bf16_f32 v102, v52, v53\n"
    "v_min_f32 v185, v221, v218\n"
    "v_min_f32 v197, v221, v219\n"
    "v_mfma_f32_16x16x32_bf16 a[60:63], a[124:127], v[108:111], a[60:63]\n"
    "v_exp_f32_e32 v55, v55\n"
    "v_max_f32 v220, s30, v246\n"
    "v_min_f32 v186, v220, v218\n"
    "v_mfma_f32_16x16x32_bf16 v[148:151], a[124:127], v[112:115], v[148:151]\n"
    "v_min_f32 v198, v220, v219\n"
    "v_max_f32 v221, s30, v247\n"
    "v_mfma_f32_16x16x32_bf16 v[180:183], a[124:127], v[116:119], v[180:183]\n"
    "ds_read_b128 a[124:127], v1 offset:7168\n"
    "v_min_f32 v187, v221, v218\n"
    "v_min_f32 v199, v221, v219\n"
    "v_cvt_pk_bf16_f32 v103, v54, v55\n"
    "s_bitcmp1_b64 s[26:27], s25\n"
    "s_cbranch_scc1 L_hkH2_%=\n"
    "L_rtH2_%=:\n"
    "s_waitcnt lgkmcnt(8)\n"
    "v_mfma_f32_16x16x32_bf16 v[24:27], a[64:67], a[128:131], v[184:187]\n"
    "v_exp_f32_e32 v56, v56\n"
    "v_mfma_f32_16x16x32_bf16 v[32:35], a[64:67], a[144:147], v[192:195]\n"
    "v_exp_f32_e32 v57, v57\n"
    "v_mfma_f32_16x16x32_bf16 v[40:43], a[64:67], a[160:163], v[200:203]\n"
    "buffer_load_dwordx4 v2, s[52:55], s34 offen offset:2048 sc1 lds\n"
    "v_mfma_f32_16x16x32_bf16 v[48:51], a[64:67], a[176:179], v[208:211]\n"
    "v_exp_f32_e32 v58, v58\n"
    "v_mfma_f32_16x16x32_bf16 v[28:31], a[68:71], a[128:131], v[188:191]\n"
    "ds_read_b128 a[64:67], v0 offset:8192\n"
    "v_cvt_pk_bf16_f32 v104, v56, v57\n"
    "v_mfma_f32_16x16x32_bf16 v[36:39], a[68:71], a[144:147], v[196:199]\n"
    "v_exp_f32_e32 v59, v59\n"
    "v_mfma_f32_16x16x32_bf16 v[44:47], a[68:71], a[160:163], v[204:207]\n"
    "buffer_load_dwordx4 v2, s[52:55], s34 offen offset:3072 sc1 lds\n"
    "v_mfma_f32_16x16x32_bf16 v[52:55], a[68:71], a[176:179], v[212:215]\n"
    "v_exp_f32_e32 v60, v60\n"
    "v_mfma_f32_16x16x32_bf16 v[24:27], a[72:75], a[132:135], v[24:27]\n"
    "ds_read_b128 a[68:71], v0 offset:9216\n"
    "v_cvt_pk_bf16_f32 v105, v58, v59\n"
    "v_mfma_f32_16x16x32_bf16 v[32:35], a[72:75], a[148:151], v[32:35]\n"
    "v_exp_f32_e32 v61, v61\n"
    "v_mfma_f32_16x16x32_bf16 v[40:43], a[72:75], a[164:167], v[40:43]\n"
    "s_add_u32 m0, s37, 16384\n"
    "buffer_load_dwordx4 v15, s[52:55], s34 offen offset:0 sc1 lds\n"
    "v_mfma_f32_16x16x32_bf16 v[48:51], a[72:75], a[180:183], v[48:51]\n"
    "v_exp_f32_e32 v62, v62\n"
    "v_mfma_f32_16x16x32_bf16 v[28:31], a[76:79], a[132:135], v[28:31]\n"
    "ds_read_b128 a[72:75], v0 offset:10240\n"
    "v_cvt_pk_bf16_f32 v106, v60, v61\n"
    "v_mfma_f32_16x16x32_bf16 v[36:39], a[76:79], a[148:151], v[36:39]\n"
    "v_exp_f32_e32 v63, v63\n"
    "v_mfma_f32_16x16x32_bf16 v[44:47], a[76:79], a[164:167], v[44:47]\n"
    "buffer_load_dwordx4 v15, s[52:55], s34 offen offset:1024 sc1 lds\n"
    "s_add_u32 s25, s25, 1\n"
    "v_mfma_f32_16x16x32_bf16 v[52:55], a[76:79], a[180:183], v[52:55]\n"
    "v_exp_f32_e32 v64, v64\n"
    "v_mfma_f32_16x16x32_bf16 v[24:27], a[80:83], a[136:139], v[24:27]\n"
    "ds_read_b128 a[76:79], v0 offset:11264\n"
    "v_cvt_pk_bf16_f32 v107, v62, v63\n"
    "v_mfma_f32_16x16x32_bf16 v[32:35], a[80:83], a[152:155], v[32:35]\n"
    "v_exp_f32_e32 v65, v65\n"
    "v_mfma_f32_16x16x32_bf16 v[40:43], a[80:83], a[168:171], v[40:43]\n"
    "buffer_load_dwordx4 v15, s[52:55], s34 offen offset:2048 sc1 lds\n"
    "s_bitcmp1_b64 s[78:79], s25\n"
    "s_cselect_b32 s31, 0xff800000, s97\n"
    "v_mfma_f32_16x16x32_bf16 v[48:51], a[80:83], a[184:187], v[48:51]\n"
    "v_exp_f32_e32 v66, v66\n"
    "v_mfma_f32_16x16x32_bf16 v[28:31], a[84:87], a[136:139], v[28:31]\n"
    "ds_read_b128 a[80:83], v0 offset:12288\n"
    "v_cvt_pk_bf16_f32 v108, v64, v65\n"
    "v_mfma_f32_16x16x32_bf16 v[36:39], a[84:87], a[152:155], v[36:39]\n"
    "v_exp_f32_e32 v67, v67\n"
    "v_mfma_f32_16x16x32_bf16 v[44:47], a[84:87], a[168:171], v[44:47]\n"
    "buffer_load_dwordx4 v15, s[52:55], s34 offen offset:3072 sc1 lds\n"
    "s_bitcmp1_b64 s[76:77], s25\n"
    "s_cselect_b32 s30, 0xff800000, s97\n"
    "v_mfma_f32_16x16x32_bf16 v[52:55], a[84:87], a[184:187], v[52:55]\n"
    "v_exp_f32_e32 v68, v68\n"
    "v_mfma_f32_16x16x32_bf16 v[24:27], a[88:91], a[140:143], v[24:27]\n"
    "ds_read_b128 a[84:87], v0 offset:13312\n"
    "v_cvt_pk_bf16_f32 v109, v66, v67\n"
    "v_mfma_f32_16x16x32_bf16 v[32:35], a[88:91], a[156:159], v[32:35]\n"
    "v_exp_f32_e32 v69, v69\n"
    "v_mfma_f32_16x16x32_bf16 v[40:43], a[88:91], a[172:175], v[40:43]\n"
    "s_min_u32 s33, s24, 4\n"
    "v_mad_u64_u32 v[252:253], vcc, v251, s33, v[254:255]\n"
    "v_mfma_f32_16x16x32_bf16 v[48:51], a[88:91], a[188:191], v[48:51]\n"
    "v_exp_f32_e32 v70, v70\n"
    "v_mfma_f32_16x16x32_bf16 v[28:31], a[92:95], a[140:143], v[28:31]\n"
    "ds_read_b128 a[88:91], v0 offset:14336\n"
    "buffer_load_dword v249, v248, s[16:19], s46 offen\n"
    "v_mfma_f32_16x16x32_bf16 v[36:39], a[92:95], a[156:159], v[36:39]\n"
    "v_cvt_pk_bf16_f32 v110, v68, v69\n"
    "v_min_f32 v218, s31, v10\n"
    "v_mfma_f32_16x16x32_bf16 v[44:47], a[92:95], a[172:175], v[44:47]\n"
    "v_min_f32 v219, s31, v11\n"
    "v_min_f32 v204, s30, v218\n"
    "v_mfma_f32_16x16x32_bf16 v[52:55], a[92:95], a[188:191], v[52:55]\n"
    "ds_read_b128 a[92:95], v0 offset:15360\n"
    "s_waitcnt lgkmcnt(8)\n"
    "v_mfma_f32_16x16x32_bf16 a[0:3], a[96:99], v[88:91], a[0:3]\n"
    "global_load_dword v249, v[252:253], off\n"
    "v_mov_b32 v205, v204\n"
    "v_mfma_f32_16x16x32_bf16 a[32:35], a[96:99], v[92:95], a[32:35]\n"
    "v_exp_f32_e32 v71, v71\n"
    "v_mfma_f32_16x16x32_bf16 v[120:123], a[96:99], v[96:99], v[120:123]\n"
    "v_mov_b32 v206, v204\n"
    "v_mov_b32 v207, v204\n"
    "v_mfma_f32_16x16x32_bf16 v[152:155], a[96:99], v[100:103], v[152:155]\n"
    "v_exp_f32_e32 v72, v72\n"
    "v_mfma_f32_16x16x32_bf16 a[192:195], a[208:211], v[88:91], a[192:195]\n"
    "ds_read_b128 a[96:99], v1 offset:8192\n"
    "s_add_u32 s46, s46, 0x200000\n"
    "v_mfma_f32_16x16x32_bf16 a[4:7], a[100:103], v[88:91], a[4:7]\n"
    "s_add_u32 s86, s86, s38\n"
    "s_add_u32 s34, s34, s39\n"
    "v_cvt_pk_bf16_f32 v111, v70, v71\n"
    "v_mfma_f32_16x16x32_bf16 a[36:39], a[100:103], v[92:95], a[36:39]\n"
    "v_exp_f32_e32 v73, v73\n"
    "v_mfma_f32_16x16x32_bf16 v[124:127], a[100:103], v[96:99], v[124:127]\n"
    "v_exp_f32_e32 v74, v74\n"
    "v_mfma_f32_16x16x32_bf16 v[156:159], a[100:103], v[100:103], v[156:159]\n"
    "ds_read_b128 a[100:103], v1 offset:9216\n"
    "s_xor_b32 s36, s36, 0x8000\n"
    "v_mfma_f32_16x16x32_bf16 a[8:11], a[104:107], v[88:91], a[8:11]\n"
    "s_xor_b32 s37, s37, 0x8000\n"
    "s_add_u32 s24, s24, 1\n"
    "v_cvt_pk_bf16_f32 v112, v72, v73\n"
    "v_mfma_f32_16x16x32_bf16 a[40:43], a[104:107], v[92:95], a[40:43]\n"
    "v_exp_f32_e32 v75, v75\n"
    "v_mfma_f32_16x16x32_bf16 v[128:131], a[104:107], v[96:99], v[128:131]\n"
    "v_exp_f32_e32 v76, v76\n"
    "v_mfma_f32_16x16x32_bf16 v[160:163], a[104:107], v[100:103], v[160:163]\n"
    "ds_read_b128 a[104:107], v1 offset:10240\n"
    "v_cvt_pk_bf16_f32 v113, v74, v75\n"
    "v_mfma_f32_16x16x32_bf16 a[196:199], a[208:211], v[92:95], a[196:199]\n"
    "v_exp_f32_e32 v77, v77\n"
    "v_mfma_f32_16x16x32_bf16 a[12:15], a[108:111], v[88:91], a[12:15]\n"
    "v_mov_b32 v208, v219\n"
    "v_mov_b32 v209, v219\n"
    "v_mfma_f32_16x16x32_bf16 a[44:47], a[108:111], v[92:95], a[44:47]\n"
    "v_exp_f32_e32 v78, v78\n"
    "v_mfma_f32_16x16x32_bf16 v[132:135], a[108:111], v[96:99], v[132:135]\n"
    "v_cvt_pk_bf16_f32 v114, v76, v77\n"
    "v_mov_b32 v210, v219\n"
    "v_mfma_f32_16x16x32_bf16 v[164:167], a[108:111], v[100:103], v[164:167]\n"
    "v_exp_f32_e32 v79, v79\n"
    "v_mov_b32 v211, v219\n"
    "v_mfma_f32_16x16x32_bf16 a[16:19], a[112:115], v[88:91], a[16:19]\n"
    "ds_read_b128 a[108:111], v1 offset:11264\n"
    "v_max_f32 v220, s30, v244\n"
    "v_min_f32 v200, v220, v218\n"
    "v_mfma_f32_16x16x32_bf16 a[48:51], a[112:115], v[92:95], a[48:51]\n"
    "v_exp_f32_e32 v80, v80\n"
    "v_min_f32 v212, v220, v219\n"
    "v_max_f32 v221, s30, v245\n"
    "v_mfma_f32_16x16x32_bf16 v[136:139], a[112:115], v[96:99], v[136:139]\n"
    "v_cvt_pk_bf16_f32 v115, v78, v79\n"
    "v_min_f32 v201, v221, v218\n"
    "v_min_f32 v213, v221, v219\n"
    "v_mfma_f32_16x16x32_bf16 v[168:171], a[112:115], v[100:103], v[168:171]\n"
    "v_exp_f32_e32 v81, v81\n"
    "v_max_f32 v220, s30, v246\n"
    "v_min_f32 v202, v220, v218\n"
    "v_mfma_f32_16x16x32_bf16 a[200:203], a[208:211], v[96:99], a[200:203]\n"
    "ds_read_b128 a[112:115], v1 offset:12288\n"
    "v_min_f32 v214, v220, v219\n"
    "v_max_f32 v221, s30, v247\n"
    "v_mfma_f32_16x16x32_bf16 a[20:23], a[116:119], v[88:91], a[20:23]\n"
    "v_exp_f32_e32 v82, v82\n"
    "v_min_f32 v203, v221, v218\n"
    "v_min_f32 v215, v221, v219\n"
    "v_mfma_f32_16x16x32_bf16 a[52:55], a[116:119], v[92:95], a[52:55]\n"
    "v_cvt_pk_bf16_f32 v116, v80, v81\n"
    "s_bitcmp1_b64 s[74:75], s25\n"
    "s_cselect_b32 s31, 0xff800000, s97\n"
    "s_bitcmp1_b64 s[72:73], s25\n"
    "s_cselect_b32 s30, 0xff800000, s97\n"
    "v_mfma_f32_16x16x32_bf16 v[140:143], a[116:119], v[96:99], v[140:143]\n"
    "v_exp_f32_e32 v83, v83\n"
    "v_min_f32 v218, s31, v8\n"
    "v_min_f32 v219, s31, v9\n"
    "v_mfma_f32_16x16x32_bf16 v[172:175], a[116:119], v[100:103], v[172:175]\n"
    "ds_read_b128 a[116:119], v1 offset:13312\n"
    "v_min_f32 v188, s30, v218\n"
    "v_mov_b32 v189, v188\n"
    "v_mfma_f32_16x16x32_bf16 a[24:27], a[120:123], v[88:91], a[24:27]\n"
    "v_exp_f32_e32 v84, v84\n"
    "v_mov_b32 v190, v188\n"
    "v_mov_b32 v191, v188\n"
    "v_mfma_f32_16x16x32_bf16 a[56:59], a[120:123], v[92:95], a[56:59]\n"
    "v_cvt_pk_bf16_f32 v117, v82, v83\n"
    "v_mov_b32 v192, v219\n"
    "v_mov_b32 v193, v219\n"
    "v_mfma_f32_16x16x32_bf16 v[144:147], a[120:123], v[96:99], v[144:147]\n"
    "v_exp_f32_e32 v85, v85\n"
    "v_mov_b32 v194, v219\n"
    "v_mov_b32 v195, v219\n"
    "v_mfma_f32_16x16x32_bf16 v[176:179], a[120:123], v[100:103], v[176:179]\n"
    "ds_read_b128 a[120:123], v1 offset:14336\n"
    "v_max_f32 v220, s30, v244\n"
    "v_min_f32 v184, v220, v218\n"
    "v_mfma_f32_16x16x32_bf16 a[204:207], a[208:211], v[100:103], a[204:207]\n"
    "v_exp_f32_e32 v86, v86\n"
    "v_min_f32 v196, v220, v219\n"
    "v_max_f32 v221, s30, v245\n"
    "v_mfma_f32_16x16x32_bf16 a[28:31], a[124:127], v[88:91], a[28:31]\n"
    "v_cvt_pk_bf16_f32 v118, v84, v85\n"
    "v_min_f32 v185, v221, v218\n"
    "v_min_f32 v197, v221, v219\n"
    "v_mfma_f32_16x16x32_bf16 a[60:63], a[124:127], v[92:95], a[60:63]\n"
    "v_exp_f32_e32 v87, v87\n"
    "v_max_f32 v220, s30, v246\n"
    "v_min_f32 v186, v220, v218\n"
    "v_mfma_f32_16x16x32_bf16 v[148:151], a[124:127], v[96:99], v[148:151]\n"
    "v_min_f32 v198, v220, v219\n"
    "v_max_f32 v221, s30, v247\n"
    "v_mfma_f32_16x16x32_bf16 v[180:183], a[124:127], v[100:103], v[180:183]\n"
    "ds_read_b128 a[124:127], v1 offset:15360\n"
    "v_min_f32 v187, v221, v218\n"
    "v_min_f32 v199, v221, v219\n"
    "v_cvt_pk_bf16_f32 v119, v86, v87\n"
    "v_xor_b32 v0, 0x8000, v0\n"
    "v_xor_b32 v1, 0x8000, v1\n"
    "s_waitcnt lgkmcnt(8)\n"
    "v_mfma_f32_16x16x32_bf16 v[56:59], a[64:67], a[128:131], v[184:187]\n"
    "s_cmp_eq_u32 s24, s23\n"
    "s_cselect_b32 s30, s80, 0\n"
    "v_mfma_f32_16x16x32_bf16 v[64:67], a[64:67], a[144:147], v[192:195]\n"
    "v_exp_f32_e32 v24, v24\n"
    "v_mfma_f32_16x16x32_bf16 v[72:75], a[64:67], a[160:163], v[200:203]\n"
    "v_add_u32 v1, s30, v1\n"
    "s_add_u32 s25, s25, 1\n"
    "v_mfma_f32_16x16x32_bf16 v[80:83], a[64:67], a[176:179], v[208:211]\n"
    "v_exp_f32_e32 v25, v25\n"
    "v_mfma_f32_16x16x32_bf16 v[60:63], a[68:71], a[128:131], v[188:191]\n"
    "v_lshrrev_b32 v12, 3, v16\n"
    "s_lshl_b32 s99, s20, 5\n"
    "v_mfma_f32_16x16x32_bf16 v[68:71], a[68:71], a[144:147], v[196:199]\n"
    "v_exp_f32_e32 v26, v26\n"
    "v_mfma_f32_16x16x32_bf16 v[76:79], a[68:71], a[160:163], v[204:207]\n"
    "v_add_u32 v12, s99, v12\n"
    "v_lshlrev_b32 v12, 10, v12\n"
    "v_mfma_f32_16x16x32_bf16 v[84:87], a[68:71], a[176:179], v[212:215]\n"
    "v_lshrrev_b32 v13, 4, v16\n"
    "v_lshlrev_b32 v13, 1, v13\n"
    "v_mfma_f32_16x16x32_bf16 v[56:59], a[72:75], a[132:135], v[56:59]\n"
    "v_and_b32 v14, 7, v16\n"
    "v_xor_b32 v13, v13, v14\n"
    "v_cvt_pk_bf16_f32 v88, v24, v25\n"
    "v_mfma_f32_16x16x32_bf16 v[64:67], a[72:75], a[148:151], v[64:67]\n"
    "v_exp_f32_e32 v27, v27\n"
    "v_mfma_f32_16x16x32_bf16 v[72:75], a[72:75], a[164:167], v[72:75]\n"
    "v_lshl_add_u32 v4, v13, 4, v12\n"
    "v_add_u32 v5, 7168, v4\n"
    "v_mfma_f32_16x16x32_bf16 v[80:83], a[72:75], a[180:183], v[80:83]\n"
    "s_waitcnt vmcnt(2) lgkmcnt(0)\n"
    "s_barrier\n"
    "v_exp_f32_e32 v28, v28\n"
    "v_mfma_f32_16x16x32_bf16 v[60:63], a[76:79], a[132:135], v[60:63]\n"
    "v_add_u32 v6, 14336, v4\n"
    "v_add_u32 v7, 21504, v4\n"
    "v_cvt_pk_bf16_f32 v89, v26, v27\n"
    "v_mfma_f32_16x16x32_bf16 v[68:71], a[76:79], a[148:151], v[68:71]\n"
    "v_exp_f32_e32 v29, v29\n"
    "v_mfma_f32_16x16x32_bf16 v[76:79], a[76:79], a[164:167], v[76:79]\n"
    "s_lshr_b32 s33, s64, 6\n"
    "s_lshl_b32 s99, s33, 8\n"
    "v_mfma_f32_16x16x32_bf16 v[84:87], a[76:79], a[180:183], v[84:87]\n"
    "v_exp_f32_e32 v30, v30\n"
    "v_mfma_f32_16x16x32_bf16 v[56:59], a[80:83], a[136:139], v[56:59]\n"
    "s_add_u32 s16, s16, s99\n\ts_addc_u32 s17, s17, 0\n"
    "s_mov_b32 s18, 523520\n"
    "v_cvt_pk_bf16_f32 v90, v28, v29\n"
    "v_mfma_f32_16x16x32_bf16 v[64:67], a[80:83], a[152:155], v[64:67]\n"
    "v_exp_f32_e32 v31, v31\n"
    "v_mfma_f32_16x16x32_bf16 v[72:75], a[80:83], a[168:171], v[72:75]\n"
    "s_add_u32 m0, s28, 32768\n\ts_add_u32 s99, s33, 0\n\ts_and_b32 s99, s99, 3\n\ts_lshl_b32 s99, s99, 17\n\ts_add_u32 s99, s99, 0\n"
    "buffer_load_dwordx4 v4, s[16:19], s99 offen offset:0 lds\n"
    "v_mfma_f32_16x16x32_bf16 v[80:83], a[80:83], a[184:187], v[80:83]\n"
    "v_exp_f32_e32 v32, v32\n"
    "v_mfma_f32_16x16x32_bf16 v[60:63], a[84:87], a[136:139], v[60:63]\n"
    "v_cvt_pk_bf16_f32 v91, v30, v31\n"
    "v_mfma_f32_16x16x32_bf16 v[68:71], a[84:87], a[152:155], v[68:71]\n"
    "v_exp_f32_e32 v33, v33\n"
    "v_mfma_f32_16x16x32_bf16 v[76:79], a[84:87], a[168:171], v[76:79]\n"
    "buffer_load_dwordx4 v5, s[16:19], s99 offen offset:1024 lds\n"
    "v_mfma_f32_16x16x32_bf16 v[84:87], a[84:87], a[184:187], v[84:87]\n"
    "v_exp_f32_e32 v34, v34\n"
    "v_mfma_f32_16x16x32_bf16 v[56:59], a[88:91], a[140:143], v[56:59]\n"
    "v_cvt_pk_bf16_f32 v92, v32, v33\n"
    "v_mfma_f32_16x16x32_bf16 v[64:67], a[88:91], a[156:159], v[64:67]\n"
    "v_exp_f32_e32 v35, v35\n"
    "v_mfma_f32_16x16x32_bf16 v[72:75], a[88:91], a[172:175], v[72:75]\n"
    "buffer_load_dwordx4 v6, s[16:19], s99 offen offset:2048 lds\n"
    "v_mfma_f32_16x16x32_bf16 v[80:83], a[88:91], a[188:191], v[80:83]\n"
    "v_exp_f32_e32 v36, v36\n"
    "v_mfma_f32_16x16x32_bf16 v[60:63], a[92:95], a[140:143], v[60:63]\n"
    "v_cvt_pk_bf16_f32 v93, v34, v35\n"
    "v_mfma_f32_16x16x32_bf16 v[68:71], a[92:95], a[156:159], v[68:71]\n"
    "v_exp_f32_e32 v37, v37\n"
    "v_mfma_f32_16x16x32_bf16 v[76:79], a[92:95], a[172:175], v[76:79]\n"
    "buffer_load_dwordx4 v7, s[16:19], s99 offen offset:3072 lds\n"
    "v_mfma_f32_16x16x32_bf16 v[84:87], a[92:95], a[188:191], v[84:87]\n"
    "v_exp_f32_e32 v38, v38\n"
    "v_mfma_f32_16x16x32_bf16 a[0:3], a[96:99], v[104:107], a[0:3]\n"
    "v_cvt_pk_bf16_f32 v94, v36, v37\n"
    "v_mfma_f32_16x16x32_bf16 a[32:35], a[96:99], v[108:111], a[32:35]\n"
    "v_exp_f32_e32 v39, v39\n"
    "v_mfma_f32_16x16x32_bf16 v[120:123], a[96:99], v[112:115], v[120:123]\n"
    "s_add_u32 m0, s28, 49152\n\ts_add_u32 s99, s33, 0\n\ts_and_b32 s99, s99, 3\n\ts_lshl_b32 s99, s99, 17\n\ts_add_u32 s99, s99, 128\n"
    "buffer_load_dwordx4 v4, s[16:19], s99 offen offset:0 lds\n"
    "v_mfma_f32_16x16x32_bf16 v[152:155], a[96:99], v[116:119], v[152:155]\n"
    "v_exp_f32_e32 v40, v40\n"
    "v_mfma_f32_16x16x32_bf16 a[192:195], a[208:211], v[104:107], a[192:195]\n"
    "ds_read_b128 a[96:99], v1 offset:0\n"
    "v_cvt_pk_bf16_f32 v95, v38, v39\n"
    "v_mfma_f32_16x16x32_bf16 a[4:7], a[100:103], v[104:107], a[4:7]\n"
    "v_exp_f32_e32 v41, v41\n"
    "v_mfma_f32_16x16x32_bf16 a[36:39], a[100:103], v[108:111], a[36:39]\n"
    "buffer_load_dwordx4 v5, s[16:19], s99 offen offset:1024 lds\n"
    "v_mfma_f32_16x16x32_bf16 v[124:127], a[100:103], v[112:115], v[124:127]\n"
    "v_exp_f32_e32 v42, v42\n"
    "v_mfma_f32_16x16x32_bf16 v[156:159], a[100:103], v[116:119], v[156:159]\n"
    "ds_read_b128 a[100:103], v1 offset:1024\n"
    "v_cvt_pk_bf16_f32 v96, v40, v41\n"
    "v_mfma_f32_16x16x32_bf16 a[8:11], a[104:107], v[104:107], a[8:11]\n"
    "v_exp_f32_e32 v43, v43\n"
    "v_mfma_f32_16x16x32_bf16 a[40:43], a[104:107], v[108:111], a[40:43]\n"
    "buffer_load_dwordx4 v6, s[16:19], s99 offen offset:2048 lds\n"
    "v_mfma_f32_16x16x32_bf16 v[128:131], a[104:107], v[112:115], v[128:131]\n"
    "v_exp_f32_e32 v44, v44\n"
    "v_mfma_f32_16x16x32_bf16 v[160:163], a[104:107], v[116:119], v[160:163]\n"
    "ds_read_b128 a[104:107], v1 offset:2048\n"
    "v_cvt_pk_bf16_f32 v97, v42, v43\n"
    "v_mfma_f32_16x16x32_bf16 a[196:199], a[208:211], v[108:111], a[196:199]\n"
    "v_exp_f32_e32 v45, v45\n"
    "v_mfma_f32_16x16x32_bf16 a[12:15], a[108:111], v[104:107], a[12:15]\n"
    "buffer_load_dwordx4 v7, s[16:19], s99 offen offset:3072 lds\n"
    "v_mfma_f32_16x16x32_bf16 a[44:47], a[108:111], v[108:111], a[44:47]\n"
    "v_exp_f32_e32 v46, v46\n"
    "v_mfma_f32_16x16x32_bf16 v[132:135], a[108:111], v[112:115], v[132:135]\n"
    "v_cvt_pk_bf16_f32 v98, v44, v45\n"
    "v_mfma_f32_16x16x32_bf16 v[164:167], a[108:111], v[116:119], v[164:167]\n"
    "v_exp_f32_e32 v47, v47\n"
    "v_mfma_f32_16x16x32_bf16 a[16:19], a[112:115], v[104:107], a[16:19]\n"
    "ds_read_b128 a[108:111], v1 offset:3072\n"
    "s_add_u32 m0, s28, 98304\n\ts_add_u32 s99, s33, 1\n\ts_and_b32 s99, s99, 3\n\ts_lshl_b32 s99, s99, 17\n\ts_add_u32 s99, s99, 0\n"
    "v_mfma_f32_16x16x32_bf16 a[48:51], a[112:115], v[108:111], a[48:51]\n"
    "v_exp_f32_e32 v48, v48\n"
    "v_mfma_f32_16x16x32_bf16 v[136:139], a[112:115], v[112:115], v[136:139]\n"
    "buffer_load_dwordx4 v4, s[16:19], s99 offen offset:0 lds\n"
    "v_cvt_pk_bf16_f32 v99, v46, v47\n"
    "v_mfma_f32_16x16x32_bf16 v[168:171], a[112:115], v[116:119], v[168:171]\n"
    "v_exp_f32_e32 v49, v49\n"
    "v_mfma_f32_16x16x32_bf16 a[200:203], a[208:211], v[112:115], a[200:203]\n"
    "ds_read_b128 a[112:115], v1 offset:4096\n"
    "v_mfma_f32_16x16x32_bf16 a[20:23], a[116:119], v[104:107], a[20:23]\n"
    "v_exp_f32_e32 v50, v50\n"
    "v_mfma_f32_16x16x32_bf16 a[52:55], a[116:119], v[108:111], a[52:55]\n"
    "buffer_load_dwordx4 v5, s[16:19], s99 offen offset:1024 lds\n"
    "v_cvt_pk_bf16_f32 v100, v48, v49\n"
    "v_mfma_f32_16x16x32_bf16 v[140:143], a[116:119], v[112:115], v[140:143]\n"
    "v_exp_f32_e32 v51, v51\n"
    "v_mfma_f32_16x16x32_bf16 v[172:175], a[116:119], v[116:119], v[172:175]\n"
    "ds_read_b128 a[116:119], v1 offset:5120\n"
    "v_mfma_f32_16x16x32_bf16 a[24:27], a[120:123], v[104:107], a[24:27]\n"
    "v_exp_f32_e32 v52, v52\n"
    "v_mfma_f32_16x16x32_bf16 a[56:59], a[120:123], v[108:111], a[56:59]\n"
    "buffer_load_dwordx4 v6, s[16:19], s99 offen offset:2048 lds\n"
    "v_cvt_pk_bf16_f32 v101, v50, v51\n"
    "v_mfma_f32_16x16x32_bf16 v[144:147], a[120:123], v[112:115], v[144:147]\n"
    "v_exp_f32_e32 v53, v53\n"
    "v_mfma_f32_16x16x32_bf16 v[176:179], a[120:123], v[116:119], v[176:179]\n"
    "ds_read_b128 a[120:123], v1 offset:6144\n"
    "v_mfma_f32_16x16x32_bf16 a[204:207], a[208:211], v[116:119], a[204:207]\n"
    "v_exp_f32_e32 v54, v54\n"
    "v_mfma_f32_16x16x32_bf16 a[28:31], a[124:127], v[104:107], a[28:31]\n"
    "v_cvt_pk_bf16_f32 v102, v52, v53\n"
    "v_mfma_f32_16x16x32_bf16 a[60:63], a[124:127], v[108:111], a[60:63]\n"
    "v_exp_f32_e32 v55, v55\n"
    "v_mfma_f32_16x16x32_bf16 v[148:151], a[124:127], v[112:115], v[148:151]\n"
    "v_mfma_f32_16x16x32_bf16 v[180:183], a[124:127], v[116:119], v[180:183]\n"
    "ds_read_b128 a[124:127], v1 offset:7168\n"
    "v_cvt_pk_bf16_f32 v103, v54, v55\n"
    "s_bitcmp1_b64 s[26:27], s25\n"
    "s_cbranch_scc1 L_hkH3_%=\n"
    "L_rtH3_%=:\n"
    "s_waitcnt lgkmcnt(0)\n"
    "v_mfma_f32_16x16x32_bf16 a[0:3], a[96:99], v[88:91], a[0:3]\n"
    "v_exp_f32_e32 v56, v56\n"
    "v_mfma_f32_16x16x32_bf16 a[32:35], a[96:99], v[92:95], a[32:35]\n"
    "buffer_load_dwordx4 v7, s[16:19], s99 offen offset:3072 lds\n"
    "v_mfma_f32_16x16x32_bf16 v[120:123], a[96:99], v[96:99], v[120:123]\n"
    "v_exp_f32_e32 v57, v57\n"
    "v_mfma_f32_16x16x32_bf16 v[152:155], a[96:99], v[100:103], v[152:155]\n"
    "v_exp_f32_e32 v58, v58\n"
    "v_mfma_f32_16x16x32_bf16 a[192:195], a[208:211], v[88:91], a[192:195]\n"
    "s_add_u32 m0, s28, 114688\n\ts_add_u32 s99, s33, 2\n\ts_and_b32 s99, s99, 3\n\ts_lshl_b32 s99, s99, 17\n\ts_add_u32 s99, s99, 0\n"
    "ds_read_b128 a[96:99], v1 offset:8192\n"
    "v_cvt_pk_bf16_f32 v104, v56, v57\n"
    "v_mfma_f32_16x16x32_bf16 a[4:7], a[100:103], v[88:91], a[4:7]\n"
    "buffer_load_dwordx4 v4, s[16:19], s99 offen offset:0 lds\n"
    "v_exp_f32_e32 v59, v59\n"
    "v_exp_f32_e32 v60, v60\n"
    "v_mfma_f32_16x16x32_bf16 a[36:39], a[100:103], v[92:95], a[36:39]\n"
    "v_cvt_pk_bf16_f32 v105, v58, v59\n"
    "v_mfma_f32_16x16x32_bf16 v[124:127], a[100:103], v[96:99], v[124:127]\n"
    "v_exp_f32_e32 v61, v61\n"
    "v_mfma_f32_16x16x32_bf16 v[156:159], a[100:103], v[100:103], v[156:159]\n"
    "buffer_load_dwordx4 v5, s[16:19], s99 offen offset:1024 lds\n"
    "ds_read_b128 a[100:103], v1 offset:9216\n"
    "v_exp_f32_e32 v62, v62\n"
    "v_cvt_pk_bf16_f32 v106, v60, v61\n"
    "v_mfma_f32_16x16x32_bf16 a[8:11], a[104:107], v[88:91], a[8:11]\n"
    "v_exp_f32_e32 v63, v63\n"
    "v_mfma_f32_16x16x32_bf16 a[40:43], a[104:107], v[92:95], a[40:43]\n"
    "v_exp_f32_e32 v64, v64\n"
    "v_cvt_pk_bf16_f32 v107, v62, v63\n"
    "v_mfma_f32_16x16x32_bf16 v[128:131], a[104:107], v[96:99], v[128:131]\n"
    "buffer_load_dwordx4 v6, s[16:19], s99 offen offset:2048 lds\n"
    "v_exp_f32_e32 v65, v65\n"
    "v_mfma_f32_16x16x32_bf16 v[160:163], a[104:107], v[100:103], v[160:163]\n"
    "v_exp_f32_e32 v66, v66\n"
    "v_cvt_pk_bf16_f32 v108, v64, v65\n"
    "v_mfma_f32_16x16x32_bf16 a[196:199], a[208:211], v[92:95], a[196:199]\n"
    "v_exp_f32_e32 v67, v67\n"
    "v_mfma_f32_16x16x32_bf16 a[12:15], a[108:111], v[88:91], a[12:15]\n"
    "buffer_load_dwordx4 v7, s[16:19], s99 offen offset:3072 lds\n"
    "ds_read_b128 a[104:107], v1 offset:10240\n"
    "v_exp_f32_e32 v68, v68\n"
    "v_cvt_pk_bf16_f32 v109, v66, v67\n"
    "v_mfma_f32_16x16x32_bf16 a[44:47], a[108:111], v[92:95], a[44:47]\n"
    "v_exp_f32_e32 v69, v69\n"
    "v_mfma_f32_16x16x32_bf16 v[132:135], a[108:111], v[96:99], v[132:135]\n"
    "v_exp_f32_e32 v70, v70\n"
    "v_cvt_pk_bf16_f32 v110, v68, v69\n"
    "v_mfma_f32_16x16x32_bf16 v[164:167], a[108:111], v[100:103], v[164:167]\n"
    "s_lshl_b32 s39, s22, 2\n\ts_add_u32 s39, s39, s20\n\ts_lshl_b32 s39, s39, 18\n"
    "ds_read_b128 a[108:111], v1 offset:11264\n"
    "v_exp_f32_e32 v71, v71\n"
    "v_mfma_f32_16x16x32_bf16 a[16:19], a[112:115], v[88:91], a[16:19]\n"
    "buffer_load_dwordx4 a[128:131], v3, s[56:59], s39 offen offset:0 sc1\n"
    "v_exp_f32_e32 v72, v72\n"
    "v_mfma_f32_16x16x32_bf16 a[48:51], a[112:115], v[92:95], a[48:51]\n"
    "v_cvt_pk_bf16_f32 v111, v70, v71\n"
    "v_exp_f32_e32 v73, v73\n"
    "v_mfma_f32_16x16x32_bf16 v[136:139], a[112:115], v[96:99], v[136:139]\n"
    "v_exp_f32_e32 v74, v74\n"
    "v_mfma_f32_16x16x32_bf16 v[168:171], a[112:115], v[100:103], v[168:171]\n"
    "buffer_load_dwordx4 a[132:135], v3, s[56:59], s39 offen offset:1024 sc1\n"
    "ds_read_b128 a[112:115], v1 offset:12288\n"
    "v_cvt_pk_bf16_f32 v112, v72, v73\n"
    "v_mfma_f32_16x16x32_bf16 a[200:203], a[208:211], v[96:99], a[200:203]\n"
    "v_exp_f32_e32 v75, v75\n"
    "v_exp_f32_e32 v76, v76\n"
    "v_mfma_f32_16x16x32_bf16 a[20:23], a[116:119], v[88:91], a[20:23]\n"
    "v_cvt_pk_bf16_f32 v113, v74, v75\n"
    "v_mfma_f32_16x16x32_bf16 a[52:55], a[116:119], v[92:95], a[52:55]\n"
    "buffer_load_dwordx4 a[136:139], v3, s[56:59], s39 offen offset:2048 sc1\n"
    "v_exp_f32_e32 v77, v77\n"
    "v_mfma_f32_16x16x32_bf16 v[140:143], a[116:119], v[96:99], v[140:143]\n"
    "v_exp_f32_e32 v78, v78\n"
    "v_cvt_pk_bf16_f32 v114, v76, v77\n"
    "v_mfma_f32_16x16x32_bf16 v[172:175], a[116:119], v[100:103], v[172:175]\n"
    "v_exp_f32_e32 v79, v79\n"
    "v_mfma_f32_16x16x32_bf16 a[24:27], a[120:123], v[88:91], a[24:27]\n"
    "buffer_load_dwordx4 a[140:143], v3, s[56:59], s39 offen offset:3072 sc1\n"
    "ds_read_b128 a[116:119], v1 offset:13312\n"
    "v_exp_f32_e32 v80, v80\n"
    "v_cvt_pk_bf16_f32 v115, v78, v79\n"
    "v_mfma_f32_16x16x32_bf16 a[56:59], a[120:123], v[92:95], a[56:59]\n"
    "v_exp_f32_e32 v81, v81\n"
    "v_mfma_f32_16x16x32_bf16 v[144:147], a[120:123], v[96:99], v[144:147]\n"
    "v_exp_f32_e32 v82, v82\n"
    "v_cvt_pk_bf16_f32 v116, v80, v81\n"
    "v_mfma_f32_16x16x32_bf16 v[176:179], a[120:123], v[100:103], v[176:179]\n"
    "v_exp_f32_e32 v83, v83\n"
    "v_mfma_f32_16x16x32_bf16 a[204:207], a[208:211], v[100:103], a[204:207]\n"
    "v_exp_f32_e32 v84, v84\n"
    "v_cvt_pk_bf16_f32 v117, v82, v83\n"
    "v_mfma_f32_16x16x32_bf16 a[28:31], a[124:127], v[88:91], a[28:31]\n"
    "v_exp_f32_e32 v85, v85\n"
    "v_mfma_f32_16x16x32_bf16 a[60:63], a[124:127], v[92:95], a[60:63]\n"
    "v_exp_f32_e32 v86, v86\n"
    "v_cvt_pk_bf16_f32 v118, v84, v85\n"
    "v_mfma_f32_16x16x32_bf16 v[148:151], a[124:127], v[96:99], v[148:151]\n"
    "ds_read_b128 a[120:123], v1 offset:14336\n"
    "v_exp_f32_e32 v87, v87\n"
    "v_mfma_f32_16x16x32_bf16 v[180:183], a[124:127], v[100:103], v[180:183]\n"
    "ds_read_b128 a[124:127], v1 offset:15360\n"
    "v_cvt_pk_bf16_f32 v119, v86, v87\n"
    "s_waitcnt lgkmcnt(0)\n"
    "s_lshl_b32 s30, s21, 14\n"
    "s_lshr_b32 s31, s20, 1\n"
    "s_lshl_b32 s31, s31, 16\n"
    "s_add_u32 s30, s30, s31\n"
    "v_add_u32 v0, s30, v3\n"
    "v_xor_b32 v1, 0x4000, v0\n"
    "s_barrier\n"
    "v_mfma_f32_16x16x32_bf16 a[200:203], a[208:211], v[112:115], a[200:203]\n"
    "v_mfma_f32_16x16x32_bf16 a[204:207], a[208:211], v[116:119], a[204:207]\n"
    "v_mfma_f32_16x16x32_bf16 v[120:123], a[96:99], v[112:115], v[120:123]\n"
    "v_mfma_f32_16x16x32_bf16 v[152:155], a[96:99], v[116:119], v[152:155]\n"
    "v_mfma_f32_16x16x32_bf16 v[124:127], a[100:103], v[112:115], v[124:127]\n"
    "v_mfma_f32_16x16x32_bf16 v[156:159], a[100:103], v[116:119], v[156:159]\n"
    "v_mfma_f32_16x16x32_bf16 v[128:131], a[104:107], v[112:115], v[128:131]\n"
    "v_mfma_f32_16x16x32_bf16 v[160:163], a[104:107], v[116:119], v[160:163]\n"
    "v_mfma_f32_16x16x32_bf16 v[132:135], a[108:111], v[112:115], v[132:135]\n"
    "v_mfma_f32_16x16x32_bf16 v[164:167], a[108:111], v[116:119], v[164:167]\n"
    "v_mfma_f32_16x16x32_bf16 v[136:139], a[112:115], v[112:115], v[136:139]\n"
    "v_mfma_f32_16x16x32_bf16 v[168:171], a[112:115], v[116:119], v[168:171]\n"
    "v_mfma_f32_16x16x32_bf16 v[140:143], a[116:119], v[112:115], v[140:143]\n"
    "v_mfma_f32_16x16x32_bf16 v[172:175], a[116:119], v[116:119], v[172:175]\n"
    "v_mfma_f32_16x16x32_bf16 v[144:147], a[120:123], v[112:115], v[144:147]\n"
    "v_mfma_f32_16x16x32_bf16 v[176:179], a[120:123], v[116:119], v[176:179]\n"
    "v_mfma_f32_16x16x32_bf16 v[148:151], a[124:127], v[112:115], v[148:151]\n"
    "v_mfma_f32_16x16x32_bf16 v[180:183], a[124:127], v[116:119], v[180:183]\n"
    "v_mfma_f32_16x16x32_bf16 a[0:3], a[96:99], v[104:107], a[0:3]\n"
    "ds_write_b32 v17, a200 offset:1024\n"
    "v_mfma_f32_16x16x32_bf16 a[32:35], a[96:99], v[108:111], a[32:35]\n"
    "ds_write_b32 v17, a204 offset:1028\n"
    "v_mfma_f32_16x16x32_bf16 a[192:195], a[208:211], v[104:107], a[192:195]\n"
    "ds_write_b128 v0, v[120:123] offset:0\n"
    "v_mfma_f32_16x16x32_bf16 a[4:7], a[100:103], v[104:107], a[4:7]\n"
    "ds_write_b128 v0, v[152:155] offset:8192\n"
    "v_mfma_f32_16x16x32_bf16 a[36:39], a[100:103], v[108:111], a[36:39]\n"
    "ds_write_b128 v0, v[124:127] offset:1024\n"
    "v_mfma_f32_16x16x32_bf16 a[8:11], a[104:107], v[104:107], a[8:11]\n"
    "ds_write_b128 v0, v[156:159] offset:9216\n"
    "v_mfma_f32_16x16x32_bf16 a[40:43], a[104:107], v[108:111], a[40:43]\n"
    "ds_write_b128 v0, v[128:131] offset:2048\n"
    "v_mfma_f32_16x16x32_bf16 a[196:199], a[208:211], v[108:111], a[196:199]\n"
    "ds_write_b128 v0, v[160:163] offset:10240\n"
    "v_mfma_f32_16x16x32_bf16 a[12:15], a[108:111], v[104:107], a[12:15]\n"
    "ds_write_b128 v0, v[132:135] offset:3072\n"
    "v_mfma_f32_16x16x32_bf16 a[44:47], a[108:111], v[108:111], a[44:47]\n"
    "ds_write_b128 v0, v[164:167] offset:11264\n"
    "v_mfma_f32_16x16x32_bf16 a[16:19], a[112:115], v[104:107], a[16:19]\n"
    "ds_write_b128 v0, v[136:139] offset:4096\n"
    "v_mfma_f32_16x16x32_bf16 a[48:51], a[112:115], v[108:111], a[48:51]\n"
    "ds_write_b128 v0, v[168:171] offset:12288\n"
    "v_mfma_f32_16x16x32_bf16 a[20:23], a[116:119], v[104:107], a[20:23]\n"
    "ds_write_b128 v0, v[140:143] offset:5120\n"
    "v_mfma_f32_16x16x32_bf16 a[52:55], a[116:119], v[108:111], a[52:55]\n"
    "ds_write_b128 v0, v[172:175] offset:13312\n"
    "v_mfma_f32_16x16x32_bf16 a[24:27], a[120:123], v[104:107], a[24:27]\n"
    "ds_write_b128 v0, v[144:147] offset:6144\n"
    "v_mfma_f32_16x16x32_bf16 a[56:59], a[120:123], v[108:111], a[56:59]\n"
    "ds_write_b128 v0, v[176:179] offset:14336\n"
    "v_mfma_f32_16x16x32_bf16 a[28:31], a[124:127], v[104:107], a[28:31]\n"
    "ds_write_b128 v0, v[148:151] offset:7168\n"
    "v_mfma_f32_16x16x32_bf16 a[60:63], a[124:127], v[108:111], a[60:63]\n"
    "ds_write_b128 v0, v[180:183] offset:15360\n"
    "v_mov_b32 v223, 0x5f800000\n"
    "s_sub_u32 s30, 15, s22\n"
    "s_lshl_b32 s30, s30, 2\n"
    "s_add_u32 s30, s30, s20\n"
    "s_lshl_b32 s30, s30, 18\n"
    "s_waitcnt lgkmcnt(0)\n"
    "s_barrier\n"
    "ds_read_b64 v[216:217], v18 offset:1024\n"
    "ds_read_b128 v[24:27], v1 offset:0\n"
    "ds_read_b128 v[28:31], v1 offset:1024\n"
    "ds_read_b128 v[32:35], v1 offset:2048\n"
    "ds_read_b128 v[36:39], v1 offset:3072\n"
    "ds_read_b128 v[40:43], v1 offset:4096\n"
    "ds_read_b128 v[44:47], v1 offset:5120\n"
    "ds_read_b128 v[48:51], v1 offset:6144\n"
    "ds_read_b128 v[52:55], v1 offset:7168\n"
    "ds_read_b128 v[56:59], v1 offset:8192\n"
    "ds_read_b128 v[60:63], v1 offset:9216\n"
    "ds_read_b128 v[64:67], v1 offset:10240\n"
    "ds_read_b128 v[68:71], v1 offset:11264\n"
    "ds_read_b128 v[72:75], v1 offset:12288\n"
    "ds_read_b128 v[76:79], v1 offset:13312\n"
    "v_accvgpr_read_b32 v218, a192\n"
    "v_accvgpr_read_b32 v219, a196\n"
    "s_add_u32 s39, s39, 0x20000\n"
    "buffer_load_dwordx4 a[192:195], v3, s[56:59], s39 offen offset:0 sc1\n"
    "buffer_load_dwordx4 a[196:199], v3, s[56:59], s39 offen offset:1024 sc1\n"
    "buffer_load_dwordx4 a[200:203], v3, s[56:59], s39 offen offset:2048 sc1\n"
    "buffer_load_dwordx4 a[204:207], v3, s[56:59], s39 offen offset:3072 sc1\n"
    "v_accvgpr_read_b32 v120, a0\n"
    "v_accvgpr_read_b32 v121, a1\n"
    "v_accvgpr_read_b32 v122, a2\n"
    "v_accvgpr_read_b32 v123, a3\n"
    "v_accvgpr_read_b32 v124, a4\n"
    "v_accvgpr_read_b32 v125, a5\n"
    "v_accvgpr_read_b32 v126, a6\n"
    "v_accvgpr_read_b32 v127, a7\n"
    "v_accvgpr_read_b32 v128, a8\n"
    "v_accvgpr_read_b32 v129, a9\n"
    "v_accvgpr_read_b32 v130, a10\n"
    "v_accvgpr_read_b32 v131, a11\n"
    "v_accvgpr_read_b32 v132, a12\n"
    "v_accvgpr_read_b32 v133, a13\n"
    "v_accvgpr_read_b32 v134, a14\n"
    "v_accvgpr_read_b32 v135, a15\n"
    "v_accvgpr_read_b32 v136, a16\n"
    "v_accvgpr_read_b32 v137, a17\n"
    "v_accvgpr_read_b32 v138, a18\n"
    "v_accvgpr_read_b32 v139, a19\n"
    "v_accvgpr_read_b32 v140, a20\n"
    "v_accvgpr_read_b32 v141, a21\n"
    "v_accvgpr_read_b32 v142, a22\n"
    "v_accvgpr_read_b32 v143, a23\n"
    "v_accvgpr_read_b32 v144, a24\n"
    "v_accvgpr_read_b32 v145, a25\n"
    "v_accvgpr_read_b32 v146, a26\n"
    "v_accvgpr_read_b32 v147, a27\n"
    "v_accvgpr_read_b32 v148, a28\n"
    "v_accvgpr_read_b32 v149, a29\n"
    "v_accvgpr_read_b32 v150, a30\n"
    "v_accvgpr_read_b32 v151, a31\n"
    "v_accvgpr_read_b32 v152, a32\n"
    "v_accvgpr_read_b32 v153, a33\n"
    "v_accvgpr_read_b32 v154, a34\n"
    "v_accvgpr_read_b32 v155, a35\n"
    "v_accvgpr_read_b32 v156, a36\n"
    "v_accvgpr_read_b32 v157, a37\n"
    "v_accvgpr_read_b32 v158, a38\n"
    "v_accvgpr_read_b32 v159, a39\n"
    "v_accvgpr_read_b32 v160, a40\n"
    "v_accvgpr_read_b32 v161, a41\n"
    "v_accvgpr_read_b32 v162, a42\n"
    "v_accvgpr_read_b32 v163, a43\n"
    "v_accvgpr_read_b32 v164, a44\n"
    "v_accvgpr_read_b32 v165, a45\n"
    "v_accvgpr_read_b32 v166, a46\n"
    "v_accvgpr_read_b32 v167, a47\n"
    "v_accvgpr_read_b32 v168, a48\n"
    "v_accvgpr_read_b32 v169, a49\n"
    "v_accvgpr_read_b32 v170, a50\n"
    "v_accvgpr_read_b32 v171, a51\n"
    "v_accvgpr_read_b32 v172, a52\n"
    "v_accvgpr_read_b32 v173, a53\n"
    "v_accvgpr_read_b32 v174, a54\n"
    "v_accvgpr_read_b32 v175, a55\n"
    "v_accvgpr_read_b32 v176, a56\n"
    "v_accvgpr_read_b32 v177, a57\n"
    "v_accvgpr_read_b32 v178, a58\n"
    "v_accvgpr_read_b32 v179, a59\n"
    "v_accvgpr_read_b32 v180, a60\n"
    "v_accvgpr_read_b32 v181, a61\n"
    "v_accvgpr_read_b32 v182, a62\n"
    "v_accvgpr_read_b32 v183, a63\n"
    "ds_read_b128 v[80:83], v1 offset:14336\n"
    "ds_read_b128 v[84:87], v1 offset:15360\n"
    "s_waitcnt lgkmcnt(2)\n"
    "v_add_f32 v216, v216, v218\n"
    "v_add_f32 v217, v217, v219\n"
    "v_max_f32 v222, v216, v217\n"
    "v_add_f32 v222, v222, v216\n"
    "v_add_f32 v222, v222, v217\n"
    "v_cmp_nlt_f32 vcc, v222, v223\n"
    "v_rcp_f32 v218, v216\n"
    "v_rcp_f32 v219, v217\n"
    "s_nop 1\n"
    "s_or_b32 s98, s98, vcc_lo\n"
    "s_or_b32 s98, s98, vcc_hi\n"
    "s_waitcnt lgkmcnt(0)\n"
    "v_pk_add_f32 v[120:121], v[120:121], v[24:25]\n"
    "v_pk_add_f32 v[122:123], v[122:123], v[26:27]\n"
    "v_pk_add_f32 v[124:125], v[124:125], v[28:29]\n"
    "v_pk_add_f32 v[126:127], v[126:127], v[30:31]\n"
    "v_pk_mul_f32 v[120:121], v[120:121], v[218:219] op_sel:[0,0] op_sel_hi:[1,0]\n"
    "v_pk_mul_f32 v[122:123], v[122:123], v[218:219] op_sel:[0,0] op_sel_hi:[1,0]\n"
    "v_pk_mul_f32 v[124:125], v[124:125], v[218:219] op_sel:[0,0] op_sel_hi:[1,0]\n"
    "v_pk_mul_f32 v[126:127], v[126:127], v[218:219] op_sel:[0,0] op_sel_hi:[1,0]\n"
    "v_cvt_pk_bf16_f32 v224, v120, v121\n"
    "v_cvt_pk_bf16_f32 v225, v122, v123\n"
    "v_cvt_pk_bf16_f32 v226, v124, v125\n"
    "v_cvt_pk_bf16_f32 v227, v126, v127\n"
    "buffer_store_dwordx4 v[224:227], v3, s[56:59], s30 offen offset:0 sc1\n"
    "v_accvgpr_write_b32 a0, v224\n"
    "v_accvgpr_write_b32 a1, v225\n"
    "v_accvgpr_write_b32 a2, v226\n"
    "v_accvgpr_write_b32 a3, v227\n"
    "v_pk_add_f32 v[128:129], v[128:129], v[32:33]\n"
    "v_pk_add_f32 v[130:131], v[130:131], v[34:35]\n"
    "v_pk_add_f32 v[132:133], v[132:133], v[36:37]\n"
    "v_pk_add_f32 v[134:135], v[134:135], v[38:39]\n"
    "v_pk_mul_f32 v[128:129], v[128:129], v[218:219] op_sel:[0,0] op_sel_hi:[1,0]\n"
    "v_pk_mul_f32 v[130:131], v[130:131], v[218:219] op_sel:[0,0] op_sel_hi:[1,0]\n"
    "v_pk_mul_f32 v[132:133], v[132:133], v[218:219] op_sel:[0,0] op_sel_hi:[1,0]\n"
    "v_pk_mul_f32 v[134:135], v[134:135], v[218:219] op_sel:[0,0] op_sel_hi:[1,0]\n"
    "v_cvt_pk_bf16_f32 v228, v128, v129\n"
    "v_cvt_pk_bf16_f32 v229, v130, v131\n"
    "v_cvt_pk_bf16_f32 v230, v132, v133\n"
    "v_cvt_pk_bf16_f32 v231, v134, v135\n"
    "buffer_store_dwordx4 v[228:231], v3, s[56:59], s30 offen offset:1024 sc1\n"
    "v_accvgpr_write_b32 a4, v228\n"
    "v_accvgpr_write_b32 a5, v229\n"
    "v_accvgpr_write_b32 a6, v230\n"
    "v_accvgpr_write_b32 a7, v231\n"
    "v_pk_add_f32 v[136:137], v[136:137], v[40:41]\n"
    "v_pk_add_f32 v[138:139], v[138:139], v[42:43]\n"
    "v_pk_add_f32 v[140:141], v[140:141], v[44:45]\n"
    "v_pk_add_f32 v[142:143], v[142:143], v[46:47]\n"
    "v_pk_mul_f32 v[136:137], v[136:137], v[218:219] op_sel:[0,0] op_sel_hi:[1,0]\n"
    "v_pk_mul_f32 v[138:139], v[138:139], v[218:219] op_sel:[0,0] op_sel_hi:[1,0]\n"
    "v_pk_mul_f32 v[140:141], v[140:141], v[218:219] op_sel:[0,0] op_sel_hi:[1,0]\n"
    "v_pk_mul_f32 v[142:143], v[142:143], v[218:219] op_sel:[0,0] op_sel_hi:[1,0]\n"
    "v_cvt_pk_bf16_f32 v232, v136, v137\n"
    "v_cvt_pk_bf16_f32 v233, v138, v139\n"
    "v_cvt_pk_bf16_f32 v234, v140, v141\n"
    "v_cvt_pk_bf16_f32 v235, v142, v143\n"
    "buffer_store_dwordx4 v[232:235], v3, s[56:59], s30 offen offset:2048 sc1\n"
    "v_accvgpr_write_b32 a8, v232\n"
    "v_accvgpr_write_b32 a9, v233\n"
    "v_accvgpr_write_b32 a10, v234\n"
    "v_accvgpr_write_b32 a11, v235\n"
    "v_pk_add_f32 v[144:145], v[144:145], v[48:49]\n"
    "v_pk_add_f32 v[146:147], v[146:147], v[50:51]\n"
    "v_pk_add_f32 v[148:149], v[148:149], v[52:53]\n"
    "v_pk_add_f32 v[150:151], v[150:151], v[54:55]\n"
    "v_pk_mul_f32 v[144:145], v[144:145], v[218:219] op_sel:[0,0] op_sel_hi:[1,0]\n"
    "v_pk_mul_f32 v[146:147], v[146:147], v[218:219] op_sel:[0,0] op_sel_hi:[1,0]\n"
    "v_pk_mul_f32 v[148:149], v[148:149], v[218:219] op_sel:[0,0] op_sel_hi:[1,0]\n"
    "v_pk_mul_f32 v[150:151], v[150:151], v[218:219] op_sel:[0,0] op_sel_hi:[1,0]\n"
    "v_cvt_pk_bf16_f32 v236, v144, v145\n"
    "v_cvt_pk_bf16_f32 v237, v146, v147\n"
    "v_cvt_pk_bf16_f32 v238, v148, v149\n"
    "v_cvt_pk_bf16_f32 v239, v150, v151\n"
    "buffer_store_dwordx4 v[236:239], v3, s[56:59], s30 offen offset:3072 sc1\n"
    "v_accvgpr_write_b32 a12, v236\n"
    "v_accvgpr_write_b32 a13, v237\n"
    "v_accvgpr_write_b32 a14, v238\n"
    "v_accvgpr_write_b32 a15, v239\n"
    "v_pk_add_f32 v[152:153], v[152:153], v[56:57]\n"
    "v_pk_add_f32 v[154:155], v[154:155], v[58:59]\n"
    "v_pk_add_f32 v[156:157], v[156:157], v[60:61]\n"
    "v_pk_add_f32 v[158:159], v[158:159], v[62:63]\n"
    "v_pk_mul_f32 v[152:153], v[152:153], v[218:219] op_sel:[0,1] op_sel_hi:[1,1]\n"
    "v_pk_mul_f32 v[154:155], v[154:155], v[218:219] op_sel:[0,1] op_sel_hi:[1,1]\n"
    "v_pk_mul_f32 v[156:157], v[156:157], v[218:219] op_sel:[0,1] op_sel_hi:[1,1]\n"
    "v_pk_mul_f32 v[158:159], v[158:159], v[218:219] op_sel:[0,1] op_sel_hi:[1,1]\n"
    "v_cvt_pk_bf16_f32 v224, v152, v153\n"
    "v_cvt_pk_bf16_f32 v225, v154, v155\n"
    "v_cvt_pk_bf16_f32 v226, v156, v157\n"
    "v_cvt_pk_bf16_f32 v227, v158, v159\n"
    "s_add_u32 s30, s30, 131072\n"
    "buffer_store_dwordx4 v[224:227], v3, s[56:59], s30 offen offset:0 sc1\n"
    "v_accvgpr_write_b32 a64, v224\n"
    "v_accvgpr_write_b32 a65, v225\n"
    "v_accvgpr_write_b32 a66, v226\n"
    "v_accvgpr_write_b32 a67, v227\n"
    "v_pk_add_f32 v[160:161], v[160:161], v[64:65]\n"
    "v_pk_add_f32 v[162:163], v[162:163], v[66:67]\n"
    "v_pk_add_f32 v[164:165], v[164:165], v[68:69]\n"
    "v_pk_add_f32 v[166:167], v[166:167], v[70:71]\n"
    "v_pk_mul_f32 v[160:161], v[160:161], v[218:219] op_sel:[0,1] op_sel_hi:[1,1]\n"
    "v_pk_mul_f32 v[162:163], v[162:163], v[218:219] op_sel:[0,1] op_sel_hi:[1,1]\n"
    "v_pk_mul_f32 v[164:165], v[164:165], v[218:219] op_sel:[0,1] op_sel_hi:[1,1]\n"
    "v_pk_mul_f32 v[166:167], v[166:167], v[218:219] op_sel:[0,1] op_sel_hi:[1,1]\n"
    "v_cvt_pk_bf16_f32 v228, v160, v161\n"
    "v_cvt_pk_bf16_f32 v229, v162, v163\n"
    "v_cvt_pk_bf16_f32 v230, v164, v165\n"
    "v_cvt_pk_bf16_f32 v231, v166, v167\n"
    "buffer_store_dwordx4 v[228:231], v3, s[56:59], s30 offen offset:1024 sc1\n"
    "v_accvgpr_write_b32 a68, v228\n"
    "v_accvgpr_write_b32 a69, v229\n"
    "v_accvgpr_write_b32 a70, v230\n"
    "v_accvgpr_write_b32 a71, v231\n"
    "v_pk_add_f32 v[168:169], v[168:169], v[72:73]\n"
    "v_pk_add_f32 v[170:171], v[170:171], v[74:75]\n"
    "v_pk_add_f32 v[172:173], v[172:173], v[76:77]\n"
    "v_pk_add_f32 v[174:175], v[174:175], v[78:79]\n"
    "v_pk_mul_f32 v[168:169], v[168:169], v[218:219] op_sel:[0,1] op_sel_hi:[1,1]\n"
    "v_pk_mul_f32 v[170:171], v[170:171], v[218:219] op_sel:[0,1] op_sel_hi:[1,1]\n"
    "v_pk_mul_f32 v[172:173], v[172:173], v[218:219] op_sel:[0,1] op_sel_hi:[1,1]\n"
    "v_pk_mul_f32 v[174:175], v[174:175], v[218:219] op_sel:[0,1] op_sel_hi:[1,1]\n"
    "v_cvt_pk_bf16_f32 v232, v168, v169\n"
    "v_cvt_pk_bf16_f32 v233, v170, v171\n"
    "v_cvt_pk_bf16_f32 v234, v172, v173\n"
    "v_cvt_pk_bf16_f32 v235, v174, v175\n"
    "buffer_store_dwordx4 v[232:235], v3, s[56:59], s30 offen offset:2048 sc1\n"
    "v_accvgpr_write_b32 a72, v232\n"
    "v_accvgpr_write_b32 a73, v233\n"
    "v_accvgpr_write_b32 a74, v234\n"
    "v_accvgpr_write_b32 a75, v235\n"
    "v_pk_add_f32 v[176:177], v[176:177], v[80:81]\n"
    "v_pk_add_f32 v[178:179], v[178:179], v[82:83]\n"
    "v_pk_add_f32 v[180:181], v[180:181], v[84:85]\n"
    "v_pk_add_f32 v[182:183], v[182:183], v[86:87]\n"
    "v_pk_mul_f32 v[176:177], v[176:177], v[218:219] op_sel:[0,1] op_sel_hi:[1,1]\n"
    "v_pk_mul_f32 v[178:179], v[178:179], v[218:219] op_sel:[0,1] op_sel_hi:[1,1]\n"
    "v_pk_mul_f32 v[180:181], v[180:181], v[218:219] op_sel:[0,1] op_sel_hi:[1,1]\n"
    "v_pk_mul_f32 v[182:183], v[182:183], v[218:219] op_sel:[0,1] op_sel_hi:[1,1]\n"
    "v_cvt_pk_bf16_f32 v236, v176, v177\n"
    "v_cvt_pk_bf16_f32 v237, v178, v179\n"
    "v_cvt_pk_bf16_f32 v238, v180, v181\n"
    "v_cvt_pk_bf16_f32 v239, v182, v183\n"
    "buffer_store_dwordx4 v[236:239], v3, s[56:59], s30 offen offset:3072 sc1\n"
    "v_accvgpr_write_b32 a76, v236\n"
    "v_accvgpr_write_b32 a77, v237\n"
    "v_accvgpr_write_b32 a78, v238\n"
    "v_accvgpr_write_b32 a79, v239\n"
    "s_mov_b32 %0, s98\n"
    "s_branch L_end_%=\n"
    "L_hkH1_%=:\n"
    "s_cmp_eq_u32 s24, s23\n"
    "s_cbranch_scc1 L_flH1_%=\n"
    "v_accvgpr_mov_b32 a160, a212\n"
    "v_accvgpr_mov_b32 a161, a213\n"
    "v_accvgpr_mov_b32 a162, a214\n"
    "v_accvgpr_mov_b32 a163, a215\n"
    "v_accvgpr_mov_b32 a164, a216\n"
    "v_accvgpr_mov_b32 a165, a217\n"
    "v_accvgpr_mov_b32 a166, a218\n"
    "v_accvgpr_mov_b32 a167, a219\n"
    "v_accvgpr_mov_b32 a168, a220\n"
    "v_accvgpr_mov_b32 a169, a221\n"
    "v_accvgpr_mov_b32 a170, a222\n"
    "v_accvgpr_mov_b32 a171, a223\n"
    "v_accvgpr_mov_b32 a172, a224\n"
    "v_accvgpr_mov_b32 a173, a225\n"
    "v_accvgpr_mov_b32 a174, a226\n"
    "v_accvgpr_mov_b32 a175, a227\n"
    "v_accvgpr_mov_b32 a176, a228\n"
    "v_accvgpr_mov_b32 a177, a229\n"
    "v_accvgpr_mov_b32 a178, a230\n"
    "v_accvgpr_mov_b32 a179, a231\n"
    "v_accvgpr_mov_b32 a180, a232\n"
    "v_accvgpr_mov_b32 a181, a233\n"
    "v_accvgpr_mov_b32 a182, a234\n"
    "v_accvgpr_mov_b32 a183, a235\n"
    "v_accvgpr_mov_b32 a184, a236\n"
    "v_accvgpr_mov_b32 a185, a237\n"
    "v_accvgpr_mov_b32 a186, a238\n"
    "v_accvgpr_mov_b32 a187, a239\n"
    "v_accvgpr_mov_b32 a188, a240\n"
    "v_accvgpr_mov_b32 a189, a241\n"
    "v_accvgpr_mov_b32 a190, a242\n"
    "v_accvgpr_mov_b32 a191, a243\n"
    "s_branch L_rtH1_%=\n"
    "L_flH1_%=:\n"
    "v_mov_b32 v223, 0x5f800000\n"
    "s_lshl_b32 s30, s22, 2\n"
    "s_add_u32 s30, s30, s20\n"
    "s_lshl_b32 s30, s30, 18\n"
    "s_nop 1\n"
    "v_accvgpr_read_b32 v216, a200\n"
    "v_accvgpr_read_b32 v217, a204\n"
    "v_max_f32 v222, v216, v217\n"
    "v_add_f32 v222, v222, v216\n"
    "v_add_f32 v222, v222, v217\n"
    "v_cmp_nlt_f32 vcc, v222, v223\n"
    "v_rcp_f32 v218, v216\n"
    "v_rcp_f32 v219, v217\n"
    "s_nop 1\n"
    "s_or_b32 s98, s98, vcc_lo\n"
    "s_or_b32 s98, s98, vcc_hi\n"
    "v_accvgpr_write_b32 a200, 0\n"
    "v_accvgpr_write_b32 a201, 0\n"
    "v_accvgpr_write_b32 a202, 0\n"
    "v_accvgpr_write_b32 a203, 0\n"
    "v_accvgpr_write_b32 a204, 0\n"
    "v_accvgpr_write_b32 a205, 0\n"
    "v_accvgpr_write_b32 a206, 0\n"
    "v_accvgpr_write_b32 a207, 0\n"
    "v_pk_mul_f32 v[120:121], v[120:121], v[218:219] op_sel:[0,0] op_sel_hi:[1,0]\n"
    "v_pk_mul_f32 v[122:123], v[122:123], v[218:219] op_sel:[0,0] op_sel_hi:[1,0]\n"
    "v_pk_mul_f32 v[124:125], v[124:125], v[218:219] op_sel:[0,0] op_sel_hi:[1,0]\n"
    "v_pk_mul_f32 v[126:127], v[126:127], v[218:219] op_sel:[0,0] op_sel_hi:[1,0]\n"
    "v_cvt_pk_bf16_f32 v224, v120, v121\n"
    "v_cvt_pk_bf16_f32 v225, v122, v123\n"
    "v_cvt_pk_bf16_f32 v226, v124, v125\n"
    "v_cvt_pk_bf16_f32 v227, v126, v127\n"
    "buffer_store_dwordx4 v[224:227], v3, s[56:59], s30 offen offset:0 sc1\n"
    "v_pk_mul_f32 v[128:129], v[128:129], v[218:219] op_sel:[0,0] op_sel_hi:[1,0]\n"
    "v_pk_mul_f32 v[130:131], v[130:131], v[218:219] op_sel:[0,0] op_sel_hi:[1,0]\n"
    "v_pk_mul_f32 v[132:133], v[132:133], v[218:219] op_sel:[0,0] op_sel_hi:[1,0]\n"
    "v_pk_mul_f32 v[134:135], v[134:135], v[218:219] op_sel:[0,0] op_sel_hi:[1,0]\n"
    "v_cvt_pk_bf16_f32 v228, v128, v129\n"
    "v_cvt_pk_bf16_f32 v229, v130, v131\n"
    "v_cvt_pk_bf16_f32 v230, v132, v133\n"
    "v_cvt_pk_bf16_f32 v231, v134, v135\n"
    "buffer_store_dwordx4 v[228:231], v3, s[56:59], s30 offen offset:1024 sc1\n"
    "v_pk_mul_f32 v[136:137], v[136:137], v[218:219] op_sel:[0,0] op_sel_hi:[1,0]\n"
    "v_pk_mul_f32 v[138:139], v[138:139], v[218:219] op_sel:[0,0] op_sel_hi:[1,0]\n"
    "v_pk_mul_f32 v[140:141], v[140:141], v[218:219] op_sel:[0,0] op_sel_hi:[1,0]\n"
    "v_pk_mul_f32 v[142:143], v[142:143], v[218:219] op_sel:[0,0] op_sel_hi:[1,0]\n"
    "v_cvt_pk_bf16_f32 v232, v136, v137\n"
    "v_cvt_pk_bf16_f32 v233, v138, v139\n"
    "v_cvt_pk_bf16_f32 v234, v140, v141\n"
    "v_cvt_pk_bf16_f32 v235, v142, v143\n"
    "buffer_store_dwordx4 v[232:235], v3, s[56:59], s30 offen offset:2048 sc1\n"
    "v_pk_mul_f32 v[144:145], v[144:145], v[218:219] op_sel:[0,0] op_sel_hi:[1,0]\n"
    "v_pk_mul_f32 v[146:147], v[146:147], v[218:219] op_sel:[0,0] op_sel_hi:[1,0]\n"
    "v_pk_mul_f32 v[148:149], v[148:149], v[218:219] op_sel:[0,0] op_sel_hi:[1,0]\n"
    "v_pk_mul_f32 v[150:151], v[150:151], v[218:219] op_sel:[0,0] op_sel_hi:[1,0]\n"
    "v_cvt_pk_bf16_f32 v236, v144, v145\n"
    "v_cvt_pk_bf16_f32 v237, v146, v147\n"
    "v_cvt_pk_bf16_f32 v238, v148, v149\n"
    "v_cvt_pk_bf16_f32 v239, v150, v151\n"
    "buffer_store_dwordx4 v[236:239], v3, s[56:59], s30 offen offset:3072 sc1\n"
    "v_pk_mul_f32 v[152:153], v[152:153], v[218:219] op_sel:[0,1] op_sel_hi:[1,1]\n"
    "v_pk_mul_f32 v[154:155], v[154:155], v[218:219] op_sel:[0,1] op_sel_hi:[1,1]\n"
    "v_pk_mul_f32 v[156:157], v[156:157], v[218:219] op_sel:[0,1] op_sel_hi:[1,1]\n"
    "v_pk_mul_f32 v[158:159], v[158:159], v[218:219] op_sel:[0,1] op_sel_hi:[1,1]\n"
    "v_cvt_pk_bf16_f32 v224, v152, v153\n"
    "v_cvt_pk_bf16_f32 v225, v154, v155\n"
    "v_cvt_pk_bf16_f32 v226, v156, v157\n"
    "v_cvt_pk_bf16_f32 v227, v158, v159\n"
    "s_add_u32 s30, s30, 131072\n"
    "buffer_store_dwordx4 v[224:227], v3, s[56:59], s30 offen offset:0 sc1\n"
    "v_pk_mul_f32 v[160:161], v[160:161], v[218:219] op_sel:[0,1] op_sel_hi:[1,1]\n"
    "v_pk_mul_f32 v[162:163], v[162:163], v[218:219] op_sel:[0,1] op_sel_hi:[1,1]\n"
    "v_pk_mul_f32 v[164:165], v[164:165], v[218:219] op_sel:[0,1] op_sel_hi:[1,1]\n"
    "v_pk_mul_f32 v[166:167], v[166:167], v[218:219] op_sel:[0,1] op_sel_hi:[1,1]\n"
    "v_cvt_pk_bf16_f32 v228, v160, v161\n"
    "v_cvt_pk_bf16_f32 v229, v162, v163\n"
    "v_cvt_pk_bf16_f32 v230, v164, v165\n"
    "v_cvt_pk_bf16_f32 v231, v166, v167\n"
    "buffer_store_dwordx4 v[228:231], v3, s[56:59], s30 offen offset:1024 sc1\n"
    "v_pk_mul_f32 v[168:169], v[168:169], v[218:219] op_sel:[0,1] op_sel_hi:[1,1]\n"
    "v_pk_mul_f32 v[170:171], v[170:171], v[218:219] op_sel:[0,1] op_sel_hi:[1,1]\n"
    "v_pk_mul_f32 v[172:173], v[172:173], v[218:219] op_sel:[0,1] op_sel_hi:[1,1]\n"
    "v_pk_mul_f32 v[174:175], v[174:175], v[218:219] op_sel:[0,1] op_sel_hi:[1,1]\n"
    "v_cvt_pk_bf16_f32 v232, v168, v169\n"
    "v_cvt_pk_bf16_f32 v233, v170, v171\n"
    "v_cvt_pk_bf16_f32 v234, v172, v173\n"
    "v_cvt_pk_bf16_f32 v235, v174, v175\n"
    "buffer_store_dwordx4 v[232:235], v3, s[56:59], s30 offen offset:2048 sc1\n"
    "v_pk_mul_f32 v[176:177], v[176:177], v[218:219] op_sel:[0,1] op_sel_hi:[1,1]\n"
    "v_pk_mul_f32 v[178:179], v[178:179], v[218:219] op_sel:[0,1] op_sel_hi:[1,1]\n"
    "v_pk_mul_f32 v[180:181], v[180:181], v[218:219] op_sel:[0,1] op_sel_hi:[1,1]\n"
    "v_pk_mul_f32 v[182:183], v[182:183], v[218:219] op_sel:[0,1] op_sel_hi:[1,1]\n"
    "v_cvt_pk_bf16_f32 v236, v176, v177\n"
    "v_cvt_pk_bf16_f32 v237, v178, v179\n"
    "v_cvt_pk_bf16_f32 v238, v180, v181\n"
    "v_cvt_pk_bf16_f32 v239, v182, v183\n"
    "buffer_store_dwordx4 v[236:239], v3, s[56:59], s30 offen offset:3072 sc1\n"
    "v_pk_mov_b32 v[120:121], 0, 0\n"
    "v_pk_mov_b32 v[122:123], 0, 0\n"
    "v_pk_mov_b32 v[124:125], 0, 0\n"
    "v_pk_mov_b32 v[126:127], 0, 0\n"
    "v_pk_mov_b32 v[128:129], 0, 0\n"
    "v_pk_mov_b32 v[130:131], 0, 0\n"
    "v_pk_mov_b32 v[132:133], 0, 0\n"
    "v_pk_mov_b32 v[134:135], 0, 0\n"
    "v_pk_mov_b32 v[136:137], 0, 0\n"
    "v_pk_mov_b32 v[138:139], 0, 0\n"
    "v_pk_mov_b32 v[140:141], 0, 0\n"
    "v_pk_mov_b32 v[142:143], 0, 0\n"
    "v_pk_mov_b32 v[144:145], 0, 0\n"
    "v_pk_mov_b32 v[146:147], 0, 0\n"
    "v_pk_mov_b32 v[148:149], 0, 0\n"
    "v_pk_mov_b32 v[150:151], 0, 0\n"
    "v_pk_mov_b32 v[152:153], 0, 0\n"
    "v_pk_mov_b32 v[154:155], 0, 0\n"
    "v_pk_mov_b32 v[156:157], 0, 0\n"
    "v_pk_mov_b32 v[158:159], 0, 0\n"
    "v_pk_mov_b32 v[160:161], 0, 0\n"
    "v_pk_mov_b32 v[162:163], 0, 0\n"
    "v_pk_mov_b32 v[164:165], 0, 0\n"
    "v_pk_mov_b32 v[166:167], 0, 0\n"
    "v_pk_mov_b32 v[168:169], 0, 0\n"
    "v_pk_mov_b32 v[170:171], 0, 0\n"
    "v_pk_mov_b32 v[172:173], 0, 0\n"
    "v_pk_mov_b32 v[174:175], 0, 0\n"
    "v_pk_mov_b32 v[176:177], 0, 0\n"
    "v_pk_mov_b32 v[178:179], 0, 0\n"
    "v_pk_mov_b32 v[180:181], 0, 0\n"
    "v_pk_mov_b32 v[182:183], 0, 0\n"
    "s_branch L_rtH1_%=\n"
    "L_hkH2_%=:\n"
    "s_cmp_eq_u32 s24, s23\n"
    "s_cbranch_scc1 L_flH2_%=\n"
    "v_accvgpr_mov_b32 a160, a212\n"
    "v_accvgpr_mov_b32 a161, a213\n"
    "v_accvgpr_mov_b32 a162, a214\n"
    "v_accvgpr_mov_b32 a163, a215\n"
    "v_accvgpr_mov_b32 a164, a216\n"
    "v_accvgpr_mov_b32 a165, a217\n"
    "v_accvgpr_mov_b32 a166, a218\n"
    "v_accvgpr_mov_b32 a167, a219\n"
    "v_accvgpr_mov_b32 a168, a220\n"
    "v_accvgpr_mov_b32 a169, a221\n"
    "v_accvgpr_mov_b32 a170, a222\n"
    "v_accvgpr_mov_b32 a171, a223\n"
    "v_accvgpr_mov_b32 a172, a224\n"
    "v_accvgpr_mov_b32 a173, a225\n"
    "v_accvgpr_mov_b32 a174, a226\n"
    "v_accvgpr_mov_b32 a175, a227\n"
    "v_accvgpr_mov_b32 a176, a228\n"
    "v_accvgpr_mov_b32 a177, a229\n"
    "v_accvgpr_mov_b32 a178, a230\n"
    "v_accvgpr_mov_b32 a179, a231\n"
    "v_accvgpr_mov_b32 a180, a232\n"
    "v_accvgpr_mov_b32 a181, a233\n"
    "v_accvgpr_mov_b32 a182, a234\n"
    "v_accvgpr_mov_b32 a183, a235\n"
    "v_accvgpr_mov_b32 a184, a236\n"
    "v_accvgpr_mov_b32 a185, a237\n"
    "v_accvgpr_mov_b32 a186, a238\n"
    "v_accvgpr_mov_b32 a187, a239\n"
    "v_accvgpr_mov_b32 a188, a240\n"
    "v_accvgpr_mov_b32 a189, a241\n"
    "v_accvgpr_mov_b32 a190, a242\n"
    "v_accvgpr_mov_b32 a191, a243\n"
    "s_branch L_rtH2_%=\n"
    "L_flH2_%=:\n"
    "v_mov_b32 v223, 0x5f800000\n"
    "s_lshl_b32 s30, s22, 2\n"
    "s_add_u32 s30, s30, s20\n"
    "s_lshl_b32 s30, s30, 18\n"
    "s_nop 1\n"
    "v_accvgpr_read_b32 v216, a200\n"
    "v_accvgpr_read_b32 v217, a204\n"
    "v_max_f32 v222, v216, v217\n"
    "v_add_f32 v222, v222, v216\n"
    "v_add_f32 v222, v222, v217\n"
    "v_cmp_nlt_f32 vcc, v222, v223\n"
    "v_rcp_f32 v218, v216\n"
    "v_rcp_f32 v219, v217\n"
    "s_nop 1\n"
    "s_or_b32 s98, s98, vcc_lo\n"
    "s_or_b32 s98, s98, vcc_hi\n"
    "v_accvgpr_write_b32 a200, 0\n"
    "v_accvgpr_write_b32 a201, 0\n"
    "v_accvgpr_write_b32 a202, 0\n"
    "v_accvgpr_write_b32 a203, 0\n"
    "v_accvgpr_write_b32 a204, 0\n"
    "v_accvgpr_write_b32 a205, 0\n"
    "v_accvgpr_write_b32 a206, 0\n"
    "v_accvgpr_write_b32 a207, 0\n"
    "v_pk_mul_f32 v[120:121], v[120:121], v[218:219] op_sel:[0,0] op_sel_hi:[1,0]\n"
    "v_pk_mul_f32 v[122:123], v[122:123], v[218:219] op_sel:[0,0] op_sel_hi:[1,0]\n"
    "v_pk_mul_f32 v[124:125], v[124:125], v[218:219] op_sel:[0,0] op_sel_hi:[1,0]\n"
    "v_pk_mul_f32 v[126:127], v[126:127], v[218:219] op_sel:[0,0] op_sel_hi:[1,0]\n"
    "v_cvt_pk_bf16_f32 v224, v120, v121\n"
    "v_cvt_pk_bf16_f32 v225, v122, v123\n"
    "v_cvt_pk_bf16_f32 v226, v124, v125\n"
    "v_cvt_pk_bf16_f32 v227, v126, v127\n"
    "buffer_store_dwordx4 v[224:227], v3, s[56:59], s30 offen offset:0 sc1\n"
    "v_pk_mul_f32 v[128:129], v[128:129], v[218:219] op_sel:[0,0] op_sel_hi:[1,0]\n"
    "v_pk_mul_f32 v[130:131], v[130:131], v[218:219] op_sel:[0,0] op_sel_hi:[1,0]\n"
    "v_pk_mul_f32 v[132:133], v[132:133], v[218:219] op_sel:[0,0] op_sel_hi:[1,0]\n"
    "v_pk_mul_f32 v[134:135], v[134:135], v[218:219] op_sel:[0,0] op_sel_hi:[1,0]\n"
    "v_cvt_pk_bf16_f32 v228, v128, v129\n"
    "v_cvt_pk_bf16_f32 v229, v130, v131\n"
    "v_cvt_pk_bf16_f32 v230, v132, v133\n"
    "v_cvt_pk_bf16_f32 v231, v134, v135\n"
    "buffer_store_dwordx4 v[228:231], v3, s[56:59], s30 offen offset:1024 sc1\n"
    "v_pk_mul_f32 v[136:137], v[136:137], v[218:219] op_sel:[0,0] op_sel_hi:[1,0]\n"
    "v_pk_mul_f32 v[138:139], v[138:139], v[218:219] op_sel:[0,0] op_sel_hi:[1,0]\n"
    "v_pk_mul_f32 v[140:141], v[140:141], v[218:219] op_sel:[0,0] op_sel_hi:[1,0]\n"
    "v_pk_mul_f32 v[142:143], v[142:143], v[218:219] op_sel:[0,0] op_sel_hi:[1,0]\n"
    "v_cvt_pk_bf16_f32 v232, v136, v137\n"
    "v_cvt_pk_bf16_f32 v233, v138, v139\n"
    "v_cvt_pk_bf16_f32 v234, v140, v141\n"
    "v_cvt_pk_bf16_f32 v235, v142, v143\n"
    "buffer_store_dwordx4 v[232:235], v3, s[56:59], s30 offen offset:2048 sc1\n"
    "v_pk_mul_f32 v[144:145], v[144:145], v[218:219] op_sel:[0,0] op_sel_hi:[1,0]\n"
    "v_pk_mul_f32 v[146:147], v[146:147], v[218:219] op_sel:[0,0] op_sel_hi:[1,0]\n"
    "v_pk_mul_f32 v[148:149], v[148:149], v[218:219] op_sel:[0,0] op_sel_hi:[1,0]\n"
    "v_pk_mul_f32 v[150:151], v[150:151], v[218:219] op_sel:[0,0] op_sel_hi:[1,0]\n"
    "v_cvt_pk_bf16_f32 v236, v144, v145\n"
    "v_cvt_pk_bf16_f32 v237, v146, v147\n"
    "v_cvt_pk_bf16_f32 v238, v148, v149\n"
    "v_cvt_pk_bf16_f32 v239, v150, v151\n"
    "buffer_store_dwordx4 v[236:239], v3, s[56:59], s30 offen offset:3072 sc1\n"
    "v_pk_mul_f32 v[152:153], v[152:153], v[218:219] op_sel:[0,1] op_sel_hi:[1,1]\n"
    "v_pk_mul_f32 v[154:155], v[154:155], v[218:219] op_sel:[0,1] op_sel_hi:[1,1]\n"
    "v_pk_mul_f32 v[156:157], v[156:157], v[218:219] op_sel:[0,1] op_sel_hi:[1,1]\n"
    "v_pk_mul_f32 v[158:159], v[158:159], v[218:219] op_sel:[0,1] op_sel_hi:[1,1]\n"
    "v_cvt_pk_bf16_f32 v224, v152, v153\n"
    "v_cvt_pk_bf16_f32 v225, v154, v155\n"
    "v_cvt_pk_bf16_f32 v226, v156, v157\n"
    "v_cvt_pk_bf16_f32 v227, v158, v159\n"
    "s_add_u32 s30, s30, 131072\n"
    "buffer_store_dwordx4 v[224:227], v3, s[56:59], s30 offen offset:0 sc1\n"
    "v_pk_mul_f32 v[160:161], v[160:161], v[218:219] op_sel:[0,1] op_sel_hi:[1,1]\n"
    "v_pk_mul_f32 v[162:163], v[162:163], v[218:219] op_sel:[0,1] op_sel_hi:[1,1]\n"
    "v_pk_mul_f32 v[164:165], v[164:165], v[218:219] op_sel:[0,1] op_sel_hi:[1,1]\n"
    "v_pk_mul_f32 v[166:167], v[166:167], v[218:219] op_sel:[0,1] op_sel_hi:[1,1]\n"
    "v_cvt_pk_bf16_f32 v228, v160, v161\n"
    "v_cvt_pk_bf16_f32 v229, v162, v163\n"
    "v_cvt_pk_bf16_f32 v230, v164, v165\n"
    "v_cvt_pk_bf16_f32 v231, v166, v167\n"
    "buffer_store_dwordx4 v[228:231], v3, s[56:59], s30 offen offset:1024 sc1\n"
    "v_pk_mul_f32 v[168:169], v[168:169], v[218:219] op_sel:[0,1] op_sel_hi:[1,1]\n"
    "v_pk_mul_f32 v[170:171], v[170:171], v[218:219] op_sel:[0,1] op_sel_hi:[1,1]\n"
    "v_pk_mul_f32 v[172:173], v[172:173], v[218:219] op_sel:[0,1] op_sel_hi:[1,1]\n"
    "v_pk_mul_f32 v[174:175], v[174:175], v[218:219] op_sel:[0,1] op_sel_hi:[1,1]\n"
    "v_cvt_pk_bf16_f32 v232, v168, v169\n"
    "v_cvt_pk_bf16_f32 v233, v170, v171\n"
    "v_cvt_pk_bf16_f32 v234, v172, v173\n"
    "v_cvt_pk_bf16_f32 v235, v174, v175\n"
    "buffer_store_dwordx4 v[232:235], v3, s[56:59], s30 offen offset:2048 sc1\n"
    "v_pk_mul_f32 v[176:177], v[176:177], v[218:219] op_sel:[0,1] op_sel_hi:[1,1]\n"
    "v_pk_mul_f32 v[178:179], v[178:179], v[218:219] op_sel:[0,1] op_sel_hi:[1,1]\n"
    "v_pk_mul_f32 v[180:181], v[180:181], v[218:219] op_sel:[0,1] op_sel_hi:[1,1]\n"
    "v_pk_mul_f32 v[182:183], v[182:183], v[218:219] op_sel:[0,1] op_sel_hi:[1,1]\n"
    "v_cvt_pk_bf16_f32 v236, v176, v177\n"
    "v_cvt_pk_bf16_f32 v237, v178, v179\n"
    "v_cvt_pk_bf16_f32 v238, v180, v181\n"
    "v_cvt_pk_bf16_f32 v239, v182, v183\n"
    "buffer_store_dwordx4 v[236:239], v3, s[56:59], s30 offen offset:3072 sc1\n"
    "v_pk_mov_b32 v[120:121], 0, 0\n"
    "v_pk_mov_b32 v[122:123], 0, 0\n"
    "v_pk_mov_b32 v[124:125], 0, 0\n"
    "v_pk_mov_b32 v[126:127], 0, 0\n"
    "v_pk_mov_b32 v[128:129], 0, 0\n"
    "v_pk_mov_b32 v[130:131], 0, 0\n"
    "v_pk_mov_b32 v[132:133], 0, 0\n"
    "v_pk_mov_b32 v[134:135], 0, 0\n"
    "v_pk_mov_b32 v[136:137], 0, 0\n"
    "v_pk_mov_b32 v[138:139], 0, 0\n"
    "v_pk_mov_b32 v[140:141], 0, 0\n"
    "v_pk_mov_b32 v[142:143], 0, 0\n"
    "v_pk_mov_b32 v[144:145], 0, 0\n"
    "v_pk_mov_b32 v[146:147], 0, 0\n"
    "v_pk_mov_b32 v[148:149], 0, 0\n"
    "v_pk_mov_b32 v[150:151], 0, 0\n"
    "v_pk_mov_b32 v[152:153], 0, 0\n"
    "v_pk_mov_b32 v[154:155], 0, 0\n"
    "v_pk_mov_b32 v[156:157], 0, 0\n"
    "v_pk_mov_b32 v[158:159], 0, 0\n"
    "v_pk_mov_b32 v[160:161], 0, 0\n"
    "v_pk_mov_b32 v[162:163], 0, 0\n"
    "v_pk_mov_b32 v[164:165], 0, 0\n"
    "v_pk_mov_b32 v[166:167], 0, 0\n"
    "v_pk_mov_b32 v[168:169], 0, 0\n"
    "v_pk_mov_b32 v[170:171], 0, 0\n"
    "v_pk_mov_b32 v[172:173], 0, 0\n"
    "v_pk_mov_b32 v[174:175], 0, 0\n"
    "v_pk_mov_b32 v[176:177], 0, 0\n"
    "v_pk_mov_b32 v[178:179], 0, 0\n"
    "v_pk_mov_b32 v[180:181], 0, 0\n"
    "v_pk_mov_b32 v[182:183], 0, 0\n"
    "s_branch L_rtH2_%=\n"
    "L_hkH3_%=:\n"
    "s_cmp_eq_u32 s24, s23\n"
    "s_cbranch_scc1 L_flH3_%=\n"
    "v_accvgpr_mov_b32 a160, a212\n"
    "v_accvgpr_mov_b32 a161, a213\n"
    "v_accvgpr_mov_b32 a162, a214\n"
    "v_accvgpr_mov_b32 a163, a215\n"
    "v_accvgpr_mov_b32 a164, a216\n"
    "v_accvgpr_mov_b32 a165, a217\n"
    "v_accvgpr_mov_b32 a166, a218\n"
    "v_accvgpr_mov_b32 a167, a219\n"
    "v_accvgpr_mov_b32 a168, a220\n"
    "v_accvgpr_mov_b32 a169, a221\n"
    "v_accvgpr_mov_b32 a170, a222\n"
    "v_accvgpr_mov_b32 a171, a223\n"
    "v_accvgpr_mov_b32 a172, a224\n"
    "v_accvgpr_mov_b32 a173, a225\n"
    "v_accvgpr_mov_b32 a174, a226\n"
    "v_accvgpr_mov_b32 a175, a227\n"
    "v_accvgpr_mov_b32 a176, a228\n"
    "v_accvgpr_mov_b32 a177, a229\n"
    "v_accvgpr_mov_b32 a178, a230\n"
    "v_accvgpr_mov_b32 a179, a231\n"
    "v_accvgpr_mov_b32 a180, a232\n"
    "v_accvgpr_mov_b32 a181, a233\n"
    "v_accvgpr_mov_b32 a182, a234\n"
    "v_accvgpr_mov_b32 a183, a235\n"
    "v_accvgpr_mov_b32 a184, a236\n"
    "v_accvgpr_mov_b32 a185, a237\n"
    "v_accvgpr_mov_b32 a186, a238\n"
    "v_accvgpr_mov_b32 a187, a239\n"
    "v_accvgpr_mov_b32 a188, a240\n"
    "v_accvgpr_mov_b32 a189, a241\n"
    "v_accvgpr_mov_b32 a190, a242\n"
    "v_accvgpr_mov_b32 a191, a243\n"
    "s_branch L_rtH3_%=\n"
    "L_flH3_%=:\n"
    "v_mov_b32 v223, 0x5f800000\n"
    "s_lshl_b32 s30, s22, 2\n"
    "s_add_u32 s30, s30, s20\n"
    "s_lshl_b32 s30, s30, 18\n"
    "s_nop 1\n"
    "v_accvgpr_read_b32 v216, a200\n"
    "v_accvgpr_read_b32 v217, a204\n"
    "v_max_f32 v222, v216, v217\n"
    "v_add_f32 v222, v222, v216\n"
    "v_add_f32 v222, v222, v217\n"
    "v_cmp_nlt_f32 vcc, v222, v223\n"
    "v_rcp_f32 v218, v216\n"
    "v_rcp_f32 v219, v217\n"
    "s_nop 1\n"
    "s_or_b32 s98, s98, vcc_lo\n"
    "s_or_b32 s98, s98, vcc_hi\n"
    "v_accvgpr_write_b32 a200, 0\n"
    "v_accvgpr_write_b32 a201, 0\n"
    "v_accvgpr_write_b32 a202, 0\n"
    "v_accvgpr_write_b32 a203, 0\n"
    "v_accvgpr_write_b32 a204, 0\n"
    "v_accvgpr_write_b32 a205, 0\n"
    "v_accvgpr_write_b32 a206, 0\n"
    "v_accvgpr_write_b32 a207, 0\n"
    "v_pk_mul_f32 v[120:121], v[120:121], v[218:219] op_sel:[0,0] op_sel_hi:[1,0]\n"
    "v_pk_mul_f32 v[122:123], v[122:123], v[218:219] op_sel:[0,0] op_sel_hi:[1,0]\n"
    "v_pk_mul_f32 v[124:125], v[124:125], v[218:219] op_sel:[0,0] op_sel_hi:[1,0]\n"
    "v_pk_mul_f32 v[126:127], v[126:127], v[218:219] op_sel:[0,0] op_sel_hi:[1,0]\n"
    "v_cvt_pk_bf16_f32 v224, v120, v121\n"
    "v_cvt_pk_bf16_f32 v225, v122, v123\n"
    "v_cvt_pk_bf16_f32 v226, v124, v125\n"
    "v_cvt_pk_bf16_f32 v227, v126, v127\n"
    "buffer_store_dwordx4 v[224:227], v3, s[56:59], s30 offen offset:0 sc1\n"
    "v_pk_mul_f32 v[128:129], v[128:129], v[218:219] op_sel:[0,0] op_sel_hi:[1,0]\n"
    "v_pk_mul_f32 v[130:131], v[130:131], v[218:219] op_sel:[0,0] op_sel_hi:[1,0]\n"
    "v_pk_mul_f32 v[132:133], v[132:133], v[218:219] op_sel:[0,0] op_sel_hi:[1,0]\n"
    "v_pk_mul_f32 v[134:135], v[134:135], v[218:219] op_sel:[0,0] op_sel_hi:[1,0]\n"
    "v_cvt_pk_bf16_f32 v228, v128, v129\n"
    "v_cvt_pk_bf16_f32 v229, v130, v131\n"
    "v_cvt_pk_bf16_f32 v230, v132, v133\n"
    "v_cvt_pk_bf16_f32 v231, v134, v135\n"
    "buffer_store_dwordx4 v[228:231], v3, s[56:59], s30 offen offset:1024 sc1\n"
    "v_pk_mul_f32 v[136:137], v[136:137], v[218:219] op_sel:[0,0] op_sel_hi:[1,0]\n"
    "v_pk_mul_f32 v[138:139], v[138:139], v[218:219] op_sel:[0,0] op_sel_hi:[1,0]\n"
    "v_pk_mul_f32 v[140:141], v[140:141], v[218:219] op_sel:[0,0] op_sel_hi:[1,0]\n"
    "v_pk_mul_f32 v[142:143], v[142:143], v[218:219] op_sel:[0,0] op_sel_hi:[1,0]\n"
    "v_cvt_pk_bf16_f32 v232, v136, v137\n"
    "v_cvt_pk_bf16_f32 v233, v138, v139\n"
    "v_cvt_pk_bf16_f32 v234, v140, v141\n"
    "v_cvt_pk_bf16_f32 v235, v142, v143\n"
    "buffer_store_dwordx4 v[232:235], v3, s[56:59], s30 offen offset:2048 sc1\n"
    "v_pk_mul_f32 v[144:145], v[144:145], v[218:219] op_sel:[0,0] op_sel_hi:[1,0]\n"
    "v_pk_mul_f32 v[146:147], v[146:147], v[218:219] op_sel:[0,0] op_sel_hi:[1,0]\n"
    "v_pk_mul_f32 v[148:149], v[148:149], v[218:219] op_sel:[0,0] op_sel_hi:[1,0]\n"
    "v_pk_mul_f32 v[150:151], v[150:151], v[218:219] op_sel:[0,0] op_sel_hi:[1,0]\n"
    "v_cvt_pk_bf16_f32 v236, v144, v145\n"
    "v_cvt_pk_bf16_f32 v237, v146, v147\n"
    "v_cvt_pk_bf16_f32 v238, v148, v149\n"
    "v_cvt_pk_bf16_f32 v239, v150, v151\n"
    "buffer_store_dwordx4 v[236:239], v3, s[56:59], s30 offen offset:3072 sc1\n"
    "v_pk_mul_f32 v[152:153], v[152:153], v[218:219] op_sel:[0,1] op_sel_hi:[1,1]\n"
    "v_pk_mul_f32 v[154:155], v[154:155], v[218:219] op_sel:[0,1] op_sel_hi:[1,1]\n"
    "v_pk_mul_f32 v[156:157], v[156:157], v[218:219] op_sel:[0,1] op_sel_hi:[1,1]\n"
    "v_pk_mul_f32 v[158:159], v[158:159], v[218:219] op_sel:[0,1] op_sel_hi:[1,1]\n"
    "v_cvt_pk_bf16_f32 v224, v152, v153\n"
    "v_cvt_pk_bf16_f32 v225, v154, v155\n"
    "v_cvt_pk_bf16_f32 v226, v156, v157\n"
    "v_cvt_pk_bf16_f32 v227, v158, v159\n"
    "s_add_u32 s30, s30, 131072\n"
    "buffer_store_dwordx4 v[224:227], v3, s[56:59], s30 offen offset:0 sc1\n"
    "v_pk_mul_f32 v[160:161], v[160:161], v[218:219] op_sel:[0,1] op_sel_hi:[1,1]\n"
    "v_pk_mul_f32 v[162:163], v[162:163], v[218:219] op_sel:[0,1] op_sel_hi:[1,1]\n"
    "v_pk_mul_f32 v[164:165], v[164:165], v[218:219] op_sel:[0,1] op_sel_hi:[1,1]\n"
    "v_pk_mul_f32 v[166:167], v[166:167], v[218:219] op_sel:[0,1] op_sel_hi:[1,1]\n"
    "v_cvt_pk_bf16_f32 v228, v160, v161\n"
    "v_cvt_pk_bf16_f32 v229, v162, v163\n"
    "v_cvt_pk_bf16_f32 v230, v164, v165\n"
    "v_cvt_pk_bf16_f32 v231, v166, v167\n"
    "buffer_store_dwordx4 v[228:231], v3, s[56:59], s30 offen offset:1024 sc1\n"
    "v_pk_mul_f32 v[168:169], v[168:169], v[218:219] op_sel:[0,1] op_sel_hi:[1,1]\n"
    "v_pk_mul_f32 v[170:171], v[170:171], v[218:219] op_sel:[0,1] op_sel_hi:[1,1]\n"
    "v_pk_mul_f32 v[172:173], v[172:173], v[218:219] op_sel:[0,1] op_sel_hi:[1,1]\n"
    "v_pk_mul_f32 v[174:175], v[174:175], v[218:219] op_sel:[0,1] op_sel_hi:[1,1]\n"
    "v_cvt_pk_bf16_f32 v232, v168, v169\n"
    "v_cvt_pk_bf16_f32 v233, v170, v171\n"
    "v_cvt_pk_bf16_f32 v234, v172, v173\n"
    "v_cvt_pk_bf16_f32 v235, v174, v175\n"
    "buffer_store_dwordx4 v[232:235], v3, s[56:59], s30 offen offset:2048 sc1\n"
    "v_pk_mul_f32 v[176:177], v[176:177], v[218:219] op_sel:[0,1] op_sel_hi:[1,1]\n"
    "v_pk_mul_f32 v[178:179], v[178:179], v[218:219] op_sel:[0,1] op_sel_hi:[1,1]\n"
    "v_pk_mul_f32 v[180:181], v[180:181], v[218:219] op_sel:[0,1] op_sel_hi:[1,1]\n"
    "v_pk_mul_f32 v[182:183], v[182:183], v[218:219] op_sel:[0,1] op_sel_hi:[1,1]\n"
    "v_cvt_pk_bf16_f32 v236, v176, v177\n"
    "v_cvt_pk_bf16_f32 v237, v178, v179\n"
    "v_cvt_pk_bf16_f32 v238, v180, v181\n"
    "v_cvt_pk_bf16_f32 v239, v182, v183\n"
    "buffer_store_dwordx4 v[236:239], v3, s[56:59], s30 offen offset:3072 sc1\n"
    "v_pk_mov_b32 v[120:121], 0, 0\n"
    "v_pk_mov_b32 v[122:123], 0, 0\n"
    "v_pk_mov_b32 v[124:125], 0, 0\n"
    "v_pk_mov_b32 v[126:127], 0, 0\n"
    "v_pk_mov_b32 v[128:129], 0, 0\n"
    "v_pk_mov_b32 v[130:131], 0, 0\n"
    "v_pk_mov_b32 v[132:133], 0, 0\n"
    "v_pk_mov_b32 v[134:135], 0, 0\n"
    "v_pk_mov_b32 v[136:137], 0, 0\n"
    "v_pk_mov_b32 v[138:139], 0, 0\n"
    "v_pk_mov_b32 v[140:141], 0, 0\n"
    "v_pk_mov_b32 v[142:143], 0, 0\n"
    "v_pk_mov_b32 v[144:145], 0, 0\n"
    "v_pk_mov_b32 v[146:147], 0, 0\n"
    "v_pk_mov_b32 v[148:149], 0, 0\n"
    "v_pk_mov_b32 v[150:151], 0, 0\n"
    "v_pk_mov_b32 v[152:153], 0, 0\n"
    "v_pk_mov_b32 v[154:155], 0, 0\n"
    "v_pk_mov_b32 v[156:157], 0, 0\n"
    "v_pk_mov_b32 v[158:159], 0, 0\n"
    "v_pk_mov_b32 v[160:161], 0, 0\n"
    "v_pk_mov_b32 v[162:163], 0, 0\n"
    "v_pk_mov_b32 v[164:165], 0, 0\n"
    "v_pk_mov_b32 v[166:167], 0, 0\n"
    "v_pk_mov_b32 v[168:169], 0, 0\n"
    "v_pk_mov_b32 v[170:171], 0, 0\n"
    "v_pk_mov_b32 v[172:173], 0, 0\n"
    "v_pk_mov_b32 v[174:175], 0, 0\n"
    "v_pk_mov_b32 v[176:177], 0, 0\n"
    "v_pk_mov_b32 v[178:179], 0, 0\n"
    "v_pk_mov_b32 v[180:181], 0, 0\n"
    "v_pk_mov_b32 v[182:183], 0, 0\n"
    "s_branch L_rtH3_%=\n"
    "L_end_%=:\n"
    "s_barrier\n"
      : "=s"(bad) : "s"(__builtin_amdgcn_kernarg_segment_ptr()), "s"(w), "s"(b), "s"(ldsb), "s"(tag)
      : "v0","v1","v2","v3","v4","v5","v6","v7","v8","v9","v10","v11","v12","v13","v14","v15","v16","v17","v18","v19","v20","v21","v22","v23",
      "v24","v25","v26","v27","v28","v29","v30","v31","v32","v33","v34","v35","v36","v37","v38","v39","v40","v41","v42","v43","v44","v45","v46","v47",
      "v48","v49","v50","v51","v52","v53","v54","v55","v56","v57","v58","v59","v60","v61","v62","v63","v64","v65","v66","v67","v68","v69","v70","v71",
      "v72","v73","v74","v75","v76","v77","v78","v79","v80","v81","v82","v83","v84","v85","v86","v87","v88","v89","v90","v91","v92","v93","v94","v95",
      "v96","v97","v98","v99","v100","v101","v102","v103","v104","v105","v106","v107","v108","v109","v110","v111","v112","v113","v114","v115","v116","v117","v118","v119",
      "v120","v121","v122","v123","v124","v125","v126","v127","v128","v129","v130","v131","v132","v133","v134","v135","v136","v137","v138","v139","v140","v141","v142","v143",
      "v144","v145","v146","v147","v148","v149","v150","v151","v152","v153","v154","v155","v156","v157","v158","v159","v160","v161","v162","v163","v164","v165","v166","v167",
      "v168","v169","v170","v171","v172","v173","v174","v175","v176","v177","v178","v179","v180","v181","v182","v183","v184","v185","v186","v187","v188","v189","v190","v191",
      "v192","v193","v194","v195","v196","v197","v198","v199","v200","v201","v202","v203","v204","v205","v206","v207","v208","v209","v210","v211","v212","v213","v214","v215",
      "v216","v217","v218","v219","v220","v221","v222","v223","v224","v225","v226","v227","v228","v229","v230","v231","v232","v233","v234","v235","v236","v237","v238","v239",
      "v240","v241","v242","v243","v244","v245","v246","v247","v248","v249","v250","v251","v252","v253","v254","v255","a0","a1","a2","a3","a4","a5","a6","a7",
      "a8","a9","a10","a11","a12","a13","a14","a15","a16","a17","a18","a19","a20","a21","a22","a23","a24","a25","a26","a27","a28","a29","a30","a31",
      "a32","a33","a34","a35","a36","a37","a38","a39","a40","a41","a42","a43","a44","a45","a46","a47","a48","a49","a50","a51","a52","a53","a54","a55",
      "a56","a57","a58","a59","a60","a61","a62","a63","a64","a65","a66","a67","a68","a69","a70","a71","a72","a73","a74","a75","a76","a77","a78","a79",
      "a80","a81","a82","a83","a84","a85","a86","a87","a88","a89","a90","a91","a92","a93","a94","a95","a96","a97","a98","a99","a100","a101","a102","a103",
      "a104","a105","a106","a107","a108","a109","a110","a111","a112","a113","a114","a115","a116","a117","a118","a119","a120","a121","a122","a123","a124","a125","a126","a127",
      "a128","a129","a130","a131","a132","a133","a134","a135","a136","a137","a138","a139","a140","a141","a142","a143","a144","a145","a146","a147","a148","a149","a150","a151",
      "a152","a153","a154","a155","a156","a157","a158","a159","a160","a161","a162","a163","a164","a165","a166","a167","a168","a169","a170","a171","a172","a173","a174","a175",
      "a176","a177","a178","a179","a180","a181","a182","a183","a184","a185","a186","a187","a188","a189","a190","a191","a192","a193","a194","a195","a196","a197","a198","a199",
      "a200","a201","a202","a203","a204","a205","a206","a207","a208","a209","a210","a211","a212","a213","a214","a215","a216","a217","a218","a219","a220","a221","a222","a223",
      "a224","a225","a226","a227","a228","a229","a230","a231","a232","a233","a234","a235","a236","a237","a238","a239","a240","a241","a242","a243","a244","a245","a246","a247",
      "a248","a249","a250","a251","a252","a253","a254","a255","s10","s11","s12","s13","s14","s15","s16","s17","s18","s19","s20","s21","s22","s23","s24","s25",
      "s26","s27","s28","s29","s30","s31","s33","s34","s35","s36","s37","s38","s39","s40","s41","s42","s43","s44","s45","s46","s47","s48","s49","s50",
      "s51","s52","s53","s54","s55","s56","s57","s58","s59","s60","s61","s62","s63","s64","s65","s66","s67","s68","s69","s70","s71","s72","s73","s74",
      "s75","s76","s77","s78","s79","s80","s81","s82","s83","s84","s85","s86","s87","s88","s89","s92","s93","s94","s96","s97","s98","s99","scc","vcc",
      "memory");
    STAMP(1);
#undef DIAGK
    if (__builtin_expect(bad != 0, 0)) {
      asm volatile("s_waitcnt vmcnt(0)" ::: "memory");
      __builtin_amdgcn_fence(__ATOMIC_ACQUIRE, "agent");
      asm volatile("" : "+s"(b), "+s"(w)); const int xcd = b & 7, j = b >> 3;
      TailArgsP A = tail_args();
      LDSP float* sc = (LDSP float*)(lds + (w >> 1) * 65536) + (w & 1) * 2304;
#pragma unroll 1
      for (int k = 0; k < 2; ++k) attn_rows_exact(A->wq, A->ws, sc, 4 * xcd + (j >> 3), (k ? 15 - (j & 7) : (j & 7)) * 128 + w * 32, lane_now());
      const char* pl = A->ws + WS_CTX + (size_t)(((15 - (j & 7)) * 8 + w * 2) * 128 + (4 * xcd + (j >> 3)) * 4) * 1024 + lane_now() * 16;
      const char* ps = A->ws + WS_CTX + (size_t)(((j & 7) * 8 + w * 2) * 128 + (4 * xcd + (j >> 3)) * 4) * 1024 + lane_now() * 16;
#define OWN4(A0, P) "global_load_dwordx4 a[" #A0 ":" #A0 "+3], " P ", off sc1\n\tglobal_load_dwordx4 a[" #A0 "+4:" #A0 "+7], " P ", off offset:1024 sc1\n\t" \
                    "global_load_dwordx4 a[" #A0 "+8:" #A0 "+11], " P ", off offset:2048 sc1\n\tglobal_load_dwordx4 a[" #A0 "+12:" #A0 "+15], " P ", off offset:3072 sc1\n\t"
      asm volatile("s_waitcnt vmcnt(0)\n\t" OWN4(0, "%0") OWN4(64, "%1") OWN4(128, "%2") OWN4(192, "%3") "s_waitcnt vmcnt(0)"
                   :: "v"(pl), "v"(pl + 131072), "v"(ps), "v"(ps + 131072) : "memory");
    }
  }
  { IDS; TailArgsP A = tail_args(); char* ws = A->ws;
#define DIAGK 1
    STAMP(0);
    asm volatile(
    "s_mov_b64 s[44:45], %1\n"
    "s_and_b32 s45, s45, 0xffff\n"
    "s_mov_b32 s46, 523520\n"
    "s_mov_b32 s47, 0x20000\n"
    "s_mov_b32 s20, %3\n"
    "v_mbcnt_lo_u32_b32 v16, -1, 0\n"
    "v_mbcnt_hi_u32_b32 v16, -1, v16\n"
    "s_add_u32 s52, %5, 0\n"
    "s_and_b32 s52, s52, 3\n"
    "s_lshl_b32 s56, s52, 17\n"
    "s_lshl_b32 s52, s52, 12\n"
    "s_add_u32 s53, %5, 1\n"
    "s_and_b32 s53, s53, 3\n"
    "s_lshl_b32 s57, s53, 17\n"
    "s_lshl_b32 s53, s53, 12\n"
    "s_add_u32 s54, %5, 2\n"
    "s_and_b32 s54, s54, 3\n"
    "s_lshl_b32 s58, s54, 17\n"
    "s_lshl_b32 s54, s54, 12\n"
    "s_add_u32 s55, %5, 3\n"
    "s_and_b32 s55, s55, 3\n"
    "s_lshl_b32 s59, s55, 17\n"
    "s_lshl_b32 s55, s55, 12\n"
    "s_mov_b64 s[40:41], %0\n"
    "s_and_b32 s41, s41, 0xffff\n"
    "s_mov_b32 s42, 16662528\n"
    "s_mov_b32 s43, 0x20000\n"
    "s_mov_b64 s[48:49], %2\n"
    "s_and_b32 s49, s49, 0xffff\n"
    "s_mov_b32 s50, 2084864\n"
    "s_mov_b32 s51, 0x20000\n"
    "s_mov_b64 s[66:67], %6\n"
    "v_lshlrev_b32 v17, 4, v16\n"
    "s_sub_u32 s23, 15, %4\n"
    "s_lshl_b32 s23, s23, 2\n"
    "s_add_u32 s23, s23, s20\n"
    "s_lshl_b32 s24, s23, 18\n"
    "v_add_u32 v0, s24, v17\n"
    "v_add_u32 v1, 0x20000, v0\n"
    "s_lshl_b32 s24, s23, 15\n"
    "v_add_u32 v20, s24, v17\n"
    "s_lshl_b32 s23, %4, 2\n"
    "s_add_u32 s23, s23, s20\n"
    "s_lshl_b32 s24, s23, 18\n"
    "v_add_u32 v2, s24, v17\n"
    "v_add_u32 v3, 0x20000, v2\n"
    "s_lshl_b32 s24, s23, 15\n"
    "v_add_u32 v21, s24, v17\n"
    "s_mov_b32 s26, 0\n"
    "s_mov_b32 s27, 16384\n"
    "v_min_u32 v22, 3, v16\n"
    "v_lshlrev_b32 v22, 2, v22\n"
    "v_add_u32 v22, %7, v22\n"
    "s_lshl_b32 s25, %5, 2\n"
    "s_add_u32 s25, s25, %7\n"
    "v_lshrrev_b32 v17, 2, v16\n"
    "v_lshlrev_b32 v17, 7, v17\n"
    "v_and_b32 v18, 3, v16\n"
    "v_lshl_add_u32 v17, v18, 3, v17\n"
    "v_bfe_u32 v18, v16, 3, 2\n"
    "v_lshlrev_b32 v18, 5, v18\n"
    "v_xor_b32 v19, 0, v18\n"
    "v_add_u32 v8, v17, v19\n"
    "v_add_u32 v12, 0x10000, v8\n"
    "v_xor_b32 v19, 32, v18\n"
    "v_add_u32 v9, v17, v19\n"
    "v_add_u32 v13, 0x10000, v9\n"
    "v_xor_b32 v19, 64, v18\n"
    "v_add_u32 v10, v17, v19\n"
    "v_add_u32 v14, 0x10000, v10\n"
    "v_xor_b32 v19, 96, v18\n"
    "v_add_u32 v11, v17, v19\n"
    "v_add_u32 v15, 0x10000, v11\n"
    "v_lshrrev_b32 v17, 3, v16\n"
    "s_lshl_b32 s23, s20, 5\n"
    "v_add_u32 v17, s23, v17\n"
    "s_mov_b32 s23, 1024\n"
    "v_mul_lo_u32 v17, v17, s23\n"
    "v_lshrrev_b32 v18, 4, v16\n"
    "v_lshlrev_b32 v18, 1, v18\n"
    "v_and_b32 v19, 7, v16\n"
    "v_xor_b32 v18, v18, v19\n"
    "v_lshl_add_u32 v4, v18, 4, v17\n"
    "v_add_u32 v5, 7168, v4\n"
    "v_add_u32 v6, 14336, v4\n"
    "v_add_u32 v7, 21504, v4\n"
    "s_lshl_b32 s21, s20, 12\n"
    "s_waitcnt vmcnt(8) lgkmcnt(0)\n"
    "s_barrier\n"
    "ds_read_b64_tr_b16 v[24:25], v8 offset:32768\n"
    "ds_read_b64_tr_b16 v[26:27], v8 offset:34816\n"
    "ds_read_b64_tr_b16 v[28:29], v9 offset:32768\n"
    "ds_read_b64_tr_b16 v[30:31], v9 offset:34816\n"
    "ds_read_b64_tr_b16 v[32:33], v10 offset:32768\n"
    "ds_read_b64_tr_b16 v[34:35], v10 offset:34816\n"
    "ds_read_b64_tr_b16 v[36:37], v11 offset:32768\n"
    "ds_read_b64_tr_b16 v[38:39], v11 offset:34816\n"
    "ds_read_b64_tr_b16 v[40:41], v8 offset:49152\n"
    "ds_read_b64_tr_b16 v[42:43], v8 offset:51200\n"
    "ds_read_b64_tr_b16 v[44:45], v9 offset:49152\n"
    "ds_read_b64_tr_b16 v[46:47], v9 offset:51200\n"
    "s_waitcnt lgkmcnt(8)\n"
    "v_mfma_f32_16x16x32_bf16 v[56:59], v[24:27], a[0:3], 0\n"
    "ds_read_b64_tr_b16 v[48:49], v10 offset:49152\n"
    "s_nop 0\n"
    "v_mfma_f32_16x16x32_bf16 v[64:67], v[24:27], a[64:67], 0\n"
    "ds_read_b64_tr_b16 v[50:51], v10 offset:51200\n"
    "s_nop 0\n"
    "v_mfma_f32_16x16x32_bf16 v[72:75], v[24:27], a[128:131], 0\n"
    "s_nop 0\n"
    "v_mfma_f32_16x16x32_bf16 v[80:83], v[24:27], a[192:195], 0\n"
    "s_nop 0\n"
    "v_mfma_f32_16x16x32_bf16 v[60:63], v[28:31], a[0:3], 0\n"
    "ds_read_b64_tr_b16 v[52:53], v11 offset:49152\n"
    "s_nop 0\n"
    "v_mfma_f32_16x16x32_bf16 v[68:71], v[28:31], a[64:67], 0\n"
    "ds_read_b64_tr_b16 v[54:55], v11 offset:51200\n"
    "s_nop 0\n"
    "v_mfma_f32_16x16x32_bf16 v[76:79], v[28:31], a[128:131], 0\n"
    "s_nop 0\n"
    "v_mfma_f32_16x16x32_bf16 v[84:87], v[28:31], a[192:195], 0\n"
    "s_nop 0\n"
    "s_waitcnt lgkmcnt(8)\n"
    "v_mfma_f32_16x16x32_bf16 v[88:91], v[32:35], a[0:3], 0\n"
    "ds_read_b64_tr_b16 v[24:25], v8 offset:36864\n"
    "s_nop 0\n"
    "v_mfma_f32_16x16x32_bf16 v[96:99], v[32:35], a[64:67], 0\n"
    "ds_read_b64_tr_b16 v[26:27], v8 offset:38912\n"
    "s_nop 0\n"
    "v_mfma_f32_16x16x32_bf16 v[104:107], v[32:35], a[128:131], 0\n"
    "s_nop 0\n"
    "v_mfma_f32_16x16x32_bf16 v[112:115], v[32:35], a[192:195], 0\n"
    "s_nop 0\n"
    "v_mfma_f32_16x16x32_bf16 v[92:95], v[36:39], a[0:3], 0\n"
    "ds_read_b64_tr_b16 v[28:29], v9 offset:36864\n"
    "s_nop 0\n"
    "v_mfma_f32_16x16x32_bf16 v[100:103], v[36:39], a[64:67], 0\n"
    "ds_read_b64_tr_b16 v[30:31], v9 offset:38912\n"
    "s_nop 0\n"
    "v_mfma_f32_16x16x32_bf16 v[108:111], v[36:39], a[128:131], 0\n"
    "s_nop 0\n"
    "v_mfma_f32_16x16x32_bf16 v[116:119], v[36:39], a[192:195], 0\n"
    "s_nop 0\n"
    "s_waitcnt lgkmcnt(8)\n"
    "v_mfma_f32_16x16x32_bf16 v[120:123], v[40:43], a[0:3], 0\n"
    "ds_read_b64_tr_b16 v[32:33], v10 offset:36864\n"
    "s_nop 0\n"
    "v_mfma_f32_16x16x32_bf16 v[128:131], v[40:43], a[64:67], 0\n"
    "ds_read_b64_tr_b16 v[34:35], v10 offset:38912\n"
    "s_nop 0\n"
    "v_mfma_f32_16x16x32_bf16 v[136:139], v[40:43], a[128:131], 0\n"
    "s_nop 0\n"
    "v_mfma_f32_16x16x32_bf16 v[144:147], v[40:43], a[192:195], 0\n"
    "s_nop 0\n"
    "v_mfma_f32_16x16x32_bf16 v[124:127], v[44:47], a[0:3], 0\n"
    "ds_read_b64_tr_b16 v[36:37], v11 offset:36864\n"
    "s_waitcnt vmcnt(0)\n"
    "s_barrier\n"
    "s_cmp_lg_u32 s20, 0\n"
    "s_cbranch_scc1 L_ar_%=\n"
    "v_mov_b32 v18, s25\n"
    "v_mov_b32 v19, %9\n"
    "s_mov_b64 exec, 1\n"
    "global_atomic_swap v18, v19, s[66:67]\n"
    "s_mov_b64 exec, -1\n"
    "L_ar_%=:\n"
    "v_mfma_f32_16x16x32_bf16 v[132:135], v[44:47], a[64:67], 0\n"
    "ds_read_b64_tr_b16 v[38:39], v11 offset:38912\n"
    "s_add_u32 s22, s59, 0\n"
    "v_mfma_f32_16x16x32_bf16 v[140:143], v[44:47], a[128:131], 0\n"
    "s_add_u32 m0, s21, 0\n"
    "v_mfma_f32_16x16x32_bf16 v[148:151], v[44:47], a[192:195], 0\n"
    "buffer_load_dwordx4 v4, s[44:47], s22 offen offset:0 lds\n"
    "s_waitcnt lgkmcnt(8)\n"
    "v_mfma_f32_16x16x32_bf16 v[152:155], v[48:51], a[0:3], 0\n"
    "ds_read_b64_tr_b16 v[40:41], v8 offset:53248\n"
    "v_mfma_f32_16x16x32_bf16 v[160:163], v[48:51], a[64:67], 0\n"
    "ds_read_b64_tr_b16 v[42:43], v8 offset:55296\n"
    "v_mfma_f32_16x16x32_bf16 v[168:171], v[48:51], a[128:131], 0\n"
    "buffer_load_dwordx4 v5, s[44:47], s22 offen offset:1024 lds\n"
    "v_mfma_f32_16x16x32_bf16 v[176:179], v[48:51], a[192:195], 0\n"
    "v_mfma_f32_16x16x32_bf16 v[156:159], v[52:55], a[0:3], 0\n"
    "ds_read_b64_tr_b16 v[44:45], v9 offset:53248\n"
    "v_mfma_f32_16x16x32_bf16 v[164:167], v[52:55], a[64:67], 0\n"
    "ds_read_b64_tr_b16 v[46:47], v9 offset:55296\n"
    "buffer_load_dwordx4 v6, s[44:47], s22 offen offset:2048 lds\n"
    "v_mfma_f32_16x16x32_bf16 v[172:175], v[52:55], a[128:131], 0\n"
    "v_mfma_f32_16x16x32_bf16 v[180:183], v[52:55], a[192:195], 0\n"
    "s_waitcnt lgkmcnt(8)\n"
    "v_mfma_f32_16x16x32_bf16 v[56:59], v[24:27], a[4:7], v[56:59]\n"
    "ds_read_b64_tr_b16 v[48:49], v10 offset:53248\n"
    "buffer_load_dwordx4 v7, s[44:47], s22 offen offset:3072 lds\n"
    "v_mfma_f32_16x16x32_bf16 v[64:67], v[24:27], a[68:71], v[64:67]\n"
    "ds_read_b64_tr_b16 v[50:51], v10 offset:55296\n"
    "s_add_u32 s22, s57, 128\n"
    "v_mfma_f32_16x16x32_bf16 v[72:75], v[24:27], a[132:135], v[72:75]\n"
    "s_add_u32 m0, s21, 16384\n"
    "v_mfma_f32_16x16x32_bf16 v[80:83], v[24:27], a[196:199], v[80:83]\n"
    "buffer_load_dwordx4 v4, s[44:47], s22 offen offset:0 lds\n"
    "v_mfma_f32_16x16x32_bf16 v[60:63], v[28:31], a[4:7], v[60:63]\n"
    "ds_read_b64_tr_b16 v[52:53], v11 offset:53248\n"
    "v_mfma_f32_16x16x32_bf16 v[68:71], v[28:31], a[68:71], v[68:71]\n"
    "ds_read_b64_tr_b16 v[54:55], v11 offset:55296\n"
    "v_mfma_f32_16x16x32_bf16 v[76:79], v[28:31], a[132:135], v[76:79]\n"
    "buffer_load_dwordx4 v5, s[44:47], s22 offen offset:1024 lds\n"
    "v_mfma_f32_16x16x32_bf16 v[84:87], v[28:31], a[196:199], v[84:87]\n"
    "s_waitcnt lgkmcnt(8)\n"
    "v_mfma_f32_16x16x32_bf16 v[88:91], v[32:35], a[4:7], v[88:91]\n"
    "ds_read_b64_tr_b16 v[24:25], v8 offset:40960\n"
    "v_mfma_f32_16x16x32_bf16 v[96:99], v[32:35], a[68:71], v[96:99]\n"
    "ds_read_b64_tr_b16 v[26:27], v8 offset:43008\n"
    "buffer_load_dwordx4 v6, s[44:47], s22 offen offset:2048 lds\n"
    "v_mfma_f32_16x16x32_bf16 v[104:107], v[32:35], a[132:135], v[104:107]\n"
    "v_mfma_f32_16x16x32_bf16 v[112:115], v[32:35], a[196:199], v[112:115]\n"
    "v_mfma_f32_16x16x32_bf16 v[92:95], v[36:39], a[4:7], v[92:95]\n"
    "ds_read_b64_tr_b16 v[28:29], v9 offset:40960\n"
    "buffer_load_dwordx4 v7, s[44:47], s22 offen offset:3072 lds\n"
    "v_mfma_f32_16x16x32_bf16 v[100:103], v[36:39], a[68:71], v[100:103]\n"
    "ds_read_b64_tr_b16 v[30:31], v9 offset:43008\n"
    "s_add_u32 s22, s58, 128\n"
    "v_mfma_f32_16x16x32_bf16 v[108:111], v[36:39], a[132:135], v[108:111]\n"
    "s_add_u32 m0, s21, 65536\n"
    "v_mfma_f32_16x16x32_bf16 v[116:119], v[36:39], a[196:199], v[116:119]\n"
    "buffer_load_dwordx4 v4, s[44:47], s22 offen offset:0 lds\n"
    "s_waitcnt lgkmcnt(8)\n"
    "v_mfma_f32_16x16x32_bf16 v[120:123], v[40:43], a[4:7], v[120:123]\n"
    "ds_read_b64_tr_b16 v[32:33], v10 offset:40960\n"
    "v_mfma_f32_16x16x32_bf16 v[128:131], v[40:43], a[68:71], v[128:131]\n"
    "ds_read_b64_tr_b16 v[34:35], v10 offset:43008\n"
    "v_mfma_f32_16x16x32_bf16 v[136:139], v[40:43], a[132:135], v[136:139]\n"
    "buffer_load_dwordx4 v5, s[44:47], s22 offen offset:1024 lds\n"
    "v_mfma_f32_16x16x32_bf16 v[144:147], v[40:43], a[196:199], v[144:147]\n"
    "v_mfma_f32_16x16x32_bf16 v[124:127], v[44:47], a[4:7], v[124:127]\n"
    "ds_read_b64_tr_b16 v[36:37], v11 offset:40960\n"
    "v_mfma_f32_16x16x32_bf16 v[132:135], v[44:47], a[68:71], v[132:135]\n"
    "ds_read_b64_tr_b16 v[38:39], v11 offset:43008\n"
    "buffer_load_dwordx4 v6, s[44:47], s22 offen offset:2048 lds\n"
    "v_mfma_f32_16x16x32_bf16 v[140:143], v[44:47], a[132:135], v[140:143]\n"
    "v_mfma_f32_16x16x32_bf16 v[148:151], v[44:47], a[196:199], v[148:151]\n"
    "s_waitcnt lgkmcnt(8)\n"
    "v_mfma_f32_16x16x32_bf16 v[152:155], v[48:51], a[4:7], v[152:155]\n"
    "ds_read_b64_tr_b16 v[40:41], v8 offset:57344\n"
    "buffer_load_dwordx4 v7, s[44:47], s22 offen offset:3072 lds\n"
    "v_mfma_f32_16x16x32_bf16 v[160:163], v[48:51], a[68:71], v[160:163]\n"
    "ds_read_b64_tr_b16 v[42:43], v8 offset:59392\n"
    "s_add_u32 s22, s59, 128\n"
    "v_mfma_f32_16x16x32_bf16 v[168:171], v[48:51], a[132:135], v[168:171]\n"
    "s_add_u32 m0, s21, 81920\n"
    "v_mfma_f32_16x16x32_bf16 v[176:179], v[48:51], a[196:199], v[176:179]\n"
    "buffer_load_dwordx4 v4, s[44:47], s22 offen offset:0 lds\n"
    "v_mfma_f32_16x16x32_bf16 v[156:159], v[52:55], a[4:7], v[156:159]\n"
    "ds_read_b64_tr_b16 v[44:45], v9 offset:57344\n"
    "v_mfma_f32_16x16x32_bf16 v[164:167], v[52:55], a[68:71], v[164:167]\n"
    "ds_read_b64_tr_b16 v[46:47], v9 offset:59392\n"
    "v_mfma_f32_16x16x32_bf16 v[172:175], v[52:55], a[132:135], v[172:175]\n"
    "buffer_load_dwordx4 v5, s[44:47], s22 offen offset:1024 lds\n"
    "v_mfma_f32_16x16x32_bf16 v[180:183], v[52:55], a[196:199], v[180:183]\n"
    "s_waitcnt lgkmcnt(8)\n"
    "v_mfma_f32_16x16x32_bf16 v[56:59], v[24:27], a[8:11], v[56:59]\n"
    "ds_read_b64_tr_b16 v[48:49], v10 offset:57344\n"
    "v_mfma_f32_16x16x32_bf16 v[64:67], v[24:27], a[72:75], v[64:67]\n"
    "ds_read_b64_tr_b16 v[50:51], v10 offset:59392\n"
    "buffer_load_dwordx4 v6, s[44:47], s22 offen offset:2048 lds\n"
    "v_mfma_f32_16x16x32_bf16 v[72:75], v[24:27], a[136:139], v[72:75]\n"
    "v_mfma_f32_16x16x32_bf16 v[80:83], v[24:27], a[200:203], v[80:83]\n"
    "v_mfma_f32_16x16x32_bf16 v[60:63], v[28:31], a[8:11], v[60:63]\n"
    "ds_read_b64_tr_b16 v[52:53], v11 offset:57344\n"
    "buffer_load_dwordx4 v7, s[44:47], s22 offen offset:3072 lds\n"
    "v_mfma_f32_16x16x32_bf16 v[68:71], v[28:31], a[72:75], v[68:71]\n"
    "ds_read_b64_tr_b16 v[54:55], v11 offset:59392\n"
    "v_mfma_f32_16x16x32_bf16 v[76:79], v[28:31], a[136:139], v[76:79]\n"
    "v_mfma_f32_16x16x32_bf16 v[84:87], v[28:31], a[200:203], v[84:87]\n"
    "global_load_dword v23, v22, s[66:67] sc1\n"
    "s_waitcnt lgkmcnt(8)\n"
    "v_mfma_f32_16x16x32_bf16 v[88:91], v[32:35], a[8:11], v[88:91]\n"
    "ds_read_b64_tr_b16 v[24:25], v8 offset:45056\n"
    "s_nop 0\n"
    "v_mfma_f32_16x16x32_bf16 v[96:99], v[32:35], a[72:75], v[96:99]\n"
    "ds_read_b64_tr_b16 v[26:27], v8 offset:47104\n"
    "s_nop 0\n"
    "v_mfma_f32_16x16x32_bf16 v[104:107], v[32:35], a[136:139], v[104:107]\n"
    "s_nop 0\n"
    "v_mfma_f32_16x16x32_bf16 v[112:115], v[32:35], a[200:203], v[112:115]\n"
    "s_nop 0\n"
    "v_mfma_f32_16x16x32_bf16 v[92:95], v[36:39], a[8:11], v[92:95]\n"
    "ds_read_b64_tr_b16 v[28:29], v9 offset:45056\n"
    "s_nop 0\n"
    "v_mfma_f32_16x16x32_bf16 v[100:103], v[36:39], a[72:75], v[100:103]\n"
    "ds_read_b64_tr_b16 v[30:31], v9 offset:47104\n"
    "s_nop 0\n"
    "v_mfma_f32_16x16x32_bf16 v[108:111], v[36:39], a[136:139], v[108:111]\n"
    "s_nop 0\n"
    "v_mfma_f32_16x16x32_bf16 v[116:119], v[36:39], a[200:203], v[116:119]\n"
    "s_nop 0\n"
    "s_waitcnt lgkmcnt(8)\n"
    "v_mfma_f32_16x16x32_bf16 v[120:123], v[40:43], a[8:11], v[120:123]\n"
    "ds_read_b64_tr_b16 v[32:33], v10 offset:45056\n"
    "s_nop 0\n"
    "v_mfma_f32_16x16x32_bf16 v[128:131], v[40:43], a[72:75], v[128:131]\n"
    "ds_read_b64_tr_b16 v[34:35], v10 offset:47104\n"
    "s_nop 0\n"
    "v_mfma_f32_16x16x32_bf16 v[136:139], v[40:43], a[136:139], v[136:139]\n"
    "s_nop 0\n"
    "v_mfma_f32_16x16x32_bf16 v[144:147], v[40:43], a[200:203], v[144:147]\n"
    "s_nop 0\n"
    "v_mfma_f32_16x16x32_bf16 v[124:127], v[44:47], a[8:11], v[124:127]\n"
    "ds_read_b64_tr_b16 v[36:37], v11 offset:45056\n"
    "s_nop 0\n"
    "v_mfma_f32_16x16x32_bf16 v[132:135], v[44:47], a[72:75], v[132:135]\n"
    "ds_read_b64_tr_b16 v[38:39], v11 offset:47104\n"
    "s_nop 0\n"
    "v_mfma_f32_16x16x32_bf16 v[140:143], v[44:47], a[136:139], v[140:143]\n"
    "s_nop 0\n"
    "v_mfma_f32_16x16x32_bf16 v[148:151], v[44:47], a[200:203], v[148:151]\n"
    "s_nop 0\n"
    "s_waitcnt lgkmcnt(8)\n"
    "v_mfma_f32_16x16x32_bf16 v[152:155], v[48:51], a[8:11], v[152:155]\n"
    "ds_read_b64_tr_b16 v[40:41], v8 offset:61440\n"
    "s_waitcnt vmcnt(0)\n"
    "v_cmp_ne_u32 vcc, %9, v23\n"
    "s_mov_b32 s23, 0x400000\n"
    "s_nop 1\n"
    "s_cbranch_vccz L_pc_%=_x\n"
    "L_pc_%=:\n"
    "s_sleep 2\n"
    "global_load_dword v23, v22, s[66:67] sc1\n"
    "s_waitcnt vmcnt(0)\n"
    "v_cmp_ne_u32 vcc, %9, v23\n"
    "s_sub_u32 s23, s23, 1\n"
    "s_cbranch_scc1 L_pc_%=_x\n"
    "s_nop 0\n"
    "s_cbranch_vccnz L_pc_%=\n"
    "L_pc_%=_x:\n"
    "v_mfma_f32_16x16x32_bf16 v[160:163], v[48:51], a[72:75], v[160:163]\n"
    "ds_read_b64_tr_b16 v[42:43], v8 offset:63488\n"
    "buffer_load_dwordx4 a[16:19], v0, s[40:43], s53 offen offset:0 sc1\n"
    "v_mfma_f32_16x16x32_bf16 v[168:171], v[48:51], a[136:139], v[168:171]\n"
    "v_mfma_f32_16x16x32_bf16 v[176:179], v[48:51], a[200:203], v[176:179]\n"
    "v_mfma_f32_16x16x32_bf16 v[156:159], v[52:55], a[8:11], v[156:159]\n"
    "ds_read_b64_tr_b16 v[44:45], v9 offset:61440\n"
    "buffer_load_dwordx4 a[80:83], v1, s[40:43], s53 offen offset:0 sc1\n"
    "v_mfma_f32_16x16x32_bf16 v[164:167], v[52:55], a[72:75], v[164:167]\n"
    "ds_read_b64_tr_b16 v[46:47], v9 offset:63488\n"
    "v_mfma_f32_16x16x32_bf16 v[172:175], v[52:55], a[136:139], v[172:175]\n"
    "v_mfma_f32_16x16x32_bf16 v[180:183], v[52:55], a[200:203], v[180:183]\n"
    "buffer_load_dwordx4 a[144:147], v2, s[40:43], s53 offen offset:0 sc1\n"
    "s_waitcnt lgkmcnt(8)\n"
    "v_mfma_f32_16x16x32_bf16 v[56:59], v[24:27], a[12:15], v[56:59]\n"
    "ds_read_b64_tr_b16 v[48:49], v10 offset:61440\n"
    "v_mfma_f32_16x16x32_bf16 v[64:67], v[24:27], a[76:79], v[64:67]\n"
    "ds_read_b64_tr_b16 v[50:51], v10 offset:63488\n"
    "v_mfma_f32_16x16x32_bf16 v[72:75], v[24:27], a[140:143], v[72:75]\n"
    "buffer_load_dwordx4 a[208:211], v3, s[40:43], s53 offen offset:0 sc1\n"
    "v_mfma_f32_16x16x32_bf16 v[80:83], v[24:27], a[204:207], v[80:83]\n"
    "v_mfma_f32_16x16x32_bf16 v[60:63], v[28:31], a[12:15], v[60:63]\n"
    "ds_read_b64_tr_b16 v[52:53], v11 offset:61440\n"
    "v_mfma_f32_16x16x32_bf16 v[68:71], v[28:31], a[76:79], v[68:71]\n"
    "ds_read_b64_tr_b16 v[54:55], v11 offset:63488\n"
    "buffer_load_dwordx4 a[20:23], v0, s[40:43], s53 offen offset:1024 sc1\n"
    "v_mfma_f32_16x16x32_bf16 v[76:79], v[28:31], a[140:143], v[76:79]\n"
    "v_mfma_f32_16x16x32_bf16 v[84:87], v[28:31], a[204:207], v[84:87]\n"
    "s_waitcnt lgkmcnt(8)\n"
    "v_mfma_f32_16x16x32_bf16 v[88:91], v[32:35], a[12:15], v[88:91]\n"
    "buffer_load_dwordx4 a[84:87], v1, s[40:43], s53 offen offset:1024 sc1\n"
    "v_mfma_f32_16x16x32_bf16 v[96:99], v[32:35], a[76:79], v[96:99]\n"
    "v_mfma_f32_16x16x32_bf16 v[104:107], v[32:35], a[140:143], v[104:107]\n"
    "v_mfma_f32_16x16x32_bf16 v[112:115], v[32:35], a[204:207], v[112:115]\n"
    "buffer_load_dwordx4 a[148:151], v2, s[40:43], s53 offen offset:1024 sc1\n"
    "v_mfma_f32_16x16x32_bf16 v[92:95], v[36:39], a[12:15], v[92:95]\n"
    "v_mfma_f32_16x16x32_bf16 v[100:103], v[36:39], a[76:79], v[100:103]\n"
    "v_mfma_f32_16x16x32_bf16 v[108:111], v[36:39], a[140:143], v[108:111]\n"
    "buffer_load_dwordx4 a[212:215], v3, s[40:43], s53 offen offset:1024 sc1\n"
    "v_mfma_f32_16x16x32_bf16 v[116:119], v[36:39], a[204:207], v[116:119]\n"
    "s_waitcnt lgkmcnt(4)\n"
    "v_mfma_f32_16x16x32_bf16 v[120:123], v[40:43], a[12:15], v[120:123]\n"
    "ds_read_b64_tr_b16 v[24:25], v12 offset:32768\n"
    "v_mfma_f32_16x16x32_bf16 v[128:131], v[40:43], a[76:79], v[128:131]\n"
    "ds_read_b64_tr_b16 v[26:27], v12 offset:34816\n"
    "buffer_load_dwordx4 a[24:27], v0, s[40:43], s53 offen offset:2048 sc1\n"
    "v_mfma_f32_16x16x32_bf16 v[136:139], v[40:43], a[140:143], v[136:139]\n"
    "ds_read_b64_tr_b16 v[28:29], v13 offset:32768\n"
    "v_mfma_f32_16x16x32_bf16 v[144:147], v[40:43], a[204:207], v[144:147]\n"
    "ds_read_b64_tr_b16 v[30:31], v13 offset:34816\n"
    "v_mfma_f32_16x16x32_bf16 v[124:127], v[44:47], a[12:15], v[124:127]\n"
    "ds_read_b64_tr_b16 v[32:33], v14 offset:32768\n"
    "buffer_load_dwordx4 a[88:91], v1, s[40:43], s53 offen offset:2048 sc1\n"
    "v_mfma_f32_16x16x32_bf16 v[132:135], v[44:47], a[76:79], v[132:135]\n"
    "ds_read_b64_tr_b16 v[34:35], v14 offset:34816\n"
    "v_mfma_f32_16x16x32_bf16 v[140:143], v[44:47], a[140:143], v[140:143]\n"
    "ds_read_b64_tr_b16 v[36:37], v15 offset:32768\n"
    "v_mfma_f32_16x16x32_bf16 v[148:151], v[44:47], a[204:207], v[148:151]\n"
    "ds_read_b64_tr_b16 v[38:39], v15 offset:34816\n"
    "buffer_load_dwordx4 a[152:155], v2, s[40:43], s53 offen offset:2048 sc1\n"
    "s_waitcnt lgkmcnt(8)\n"
    "v_mfma_f32_16x16x32_bf16 v[152:155], v[48:51], a[12:15], v[152:155]\n"
    "ds_read_b64_tr_b16 v[40:41], v12 offset:36864\n"
    "v_mfma_f32_16x16x32_bf16 v[160:163], v[48:51], a[76:79], v[160:163]\n"
    "ds_read_b64_tr_b16 v[42:43], v12 offset:38912\n"
    "v_mfma_f32_16x16x32_bf16 v[168:171], v[48:51], a[140:143], v[168:171]\n"
    "buffer_load_dwordx4 a[216:219], v3, s[40:43], s53 offen offset:2048 sc1\n"
    "v_mfma_f32_16x16x32_bf16 v[176:179], v[48:51], a[204:207], v[176:179]\n"
    "v_mfma_f32_16x16x32_bf16 v[156:159], v[52:55], a[12:15], v[156:159]\n"
    "ds_read_b64_tr_b16 v[44:45], v13 offset:36864\n"
    "v_mfma_f32_16x16x32_bf16 v[164:167], v[52:55], a[76:79], v[164:167]\n"
    "ds_read_b64_tr_b16 v[46:47], v13 offset:38912\n"
    "buffer_load_dwordx4 a[28:31], v0, s[40:43], s53 offen offset:3072 sc1\n"
    "v_mfma_f32_16x16x32_bf16 v[172:175], v[52:55], a[140:143], v[172:175]\n"
    "v_mfma_f32_16x16x32_bf16 v[180:183], v[52:55], a[204:207], v[180:183]\n"
    "s_waitcnt vmcnt(9)\n"
    "s_waitcnt lgkmcnt(8)\n"
    "v_mfma_f32_16x16x32_bf16 v[56:59], v[24:27], a[16:19], v[56:59]\n"
    "ds_read_b64_tr_b16 v[48:49], v14 offset:36864\n"
    "buffer_load_dwordx4 a[92:95], v1, s[40:43], s53 offen offset:3072 sc1\n"
    "v_mfma_f32_16x16x32_bf16 v[64:67], v[24:27], a[80:83], v[64:67]\n"
    "ds_read_b64_tr_b16 v[50:51], v14 offset:38912\n"
    "v_mfma_f32_16x16x32_bf16 v[72:75], v[24:27], a[144:147], v[72:75]\n"
    "buffer_load_dwordx4 a[156:159], v2, s[40:43], s53 offen offset:3072 sc1\n"
    "v_mfma_f32_16x16x32_bf16 v[80:83], v[24:27], a[208:211], v[80:83]\n"
    "v_mfma_f32_16x16x32_bf16 v[60:63], v[28:31], a[16:19], v[60:63]\n"
    "ds_read_b64_tr_b16 v[52:53], v15 offset:36864\n"
    "v_mfma_f32_16x16x32_bf16 v[68:71], v[28:31], a[80:83], v[68:71]\n"
    "ds_read_b64_tr_b16 v[54:55], v15 offset:38912\n"
    "buffer_load_dwordx4 a[220:223], v3, s[40:43], s53 offen offset:3072 sc1\n"
    "v_mfma_f32_16x16x32_bf16 v[76:79], v[28:31], a[144:147], v[76:79]\n"
    "v_mfma_f32_16x16x32_bf16 v[84:87], v[28:31], a[208:211], v[84:87]\n"
    "buffer_load_dwordx4 a[32:35], v0, s[40:43], s54 offen offset:0 sc1\n"
    "s_waitcnt lgkmcnt(8)\n"
    "v_mfma_f32_16x16x32_bf16 v[88:91], v[32:35], a[16:19], v[88:91]\n"
    "ds_read_b64_tr_b16 v[24:25], v12 offset:40960\n"
    "v_mfma_f32_16x16x32_bf16 v[96:99], v[32:35], a[80:83], v[96:99]\n"
    "ds_read_b64_tr_b16 v[26:27], v12 offset:43008\n"
    "v_mfma_f32_16x16x32_bf16 v[104:107], v[32:35], a[144:147], v[104:107]\n"
    "buffer_load_dwordx4 a[96:99], v1, s[40:43], s54 offen offset:0 sc1\n"
    "v_mfma_f32_16x16x32_bf16 v[112:115], v[32:35], a[208:211], v[112:115]\n"
    "v_mfma_f32_16x16x32_bf16 v[92:95], v[36:39], a[16:19], v[92:95]\n"
    "ds_read_b64_tr_b16 v[28:29], v13 offset:40960\n"
    "buffer_load_dwordx4 a[160:163], v2, s[40:43], s54 offen offset:0 sc1\n"
    "v_mfma_f32_16x16x32_bf16 v[100:103], v[36:39], a[80:83], v[100:103]\n"
    "ds_read_b64_tr_b16 v[30:31], v13 offset:43008\n"
    "v_mfma_f32_16x16x32_bf16 v[108:111], v[36:39], a[144:147], v[108:111]\n"
    "v_mfma_f32_16x16x32_bf16 v[116:119], v[36:39], a[208:211], v[116:119]\n"
    "buffer_load_dwordx4 a[224:227], v3, s[40:43], s54 offen offset:0 sc1\n"
    "s_waitcnt vmcnt(12)\n"
    "s_waitcnt lgkmcnt(8)\n"
    "v_mfma_f32_16x16x32_bf16 v[56:59], v[40:43], a[20:23], v[56:59]\n"
    "ds_read_b64_tr_b16 v[32:33], v14 offset:40960\n"
    "v_mfma_f32_16x16x32_bf16 v[64:67], v[40:43], a[84:87], v[64:67]\n"
    "ds_read_b64_tr_b16 v[34:35], v14 offset:43008\n"
    "buffer_load_dwordx4 a[36:39], v0, s[40:43], s54 offen offset:1024 sc1\n"
    "v_mfma_f32_16x16x32_bf16 v[72:75], v[40:43], a[148:151], v[72:75]\n"
    "v_mfma_f32_16x16x32_bf16 v[80:83], v[40:43], a[212:215], v[80:83]\n"
    "v_mfma_f32_16x16x32_bf16 v[60:63], v[44:47], a[20:23], v[60:63]\n"
    "ds_read_b64_tr_b16 v[36:37], v15 offset:40960\n"
    "buffer_load_dwordx4 a[100:103], v1, s[40:43], s54 offen offset:1024 sc1\n"
    "v_mfma_f32_16x16x32_bf16 v[68:71], v[44:47], a[84:87], v[68:71]\n"
    "ds_read_b64_tr_b16 v[38:39], v15 offset:43008\n"
    "v_mfma_f32_16x16x32_bf16 v[76:79], v[44:47], a[148:151], v[76:79]\n"
    "buffer_load_dwordx4 a[164:167], v2, s[40:43], s54 offen offset:1024 sc1\n"
    "v_mfma_f32_16x16x32_bf16 v[84:87], v[44:47], a[212:215], v[84:87]\n"
    "s_waitcnt lgkmcnt(8)\n"
    "v_mfma_f32_16x16x32_bf16 v[88:91], v[48:51], a[20:23], v[88:91]\n"
    "ds_read_b64_tr_b16 v[40:41], v12 offset:45056\n"
    "v_mfma_f32_16x16x32_bf16 v[96:99], v[48:51], a[84:87], v[96:99]\n"
    "ds_read_b64_tr_b16 v[42:43], v12 offset:47104\n"
    "buffer_load_dwordx4 a[228:231], v3, s[40:43], s54 offen offset:1024 sc1\n"
    "v_mfma_f32_16x16x32_bf16 v[104:107], v[48:51], a[148:151], v[104:107]\n"
    "v_mfma_f32_16x16x32_bf16 v[112:115], v[48:51], a[212:215], v[112:115]\n"
    "buffer_load_dwordx4 a[40:43], v0, s[40:43], s54 offen offset:2048 sc1\n"
    "v_mfma_f32_16x16x32_bf16 v[92:95], v[52:55], a[20:23], v[92:95]\n"
    "ds_read_b64_tr_b16 v[44:45], v13 offset:45056\n"
    "v_mfma_f32_16x16x32_bf16 v[100:103], v[52:55], a[84:87], v[100:103]\n"
    "ds_read_b64_tr_b16 v[46:47], v13 offset:47104\n"
    "v_mfma_f32_16x16x32_bf16 v[108:111], v[52:55], a[148:151], v[108:111]\n"
    "buffer_load_dwordx4 a[104:107], v1, s[40:43], s54 offen offset:2048 sc1\n"
    "v_mfma_f32_16x16x32_bf16 v[116:119], v[52:55], a[212:215], v[116:119]\n"
    "s_waitcnt vmcnt(14)\n"
    "s_waitcnt lgkmcnt(8)\n"
    "v_mfma_f32_16x16x32_bf16 v[56:59], v[24:27], a[24:27], v[56:59]\n"
    "ds_read_b64_tr_b16 v[48:49], v14 offset:45056\n"
    "buffer_load_dwordx4 a[168:171], v2, s[40:43], s54 offen offset:2048 sc1\n"
    "v_mfma_f32_16x16x32_bf16 v[64:67], v[24:27], a[88:91], v[64:67]\n"
    "ds_read_b64_tr_b16 v[50:51], v14 offset:47104\n"
    "v_mfma_f32_16x16x32_bf16 v[72:75], v[24:27], a[152:155], v[72:75]\n"
    "v_mfma_f32_16x16x32_bf16 v[80:83], v[24:27], a[216:219], v[80:83]\n"
    "buffer_load_dwordx4 a[232:235], v3, s[40:43], s54 offen offset:2048 sc1\n"
    "v_mfma_f32_16x16x32_bf16 v[60:63], v[28:31], a[24:27], v[60:63]\n"
    "ds_read_b64_tr_b16 v[52:53], v15 offset:45056\n"
    "v_mfma_f32_16x16x32_bf16 v[68:71], v[28:31], a[88:91], v[68:71]\n"
    "ds_read_b64_tr_b16 v[54:55], v15 offset:47104\n"
    "buffer_load_dwordx4 a[44:47], v0, s[40:43], s54 offen offset:3072 sc1\n"
    "v_mfma_f32_16x16x32_bf16 v[76:79], v[28:31], a[152:155], v[76:79]\n"
    "v_mfma_f32_16x16x32_bf16 v[84:87], v[28:31], a[216:219], v[84:87]\n"
    "s_waitcnt lgkmcnt(8)\n"
    "v_mfma_f32_16x16x32_bf16 v[88:91], v[32:35], a[24:27], v[88:91]\n"
    "buffer_load_dwordx4 a[108:111], v1, s[40:43], s54 offen offset:3072 sc1\n"
    "v_mfma_f32_16x16x32_bf16 v[96:99], v[32:35], a[88:91], v[96:99]\n"
    "v_mfma_f32_16x16x32_bf16 v[104:107], v[32:35], a[152:155], v[104:107]\n"
    "buffer_load_dwordx4 a[172:175], v2, s[40:43], s54 offen offset:3072 sc1\n"
    "v_mfma_f32_16x16x32_bf16 v[112:115], v[32:35], a[216:219], v[112:115]\n"
    "v_mfma_f32_16x16x32_bf16 v[92:95], v[36:39], a[24:27], v[92:95]\n"
    "v_mfma_f32_16x16x32_bf16 v[100:103], v[36:39], a[88:91], v[100:103]\n"
    "buffer_load_dwordx4 a[236:239], v3, s[40:43], s54 offen offset:3072 sc1\n"
    "v_mfma_f32_16x16x32_bf16 v[108:111], v[36:39], a[152:155], v[108:111]\n"
    "v_mfma_f32_16x16x32_bf16 v[116:119], v[36:39], a[216:219], v[116:119]\n"
    "buffer_load_dwordx4 a[48:51], v0, s[40:43], s55 offen offset:0 sc1\n"
    "s_waitcnt vmcnt(17)\n"
    "s_waitcnt lgkmcnt(4)\n"
    "v_mfma_f32_16x16x32_bf16 v[56:59], v[40:43], a[28:31], v[56:59]\n"
    "ds_read_b64_tr_b16 v[24:25], v12 offset:49152\n"
    "v_mfma_f32_16x16x32_bf16 v[64:67], v[40:43], a[92:95], v[64:67]\n"
    "ds_read_b64_tr_b16 v[26:27], v12 offset:51200\n"
    "v_mfma_f32_16x16x32_bf16 v[72:75], v[40:43], a[156:159], v[72:75]\n"
    "ds_read_b64_tr_b16 v[28:29], v13 offset:49152\n"
    "buffer_load_dwordx4 a[112:115], v1, s[40:43], s55 offen offset:0 sc1\n"
    "v_mfma_f32_16x16x32_bf16 v[80:83], v[40:43], a[220:223], v[80:83]\n"
    "ds_read_b64_tr_b16 v[30:31], v13 offset:51200\n"
    "v_mfma_f32_16x16x32_bf16 v[60:63], v[44:47], a[28:31], v[60:63]\n"
    "ds_read_b64_tr_b16 v[32:33], v14 offset:49152\n"
    "buffer_load_dwordx4 a[176:179], v2, s[40:43], s55 offen offset:0 sc1\n"
    "v_mfma_f32_16x16x32_bf16 v[68:71], v[44:47], a[92:95], v[68:71]\n"
    "ds_read_b64_tr_b16 v[34:35], v14 offset:51200\n"
    "v_mfma_f32_16x16x32_bf16 v[76:79], v[44:47], a[156:159], v[76:79]\n"
    "ds_read_b64_tr_b16 v[36:37], v15 offset:49152\n"
    "v_mfma_f32_16x16x32_bf16 v[84:87], v[44:47], a[220:223], v[84:87]\n"
    "ds_read_b64_tr_b16 v[38:39], v15 offset:51200\n"
    "buffer_load_dwordx4 a[240:243], v3, s[40:43], s55 offen offset:0 sc1\n"
    "s_waitcnt lgkmcnt(8)\n"
    "v_mfma_f32_16x16x32_bf16 v[88:91], v[48:51], a[28:31], v[88:91]\n"
    "ds_read_b64_tr_b16 v[40:41], v12 offset:53248\n"
    "v_mfma_f32_16x16x32_bf16 v[96:99], v[48:51], a[92:95], v[96:99]\n"
    "ds_read_b64_tr_b16 v[42:43], v12 offset:55296\n"
    "buffer_load_dwordx4 a[52:55], v0, s[40:43], s55 offen offset:1024 sc1\n"
    "v_mfma_f32_16x16x32_bf16 v[104:107], v[48:51], a[156:159], v[104:107]\n"
    "v_mfma_f32_16x16x32_bf16 v[112:115], v[48:51], a[220:223], v[112:115]\n"
    "v_mfma_f32_16x16x32_bf16 v[92:95], v[52:55], a[28:31], v[92:95]\n"
    "ds_read_b64_tr_b16 v[44:45], v13 offset:53248\n"
    "buffer_load_dwordx4 a[116:119], v1, s[40:43], s55 offen offset:1024 sc1\n"
    "v_mfma_f32_16x16x32_bf16 v[100:103], v[52:55], a[92:95], v[100:103]\n"
    "ds_read_b64_tr_b16 v[46:47], v13 offset:55296\n"
    "v_mfma_f32_16x16x32_bf16 v[108:111], v[52:55], a[156:159], v[108:111]\n"
    "buffer_load_dwordx4 a[180:183], v2, s[40:43], s55 offen offset:1024 sc1\n"
    "v_mfma_f32_16x16x32_bf16 v[116:119], v[52:55], a[220:223], v[116:119]\n"
    "s_waitcnt vmcnt(19)\n"
    "s_waitcnt lgkmcnt(8)\n"
    "v_mfma_f32_16x16x32_bf16 v[56:59], v[24:27], a[32:35], v[56:59]\n"
    "ds_read_b64_tr_b16 v[48:49], v14 offset:53248\n"
    "v_mfma_f32_16x16x32_bf16 v[64:67], v[24:27], a[96:99], v[64:67]\n"
    "ds_read_b64_tr_b16 v[50:51], v14 offset:55296\n"
    "buffer_load_dwordx4 a[244:247], v3, s[40:43], s55 offen offset:1024 sc1\n"
    "v_mfma_f32_16x16x32_bf16 v[72:75], v[24:27], a[160:163], v[72:75]\n"
    "v_mfma_f32_16x16x32_bf16 v[80:83], v[24:27], a[224:227], v[80:83]\n"
    "buffer_load_dwordx4 a[56:59], v0, s[40:43], s55 offen offset:2048 sc1\n"
    "v_mfma_f32_16x16x32_bf16 v[60:63], v[28:31], a[32:35], v[60:63]\n"
    "ds_read_b64_tr_b16 v[52:53], v15 offset:53248\n"
    "v_mfma_f32_16x16x32_bf16 v[68:71], v[28:31], a[96:99], v[68:71]\n"
    "ds_read_b64_tr_b16 v[54:55], v15 offset:55296\n"
    "v_mfma_f32_16x16x32_bf16 v[76:79], v[28:31], a[160:163], v[76:79]\n"
    "buffer_load_dwordx4 a[120:123], v1, s[40:43], s55 offen offset:2048 sc1\n"
    "v_mfma_f32_16x16x32_bf16 v[84:87], v[28:31], a[224:227], v[84:87]\n"
    "s_waitcnt lgkmcnt(8)\n"
    "v_mfma_f32_16x16x32_bf16 v[88:91], v[32:35], a[32:35], v[88:91]\n"
    "ds_read_b64_tr_b16 v[24:25], v12 offset:57344\n"
    "buffer_load_dwordx4 a[184:187], v2, s[40:43], s55 offen offset:2048 sc1\n"
    "v_mfma_f32_16x16x32_bf16 v[96:99], v[32:35], a[96:99], v[96:99]\n"
    "ds_read_b64_tr_b16 v[26:27], v12 offset:59392\n"
    "v_mfma_f32_16x16x32_bf16 v[104:107], v[32:35], a[160:163], v[104:107]\n"
    "v_mfma_f32_16x16x32_bf16 v[112:115], v[32:35], a[224:227], v[112:115]\n"
    "buffer_load_dwordx4 a[248:251], v3, s[40:43], s55 offen offset:2048 sc1\n"
    "v_mfma_f32_16x16x32_bf16 v[92:95], v[36:39], a[32:35], v[92:95]\n"
    "ds_read_b64_tr_b16 v[28:29], v13 offset:57344\n"
    "v_mfma_f32_16x16x32_bf16 v[100:103], v[36:39], a[96:99], v[100:103]\n"
    "ds_read_b64_tr_b16 v[30:31], v13 offset:59392\n"
    "buffer_load_dwordx4 a[60:63], v0, s[40:43], s55 offen offset:3072 sc1\n"
    "v_mfma_f32_16x16x32_bf16 v[108:111], v[36:39], a[160:163], v[108:111]\n"
    "v_mfma_f32_16x16x32_bf16 v[116:119], v[36:39], a[224:227], v[116:119]\n"
    "s_waitcnt vmcnt(21)\n"
    "s_waitcnt lgkmcnt(8)\n"
    "v_mfma_f32_16x16x32_bf16 v[56:59], v[40:43], a[36:39], v[56:59]\n"
    "ds_read_b64_tr_b16 v[32:33], v14 offset:57344\n"
    "buffer_load_dwordx4 a[124:127], v1, s[40:43], s55 offen offset:3072 sc1\n"
    "v_mfma_f32_16x16x32_bf16 v[64:67], v[40:43], a[100:103], v[64:67]\n"
    "ds_read_b64_tr_b16 v[34:35], v14 offset:59392\n"
    "v_mfma_f32_16x16x32_bf16 v[72:75], v[40:43], a[164:167], v[72:75]\n"
    "buffer_load_dwordx4 a[188:191], v2, s[40:43], s55 offen offset:3072 sc1\n"
    "v_mfma_f32_16x16x32_bf16 v[80:83], v[40:43], a[228:231], v[80:83]\n"
    "v_mfma_f32_16x16x32_bf16 v[60:63], v[44:47], a[36:39], v[60:63]\n"
    "ds_read_b64_tr_b16 v[36:37], v15 offset:57344\n"
    "v_mfma_f32_16x16x32_bf16 v[68:71], v[44:47], a[100:103], v[68:71]\n"
    "ds_read_b64_tr_b16 v[38:39], v15 offset:59392\n"
    "buffer_load_dwordx4 a[252:255], v3, s[40:43], s55 offen offset:3072 sc1\n"
    "v_mfma_f32_16x16x32_bf16 v[76:79], v[44:47], a[164:167], v[76:79]\n"
    "v_mfma_f32_16x16x32_bf16 v[84:87], v[44:47], a[228:231], v[84:87]\n"
    "s_waitcnt lgkmcnt(8)\n"
    "v_mfma_f32_16x16x32_bf16 v[88:91], v[48:51], a[36:39], v[88:91]\n"
    "ds_read_b64_tr_b16 v[40:41], v12 offset:61440\n"
    "v_mfma_f32_16x16x32_bf16 v[96:99], v[48:51], a[100:103], v[96:99]\n"
    "ds_read_b64_tr_b16 v[42:43], v12 offset:63488\n"
    "v_mfma_f32_16x16x32_bf16 v[104:107], v[48:51], a[164:167], v[104:107]\n"
    "v_mfma_f32_16x16x32_bf16 v[112:115], v[48:51], a[228:231], v[112:115]\n"
    "v_mfma_f32_16x16x32_bf16 v[92:95], v[52:55], a[36:39], v[92:95]\n"
    "ds_read_b64_tr_b16 v[44:45], v13 offset:61440\n"
    "v_mfma_f32_16x16x32_bf16 v[100:103], v[52:55], a[100:103], v[100:103]\n"
    "ds_read_b64_tr_b16 v[46:47], v13 offset:63488\n"
    "v_mfma_f32_16x16x32_bf16 v[108:111], v[52:55], a[164:167], v[108:111]\n"
    "v_mfma_f32_16x16x32_bf16 v[116:119], v[52:55], a[228:231], v[116:119]\n"
    "s_waitcnt vmcnt(20)\n"
    "s_waitcnt lgkmcnt(8)\n"
    "v_mfma_f32_16x16x32_bf16 v[56:59], v[24:27], a[40:43], v[56:59]\n"
    "ds_read_b64_tr_b16 v[48:49], v14 offset:61440\n"
    "v_mfma_f32_16x16x32_bf16 v[64:67], v[24:27], a[104:107], v[64:67]\n"
    "ds_read_b64_tr_b16 v[50:51], v14 offset:63488\n"
    "v_mfma_f32_16x16x32_bf16 v[72:75], v[24:27], a[168:171], v[72:75]\n"
    "v_mfma_f32_16x16x32_bf16 v[80:83], v[24:27], a[232:235], v[80:83]\n"
    "v_mfma_f32_16x16x32_bf16 v[60:63], v[28:31], a[40:43], v[60:63]\n"
    "ds_read_b64_tr_b16 v[52:53], v15 offset:61440\n"
    "v_mfma_f32_16x16x32_bf16 v[68:71], v[28:31], a[104:107], v[68:71]\n"
    "ds_read_b64_tr_b16 v[54:55], v15 offset:63488\n"
    "v_mfma_f32_16x16x32_bf16 v[76:79], v[28:31], a[168:171], v[76:79]\n"
    "v_mfma_f32_16x16x32_bf16 v[84:87], v[28:31], a[232:235], v[84:87]\n"
    "s_waitcnt lgkmcnt(8)\n"
    "v_mfma_f32_16x16x32_bf16 v[88:91], v[32:35], a[40:43], v[88:91]\n"
    "v_mfma_f32_16x16x32_bf16 v[96:99], v[32:35], a[104:107], v[96:99]\n"
    "v_mfma_f32_16x16x32_bf16 v[104:107], v[32:35], a[168:171], v[104:107]\n"
    "v_mfma_f32_16x16x32_bf16 v[112:115], v[32:35], a[232:235], v[112:115]\n"
    "v_mfma_f32_16x16x32_bf16 v[92:95], v[36:39], a[40:43], v[92:95]\n"
    "v_mfma_f32_16x16x32_bf16 v[100:103], v[36:39], a[104:107], v[100:103]\n"
    "v_mfma_f32_16x16x32_bf16 v[108:111], v[36:39], a[168:171], v[108:111]\n"
    "v_mfma_f32_16x16x32_bf16 v[116:119], v[36:39], a[232:235], v[116:119]\n"
    "s_waitcnt vmcnt(16)\n"
    "s_waitcnt vmcnt(16) lgkmcnt(0)\n"
    "s_barrier\n"
    "v_mfma_f32_16x16x32_bf16 v[56:59], v[40:43], a[44:47], v[56:59]\n"
    "ds_read_b64_tr_b16 v[24:25], v8 offset:0\n"
    "v_mfma_f32_16x16x32_bf16 v[64:67], v[40:43], a[108:111], v[64:67]\n"
    "ds_read_b64_tr_b16 v[26:27], v8 offset:2048\n"
    "v_mfma_f32_16x16x32_bf16 v[72:75], v[40:43], a[172:175], v[72:75]\n"
    "ds_read_b64_tr_b16 v[28:29], v9 offset:0\n"
    "v_mfma_f32_16x16x32_bf16 v[80:83], v[40:43], a[236:239], v[80:83]\n"
    "ds_read_b64_tr_b16 v[30:31], v9 offset:2048\n"
    "v_mfma_f32_16x16x32_bf16 v[60:63], v[44:47], a[44:47], v[60:63]\n"
    "ds_read_b64_tr_b16 v[32:33], v10 offset:0\n"
    "v_mfma_f32_16x16x32_bf16 v[68:71], v[44:47], a[108:111], v[68:71]\n"
    "ds_read_b64_tr_b16 v[34:35], v10 offset:2048\n"
    "v_mfma_f32_16x16x32_bf16 v[76:79], v[44:47], a[172:175], v[76:79]\n"
    "ds_read_b64_tr_b16 v[36:37], v11 offset:0\n"
    "v_mfma_f32_16x16x32_bf16 v[84:87], v[44:47], a[236:239], v[84:87]\n"
    "ds_read_b64_tr_b16 v[38:39], v11 offset:2048\n"
    "v_mfma_f32_16x16x32_bf16 v[88:91], v[48:51], a[44:47], v[88:91]\n"
    "ds_read_b64_tr_b16 v[40:41], v8 offset:4096\n"
    "v_mfma_f32_16x16x32_bf16 v[96:99], v[48:51], a[108:111], v[96:99]\n"
    "ds_read_b64_tr_b16 v[42:43], v8 offset:6144\n"
    "v_mfma_f32_16x16x32_bf16 v[104:107], v[48:51], a[172:175], v[104:107]\n"
    "v_mfma_f32_16x16x32_bf16 v[112:115], v[48:51], a[236:239], v[112:115]\n"
    "v_mfma_f32_16x16x32_bf16 v[92:95], v[52:55], a[44:47], v[92:95]\n"
    "ds_read_b64_tr_b16 v[44:45], v9 offset:4096\n"
    "v_mfma_f32_16x16x32_bf16 v[100:103], v[52:55], a[108:111], v[100:103]\n"
    "ds_read_b64_tr_b16 v[46:47], v9 offset:6144\n"
    "v_mfma_f32_16x16x32_bf16 v[108:111], v[52:55], a[172:175], v[108:111]\n"
    "v_mfma_f32_16x16x32_bf16 v[116:119], v[52:55], a[236:239], v[116:119]\n"
    "s_waitcnt vmcnt(12)\n"
    "s_waitcnt lgkmcnt(8)\n"
    "v_mfma_f32_16x16x32_bf16 v[56:59], v[24:27], a[48:51], v[56:59]\n"
    "ds_read_b64_tr_b16 v[48:49], v10 offset:4096\n"
    "v_mfma_f32_16x16x32_bf16 v[64:67], v[24:27], a[112:115], v[64:67]\n"
    "ds_read_b64_tr_b16 v[50:51], v10 offset:6144\n"
    "v_mfma_f32_16x16x32_bf16 v[72:75], v[24:27], a[176:179], v[72:75]\n"
    "v_mfma_f32_16x16x32_bf16 v[80:83], v[24:27], a[240:243], v[80:83]\n"
    "v_mfma_f32_16x16x32_bf16 v[60:63], v[28:31], a[48:51], v[60:63]\n"
    "ds_read_b64_tr_b16 v[52:53], v11 offset:4096\n"
    "v_mfma_f32_16x16x32_bf16 v[68:71], v[28:31], a[112:115], v[68:71]\n"
    "ds_read_b64_tr_b16 v[54:55], v11 offset:6144\n"
    "v_mfma_f32_16x16x32_bf16 v[76:79], v[28:31], a[176:179], v[76:79]\n"
    "v_mfma_f32_16x16x32_bf16 v[84:87], v[28:31], a[240:243], v[84:87]\n"
    "s_waitcnt lgkmcnt(8)\n"
    "v_mfma_f32_16x16x32_bf16 v[88:91], v[32:35], a[48:51], v[88:91]\n"
    "ds_read_b64_tr_b16 v[24:25], v8 offset:8192\n"
    "v_mfma_f32_16x16x32_bf16 v[96:99], v[32:35], a[112:115], v[96:99]\n"
    "ds_read_b64_tr_b16 v[26:27], v8 offset:10240\n"
    "v_mfma_f32_16x16x32_bf16 v[104:107], v[32:35], a[176:179], v[104:107]\n"
    "v_mfma_f32_16x16x32_bf16 v[112:115], v[32:35], a[240:243], v[112:115]\n"
    "v_mfma_f32_16x16x32_bf16 v[92:95], v[36:39], a[48:51], v[92:95]\n"
    "ds_read_b64_tr_b16 v[28:29], v9 offset:8192\n"
    "v_mfma_f32_16x16x32_bf16 v[100:103], v[36:39], a[112:115], v[100:103]\n"
    "ds_read_b64_tr_b16 v[30:31], v9 offset:10240\n"
    "v_mfma_f32_16x16x32_bf16 v[108:111], v[36:39], a[176:179], v[108:111]\n"
    "v_mfma_f32_16x16x32_bf16 v[116:119], v[36:39], a[240:243], v[116:119]\n"
    "s_waitcnt vmcnt(8)\n"
    "s_waitcnt lgkmcnt(8)\n"
    "v_mfma_f32_16x16x32_bf16 v[56:59], v[40:43], a[52:55], v[56:59]\n"
    "ds_read_b64_tr_b16 v[32:33], v10 offset:8192\n"
    "v_mfma_f32_16x16x32_bf16 v[64:67], v[40:43], a[116:119], v[64:67]\n"
    "ds_read_b64_tr_b16 v[34:35], v10 offset:10240\n"
    "v_mfma_f32_16x16x32_bf16 v[72:75], v[40:43], a[180:183], v[72:75]\n"
    "v_mfma_f32_16x16x32_bf16 v[80:83], v[40:43], a[244:247], v[80:83]\n"
    "v_mfma_f32_16x16x32_bf16 v[60:63], v[44:47], a[52:55], v[60:63]\n"
    "ds_read_b64_tr_b16 v[36:37], v11 offset:8192\n"
    "v_mfma_f32_16x16x32_bf16 v[68:71], v[44:47], a[116:119], v[68:71]\n"
    "ds_read_b64_tr_b16 v[38:39], v11 offset:10240\n"
    "v_mfma_f32_16x16x32_bf16 v[76:79], v[44:47], a[180:183], v[76:79]\n"
    "v_mfma_f32_16x16x32_bf16 v[84:87], v[44:47], a[244:247], v[84:87]\n"
    "s_waitcnt lgkmcnt(8)\n"
    "v_mfma_f32_16x16x32_bf16 v[88:91], v[48:51], a[52:55], v[88:91]\n"
    "ds_read_b64_tr_b16 v[40:41], v8 offset:12288\n"
    "v_mfma_f32_16x16x32_bf16 v[96:99], v[48:51], a[116:119], v[96:99]\n"
    "ds_read_b64_tr_b16 v[42:43], v8 offset:14336\n"
    "v_mfma_f32_16x16x32_bf16 v[104:107], v[48:51], a[180:183], v[104:107]\n"
    "v_mfma_f32_16x16x32_bf16 v[112:115], v[48:51], a[244:247], v[112:115]\n"
    "v_mfma_f32_16x16x32_bf16 v[92:95], v[52:55], a[52:55], v[92:95]\n"
    "ds_read_b64_tr_b16 v[44:45], v9 offset:12288\n"
    "v_mfma_f32_16x16x32_bf16 v[100:103], v[52:55], a[116:119], v[100:103]\n"
    "ds_read_b64_tr_b16 v[46:47], v9 offset:14336\n"
    "v_mfma_f32_16x16x32_bf16 v[108:111], v[52:55], a[180:183], v[108:111]\n"
    "v_mfma_f32_16x16x32_bf16 v[116:119], v[52:55], a[244:247], v[116:119]\n"
    "s_waitcnt vmcnt(4)\n"
    "s_waitcnt lgkmcnt(8)\n"
    "v_mfma_f32_16x16x32_bf16 v[56:59], v[24:27], a[56:59], v[56:59]\n"
    "ds_read_b64_tr_b16 v[48:49], v10 offset:12288\n"
    "v_mfma_f32_16x16x32_bf16 v[64:67], v[24:27], a[120:123], v[64:67]\n"
    "ds_read_b64_tr_b16 v[50:51], v10 offset:14336\n"
    "v_mfma_f32_16x16x32_bf16 v[72:75], v[24:27], a[184:187], v[72:75]\n"
    "v_mfma_f32_16x16x32_bf16 v[80:83], v[24:27], a[248:251], v[80:83]\n"
    "v_mfma_f32_16x16x32_bf16 v[60:63], v[28:31], a[56:59], v[60:63]\n"
    "ds_read_b64_tr_b16 v[52:53], v11 offset:12288\n"
    "v_mfma_f32_16x16x32_bf16 v[68:71], v[28:31], a[120:123], v[68:71]\n"
    "ds_read_b64_tr_b16 v[54:55], v11 offset:14336\n"
    "v_mfma_f32_16x16x32_bf16 v[76:79], v[28:31], a[184:187], v[76:79]\n"
    "v_mfma_f32_16x16x32_bf16 v[84:87], v[28:31], a[248:251], v[84:87]\n"
    "s_waitcnt lgkmcnt(8)\n"
    "v_mfma_f32_16x16x32_bf16 v[88:91], v[32:35], a[56:59], v[88:91]\n"
    "v_mfma_f32_16x16x32_bf16 v[96:99], v[32:35], a[120:123], v[96:99]\n"
    "v_mfma_f32_16x16x32_bf16 v[104:107], v[32:35], a[184:187], v[104:107]\n"
    "v_mfma_f32_16x16x32_bf16 v[112:115], v[32:35], a[248:251], v[112:115]\n"
    "v_mfma_f32_16x16x32_bf16 v[92:95], v[36:39], a[56:59], v[92:95]\n"
    "v_mfma_f32_16x16x32_bf16 v[100:103], v[36:39], a[120:123], v[100:103]\n"
    "v_mfma_f32_16x16x32_bf16 v[108:111], v[36:39], a[184:187], v[108:111]\n"
    "v_mfma_f32_16x16x32_bf16 v[116:119], v[36:39], a[248:251], v[116:119]\n"
    "s_waitcnt vmcnt(0)\n"
    "s_waitcnt vmcnt(0) lgkmcnt(0)\n"
    "s_barrier\n"
    "v_mfma_f32_16x16x32_bf16 v[56:59], v[40:43], a[60:63], v[56:59]\n"
    "ds_read_b64_tr_b16 v[24:25], v8 offset:16384\n"
    "v_mfma_f32_16x16x32_bf16 v[64:67], v[40:43], a[124:127], v[64:67]\n"
    "ds_read_b64_tr_b16 v[26:27], v8 offset:18432\n"
    "v_mfma_f32_16x16x32_bf16 v[72:75], v[40:43], a[188:191], v[72:75]\n"
    "ds_read_b64_tr_b16 v[28:29], v9 offset:16384\n"
    "v_mfma_f32_16x16x32_bf16 v[80:83], v[40:43], a[252:255], v[80:83]\n"
    "ds_read_b64_tr_b16 v[30:31], v9 offset:18432\n"
    "v_mfma_f32_16x16x32_bf16 v[60:63], v[44:47], a[60:63], v[60:63]\n"
    "ds_read_b64_tr_b16 v[32:33], v8 offset:20480\n"
    "v_mfma_f32_16x16x32_bf16 v[68:71], v[44:47], a[124:127], v[68:71]\n"
    "ds_read_b64_tr_b16 v[34:35], v8 offset:22528\n"
    "v_mfma_f32_16x16x32_bf16 v[76:79], v[44:47], a[188:191], v[76:79]\n"
    "ds_read_b64_tr_b16 v[36:37], v9 offset:20480\n"
    "v_mfma_f32_16x16x32_bf16 v[84:87], v[44:47], a[252:255], v[84:87]\n"
    "ds_read_b64_tr_b16 v[38:39], v9 offset:22528\n"
    "v_mfma_f32_16x16x32_bf16 v[88:91], v[48:51], a[60:63], v[88:91]\n"
    "ds_read_b64_tr_b16 v[40:41], v8 offset:24576\n"
    "v_mfma_f32_16x16x32_bf16 v[96:99], v[48:51], a[124:127], v[96:99]\n"
    "ds_read_b64_tr_b16 v[42:43], v8 offset:26624\n"
    "v_mfma_f32_16x16x32_bf16 v[104:107], v[48:51], a[188:191], v[104:107]\n"
    "v_mfma_f32_16x16x32_bf16 v[112:115], v[48:51], a[252:255], v[112:115]\n"
    "v_mfma_f32_16x16x32_bf16 v[92:95], v[52:55], a[60:63], v[92:95]\n"
    "ds_read_b64_tr_b16 v[44:45], v9 offset:24576\n"
    "v_mfma_f32_16x16x32_bf16 v[100:103], v[52:55], a[124:127], v[100:103]\n"
    "ds_read_b64_tr_b16 v[46:47], v9 offset:26624\n"
    "v_mfma_f32_16x16x32_bf16 v[108:111], v[52:55], a[188:191], v[108:111]\n"
    "v_mfma_f32_16x16x32_bf16 v[116:119], v[52:55], a[252:255], v[116:119]\n"
    "s_waitcnt lgkmcnt(8)\n"
    "v_mfma_f32_16x16x32_bf16 v[120:123], v[24:27], a[16:19], v[120:123]\n"
    "ds_read_b64_tr_b16 v[48:49], v8 offset:28672\n"
    "v_mfma_f32_16x16x32_bf16 v[128:131], v[24:27], a[80:83], v[128:131]\n"
    "ds_read_b64_tr_b16 v[50:51], v8 offset:30720\n"
    "v_mfma_f32_16x16x32_bf16 v[136:139], v[24:27], a[144:147], v[136:139]\n"
    "v_mfma_f32_16x16x32_bf16 v[144:147], v[24:27], a[208:211], v[144:147]\n"
    "v_mfma_f32_16x16x32_bf16 v[124:127], v[28:31], a[16:19], v[124:127]\n"
    "ds_read_b64_tr_b16 v[52:53], v9 offset:28672\n"
    "v_mfma_f32_16x16x32_bf16 v[132:135], v[28:31], a[80:83], v[132:135]\n"
    "ds_read_b64_tr_b16 v[54:55], v9 offset:30720\n"
    "v_mfma_f32_16x16x32_bf16 v[140:143], v[28:31], a[144:147], v[140:143]\n"
    "v_mfma_f32_16x16x32_bf16 v[148:151], v[28:31], a[208:211], v[148:151]\n"
    "s_waitcnt lgkmcnt(8)\n"
    "v_mfma_f32_16x16x32_bf16 v[120:123], v[32:35], a[20:23], v[120:123]\n"
    "v_mfma_f32_16x16x32_bf16 v[128:131], v[32:35], a[84:87], v[128:131]\n"
    "v_mfma_f32_16x16x32_bf16 v[136:139], v[32:35], a[148:151], v[136:139]\n"
    "v_cvt_pk_bf16_f32 v184, v56, v57\n"
    "v_mfma_f32_16x16x32_bf16 v[144:147], v[32:35], a[212:215], v[144:147]\n"
    "v_mfma_f32_16x16x32_bf16 v[124:127], v[36:39], a[20:23], v[124:127]\n"
    "v_mfma_f32_16x16x32_bf16 v[132:135], v[36:39], a[84:87], v[132:135]\n"
    "v_cvt_pk_bf16_f32 v185, v58, v59\n"
    "v_mfma_f32_16x16x32_bf16 v[140:143], v[36:39], a[148:151], v[140:143]\n"
    "v_mfma_f32_16x16x32_bf16 v[148:151], v[36:39], a[212:215], v[148:151]\n"
    "s_waitcnt vmcnt(0) lgkmcnt(0)\n"
    "s_barrier\n"
    "v_mfma_f32_16x16x32_bf16 v[120:123], v[40:43], a[24:27], v[120:123]\n"
    "ds_read_b64_tr_b16 v[24:25], v12 offset:0\n"
    "v_cvt_pk_bf16_f32 v186, v60, v61\n"
    "v_mfma_f32_16x16x32_bf16 v[128:131], v[40:43], a[88:91], v[128:131]\n"
    "ds_read_b64_tr_b16 v[26:27], v12 offset:2048\n"
    "v_mfma_f32_16x16x32_bf16 v[136:139], v[40:43], a[152:155], v[136:139]\n"
    "ds_read_b64_tr_b16 v[28:29], v13 offset:0\n"
    "v_mfma_f32_16x16x32_bf16 v[144:147], v[40:43], a[216:219], v[144:147]\n"
    "ds_read_b64_tr_b16 v[30:31], v13 offset:2048\n"
    "v_cvt_pk_bf16_f32 v187, v62, v63\n"
    "v_mfma_f32_16x16x32_bf16 v[124:127], v[44:47], a[24:27], v[124:127]\n"
    "ds_read_b64_tr_b16 v[32:33], v12 offset:4096\n"
    "v_mfma_f32_16x16x32_bf16 v[132:135], v[44:47], a[88:91], v[132:135]\n"
    "ds_read_b64_tr_b16 v[34:35], v12 offset:6144\n"
    "buffer_store_dwordx4 v[184:187], v20, s[48:51], s26 offen offset:0 sc1\n"
    "v_mfma_f32_16x16x32_bf16 v[140:143], v[44:47], a[152:155], v[140:143]\n"
    "ds_read_b64_tr_b16 v[36:37], v13 offset:4096\n"
    "v_mfma_f32_16x16x32_bf16 v[148:151], v[44:47], a[216:219], v[148:151]\n"
    "ds_read_b64_tr_b16 v[38:39], v13 offset:6144\n"
    "v_mfma_f32_16x16x32_bf16 v[120:123], v[48:51], a[28:31], v[120:123]\n"
    "ds_read_b64_tr_b16 v[40:41], v12 offset:8192\n"
    "v_cvt_pk_bf16_f32 v188, v64, v65\n"
    "v_mfma_f32_16x16x32_bf16 v[128:131], v[48:51], a[92:95], v[128:131]\n"
    "ds_read_b64_tr_b16 v[42:43], v12 offset:10240\n"
    "v_mfma_f32_16x16x32_bf16 v[136:139], v[48:51], a[156:159], v[136:139]\n"
    "v_mfma_f32_16x16x32_bf16 v[144:147], v[48:51], a[220:223], v[144:147]\n"
    "v_cvt_pk_bf16_f32 v189, v66, v67\n"
    "v_mfma_f32_16x16x32_bf16 v[124:127], v[52:55], a[28:31], v[124:127]\n"
    "ds_read_b64_tr_b16 v[44:45], v13 offset:8192\n"
    "v_mfma_f32_16x16x32_bf16 v[132:135], v[52:55], a[92:95], v[132:135]\n"
    "ds_read_b64_tr_b16 v[46:47], v13 offset:10240\n"
    "v_mfma_f32_16x16x32_bf16 v[140:143], v[52:55], a[156:159], v[140:143]\n"
    "v_cvt_pk_bf16_f32 v190, v68, v69\n"
    "v_mfma_f32_16x16x32_bf16 v[148:151], v[52:55], a[220:223], v[148:151]\n"
    "s_waitcnt lgkmcnt(8)\n"
    "v_mfma_f32_16x16x32_bf16 v[120:123], v[24:27], a[32:35], v[120:123]\n"
    "ds_read_b64_tr_b16 v[48:49], v12 offset:12288\n"
    "v_cvt_pk_bf16_f32 v191, v70, v71\n"
    "v_mfma_f32_16x16x32_bf16 v[128:131], v[24:27], a[96:99], v[128:131]\n"
    "ds_read_b64_tr_b16 v[50:51], v12 offset:14336\n"
    "v_mfma_f32_16x16x32_bf16 v[136:139], v[24:27], a[160:163], v[136:139]\n"
    "v_mfma_f32_16x16x32_bf16 v[144:147], v[24:27], a[224:227], v[144:147]\n"
    "buffer_store_dwordx4 v[188:191], v20, s[48:51], s27 offen offset:0 sc1\n"
    "v_mfma_f32_16x16x32_bf16 v[124:127], v[28:31], a[32:35], v[124:127]\n"
    "ds_read_b64_tr_b16 v[52:53], v13 offset:12288\n"
    "v_mfma_f32_16x16x32_bf16 v[132:135], v[28:31], a[96:99], v[132:135]\n"
    "ds_read_b64_tr_b16 v[54:55], v13 offset:14336\n"
    "v_mfma_f32_16x16x32_bf16 v[140:143], v[28:31], a[160:163], v[140:143]\n"
    "v_cvt_pk_bf16_f32 v192, v72, v73\n"
    "v_mfma_f32_16x16x32_bf16 v[148:151], v[28:31], a[224:227], v[148:151]\n"
    "s_waitcnt lgkmcnt(8)\n"
    "v_mfma_f32_16x16x32_bf16 v[120:123], v[32:35], a[36:39], v[120:123]\n"
    "v_mfma_f32_16x16x32_bf16 v[128:131], v[32:35], a[100:103], v[128:131]\n"
    "v_cvt_pk_bf16_f32 v193, v74, v75\n"
    "v_mfma_f32_16x16x32_bf16 v[136:139], v[32:35], a[164:167], v[136:139]\n"
    "v_mfma_f32_16x16x32_bf16 v[144:147], v[32:35], a[228:231], v[144:147]\n"
    "v_mfma_f32_16x16x32_bf16 v[124:127], v[36:39], a[36:39], v[124:127]\n"
    "v_cvt_pk_bf16_f32 v194, v76, v77\n"
    "v_mfma_f32_16x16x32_bf16 v[132:135], v[36:39], a[100:103], v[132:135]\n"
    "v_mfma_f32_16x16x32_bf16 v[140:143], v[36:39], a[164:167], v[140:143]\n"
    "v_cvt_pk_bf16_f32 v195, v78, v79\n"
    "v_mfma_f32_16x16x32_bf16 v[148:151], v[36:39], a[228:231], v[148:151]\n"
    "s_waitcnt vmcnt(2) lgkmcnt(0)\n"
    "s_barrier\n"
    "v_mfma_f32_16x16x32_bf16 v[120:123], v[40:43], a[40:43], v[120:123]\n"
    "ds_read_b64_tr_b16 v[24:25], v12 offset:16384\n"
    "v_mfma_f32_16x16x32_bf16 v[128:131], v[40:43], a[104:107], v[128:131]\n"
    "ds_read_b64_tr_b16 v[26:27], v12 offset:18432\n"
    "buffer_store_dwordx4 v[192:195], v21, s[48:51], s26 offen offset:0 sc1\n"
    "v_mfma_f32_16x16x32_bf16 v[136:139], v[40:43], a[168:171], v[136:139]\n"
    "ds_read_b64_tr_b16 v[28:29], v13 offset:16384\n"
    "v_mfma_f32_16x16x32_bf16 v[144:147], v[40:43], a[232:235], v[144:147]\n"
    "ds_read_b64_tr_b16 v[30:31], v13 offset:18432\n"
    "v_mfma_f32_16x16x32_bf16 v[124:127], v[44:47], a[40:43], v[124:127]\n"
    "ds_read_b64_tr_b16 v[32:33], v12 offset:20480\n"
    "v_cvt_pk_bf16_f32 v196, v80, v81\n"
    "v_mfma_f32_16x16x32_bf16 v[132:135], v[44:47], a[104:107], v[132:135]\n"
    "ds_read_b64_tr_b16 v[34:35], v12 offset:22528\n"
    "v_mfma_f32_16x16x32_bf16 v[140:143], v[44:47], a[168:171], v[140:143]\n"
    "ds_read_b64_tr_b16 v[36:37], v13 offset:20480\n"
    "v_mfma_f32_16x16x32_bf16 v[148:151], v[44:47], a[232:235], v[148:151]\n"
    "ds_read_b64_tr_b16 v[38:39], v13 offset:22528\n"
    "v_cvt_pk_bf16_f32 v197, v82, v83\n"
    "v_mfma_f32_16x16x32_bf16 v[120:123], v[48:51], a[44:47], v[120:123]\n"
    "ds_read_b64_tr_b16 v[40:41], v12 offset:24576\n"
    "v_mfma_f32_16x16x32_bf16 v[128:131], v[48:51], a[108:111], v[128:131]\n"
    "ds_read_b64_tr_b16 v[42:43], v12 offset:26624\n"
    "v_cvt_pk_bf16_f32 v198, v84, v85\n"
    "v_mfma_f32_16x16x32_bf16 v[136:139], v[48:51], a[172:175], v[136:139]\n"
    "v_mfma_f32_16x16x32_bf16 v[144:147], v[48:51], a[236:239], v[144:147]\n"
    "v_mfma_f32_16x16x32_bf16 v[124:127], v[52:55], a[44:47], v[124:127]\n"
    "ds_read_b64_tr_b16 v[44:45], v13 offset:24576\n"
    "v_cvt_pk_bf16_f32 v199, v86, v87\n"
    "v_mfma_f32_16x16x32_bf16 v[132:135], v[52:55], a[108:111], v[132:135]\n"
    "ds_read_b64_tr_b16 v[46:47], v13 offset:26624\n"
    "v_mfma_f32_16x16x32_bf16 v[140:143], v[52:55], a[172:175], v[140:143]\n"
    "v_mfma_f32_16x16x32_bf16 v[148:151], v[52:55], a[236:239], v[148:151]\n"
    "buffer_store_dwordx4 v[196:199], v21, s[48:51], s27 offen offset:0 sc1\n"
    "s_waitcnt lgkmcnt(8)\n"
    "v_mfma_f32_16x16x32_bf16 v[120:123], v[24:27], a[48:51], v[120:123]\n"
    "ds_read_b64_tr_b16 v[48:49], v12 offset:28672\n"
    "v_mfma_f32_16x16x32_bf16 v[128:131], v[24:27], a[112:115], v[128:131]\n"
    "ds_read_b64_tr_b16 v[50:51], v12 offset:30720\n"
    "v_mfma_f32_16x16x32_bf16 v[136:139], v[24:27], a[176:179], v[136:139]\n"
    "v_cvt_pk_bf16_f32 v184, v88, v89\n"
    "v_mfma_f32_16x16x32_bf16 v[144:147], v[24:27], a[240:243], v[144:147]\n"
    "v_mfma_f32_16x16x32_bf16 v[124:127], v[28:31], a[48:51], v[124:127]\n"
    "ds_read_b64_tr_b16 v[52:53], v13 offset:28672\n"
    "v_mfma_f32_16x16x32_bf16 v[132:135], v[28:31], a[112:115], v[132:135]\n"
    "ds_read_b64_tr_b16 v[54:55], v13 offset:30720\n"
    "v_cvt_pk_bf16_f32 v185, v90, v91\n"
    "v_mfma_f32_16x16x32_bf16 v[140:143], v[28:31], a[176:179], v[140:143]\n"
    "v_mfma_f32_16x16x32_bf16 v[148:151], v[28:31], a[240:243], v[148:151]\n"
    "v_cvt_pk_bf16_f32 v186, v92, v93\n"
    "s_waitcnt lgkmcnt(8)\n"
    "v_mfma_f32_16x16x32_bf16 v[120:123], v[32:35], a[52:55], v[120:123]\n"
    "v_mfma_f32_16x16x32_bf16 v[128:131], v[32:35], a[116:119], v[128:131]\n"
    "v_mfma_f32_16x16x32_bf16 v[136:139], v[32:35], a[180:183], v[136:139]\n"
    "v_cvt_pk_bf16_f32 v187, v94, v95\n"
    "v_mfma_f32_16x16x32_bf16 v[144:147], v[32:35], a[244:247], v[144:147]\n"
    "v_mfma_f32_16x16x32_bf16 v[124:127], v[36:39], a[52:55], v[124:127]\n"
    "v_mfma_f32_16x16x32_bf16 v[132:135], v[36:39], a[116:119], v[132:135]\n"
    "buffer_store_dwordx4 v[184:187], v20, s[48:51], s26 offen offset:1024 sc1\n"
    "v_mfma_f32_16x16x32_bf16 v[140:143], v[36:39], a[180:183], v[140:143]\n"
    "v_mfma_f32_16x16x32_bf16 v[148:151], v[36:39], a[244:247], v[148:151]\n"
    "s_waitcnt lgkmcnt(4)\n"
    "v_mfma_f32_16x16x32_bf16 v[120:123], v[40:43], a[56:59], v[120:123]\n"
    "ds_read_b64_tr_b16 v[24:25], v10 offset:16384\n"
    "v_cvt_pk_bf16_f32 v188, v96, v97\n"
    "v_mfma_f32_16x16x32_bf16 v[128:131], v[40:43], a[120:123], v[128:131]\n"
    "ds_read_b64_tr_b16 v[26:27], v10 offset:18432\n"
    "v_mfma_f32_16x16x32_bf16 v[136:139], v[40:43], a[184:187], v[136:139]\n"
    "ds_read_b64_tr_b16 v[28:29], v11 offset:16384\n"
    "v_cvt_pk_bf16_f32 v189, v98, v99\n"
    "v_mfma_f32_16x16x32_bf16 v[144:147], v[40:43], a[248:251], v[144:147]\n"
    "ds_read_b64_tr_b16 v[30:31], v11 offset:18432\n"
    "v_mfma_f32_16x16x32_bf16 v[124:127], v[44:47], a[56:59], v[124:127]\n"
    "ds_read_b64_tr_b16 v[32:33], v10 offset:20480\n"
    "v_mfma_f32_16x16x32_bf16 v[132:135], v[44:47], a[120:123], v[132:135]\n"
    "ds_read_b64_tr_b16 v[34:35], v10 offset:22528\n"
    "v_cvt_pk_bf16_f32 v190, v100, v101\n"
    "v_mfma_f32_16x16x32_bf16 v[140:143], v[44:47], a[184:187], v[140:143]\n"
    "ds_read_b64_tr_b16 v[36:37], v11 offset:20480\n"
    "v_mfma_f32_16x16x32_bf16 v[148:151], v[44:47], a[248:251], v[148:151]\n"
    "ds_read_b64_tr_b16 v[38:39], v11 offset:22528\n"
    "s_waitcnt lgkmcnt(8)\n"
    "v_mfma_f32_16x16x32_bf16 v[120:123], v[48:51], a[60:63], v[120:123]\n"
    "ds_read_b64_tr_b16 v[40:41], v10 offset:24576\n"
    "v_cvt_pk_bf16_f32 v191, v102, v103\n"
    "v_mfma_f32_16x16x32_bf16 v[128:131], v[48:51], a[124:127], v[128:131]\n"
    "ds_read_b64_tr_b16 v[42:43], v10 offset:26624\n"
    "v_mfma_f32_16x16x32_bf16 v[136:139], v[48:51], a[188:191], v[136:139]\n"
    "v_mfma_f32_16x16x32_bf16 v[144:147], v[48:51], a[252:255], v[144:147]\n"
    "buffer_store_dwordx4 v[188:191], v20, s[48:51], s27 offen offset:1024 sc1\n"
    "v_mfma_f32_16x16x32_bf16 v[124:127], v[52:55], a[60:63], v[124:127]\n"
    "ds_read_b64_tr_b16 v[44:45], v11 offset:24576\n"
    "v_mfma_f32_16x16x32_bf16 v[132:135], v[52:55], a[124:127], v[132:135]\n"
    "ds_read_b64_tr_b16 v[46:47], v11 offset:26624\n"
    "v_mfma_f32_16x16x32_bf16 v[140:143], v[52:55], a[188:191], v[140:143]\n"
    "v_mfma_f32_16x16x32_bf16 v[148:151], v[52:55], a[252:255], v[148:151]\n"
    "s_waitcnt lgkmcnt(8)\n"
    "v_mfma_f32_16x16x32_bf16 v[152:155], v[24:27], a[16:19], v[152:155]\n"
    "ds_read_b64_tr_b16 v[48:49], v10 offset:28672\n"
    "v_mfma_f32_16x16x32_bf16 v[160:163], v[24:27], a[80:83], v[160:163]\n"
    "ds_read_b64_tr_b16 v[50:51], v10 offset:30720\n"
    "v_mfma_f32_16x16x32_bf16 v[168:171], v[24:27], a[144:147], v[168:171]\n"
    "v_mfma_f32_16x16x32_bf16 v[176:179], v[24:27], a[208:211], v[176:179]\n"
    "v_mfma_f32_16x16x32_bf16 v[156:159], v[28:31], a[16:19], v[156:159]\n"
    "ds_read_b64_tr_b16 v[52:53], v11 offset:28672\n"
    "v_mfma_f32_16x16x32_bf16 v[164:167], v[28:31], a[80:83], v[164:167]\n"
    "ds_read_b64_tr_b16 v[54:55], v11 offset:30720\n"
    "v_mfma_f32_16x16x32_bf16 v[172:175], v[28:31], a[144:147], v[172:175]\n"
    "v_mfma_f32_16x16x32_bf16 v[180:183], v[28:31], a[208:211], v[180:183]\n"
    "s_waitcnt lgkmcnt(8)\n"
    "v_mfma_f32_16x16x32_bf16 v[152:155], v[32:35], a[20:23], v[152:155]\n"
    "v_mfma_f32_16x16x32_bf16 v[160:163], v[32:35], a[84:87], v[160:163]\n"
    "v_mfma_f32_16x16x32_bf16 v[168:171], v[32:35], a[148:151], v[168:171]\n"
    "v_cvt_pk_bf16_f32 v192, v104, v105\n"
    "v_mfma_f32_16x16x32_bf16 v[176:179], v[32:35], a[212:215], v[176:179]\n"
    "v_mfma_f32_16x16x32_bf16 v[156:159], v[36:39], a[20:23], v[156:159]\n"
    "v_mfma_f32_16x16x32_bf16 v[164:167], v[36:39], a[84:87], v[164:167]\n"
    "v_cvt_pk_bf16_f32 v193, v106, v107\n"
    "v_mfma_f32_16x16x32_bf16 v[172:175], v[36:39], a[148:151], v[172:175]\n"
    "v_mfma_f32_16x16x32_bf16 v[180:183], v[36:39], a[212:215], v[180:183]\n"
    "s_waitcnt lgkmcnt(4)\n"
    "v_mfma_f32_16x16x32_bf16 v[152:155], v[40:43], a[24:27], v[152:155]\n"
    "ds_read_b64_tr_b16 v[24:25], v14 offset:0\n"
    "v_cvt_pk_bf16_f32 v194, v108, v109\n"
    "v_mfma_f32_16x16x32_bf16 v[160:163], v[40:43], a[88:91], v[160:163]\n"
    "ds_read_b64_tr_b16 v[26:27], v14 offset:2048\n"
    "v_mfma_f32_16x16x32_bf16 v[168:171], v[40:43], a[152:155], v[168:171]\n"
    "ds_read_b64_tr_b16 v[28:29], v15 offset:0\n"
    "v_mfma_f32_16x16x32_bf16 v[176:179], v[40:43], a[216:219], v[176:179]\n"
    "ds_read_b64_tr_b16 v[30:31], v15 offset:2048\n"
    "v_cvt_pk_bf16_f32 v195, v110, v111\n"
    "v_mfma_f32_16x16x32_bf16 v[156:159], v[44:47], a[24:27], v[156:159]\n"
    "ds_read_b64_tr_b16 v[32:33], v14 offset:4096\n"
    "v_mfma_f32_16x16x32_bf16 v[164:167], v[44:47], a[88:91], v[164:167]\n"
    "ds_read_b64_tr_b16 v[34:35], v14 offset:6144\n"
    "buffer_store_dwordx4 v[192:195], v21, s[48:51], s26 offen offset:1024 sc1\n"
    "v_mfma_f32_16x16x32_bf16 v[172:175], v[44:47], a[152:155], v[172:175]\n"
    "ds_read_b64_tr_b16 v[36:37], v15 offset:4096\n"
    "v_mfma_f32_16x16x32_bf16 v[180:183], v[44:47], a[216:219], v[180:183]\n"
    "ds_read_b64_tr_b16 v[38:39], v15 offset:6144\n"
    "s_waitcnt lgkmcnt(8)\n"
    "v_mfma_f32_16x16x32_bf16 v[152:155], v[48:51], a[28:31], v[152:155]\n"
    "ds_read_b64_tr_b16 v[40:41], v14 offset:8192\n"
    "v_cvt_pk_bf16_f32 v196, v112, v113\n"
    "v_mfma_f32_16x16x32_bf16 v[160:163], v[48:51], a[92:95], v[160:163]\n"
    "ds_read_b64_tr_b16 v[42:43], v14 offset:10240\n"
    "v_mfma_f32_16x16x32_bf16 v[168:171], v[48:51], a[156:159], v[168:171]\n"
    "v_mfma_f32_16x16x32_bf16 v[176:179], v[48:51], a[220:223], v[176:179]\n"
    "v_cvt_pk_bf16_f32 v197, v114, v115\n"
    "v_mfma_f32_16x16x32_bf16 v[156:159], v[52:55], a[28:31], v[156:159]\n"
    "ds_read_b64_tr_b16 v[44:45], v15 offset:8192\n"
    "v_mfma_f32_16x16x32_bf16 v[164:167], v[52:55], a[92:95], v[164:167]\n"
    "ds_read_b64_tr_b16 v[46:47], v15 offset:10240\n"
    "v_mfma_f32_16x16x32_bf16 v[172:175], v[52:55], a[156:159], v[172:175]\n"
    "v_cvt_pk_bf16_f32 v198, v116, v117\n"
    "v_mfma_f32_16x16x32_bf16 v[180:183], v[52:55], a[220:223], v[180:183]\n"
    "s_waitcnt lgkmcnt(8)\n"
    "v_mfma_f32_16x16x32_bf16 v[152:155], v[24:27], a[32:35], v[152:155]\n"
    "ds_read_b64_tr_b16 v[48:49], v14 offset:12288\n"
    "v_cvt_pk_bf16_f32 v199, v118, v119\n"
    "v_mfma_f32_16x16x32_bf16 v[160:163], v[24:27], a[96:99], v[160:163]\n"
    "ds_read_b64_tr_b16 v[50:51], v14 offset:14336\n"
    "v_mfma_f32_16x16x32_bf16 v[168:171], v[24:27], a[160:163], v[168:171]\n"
    "v_mfma_f32_16x16x32_bf16 v[176:179], v[24:27], a[224:227], v[176:179]\n"
    "buffer_store_dwordx4 v[196:199], v21, s[48:51], s27 offen offset:1024 sc1\n"
    "v_mfma_f32_16x16x32_bf16 v[156:159], v[28:31], a[32:35], v[156:159]\n"
    "ds_read_b64_tr_b16 v[52:53], v15 offset:12288\n"
    "v_mfma_f32_16x16x32_bf16 v[164:167], v[28:31], a[96:99], v[164:167]\n"
    "ds_read_b64_tr_b16 v[54:55], v15 offset:14336\n"
    "v_mfma_f32_16x16x32_bf16 v[172:175], v[28:31], a[160:163], v[172:175]\n"
    "v_cvt_pk_bf16_f32 v184, v120, v121\n"
    "v_mfma_f32_16x16x32_bf16 v[180:183], v[28:31], a[224:227], v[180:183]\n"
    "s_waitcnt lgkmcnt(8)\n"
    "v_mfma_f32_16x16x32_bf16 v[152:155], v[32:35], a[36:39], v[152:155]\n"
    "v_mfma_f32_16x16x32_bf16 v[160:163], v[32:35], a[100:103], v[160:163]\n"
    "v_cvt_pk_bf16_f32 v185, v122, v123\n"
    "v_mfma_f32_16x16x32_bf16 v[168:171], v[32:35], a[164:167], v[168:171]\n"
    "v_mfma_f32_16x16x32_bf16 v[176:179], v[32:35], a[228:231], v[176:179]\n"
    "v_mfma_f32_16x16x32_bf16 v[156:159], v[36:39], a[36:39], v[156:159]\n"
    "v_cvt_pk_bf16_f32 v186, v124, v125\n"
    "v_mfma_f32_16x16x32_bf16 v[164:167], v[36:39], a[100:103], v[164:167]\n"
    "v_mfma_f32_16x16x32_bf16 v[172:175], v[36:39], a[164:167], v[172:175]\n"
    "v_cvt_pk_bf16_f32 v187, v126, v127\n"
    "v_mfma_f32_16x16x32_bf16 v[180:183], v[36:39], a[228:231], v[180:183]\n"
    "s_waitcnt lgkmcnt(4)\n"
    "v_mfma_f32_16x16x32_bf16 v[152:155], v[40:43], a[40:43], v[152:155]\n"
    "ds_read_b64_tr_b16 v[24:25], v14 offset:16384\n"
    "v_mfma_f32_16x16x32_bf16 v[160:163], v[40:43], a[104:107], v[160:163]\n"
    "ds_read_b64_tr_b16 v[26:27], v14 offset:18432\n"
    "buffer_store_dwordx4 v[184:187], v20, s[48:51], s26 offen offset:2048 sc1\n"
    "v_mfma_f32_16x16x32_bf16 v[168:171], v[40:43], a[168:171], v[168:171]\n"
    "ds_read_b64_tr_b16 v[28:29], v15 offset:16384\n"
    "v_mfma_f32_16x16x32_bf16 v[176:179], v[40:43], a[232:235], v[176:179]\n"
    "ds_read_b64_tr_b16 v[30:31], v15 offset:18432\n"
    "v_mfma_f32_16x16x32_bf16 v[156:159], v[44:47], a[40:43], v[156:159]\n"
    "ds_read_b64_tr_b16 v[32:33], v14 offset:20480\n"
    "v_cvt_pk_bf16_f32 v188, v128, v129\n"
    "v_mfma_f32_16x16x32_bf16 v[164:167], v[44:47], a[104:107], v[164:167]\n"
    "ds_read_b64_tr_b16 v[34:35], v14 offset:22528\n"
    "v_mfma_f32_16x16x32_bf16 v[172:175], v[44:47], a[168:171], v[172:175]\n"
    "ds_read_b64_tr_b16 v[36:37], v15 offset:20480\n"
    "v_mfma_f32_16x16x32_bf16 v[180:183], v[44:47], a[232:235], v[180:183]\n"
    "ds_read_b64_tr_b16 v[38:39], v15 offset:22528\n"
    "v_cvt_pk_bf16_f32 v189, v130, v131\n"
    "s_waitcnt lgkmcnt(8)\n"
    "v_mfma_f32_16x16x32_bf16 v[152:155], v[48:51], a[44:47], v[152:155]\n"
    "ds_read_b64_tr_b16 v[40:41], v14 offset:24576\n"
    "v_mfma_f32_16x16x32_bf16 v[160:163], v[48:51], a[108:111], v[160:163]\n"
    "ds_read_b64_tr_b16 v[42:43], v14 offset:26624\n"
    "v_cvt_pk_bf16_f32 v190, v132, v133\n"
    "v_mfma_f32_16x16x32_bf16 v[168:171], v[48:51], a[172:175], v[168:171]\n"
    "v_mfma_f32_16x16x32_bf16 v[176:179], v[48:51], a[236:239], v[176:179]\n"
    "v_mfma_f32_16x16x32_bf16 v[156:159], v[52:55], a[44:47], v[156:159]\n"
    "ds_read_b64_tr_b16 v[44:45], v15 offset:24576\n"
    "v_cvt_pk_bf16_f32 v191, v134, v135\n"
    "v_mfma_f32_16x16x32_bf16 v[164:167], v[52:55], a[108:111], v[164:167]\n"
    "ds_read_b64_tr_b16 v[46:47], v15 offset:26624\n"
    "v_mfma_f32_16x16x32_bf16 v[172:175], v[52:55], a[172:175], v[172:175]\n"
    "v_mfma_f32_16x16x32_bf16 v[180:183], v[52:55], a[236:239], v[180:183]\n"
    "buffer_store_dwordx4 v[188:191], v20, s[48:51], s27 offen offset:2048 sc1\n"
    "s_waitcnt lgkmcnt(8)\n"
    "v_mfma_f32_16x16x32_bf16 v[152:155], v[24:27], a[48:51], v[152:155]\n"
    "ds_read_b64_tr_b16 v[48:49], v14 offset:28672\n"
    "v_mfma_f32_16x16x32_bf16 v[160:163], v[24:27], a[112:115], v[160:163]\n"
    "ds_read_b64_tr_b16 v[50:51], v14 offset:30720\n"
    "v_mfma_f32_16x16x32_bf16 v[168:171], v[24:27], a[176:179], v[168:171]\n"
    "v_cvt_pk_bf16_f32 v192, v136, v137\n"
    "v_mfma_f32_16x16x32_bf16 v[176:179], v[24:27], a[240:243], v[176:179]\n"
    "v_mfma_f32_16x16x32_bf16 v[156:159], v[28:31], a[48:51], v[156:159]\n"
    "ds_read_b64_tr_b16 v[52:53], v15 offset:28672\n"
    "v_mfma_f32_16x16x32_bf16 v[164:167], v[28:31], a[112:115], v[164:167]\n"
    "ds_read_b64_tr_b16 v[54:55], v15 offset:30720\n"
    "v_cvt_pk_bf16_f32 v193, v138, v139\n"
    "v_mfma_f32_16x16x32_bf16 v[172:175], v[28:31], a[176:179], v[172:175]\n"
    "v_mfma_f32_16x16x32_bf16 v[180:183], v[28:31], a[240:243], v[180:183]\n"
    "v_cvt_pk_bf16_f32 v194, v140, v141\n"
    "s_waitcnt lgkmcnt(8)\n"
    "v_mfma_f32_16x16x32_bf16 v[152:155], v[32:35], a[52:55], v[152:155]\n"
    "v_mfma_f32_16x16x32_bf16 v[160:163], v[32:35], a[116:119], v[160:163]\n"
    "v_mfma_f32_16x16x32_bf16 v[168:171], v[32:35], a[180:183], v[168:171]\n"
    "v_cvt_pk_bf16_f32 v195, v142, v143\n"
    "v_mfma_f32_16x16x32_bf16 v[176:179], v[32:35], a[244:247], v[176:179]\n"
    "v_mfma_f32_16x16x32_bf16 v[156:159], v[36:39], a[52:55], v[156:159]\n"
    "v_mfma_f32_16x16x32_bf16 v[164:167], v[36:39], a[116:119], v[164:167]\n"
    "buffer_store_dwordx4 v[192:195], v21, s[48:51], s26 offen offset:2048 sc1\n"
    "v_mfma_f32_16x16x32_bf16 v[172:175], v[36:39], a[180:183], v[172:175]\n"
    "v_mfma_f32_16x16x32_bf16 v[180:183], v[36:39], a[244:247], v[180:183]\n"
    "s_waitcnt lgkmcnt(4)\n"
    "v_mfma_f32_16x16x32_bf16 v[152:155], v[40:43], a[56:59], v[152:155]\n"
    "v_cvt_pk_bf16_f32 v196, v144, v145\n"
    "v_mfma_f32_16x16x32_bf16 v[160:163], v[40:43], a[120:123], v[160:163]\n"
    "v_mfma_f32_16x16x32_bf16 v[168:171], v[40:43], a[184:187], v[168:171]\n"
    "v_cvt_pk_bf16_f32 v197, v146, v147\n"
    "v_mfma_f32_16x16x32_bf16 v[176:179], v[40:43], a[248:251], v[176:179]\n"
    "v_mfma_f32_16x16x32_bf16 v[156:159], v[44:47], a[56:59], v[156:159]\n"
    "v_mfma_f32_16x16x32_bf16 v[164:167], v[44:47], a[120:123], v[164:167]\n"
    "v_cvt_pk_bf16_f32 v198, v148, v149\n"
    "v_mfma_f32_16x16x32_bf16 v[172:175], v[44:47], a[184:187], v[172:175]\n"
    "v_mfma_f32_16x16x32_bf16 v[180:183], v[44:47], a[248:251], v[180:183]\n"
    "s_waitcnt lgkmcnt(0)\n"
    "v_mfma_f32_16x16x32_bf16 v[152:155], v[48:51], a[60:63], v[152:155]\n"
    "v_cvt_pk_bf16_f32 v199, v150, v151\n"
    "v_mfma_f32_16x16x32_bf16 v[160:163], v[48:51], a[124:127], v[160:163]\n"
    "v_mfma_f32_16x16x32_bf16 v[168:171], v[48:51], a[188:191], v[168:171]\n"
    "v_mfma_f32_16x16x32_bf16 v[176:179], v[48:51], a[252:255], v[176:179]\n"
    "buffer_store_dwordx4 v[196:199], v21, s[48:51], s27 offen offset:2048 sc1\n"
    "v_mfma_f32_16x16x32_bf16 v[156:159], v[52:55], a[60:63], v[156:159]\n"
    "v_mfma_f32_16x16x32_bf16 v[164:167], v[52:55], a[124:127], v[164:167]\n"
    "v_mfma_f32_16x16x32_bf16 v[172:175], v[52:55], a[188:191], v[172:175]\n"
    "v_mfma_f32_16x16x32_bf16 v[180:183], v[52:55], a[252:255], v[180:183]\n"
    "s_nop 7\n"
    "v_cvt_pk_bf16_f32 v184, v152, v153\n"
    "v_cvt_pk_bf16_f32 v185, v154, v155\n"
    "v_cvt_pk_bf16_f32 v186, v156, v157\n"
    "v_cvt_pk_bf16_f32 v187, v158, v159\n"
    "buffer_store_dwordx4 v[184:187], v20, s[48:51], s26 offen offset:3072 sc1\n"
    "v_cvt_pk_bf16_f32 v188, v160, v161\n"
    "v_cvt_pk_bf16_f32 v189, v162, v163\n"
    "v_cvt_pk_bf16_f32 v190, v164, v165\n"
    "v_cvt_pk_bf16_f32 v191, v166, v167\n"
    "buffer_store_dwordx4 v[188:191], v20, s[48:51], s27 offen offset:3072 sc1\n"
    "v_cvt_pk_bf16_f32 v192, v168, v169\n"
    "v_cvt_pk_bf16_f32 v193, v170, v171\n"
    "v_cvt_pk_bf16_f32 v194, v172, v173\n"
    "v_cvt_pk_bf16_f32 v195, v174, v175\n"
    "buffer_store_dwordx4 v[192:195], v21, s[48:51], s26 offen offset:3072 sc1\n"
    "v_cvt_pk_bf16_f32 v196, v176, v177\n"
    "v_cvt_pk_bf16_f32 v197, v178, v179\n"
    "v_cvt_pk_bf16_f32 v198, v180, v181\n"
    "v_cvt_pk_bf16_f32 v199, v182, v183\n"
    "buffer_store_dwordx4 v[196:199], v21, s[48:51], s27 offen offset:3072 sc1\n"
      :: "s"(ws + WS_CTX + xcd * 16384), "s"((const char*)A->wo + xcd * 524288 + (j >> 3) * 256), "s"(ws + WS_PART + xcd * 2097152 + (j >> 3) * 4096), "s"(w), "s"(j & 7), "s"(j >> 3),
         "s"(ws + WS_FLAG), "s"(FL_C4 + (xcd * 8 + (j & 7)) * 128), "s"(ldsb), "s"(tag)
      : "v0","v1","v2","v3","v4","v5","v6","v7","v8","v9","v10","v11","v12","v13","v14","v15","v16","v17","v18","v19","v20","v21","v22","v23",
      "v24","v25","v26","v27","v28","v29","v30","v31","v32","v33","v34","v35","v36","v37","v38","v39","v40","v41","v42","v43","v44","v45","v46","v47",
      "v48","v49","v50","v51","v52","v53","v54","v55","v56","v57","v58","v59","v60","v61","v62","v63","v64","v65","v66","v67","v68","v69","v70","v71",
      "v72","v73","v74","v75","v76","v77","v78","v79","v80","v81","v82","v83","v84","v85","v86","v87","v88","v89","v90","v91","v92","v93","v94","v95",
      "v96","v97","v98","v99","v100","v101","v102","v103","v104","v105","v106","v107","v108","v109","v110","v111","v112","v113","v114","v115","v116","v117","v118","v119",
      "v120","v121","v122","v123","v124","v125","v126","v127","v128","v129","v130","v131","v132","v133","v134","v135","v136","v137","v138","v139","v140","v141","v142","v143",
      "v144","v145","v146","v147","v148","v149","v150","v151","v152","v153","v154","v155","v156","v157","v158","v159","v160","v161","v162","v163","v164","v165","v166","v167",
      "v168","v169","v170","v171","v172","v173","v174","v175","v176","v177","v178","v179","v180","v181","v182","v183","v184","v185","v186","v187","v188","v189","v190","v191",
      "v192","v193","v194","v195","v196","v197","v198","v199","a0","a1","a2","a3","a4","a5","a6","a7","a8","a9","a10","a11","a12","a13","a14","a15",
      "a16","a17","a18","a19","a20","a21","a22","a23","a24","a25","a26","a27","a28","a29","a30","a31","a32","a33","a34","a35","a36","a37","a38","a39",
      "a40","a41","a42","a43","a44","a45","a46","a47","a48","a49","a50","a51","a52","a53","a54","a55","a56","a57","a58","a59","a60","a61","a62","a63",
      "a64","a65","a66","a67","a68","a69","a70","a71","a72","a73","a74","a75","a76","a77","a78","a79","a80","a81","a82","a83","a84","a85","a86","a87",
      "a88","a89","a90","a91","a92","a93","a94","a95","a96","a97","a98","a99","a100","a101","a102","a103","a104","a105","a106","a107","a108","a109","a110","a111",
      "a112","a113","a114","a115","a116","a117","a118","a119","a120","a121","a122","a123","a124","a125","a126","a127","a128","a129","a130","a131","a132","a133","a134","a135",
      "a136","a137","a138","a139","a140","a141","a142","a143","a144","a145","a146","a147","a148","a149","a150","a151","a152","a153","a154","a155","a156","a157","a158","a159",
      "a160","a161","a162","a163","a164","a165","a166","a167","a168","a169","a170","a171","a172","a173","a174","a175","a176","a177","a178","a179","a180","a181","a182","a183",
      "a184","a185","a186","a187","a188","a189","a190","a191","a192","a193","a194","a195","a196","a197","a198","a199","a200","a201","a202","a203","a204","a205","a206","a207",
      "a208","a209","a210","a211","a212","a213","a214","a215","a216","a217","a218","a219","a220","a221","a222","a223","a224","a225","a226","a227","a228","a229","a230","a231",
      "a232","a233","a234","a235","a236","a237","a238","a239","a240","a241","a242","a243","a244","a245","a246","a247","a248","a249","a250","a251","a252","a253","a254","a255",
      "s20","s21","s22","s23","s24","s25","s26","s27","s40","s41","s42","s43","s44","s45","s46","s47","s48","s49","s50","s51","s52","s53","s54","s55",
      "s56","s57","s58","s59","s60","s61","s62","s63","s64","s65","s66","s67","scc","vcc","memory");
    STAMP3(); STAMP(4);
#undef DIAGK
  }
  { IDS; TailArgsP A = tail_args(); char* ws = A->ws;
#define DIAGK 2
    STAMP(0);
    asm volatile("s_waitcnt vmcnt(0)\n\ts_barrier" ::: "memory");
    const int lane = lane_now(), rb = R3_RB, half = j & 1;
    if (w == 0 && lane == 0) flag_set(ws, FL_P + (j & 7) * 128 + (xcd * 4 + (j >> 3)) * 4, tag);
    const int c = half * 8 + (lane & 7), g = (lane >> 3) & 3, t0 = w * 4 + (lane >> 5), row = rb * 16 + c;
    u32x2 xr[2][2]; f32x4 ga[2][2], cb[2][2], sb[2][2];
    const __amdgpu_buffer_rsrc_t rp = __builtin_amdgcn_make_buffer_rsrc(ws + WS_P3, 0, 12288, 0x20000);
#pragma unroll
    for (int k = 0; k < 2; ++k)
#pragma unroll
      for (int hb = 0; hb < 2; ++hb) { const int col = (t0 + 2 * k) * 32 + hb * 16 + g * 4;
        xr[k][hb] = *(const u32x2*)(A->x + (size_t)row * 512 + col);
        ga[k][hb] = __builtin_bit_cast(f32x4, __builtin_amdgcn_raw_buffer_load_b128(rp, col * 4, 0, 16));
        cb[k][hb] = __builtin_bit_cast(f32x4, __builtin_amdgcn_raw_buffer_load_b128(rp, 2048 + col * 4, 0, 16));
        sb[k][hb] = __builtin_bit_cast(f32x4, __builtin_amdgcn_raw_buffer_load_b128(rp, 4096 + col * 4, 0, 16)); }
    const __amdgpu_buffer_rsrc_t rq = __builtin_amdgcn_make_buffer_rsrc(ws + WS_PART, 0, 16777216, 0x20000);
    const int fo = (rb * 16 + t0) * 1024 + (g * 16 + c) * 16;
    u32x4 pp[8][2];
    { const unsigned* fl = (const unsigned*)(ws + WS_FLAG + FL_P + ((rb >> 3) < 8 ? (rb >> 3) : 15 - (rb >> 3)) * 128) + (lane & 31);
      unsigned done = 0;
      for (unsigned n = 0x400000; ; --n) {
        unsigned m = (unsigned)__ballot(__hip_atomic_load(fl, __ATOMIC_RELAXED, __HIP_MEMORY_SCOPE_AGENT) == tag || n == 0);
        m &= m >> 2; m &= m >> 1;
        const unsigned rdy = m & 0x11111111u & ~done;
        asm volatile("" ::: "memory");
#pragma unroll
        for (int p = 0; p < 8; ++p) if (rdy & (1u << (4 * p))) {
          pp[p][0] = __builtin_amdgcn_raw_buffer_load_b128(rq, fo, p * 2097152, 16); pp[p][1] = __builtin_amdgcn_raw_buffer_load_b128(rq, fo + 2048, p * 2097152, 16); }
        done |= rdy;
        if (done == 0x11111111u) break;
        if (!rdy) __builtin_amdgcn_s_sleep(4);
      } }
    STAMP(1);
    unsigned sel_lo = 0x00003f80u, sel_hi = 0x3f800000u; asm("" : "+v"(sel_lo), "+v"(sel_hi));
    float x1[16], sm = 0.f;
#pragma unroll
    for (int k = 0; k < 2; ++k)
#pragma unroll
      for (int e = 0; e < 4; ++e) { float lo = 0.f, hi = 0.f;
#pragma unroll
        for (int p = 0; p < 8; ++p) { lo = dot2(pp[p][k][e], sel_lo, lo); hi = dot2(pp[p][k][e], sel_hi, hi); }
        const unsigned xw = xr[k][e >> 1][e & 1]; const int q = k * 8 + e * 2;
        x1[q] = rbf(fmaf(ga[k][e >> 1][(e & 1) * 2], lo, bflo(xw))); x1[q + 1] = rbf(fmaf(ga[k][e >> 1][(e & 1) * 2 + 1], hi, bfhi(xw)));
        sm += x1[q] + x1[q + 1]; }
    sm += dpp<0x128>(sm); sm = xsum(sm);
    const float mt = sm * (1.f / 128.f); float m2 = 0.f;
#pragma unroll
    for (int q = 0; q < 16; ++q) m2 = fmaf(x1[q] - mt, x1[q] - mt, m2);
    m2 += dpp<0x128>(m2); m2 = xsum(m2);
    LDSP f32x2* st = (LDSP f32x2*)(lds + 131072);
    if (lane < 8) st[lane * 4 + w] = f32x2{sm, m2};
#pragma unroll
    for (int k = 0; k < 2; ++k) st_wt(ws + WS_X1 + fo + k * 2048, u32x4{pk2(x1[k * 8], x1[k * 8 + 1]), pk2(x1[k * 8 + 2], x1[k * 8 + 3]), pk2(x1[k * 8 + 4], x1[k * 8 + 5]), pk2(x1[k * 8 + 6], x1[k * 8 + 7])});
    wg_barrier();
    const f32x4 s01 = *(LDSP const f32x4*)(st + (lane & 7) * 4), s23 = *(LDSP const f32x4*)(st + (lane & 7) * 4 + 2);
    const float mean = ((s01[0] + s01[2]) + (s23[0] + s23[2])) * (1.f / 512.f);
    const float d0 = s01[0] * (1.f / 128.f) - mean, d1 = s01[2] * (1.f / 128.f) - mean, d2 = s23[0] * (1.f / 128.f) - mean, d3 = s23[2] * (1.f / 128.f) - mean;
    const float var = (((s01[1] + s01[3]) + (s23[1] + s23[3])) + 128.f * ((d0 * d0 + d1 * d1) + (d2 * d2 + d3 * d3))) * (1.f / 512.f);
    const float rs = rsqrtf(var + 1e-5f);
#pragma unroll
    for (int k = 0; k < 2; ++k) { u32x4 o;
#pragma unroll
      for (int e = 0; e < 4; ++e) { const int q = k * 8 + e * 2, hb = e >> 1, p = (e & 1) * 2;
        o[e] = pk2(fmaf((x1[q] - mean) * rs, cb[k][hb][p], sb[k][hb][p]), fmaf((x1[q + 1] - mean) * rs, cb[k][hb][p + 1], sb[k][hb][p + 1])); }
      st_wt(ws + WS_A2 + (size_t)(((rb >> 3) * 16 + t0 + 2 * k) * 8 + (rb & 7)) * 1024 + (g * 16 + c) * 16, o); }
    STAMP(2);
#undef DIAGK
  }
  { IDS; TailArgsP A = tail_args(); char* ws = A->ws;
    const int arr = FL_A + xcd * 128 + j * 4;
#define DIAGK 3
    STAMP(0);
    if (j < 28) {
      const int rt = K4_RT, ct = K4_CT;
      asm volatile(
    "s_mov_b64 s[40:41], %0\n"
    "s_and_b32 s41, s41, 0xffff\n"
    "s_mov_b32 s42, 262144\n"
    "s_mov_b32 s43, 0x20000\n"
    "s_mov_b64 s[48:49], %1\n"
    "s_and_b32 s49, s49, 0xffff\n"
    "s_mov_b32 s50, 917504\n"
    "s_mov_b32 s51, 0x20000\n"
    "s_mov_b32 s20, %2\n"
    "s_mov_b64 s[66:67], %3\n"
    "v_mbcnt_lo_u32_b32 v16, -1, 0\n"
    "v_mbcnt_hi_u32_b32 v16, -1, v16\n"
    "s_mov_b64 s[44:45], %7\n"
    "s_and_b32 s45, s45, 0xffff\n"
    "s_mov_b32 s46, 1831552\n"
    "s_mov_b32 s47, 0x20000\n"
    "s_mov_b64 s[52:53], %8\n"
    "s_and_b32 s53, s53, 0xffff\n"
    "s_mov_b32 s54, 1831552\n"
    "s_mov_b32 s55, 0x20000\n"
    "v_lshrrev_b32 v17, 3, v16\n"
    "s_lshl_b32 s23, s20, 5\n"
    "v_add_u32 v17, s23, v17\n"
    "s_mov_b32 s23, 3584\n"
    "v_mul_lo_u32 v17, v17, s23\n"
    "v_lshrrev_b32 v18, 4, v16\n"
    "v_lshlrev_b32 v18, 1, v18\n"
    "v_and_b32 v19, 7, v16\n"
    "v_xor_b32 v18, v18, v19\n"
    "v_lshl_add_u32 v4, v18, 4, v17\n"
    "v_add_u32 v5, 27648, v4\n"
    "v_add_u32 v6, 55296, v4\n"
    "v_add_u32 v7, 82944, v4\n"
    "s_lshl_b32 s21, s20, 12\n"
    "s_mov_b32 s22, 0\n"
    "s_add_u32 m0, s21, 0\n"
    "s_nop 0\n"
    "buffer_load_dwordx4 v4, s[44:47], s22 offen offset:0 lds\n"
    "buffer_load_dwordx4 v5, s[44:47], s22 offen offset:1024 lds\n"
    "buffer_load_dwordx4 v6, s[44:47], s22 offen offset:2048 lds\n"
    "buffer_load_dwordx4 v7, s[44:47], s22 offen offset:3072 lds\n"
    "s_mov_b32 s22, 0\n"
    "s_add_u32 m0, s21, 16384\n"
    "s_nop 0\n"
    "buffer_load_dwordx4 v4, s[52:55], s22 offen offset:0 lds\n"
    "buffer_load_dwordx4 v5, s[52:55], s22 offen offset:1024 lds\n"
    "buffer_load_dwordx4 v6, s[52:55], s22 offen offset:2048 lds\n"
    "buffer_load_dwordx4 v7, s[52:55], s22 offen offset:3072 lds\n"
    "s_lshr_b32 s23, s20, 1\n"
    "s_lshl_b32 s23, s23, 17\n"
    "s_and_b32 s24, s20, 1\n"
    "s_lshl_b32 s24, s24, 12\n"
    "s_add_u32 s23, s23, s24\n"
    "v_lshlrev_b32 v17, 4, v16\n"
    "v_add_u32 v0, s23, v17\n"
    "v_add_u32 v1, 2048, v0\n"
    "s_mul_i32 s23, s20, 229376\n"
    "v_add_u32 v20, s23, v17\n"
    "v_lshrrev_b32 v17, 2, v16\n"
    "v_lshlrev_b32 v17, 7, v17\n"
    "v_and_b32 v18, 3, v16\n"
    "v_lshl_add_u32 v17, v18, 3, v17\n"
    "v_bfe_u32 v18, v16, 3, 2\n"
    "v_lshlrev_b32 v18, 5, v18\n"
    "v_xor_b32 v19, 0, v18\n"
    "v_add_u32 v8, v17, v19\n"
    "v_add_u32 v12, 0x10000, v8\n"
    "v_xor_b32 v19, 32, v18\n"
    "v_add_u32 v9, v17, v19\n"
    "v_add_u32 v13, 0x10000, v9\n"
    "v_xor_b32 v19, 64, v18\n"
    "v_add_u32 v10, v17, v19\n"
    "v_add_u32 v14, 0x10000, v10\n"
    "v_xor_b32 v19, 96, v18\n"
    "v_add_u32 v11, v17, v19\n"
    "v_add_u32 v15, 0x10000, v11\n"
    "s_waitcnt vmcnt(8)\n"
    "s_barrier\n"
    "s_cmp_lg_u32 s20, 0\n"
    "s_cbranch_scc1 L_ar_%=\n"
    "v_mov_b32 v24, %4\n"
    "v_mov_b32 v25, %10\n"
    "s_mov_b64 exec, 1\n"
    "global_atomic_swap v24, v25, s[66:67]\n"
    "s_mov_b64 exec, -1\n"
    "L_ar_%=:\n"
    "s_mov_b32 s22, 458752\n"
    "s_add_u32 m0, s21, 32768\n"
    "s_nop 0\n"
    "buffer_load_dwordx4 v4, s[44:47], s22 offen offset:0 lds\n"
    "buffer_load_dwordx4 v5, s[44:47], s22 offen offset:1024 lds\n"
    "buffer_load_dwordx4 v6, s[44:47], s22 offen offset:2048 lds\n"
    "buffer_load_dwordx4 v7, s[44:47], s22 offen offset:3072 lds\n"
    "s_mov_b32 s22, 458752\n"
    "s_add_u32 m0, s21, 49152\n"
    "s_nop 0\n"
    "buffer_load_dwordx4 v4, s[52:55], s22 offen offset:0 lds\n"
    "buffer_load_dwordx4 v5, s[52:55], s22 offen offset:1024 lds\n"
    "buffer_load_dwordx4 v6, s[52:55], s22 offen offset:2048 lds\n"
    "buffer_load_dwordx4 v7, s[52:55], s22 offen offset:3072 lds\n"
    "s_mov_b32 s22, 917504\n"
    "s_add_u32 m0, s21, 65536\n"
    "s_nop 0\n"
    "buffer_load_dwordx4 v4, s[44:47], s22 offen offset:0 lds\n"
    "buffer_load_dwordx4 v5, s[44:47], s22 offen offset:1024 lds\n"
    "buffer_load_dwordx4 v6, s[44:47], s22 offen offset:2048 lds\n"
    "buffer_load_dwordx4 v7, s[44:47], s22 offen offset:3072 lds\n"
    "s_mov_b32 s22, 917504\n"
    "s_add_u32 m0, s21, 81920\n"
    "s_nop 0\n"
    "buffer_load_dwordx4 v4, s[52:55], s22 offen offset:0 lds\n"
    "buffer_load_dwordx4 v5, s[52:55], s22 offen offset:1024 lds\n"
    "buffer_load_dwordx4 v6, s[52:55], s22 offen offset:2048 lds\n"
    "buffer_load_dwordx4 v7, s[52:55], s22 offen offset:3072 lds\n"
    "s_mov_b32 s22, 1376256\n"
    "s_add_u32 m0, s21, 98304\n"
    "s_nop 0\n"
    "buffer_load_dwordx4 v4, s[44:47], s22 offen offset:0 lds\n"
    "buffer_load_dwordx4 v5, s[44:47], s22 offen offset:1024 lds\n"
    "buffer_load_dwordx4 v6, s[44:47], s22 offen offset:2048 lds\n"
    "buffer_load_dwordx4 v7, s[44:47], s22 offen offset:3072 lds\n"
    "s_mov_b32 s22, 1376256\n"
    "s_add_u32 m0, s21, 114688\n"
    "s_nop 0\n"
    "buffer_load_dwordx4 v4, s[52:55], s22 offen offset:0 lds\n"
    "buffer_load_dwordx4 v5, s[52:55], s22 offen offset:1024 lds\n"
    "buffer_load_dwordx4 v6, s[52:55], s22 offen offset:2048 lds\n"
    "buffer_load_dwordx4 v7, s[52:55], s22 offen offset:3072 lds\n"
    "s_cmp_lg_u32 s20, 0\n"
    "s_cbranch_scc1 L_po_%=_x\n"
    "v_min_u32 v24, 31, v16\n"
    "v_lshlrev_b32 v24, 2, v24\n"
    "v_add_u32 v24, %5, v24\n"
    "global_load_dword v25, v24, s[66:67] sc1\n"
    "s_waitcnt vmcnt(0)\n"
    "v_cmp_ne_u32 vcc, %10, v25\n"
    "s_mov_b32 s23, 0x400000\n"
    "s_nop 1\n"
    "s_cbranch_vccz L_po_%=_x\n"
    "L_po_%=:\n"
    "s_sleep 2\n"
    "global_load_dword v25, v24, s[66:67] sc1\n"
    "s_waitcnt vmcnt(0)\n"
    "v_cmp_ne_u32 vcc, %10, v25\n"
    "s_sub_u32 s23, s23, 1\n"
    "s_cbranch_scc1 L_po_%=_x\n"
    "s_nop 0\n"
    "s_cbranch_vccnz L_po_%=\n"
    "L_po_%=_x:\n"
    "s_waitcnt vmcnt(0)\n"
    "s_barrier\n"
    "s_mov_b32 s25, 0\n"
    "buffer_load_dwordx4 a[0:3], v0, s[40:43], s25 offen offset:0 sc1\n"
    "buffer_load_dwordx4 a[64:67], v0, s[40:43], s25 offen offset:1024 sc1\n"
    "buffer_load_dwordx4 a[128:131], v1, s[40:43], s25 offen offset:0 sc1\n"
    "buffer_load_dwordx4 a[192:195], v1, s[40:43], s25 offen offset:1024 sc1\n"
    "s_mov_b32 s25, 8192\n"
    "buffer_load_dwordx4 a[4:7], v0, s[40:43], s25 offen offset:0 sc1\n"
    "buffer_load_dwordx4 a[68:71], v0, s[40:43], s25 offen offset:1024 sc1\n"
    "buffer_load_dwordx4 a[132:135], v1, s[40:43], s25 offen offset:0 sc1\n"
    "buffer_load_dwordx4 a[196:199], v1, s[40:43], s25 offen offset:1024 sc1\n"
    "s_mov_b32 s25, 16384\n"
    "buffer_load_dwordx4 a[8:11], v0, s[40:43], s25 offen offset:0 sc1\n"
    "buffer_load_dwordx4 a[72:75], v0, s[40:43], s25 offen offset:1024 sc1\n"
    "buffer_load_dwordx4 a[136:139], v1, s[40:43], s25 offen offset:0 sc1\n"
    "buffer_load_dwordx4 a[200:203], v1, s[40:43], s25 offen offset:1024 sc1\n"
    "s_waitcnt vmcnt(12) lgkmcnt(0)\n"
    "s_barrier\n"
    "ds_read_b64_tr_b16 v[24:25], v8 offset:0\n"
    "ds_read_b64_tr_b16 v[26:27], v8 offset:2048\n"
    "ds_read_b64_tr_b16 v[28:29], v9 offset:0\n"
    "ds_read_b64_tr_b16 v[30:31], v9 offset:2048\n"
    "ds_read_b64_tr_b16 v[32:33], v8 offset:16384\n"
    "ds_read_b64_tr_b16 v[34:35], v8 offset:18432\n"
    "ds_read_b64_tr_b16 v[36:37], v9 offset:16384\n"
    "ds_read_b64_tr_b16 v[38:39], v9 offset:18432\n"
    "ds_read_b64_tr_b16 v[40:41], v8 offset:4096\n"
    "ds_read_b64_tr_b16 v[42:43], v8 offset:6144\n"
    "ds_read_b64_tr_b16 v[44:45], v9 offset:4096\n"
    "ds_read_b64_tr_b16 v[46:47], v9 offset:6144\n"
    "s_waitcnt vmcnt(8)\n"
    "s_waitcnt lgkmcnt(8)\n"
    "v_mfma_f32_16x16x32_bf16 v[56:59], v[24:27], a[0:3], 0\n"
    "ds_read_b64_tr_b16 v[48:49], v8 offset:20480\n"
    "s_mov_b32 s25, 24576\n"
    "v_mfma_f32_16x16x32_bf16 v[88:91], v[24:27], a[64:67], 0\n"
    "ds_read_b64_tr_b16 v[50:51], v8 offset:22528\n"
    "buffer_load_dwordx4 a[12:15], v0, s[40:43], s25 offen offset:0 sc1\n"
    "v_mfma_f32_16x16x32_bf16 v[120:123], v[24:27], a[128:131], 0\n"
    "v_mfma_f32_16x16x32_bf16 v[152:155], v[24:27], a[192:195], 0\n"
    "v_mfma_f32_16x16x32_bf16 v[60:63], v[28:31], a[0:3], 0\n"
    "ds_read_b64_tr_b16 v[52:53], v9 offset:20480\n"
    "buffer_load_dwordx4 a[76:79], v0, s[40:43], s25 offen offset:1024 sc1\n"
    "v_mfma_f32_16x16x32_bf16 v[92:95], v[28:31], a[64:67], 0\n"
    "ds_read_b64_tr_b16 v[54:55], v9 offset:22528\n"
    "v_mfma_f32_16x16x32_bf16 v[124:127], v[28:31], a[128:131], 0\n"
    "v_mfma_f32_16x16x32_bf16 v[156:159], v[28:31], a[192:195], 0\n"
    "buffer_load_dwordx4 a[140:143], v1, s[40:43], s25 offen offset:0 sc1\n"
    "s_waitcnt lgkmcnt(8)\n"
    "v_mfma_f32_16x16x32_bf16 v[72:75], v[32:35], a[0:3], 0\n"
    "ds_read_b64_tr_b16 v[24:25], v8 offset:8192\n"
    "v_mfma_f32_16x16x32_bf16 v[104:107], v[32:35], a[64:67], 0\n"
    "ds_read_b64_tr_b16 v[26:27], v8 offset:10240\n"
    "v_mfma_f32_16x16x32_bf16 v[136:139], v[32:35], a[128:131], 0\n"
    "buffer_load_dwordx4 a[204:207], v1, s[40:43], s25 offen offset:1024 sc1\n"
    "v_mfma_f32_16x16x32_bf16 v[168:171], v[32:35], a[192:195], 0\n"
    "s_mov_b32 s25, 32768\n"
    "v_mfma_f32_16x16x32_bf16 v[76:79], v[36:39], a[0:3], 0\n"
    "ds_read_b64_tr_b16 v[28:29], v9 offset:8192\n"
    "v_mfma_f32_16x16x32_bf16 v[108:111], v[36:39], a[64:67], 0\n"
    "ds_read_b64_tr_b16 v[30:31], v9 offset:10240\n"
    "buffer_load_dwordx4 a[16:19], v0, s[40:43], s25 offen offset:0 sc1\n"
    "v_mfma_f32_16x16x32_bf16 v[140:143], v[36:39], a[128:131], 0\n"
    "v_mfma_f32_16x16x32_bf16 v[172:175], v[36:39], a[192:195], 0\n"
    "s_waitcnt vmcnt(9)\n"
    "s_waitcnt lgkmcnt(8)\n"
    "v_mfma_f32_16x16x32_bf16 v[56:59], v[40:43], a[4:7], v[56:59]\n"
    "ds_read_b64_tr_b16 v[32:33], v8 offset:24576\n"
    "buffer_load_dwordx4 a[80:83], v0, s[40:43], s25 offen offset:1024 sc1\n"
    "v_mfma_f32_16x16x32_bf16 v[88:91], v[40:43], a[68:71], v[88:91]\n"
    "ds_read_b64_tr_b16 v[34:35], v8 offset:26624\n"
    "v_mfma_f32_16x16x32_bf16 v[120:123], v[40:43], a[132:135], v[120:123]\n"
    "v_mfma_f32_16x16x32_bf16 v[152:155], v[40:43], a[196:199], v[152:155]\n"
    "buffer_load_dwordx4 a[144:147], v1, s[40:43], s25 offen offset:0 sc1\n"
    "v_mfma_f32_16x16x32_bf16 v[60:63], v[44:47], a[4:7], v[60:63]\n"
    "ds_read_b64_tr_b16 v[36:37], v9 offset:24576\n"
    "v_mfma_f32_16x16x32_bf16 v[92:95], v[44:47], a[68:71], v[92:95]\n"
    "ds_read_b64_tr_b16 v[38:39], v9 offset:26624\n"
    "v_mfma_f32_16x16x32_bf16 v[124:127], v[44:47], a[132:135], v[124:127]\n"
    "buffer_load_dwordx4 a[208:211], v1, s[40:43], s25 offen offset:1024 sc1\n"
    "v_mfma_f32_16x16x32_bf16 v[156:159], v[44:47], a[196:199], v[156:159]\n"
    "s_mov_b32 s25, 40960\n"
    "s_waitcnt lgkmcnt(8)\n"
    "v_mfma_f32_16x16x32_bf16 v[72:75], v[48:51], a[4:7], v[72:75]\n"
    "ds_read_b64_tr_b16 v[40:41], v8 offset:12288\n"
    "v_mfma_f32_16x16x32_bf16 v[104:107], v[48:51], a[68:71], v[104:107]\n"
    "ds_read_b64_tr_b16 v[42:43], v8 offset:14336\n"
    "buffer_load_dwordx4 a[20:23], v0, s[40:43], s25 offen offset:0 sc1\n"
    "v_mfma_f32_16x16x32_bf16 v[136:139], v[48:51], a[132:135], v[136:139]\n"
    "v_mfma_f32_16x16x32_bf16 v[168:171], v[48:51], a[196:199], v[168:171]\n"
    "v_mfma_f32_16x16x32_bf16 v[76:79], v[52:55], a[4:7], v[76:79]\n"
    "ds_read_b64_tr_b16 v[44:45], v9 offset:12288\n"
    "buffer_load_dwordx4 a[84:87], v0, s[40:43], s25 offen offset:1024 sc1\n"
    "v_mfma_f32_16x16x32_bf16 v[108:111], v[52:55], a[68:71], v[108:111]\n"
    "ds_read_b64_tr_b16 v[46:47], v9 offset:14336\n"
    "v_mfma_f32_16x16x32_bf16 v[140:143], v[52:55], a[132:135], v[140:143]\n"
    "v_mfma_f32_16x16x32_bf16 v[172:175], v[52:55], a[196:199], v[172:175]\n"
    "buffer_load_dwordx4 a[148:151], v1, s[40:43], s25 offen offset:0 sc1\n"
    "s_waitcnt vmcnt(11)\n"
    "s_waitcnt lgkmcnt(8)\n"
    "v_mfma_f32_16x16x32_bf16 v[56:59], v[24:27], a[8:11], v[56:59]\n"
    "ds_read_b64_tr_b16 v[48:49], v8 offset:28672\n"
    "v_mfma_f32_16x16x32_bf16 v[88:91], v[24:27], a[72:75], v[88:91]\n"
    "ds_read_b64_tr_b16 v[50:51], v8 offset:30720\n"
    "v_mfma_f32_16x16x32_bf16 v[120:123], v[24:27], a[136:139], v[120:123]\n"
    "buffer_load_dwordx4 a[212:215], v1, s[40:43], s25 offen offset:1024 sc1\n"
    "v_mfma_f32_16x16x32_bf16 v[152:155], v[24:27], a[200:203], v[152:155]\n"
    "s_mov_b32 s25, 49152\n"
    "v_mfma_f32_16x16x32_bf16 v[60:63], v[28:31], a[8:11], v[60:63]\n"
    "ds_read_b64_tr_b16 v[52:53], v9 offset:28672\n"
    "v_mfma_f32_16x16x32_bf16 v[92:95], v[28:31], a[72:75], v[92:95]\n"
    "ds_read_b64_tr_b16 v[54:55], v9 offset:30720\n"
    "buffer_load_dwordx4 a[24:27], v0, s[40:43], s25 offen offset:0 sc1\n"
    "v_mfma_f32_16x16x32_bf16 v[124:127], v[28:31], a[136:139], v[124:127]\n"
    "v_mfma_f32_16x16x32_bf16 v[156:159], v[28:31], a[200:203], v[156:159]\n"
    "s_waitcnt lgkmcnt(8)\n"
    "v_mfma_f32_16x16x32_bf16 v[72:75], v[32:35], a[8:11], v[72:75]\n"
    "buffer_load_dwordx4 a[88:91], v0, s[40:43], s25 offen offset:1024 sc1\n"
    "v_mfma_f32_16x16x32_bf16 v[104:107], v[32:35], a[72:75], v[104:107]\n"
    "v_mfma_f32_16x16x32_bf16 v[136:139], v[32:35], a[136:139], v[136:139]\n"
    "v_mfma_f32_16x16x32_bf16 v[168:171], v[32:35], a[200:203], v[168:171]\n"
    "buffer_load_dwordx4 a[152:155], v1, s[40:43], s25 offen offset:0 sc1\n"
    "v_mfma_f32_16x16x32_bf16 v[76:79], v[36:39], a[8:11], v[76:79]\n"
    "v_mfma_f32_16x16x32_bf16 v[108:111], v[36:39], a[72:75], v[108:111]\n"
    "v_mfma_f32_16x16x32_bf16 v[140:143], v[36:39], a[136:139], v[140:143]\n"
    "buffer_load_dwordx4 a[216:219], v1, s[40:43], s25 offen offset:1024 sc1\n"
    "v_mfma_f32_16x16x32_bf16 v[172:175], v[36:39], a[200:203], v[172:175]\n"
    "s_mov_b32 s25, 57344\n"
    "s_waitcnt vmcnt(12)\n"
    "s_waitcnt lgkmcnt(4)\n"
    "v_mfma_f32_16x16x32_bf16 v[56:59], v[40:43], a[12:15], v[56:59]\n"
    "ds_read_b64_tr_b16 v[24:25], v8 offset:32768\n"
    "v_mfma_f32_16x16x32_bf16 v[88:91], v[40:43], a[76:79], v[88:91]\n"
    "ds_read_b64_tr_b16 v[26:27], v8 offset:34816\n"
    "buffer_load_dwordx4 a[28:31], v0, s[40:43], s25 offen offset:0 sc1\n"
    "v_mfma_f32_16x16x32_bf16 v[120:123], v[40:43], a[140:143], v[120:123]\n"
    "ds_read_b64_tr_b16 v[28:29], v9 offset:32768\n"
    "v_mfma_f32_16x16x32_bf16 v[152:155], v[40:43], a[204:207], v[152:155]\n"
    "ds_read_b64_tr_b16 v[30:31], v9 offset:34816\n"
    "v_mfma_f32_16x16x32_bf16 v[60:63], v[44:47], a[12:15], v[60:63]\n"
    "ds_read_b64_tr_b16 v[32:33], v8 offset:49152\n"
    "buffer_load_dwordx4 a[92:95], v0, s[40:43], s25 offen offset:1024 sc1\n"
    "v_mfma_f32_16x16x32_bf16 v[92:95], v[44:47], a[76:79], v[92:95]\n"
    "ds_read_b64_tr_b16 v[34:35], v8 offset:51200\n"
    "v_mfma_f32_16x16x32_bf16 v[124:127], v[44:47], a[140:143], v[124:127]\n"
    "ds_read_b64_tr_b16 v[36:37], v9 offset:49152\n"
    "v_mfma_f32_16x16x32_bf16 v[156:159], v[44:47], a[204:207], v[156:159]\n"
    "ds_read_b64_tr_b16 v[38:39], v9 offset:51200\n"
    "buffer_load_dwordx4 a[156:159], v1, s[40:43], s25 offen offset:0 sc1\n"
    "s_waitcnt lgkmcnt(8)\n"
    "v_mfma_f32_16x16x32_bf16 v[72:75], v[48:51], a[12:15], v[72:75]\n"
    "ds_read_b64_tr_b16 v[40:41], v8 offset:36864\n"
    "v_mfma_f32_16x16x32_bf16 v[104:107], v[48:51], a[76:79], v[104:107]\n"
    "ds_read_b64_tr_b16 v[42:43], v8 offset:38912\n"
    "v_mfma_f32_16x16x32_bf16 v[136:139], v[48:51], a[140:143], v[136:139]\n"
    "buffer_load_dwordx4 a[220:223], v1, s[40:43], s25 offen offset:1024 sc1\n"
    "v_mfma_f32_16x16x32_bf16 v[168:171], v[48:51], a[204:207], v[168:171]\n"
    "s_mov_b32 s25, 65536\n"
    "v_mfma_f32_16x16x32_bf16 v[76:79], v[52:55], a[12:15], v[76:79]\n"
    "ds_read_b64_tr_b16 v[44:45], v9 offset:36864\n"
    "v_mfma_f32_16x16x32_bf16 v[108:111], v[52:55], a[76:79], v[108:111]\n"
    "ds_read_b64_tr_b16 v[46:47], v9 offset:38912\n"
    "buffer_load_dwordx4 a[32:35], v0, s[40:43], s25 offen offset:0 sc1\n"
    "v_mfma_f32_16x16x32_bf16 v[140:143], v[52:55], a[140:143], v[140:143]\n"
    "v_mfma_f32_16x16x32_bf16 v[172:175], v[52:55], a[204:207], v[172:175]\n"
    "s_waitcnt vmcnt(13)\n"
    "s_waitcnt lgkmcnt(8)\n"
    "v_mfma_f32_16x16x32_bf16 v[56:59], v[24:27], a[16:19], v[56:59]\n"
    "ds_read_b64_tr_b16 v[48:49], v8 offset:53248\n"
    "buffer_load_dwordx4 a[96:99], v0, s[40:43], s25 offen offset:1024 sc1\n"
    "v_mfma_f32_16x16x32_bf16 v[88:91], v[24:27], a[80:83], v[88:91]\n"
    "ds_read_b64_tr_b16 v[50:51], v8 offset:55296\n"
    "v_mfma_f32_16x16x32_bf16 v[120:123], v[24:27], a[144:147], v[120:123]\n"
    "v_mfma_f32_16x16x32_bf16 v[152:155], v[24:27], a[208:211], v[152:155]\n"
    "buffer_load_dwordx4 a[160:163], v1, s[40:43], s25 offen offset:0 sc1\n"
    "v_mfma_f32_16x16x32_bf16 v[60:63], v[28:31], a[16:19], v[60:63]\n"
    "ds_read_b64_tr_b16 v[52:53], v9 offset:53248\n"
    "v_mfma_f32_16x16x32_bf16 v[92:95], v[28:31], a[80:83], v[92:95]\n"
    "ds_read_b64_tr_b16 v[54:55], v9 offset:55296\n"
    "v_mfma_f32_16x16x32_bf16 v[124:127], v[28:31], a[144:147], v[124:127]\n"
    "buffer_load_dwordx4 a[224:227], v1, s[40:43], s25 offen offset:1024 sc1\n"
    "v_mfma_f32_16x16x32_bf16 v[156:159], v[28:31], a[208:211], v[156:159]\n"
    "s_mov_b32 s25, 73728\n"
    "s_waitcnt lgkmcnt(8)\n"
    "v_mfma_f32_16x16x32_bf16 v[72:75], v[32:35], a[16:19], v[72:75]\n"
    "ds_read_b64_tr_b16 v[24:25], v8 offset:40960\n"
    "v_mfma_f32_16x16x32_bf16 v[104:107], v[32:35], a[80:83], v[104:107]\n"
    "ds_read_b64_tr_b16 v[26:27], v8 offset:43008\n"
    "buffer_load_dwordx4 a[36:39], v0, s[40:43], s25 offen offset:0 sc1\n"
    "v_mfma_f32_16x16x32_bf16 v[136:139], v[32:35], a[144:147], v[136:139]\n"
    "v_mfma_f32_16x16x32_bf16 v[168:171], v[32:35], a[208:211], v[168:171]\n"
    "v_mfma_f32_16x16x32_bf16 v[76:79], v[36:39], a[16:19], v[76:79]\n"
    "ds_read_b64_tr_b16 v[28:29], v9 offset:40960\n"
    "buffer_load_dwordx4 a[100:103], v0, s[40:43], s25 offen offset:1024 sc1\n"
    "v_mfma_f32_16x16x32_bf16 v[108:111], v[36:39], a[80:83], v[108:111]\n"
    "ds_read_b64_tr_b16 v[30:31], v9 offset:43008\n"
    "v_mfma_f32_16x16x32_bf16 v[140:143], v[36:39], a[144:147], v[140:143]\n"
    "v_mfma_f32_16x16x32_bf16 v[172:175], v[36:39], a[208:211], v[172:175]\n"
    "buffer_load_dwordx4 a[164:167], v1, s[40:43], s25 offen offset:0 sc1\n"
    "s_waitcnt vmcnt(15)\n"
    "s_waitcnt lgkmcnt(8)\n"
    "v_mfma_f32_16x16x32_bf16 v[56:59], v[40:43], a[20:23], v[56:59]\n"
    "ds_read_b64_tr_b16 v[32:33], v8 offset:57344\n"
    "v_mfma_f32_16x16x32_bf16 v[88:91], v[40:43], a[84:87], v[88:91]\n"
    "ds_read_b64_tr_b16 v[34:35], v8 offset:59392\n"
    "v_mfma_f32_16x16x32_bf16 v[120:123], v[40:43], a[148:151], v[120:123]\n"
    "buffer_load_dwordx4 a[228:231], v1, s[40:43], s25 offen offset:1024 sc1\n"
    "v_mfma_f32_16x16x32_bf16 v[152:155], v[40:43], a[212:215], v[152:155]\n"
    "s_mov_b32 s25, 81920\n"
    "v_mfma_f32_16x16x32_bf16 v[60:63], v[44:47], a[20:23], v[60:63]\n"
    "ds_read_b64_tr_b16 v[36:37], v9 offset:57344\n"
    "v_mfma_f32_16x16x32_bf16 v[92:95], v[44:47], a[84:87], v[92:95]\n"
    "ds_read_b64_tr_b16 v[38:39], v9 offset:59392\n"
    "buffer_load_dwordx4 a[40:43], v0, s[40:43], s25 offen offset:0 sc1\n"
    "v_mfma_f32_16x16x32_bf16 v[124:127], v[44:47], a[148:151], v[124:127]\n"
    "v_mfma_f32_16x16x32_bf16 v[156:159], v[44:47], a[212:215], v[156:159]\n"
    "s_waitcnt lgkmcnt(8)\n"
    "v_mfma_f32_16x16x32_bf16 v[72:75], v[48:51], a[20:23], v[72:75]\n"
    "ds_read_b64_tr_b16 v[40:41], v8 offset:45056\n"
    "buffer_load_dwordx4 a[104:107], v0, s[40:43], s25 offen offset:1024 sc1\n"
    "v_mfma_f32_16x16x32_bf16 v[104:107], v[48:51], a[84:87], v[104:107]\n"
    "ds_read_b64_tr_b16 v[42:43], v8 offset:47104\n"
    "v_mfma_f32_16x16x32_bf16 v[136:139], v[48:51], a[148:151], v[136:139]\n"
    "v_mfma_f32_16x16x32_bf16 v[168:171], v[48:51], a[212:215], v[168:171]\n"
    "buffer_load_dwordx4 a[168:171], v1, s[40:43], s25 offen offset:0 sc1\n"
    "v_mfma_f32_16x16x32_bf16 v[76:79], v[52:55], a[20:23], v[76:79]\n"
    "ds_read_b64_tr_b16 v[44:45], v9 offset:45056\n"
    "v_mfma_f32_16x16x32_bf16 v[108:111], v[52:55], a[84:87], v[108:111]\n"
    "ds_read_b64_tr_b16 v[46:47], v9 offset:47104\n"
    "v_mfma_f32_16x16x32_bf16 v[140:143], v[52:55], a[148:151], v[140:143]\n"
    "buffer_load_dwordx4 a[232:235], v1, s[40:43], s25 offen offset:1024 sc1\n"
    "v_mfma_f32_16x16x32_bf16 v[172:175], v[52:55], a[212:215], v[172:175]\n"
    "s_mov_b32 s25, 90112\n"
    "s_waitcnt vmcnt(16)\n"
    "s_waitcnt lgkmcnt(8)\n"
    "v_mfma_f32_16x16x32_bf16 v[56:59], v[24:27], a[24:27], v[56:59]\n"
    "ds_read_b64_tr_b16 v[48:49], v8 offset:61440\n"
    "v_mfma_f32_16x16x32_bf16 v[88:91], v[24:27], a[88:91], v[88:91]\n"
    "ds_read_b64_tr_b16 v[50:51], v8 offset:63488\n"
    "buffer_load_dwordx4 a[44:47], v0, s[40:43], s25 offen offset:0 sc1\n"
    "v_mfma_f32_16x16x32_bf16 v[120:123], v[24:27], a[152:155], v[120:123]\n"
    "v_mfma_f32_16x16x32_bf16 v[152:155], v[24:27], a[216:219], v[152:155]\n"
    "v_mfma_f32_16x16x32_bf16 v[60:63], v[28:31], a[24:27], v[60:63]\n"
    "ds_read_b64_tr_b16 v[52:53], v9 offset:61440\n"
    "buffer_load_dwordx4 a[108:111], v0, s[40:43], s25 offen offset:1024 sc1\n"
    "v_mfma_f32_16x16x32_bf16 v[92:95], v[28:31], a[88:91], v[92:95]\n"
    "ds_read_b64_tr_b16 v[54:55], v9 offset:63488\n"
    "v_mfma_f32_16x16x32_bf16 v[124:127], v[28:31], a[152:155], v[124:127]\n"
    "v_mfma_f32_16x16x32_bf16 v[156:159], v[28:31], a[216:219], v[156:159]\n"
    "buffer_load_dwordx4 a[172:175], v1, s[40:43], s25 offen offset:0 sc1\n"
    "s_waitcnt lgkmcnt(8)\n"
    "v_mfma_f32_16x16x32_bf16 v[72:75], v[32:35], a[24:27], v[72:75]\n"
    "v_mfma_f32_16x16x32_bf16 v[104:107], v[32:35], a[88:91], v[104:107]\n"
    "v_mfma_f32_16x16x32_bf16 v[136:139], v[32:35], a[152:155], v[136:139]\n"
    "buffer_load_dwordx4 a[236:239], v1, s[40:43], s25 offen offset:1024 sc1\n"
    "v_mfma_f32_16x16x32_bf16 v[168:171], v[32:35], a[216:219], v[168:171]\n"
    "s_mov_b32 s25, 98304\n"
    "v_mfma_f32_16x16x32_bf16 v[76:79], v[36:39], a[24:27], v[76:79]\n"
    "v_mfma_f32_16x16x32_bf16 v[108:111], v[36:39], a[88:91], v[108:111]\n"
    "buffer_load_dwordx4 a[48:51], v0, s[40:43], s25 offen offset:0 sc1\n"
    "v_mfma_f32_16x16x32_bf16 v[140:143], v[36:39], a[152:155], v[140:143]\n"
    "v_mfma_f32_16x16x32_bf16 v[172:175], v[36:39], a[216:219], v[172:175]\n"
    "s_waitcnt vmcnt(17)\n"
    "s_waitcnt lgkmcnt(4)\n"
    "v_mfma_f32_16x16x32_bf16 v[56:59], v[40:43], a[28:31], v[56:59]\n"
    "ds_read_b64_tr_b16 v[24:25], v12 offset:0\n"
    "buffer_load_dwordx4 a[112:115], v0, s[40:43], s25 offen offset:1024 sc1\n"
    "v_mfma_f32_16x16x32_bf16 v[88:91], v[40:43], a[92:95], v[88:91]\n"
    "ds_read_b64_tr_b16 v[26:27], v12 offset:2048\n"
    "v_mfma_f32_16x16x32_bf16 v[120:123], v[40:43], a[156:159], v[120:123]\n"
    "ds_read_b64_tr_b16 v[28:29], v13 offset:0\n"
    "v_mfma_f32_16x16x32_bf16 v[152:155], v[40:43], a[220:223], v[152:155]\n"
    "ds_read_b64_tr_b16 v[30:31], v13 offset:2048\n"
    "buffer_load_dwordx4 a[176:179], v1, s[40:43], s25 offen offset:0 sc1\n"
    "v_mfma_f32_16x16x32_bf16 v[60:63], v[44:47], a[28:31], v[60:63]\n"
    "ds_read_b64_tr_b16 v[32:33], v12 offset:16384\n"
    "v_mfma_f32_16x16x32_bf16 v[92:95], v[44:47], a[92:95], v[92:95]\n"
    "ds_read_b64_tr_b16 v[34:35], v12 offset:18432\n"
    "v_mfma_f32_16x16x32_bf16 v[124:127], v[44:47], a[156:159], v[124:127]\n"
    "ds_read_b64_tr_b16 v[36:37], v13 offset:16384\n"
    "buffer_load_dwordx4 a[240:243], v1, s[40:43], s25 offen offset:1024 sc1\n"
    "v_mfma_f32_16x16x32_bf16 v[156:159], v[44:47], a[220:223], v[156:159]\n"
    "ds_read_b64_tr_b16 v[38:39], v13 offset:18432\n"
    "s_mov_b32 s25, 106496\n"
    "s_waitcnt lgkmcnt(8)\n"
    "v_mfma_f32_16x16x32_bf16 v[72:75], v[48:51], a[28:31], v[72:75]\n"
    "ds_read_b64_tr_b16 v[40:41], v12 offset:4096\n"
    "v_mfma_f32_16x16x32_bf16 v[104:107], v[48:51], a[92:95], v[104:107]\n"
    "ds_read_b64_tr_b16 v[42:43], v12 offset:6144\n"
    "buffer_load_dwordx4 a[52:55], v0, s[40:43], s25 offen offset:0 sc1\n"
    "v_mfma_f32_16x16x32_bf16 v[136:139], v[48:51], a[156:159], v[136:139]\n"
    "v_mfma_f32_16x16x32_bf16 v[168:171], v[48:51], a[220:223], v[168:171]\n"
    "v_mfma_f32_16x16x32_bf16 v[76:79], v[52:55], a[28:31], v[76:79]\n"
    "ds_read_b64_tr_b16 v[44:45], v13 offset:4096\n"
    "buffer_load_dwordx4 a[116:119], v0, s[40:43], s25 offen offset:1024 sc1\n"
    "v_mfma_f32_16x16x32_bf16 v[108:111], v[52:55], a[92:95], v[108:111]\n"
    "ds_read_b64_tr_b16 v[46:47], v13 offset:6144\n"
    "v_mfma_f32_16x16x32_bf16 v[140:143], v[52:55], a[156:159], v[140:143]\n"
    "v_mfma_f32_16x16x32_bf16 v[172:175], v[52:55], a[220:223], v[172:175]\n"
    "buffer_load_dwordx4 a[180:183], v1, s[40:43], s25 offen offset:0 sc1\n"
    "s_waitcnt vmcnt(19)\n"
    "s_waitcnt lgkmcnt(8)\n"
    "v_mfma_f32_16x16x32_bf16 v[56:59], v[24:27], a[32:35], v[56:59]\n"
    "ds_read_b64_tr_b16 v[48:49], v12 offset:20480\n"
    "v_mfma_f32_16x16x32_bf16 v[88:91], v[24:27], a[96:99], v[88:91]\n"
    "ds_read_b64_tr_b16 v[50:51], v12 offset:22528\n"
    "v_mfma_f32_16x16x32_bf16 v[120:123], v[24:27], a[160:163], v[120:123]\n"
    "buffer_load_dwordx4 a[244:247], v1, s[40:43], s25 offen offset:1024 sc1\n"
    "v_mfma_f32_16x16x32_bf16 v[152:155], v[24:27], a[224:227], v[152:155]\n"
    "s_mov_b32 s25, 114688\n"
    "v_mfma_f32_16x16x32_bf16 v[60:63], v[28:31], a[32:35], v[60:63]\n"
    "ds_read_b64_tr_b16 v[52:53], v13 offset:20480\n"
    "v_mfma_f32_16x16x32_bf16 v[92:95], v[28:31], a[96:99], v[92:95]\n"
    "ds_read_b64_tr_b16 v[54:55], v13 offset:22528\n"
    "buffer_load_dwordx4 a[56:59], v0, s[40:43], s25 offen offset:0 sc1\n"
    "v_mfma_f32_16x16x32_bf16 v[124:127], v[28:31], a[160:163], v[124:127]\n"
    "v_mfma_f32_16x16x32_bf16 v[156:159], v[28:31], a[224:227], v[156:159]\n"
    "s_waitcnt lgkmcnt(8)\n"
    "v_mfma_f32_16x16x32_bf16 v[72:75], v[32:35], a[32:35], v[72:75]\n"
    "ds_read_b64_tr_b16 v[24:25], v12 offset:8192\n"
    "buffer_load_dwordx4 a[120:123], v0, s[40:43], s25 offen offset:1024 sc1\n"
    "v_mfma_f32_16x16x32_bf16 v[104:107], v[32:35], a[96:99], v[104:107]\n"
    "ds_read_b64_tr_b16 v[26:27], v12 offset:10240\n"
    "v_mfma_f32_16x16x32_bf16 v[136:139], v[32:35], a[160:163], v[136:139]\n"
    "v_mfma_f32_16x16x32_bf16 v[168:171], v[32:35], a[224:227], v[168:171]\n"
    "buffer_load_dwordx4 a[184:187], v1, s[40:43], s25 offen offset:0 sc1\n"
    "v_mfma_f32_16x16x32_bf16 v[76:79], v[36:39], a[32:35], v[76:79]\n"
    "ds_read_b64_tr_b16 v[28:29], v13 offset:8192\n"
    "v_mfma_f32_16x16x32_bf16 v[108:111], v[36:39], a[96:99], v[108:111]\n"
    "ds_read_b64_tr_b16 v[30:31], v13 offset:10240\n"
    "v_mfma_f32_16x16x32_bf16 v[140:143], v[36:39], a[160:163], v[140:143]\n"
    "buffer_load_dwordx4 a[248:251], v1, s[40:43], s25 offen offset:1024 sc1\n"
    "v_mfma_f32_16x16x32_bf16 v[172:175], v[36:39], a[224:227], v[172:175]\n"
    "s_mov_b32 s25, 122880\n"
    "s_waitcnt vmcnt(20)\n"
    "s_waitcnt lgkmcnt(8)\n"
    "v_mfma_f32_16x16x32_bf16 v[56:59], v[40:43], a[36:39], v[56:59]\n"
    "ds_read_b64_tr_b16 v[32:33], v12 offset:24576\n"
    "v_mfma_f32_16x16x32_bf16 v[88:91], v[40:43], a[100:103], v[88:91]\n"
    "ds_read_b64_tr_b16 v[34:35], v12 offset:26624\n"
    "buffer_load_dwordx4 a[60:63], v0, s[40:43], s25 offen offset:0 sc1\n"
    "v_mfma_f32_16x16x32_bf16 v[120:123], v[40:43], a[164:167], v[120:123]\n"
    "v_mfma_f32_16x16x32_bf16 v[152:155], v[40:43], a[228:231], v[152:155]\n"
    "v_mfma_f32_16x16x32_bf16 v[60:63], v[44:47], a[36:39], v[60:63]\n"
    "ds_read_b64_tr_b16 v[36:37], v13 offset:24576\n"
    "buffer_load_dwordx4 a[124:127], v0, s[40:43], s25 offen offset:1024 sc1\n"
    "v_mfma_f32_16x16x32_bf16 v[92:95], v[44:47], a[100:103], v[92:95]\n"
    "ds_read_b64_tr_b16 v[38:39], v13 offset:26624\n"
    "v_mfma_f32_16x16x32_bf16 v[124:127], v[44:47], a[164:167], v[124:127]\n"
    "v_mfma_f32_16x16x32_bf16 v[156:159], v[44:47], a[228:231], v[156:159]\n"
    "buffer_load_dwordx4 a[188:191], v1, s[40:43], s25 offen offset:0 sc1\n"
    "s_waitcnt lgkmcnt(8)\n"
    "v_mfma_f32_16x16x32_bf16 v[72:75], v[48:51], a[36:39], v[72:75]\n"
    "ds_read_b64_tr_b16 v[40:41], v12 offset:12288\n"
    "v_mfma_f32_16x16x32_bf16 v[104:107], v[48:51], a[100:103], v[104:107]\n"
    "ds_read_b64_tr_b16 v[42:43], v12 offset:14336\n"
    "v_mfma_f32_16x16x32_bf16 v[136:139], v[48:51], a[164:167], v[136:139]\n"
    "buffer_load_dwordx4 a[252:255], v1, s[40:43], s25 offen offset:1024 sc1\n"
    "v_mfma_f32_16x16x32_bf16 v[168:171], v[48:51], a[228:231], v[168:171]\n"
    "v_mfma_f32_16x16x32_bf16 v[76:79], v[52:55], a[36:39], v[76:79]\n"
    "ds_read_b64_tr_b16 v[44:45], v13 offset:12288\n"
    "v_mfma_f32_16x16x32_bf16 v[108:111], v[52:55], a[100:103], v[108:111]\n"
    "ds_read_b64_tr_b16 v[46:47], v13 offset:14336\n"
    "v_mfma_f32_16x16x32_bf16 v[140:143], v[52:55], a[164:167], v[140:143]\n"
    "v_mfma_f32_16x16x32_bf16 v[172:175], v[52:55], a[228:231], v[172:175]\n"
    "s_waitcnt vmcnt(20)\n"
    "s_waitcnt lgkmcnt(8)\n"
    "v_mfma_f32_16x16x32_bf16 v[56:59], v[24:27], a[40:43], v[56:59]\n"
    "ds_read_b64_tr_b16 v[48:49], v12 offset:28672\n"
    "v_mfma_f32_16x16x32_bf16 v[88:91], v[24:27], a[104:107], v[88:91]\n"
    "ds_read_b64_tr_b16 v[50:51], v12 offset:30720\n"
    "v_mfma_f32_16x16x32_bf16 v[120:123], v[24:27], a[168:171], v[120:123]\n"
    "v_mfma_f32_16x16x32_bf16 v[152:155], v[24:27], a[232:235], v[152:155]\n"
    "v_mfma_f32_16x16x32_bf16 v[60:63], v[28:31], a[40:43], v[60:63]\n"
    "ds_read_b64_tr_b16 v[52:53], v13 offset:28672\n"
    "v_mfma_f32_16x16x32_bf16 v[92:95], v[28:31], a[104:107], v[92:95]\n"
    "ds_read_b64_tr_b16 v[54:55], v13 offset:30720\n"
    "v_mfma_f32_16x16x32_bf16 v[124:127], v[28:31], a[168:171], v[124:127]\n"
    "v_mfma_f32_16x16x32_bf16 v[156:159], v[28:31], a[232:235], v[156:159]\n"
    "s_waitcnt lgkmcnt(8)\n"
    "v_mfma_f32_16x16x32_bf16 v[72:75], v[32:35], a[40:43], v[72:75]\n"
    "v_mfma_f32_16x16x32_bf16 v[104:107], v[32:35], a[104:107], v[104:107]\n"
    "v_mfma_f32_16x16x32_bf16 v[136:139], v[32:35], a[168:171], v[136:139]\n"
    "v_mfma_f32_16x16x32_bf16 v[168:171], v[32:35], a[232:235], v[168:171]\n"
    "v_mfma_f32_16x16x32_bf16 v[76:79], v[36:39], a[40:43], v[76:79]\n"
    "v_mfma_f32_16x16x32_bf16 v[108:111], v[36:39], a[104:107], v[108:111]\n"
    "v_mfma_f32_16x16x32_bf16 v[140:143], v[36:39], a[168:171], v[140:143]\n"
    "v_mfma_f32_16x16x32_bf16 v[172:175], v[36:39], a[232:235], v[172:175]\n"
    "s_waitcnt vmcnt(16)\n"
    "s_waitcnt lgkmcnt(4)\n"
    "v_mfma_f32_16x16x32_bf16 v[56:59], v[40:43], a[44:47], v[56:59]\n"
    "ds_read_b64_tr_b16 v[24:25], v12 offset:32768\n"
    "v_mfma_f32_16x16x32_bf16 v[88:91], v[40:43], a[108:111], v[88:91]\n"
    "ds_read_b64_tr_b16 v[26:27], v12 offset:34816\n"
    "v_mfma_f32_16x16x32_bf16 v[120:123], v[40:43], a[172:175], v[120:123]\n"
    "ds_read_b64_tr_b16 v[28:29], v13 offset:32768\n"
    "v_mfma_f32_16x16x32_bf16 v[152:155], v[40:43], a[236:239], v[152:155]\n"
    "ds_read_b64_tr_b16 v[30:31], v13 offset:34816\n"
    "v_mfma_f32_16x16x32_bf16 v[60:63], v[44:47], a[44:47], v[60:63]\n"
    "ds_read_b64_tr_b16 v[32:33], v12 offset:49152\n"
    "v_mfma_f32_16x16x32_bf16 v[92:95], v[44:47], a[108:111], v[92:95]\n"
    "ds_read_b64_tr_b16 v[34:35], v12 offset:51200\n"
    "v_mfma_f32_16x16x32_bf16 v[124:127], v[44:47], a[172:175], v[124:127]\n"
    "ds_read_b64_tr_b16 v[36:37], v13 offset:49152\n"
    "v_mfma_f32_16x16x32_bf16 v[156:159], v[44:47], a[236:239], v[156:159]\n"
    "ds_read_b64_tr_b16 v[38:39], v13 offset:51200\n"
    "s_waitcnt lgkmcnt(8)\n"
    "v_mfma_f32_16x16x32_bf16 v[72:75], v[48:51], a[44:47], v[72:75]\n"
    "ds_read_b64_tr_b16 v[40:41], v12 offset:36864\n"
    "v_mfma_f32_16x16x32_bf16 v[104:107], v[48:51], a[108:111], v[104:107]\n"
    "ds_read_b64_tr_b16 v[42:43], v12 offset:38912\n"
    "v_mfma_f32_16x16x32_bf16 v[136:139], v[48:51], a[172:175], v[136:139]\n"
    "v_mfma_f32_16x16x32_bf16 v[168:171], v[48:51], a[236:239], v[168:171]\n"
    "v_mfma_f32_16x16x32_bf16 v[76:79], v[52:55], a[44:47], v[76:79]\n"
    "ds_read_b64_tr_b16 v[44:45], v13 offset:36864\n"
    "v_mfma_f32_16x16x32_bf16 v[108:111], v[52:55], a[108:111], v[108:111]\n"
    "ds_read_b64_tr_b16 v[46:47], v13 offset:38912\n"
    "v_mfma_f32_16x16x32_bf16 v[140:143], v[52:55], a[172:175], v[140:143]\n"
    "v_mfma_f32_16x16x32_bf16 v[172:175], v[52:55], a[236:239], v[172:175]\n"
    "s_waitcnt vmcnt(12)\n"
    "s_waitcnt lgkmcnt(8)\n"
    "v_mfma_f32_16x16x32_bf16 v[56:59], v[24:27], a[48:51], v[56:59]\n"
    "ds_read_b64_tr_b16 v[48:49], v12 offset:53248\n"
    "v_mfma_f32_16x16x32_bf16 v[88:91], v[24:27], a[112:115], v[88:91]\n"
    "ds_read_b64_tr_b16 v[50:51], v12 offset:55296\n"
    "v_mfma_f32_16x16x32_bf16 v[120:123], v[24:27], a[176:179], v[120:123]\n"
    "v_mfma_f32_16x16x32_bf16 v[152:155], v[24:27], a[240:243], v[152:155]\n"
    "v_mfma_f32_16x16x32_bf16 v[60:63], v[28:31], a[48:51], v[60:63]\n"
    "ds_read_b64_tr_b16 v[52:53], v13 offset:53248\n"
    "v_mfma_f32_16x16x32_bf16 v[92:95], v[28:31], a[112:115], v[92:95]\n"
    "ds_read_b64_tr_b16 v[54:55], v13 offset:55296\n"
    "v_mfma_f32_16x16x32_bf16 v[124:127], v[28:31], a[176:179], v[124:127]\n"
    "v_mfma_f32_16x16x32_bf16 v[156:159], v[28:31], a[240:243], v[156:159]\n"
    "s_waitcnt lgkmcnt(8)\n"
    "v_mfma_f32_16x16x32_bf16 v[72:75], v[32:35], a[48:51], v[72:75]\n"
    "ds_read_b64_tr_b16 v[24:25], v12 offset:40960\n"
    "v_mfma_f32_16x16x32_bf16 v[104:107], v[32:35], a[112:115], v[104:107]\n"
    "ds_read_b64_tr_b16 v[26:27], v12 offset:43008\n"
    "v_mfma_f32_16x16x32_bf16 v[136:139], v[32:35], a[176:179], v[136:139]\n"
    "v_mfma_f32_16x16x32_bf16 v[168:171], v[32:35], a[240:243], v[168:171]\n"
    "v_mfma_f32_16x16x32_bf16 v[76:79], v[36:39], a[48:51], v[76:79]\n"
    "ds_read_b64_tr_b16 v[28:29], v13 offset:40960\n"
    "v_mfma_f32_16x16x32_bf16 v[108:111], v[36:39], a[112:115], v[108:111]\n"
    "ds_read_b64_tr_b16 v[30:31], v13 offset:43008\n"
    "v_mfma_f32_16x16x32_bf16 v[140:143], v[36:39], a[176:179], v[140:143]\n"
    "v_mfma_f32_16x16x32_bf16 v[172:175], v[36:39], a[240:243], v[172:175]\n"
    "s_waitcnt vmcnt(8)\n"
    "s_waitcnt lgkmcnt(8)\n"
    "v_mfma_f32_16x16x32_bf16 v[56:59], v[40:43], a[52:55], v[56:59]\n"
    "ds_read_b64_tr_b16 v[32:33], v12 offset:57344\n"
    "v_mfma_f32_16x16x32_bf16 v[88:91], v[40:43], a[116:119], v[88:91]\n"
    "ds_read_b64_tr_b16 v[34:35], v12 offset:59392\n"
    "v_mfma_f32_16x16x32_bf16 v[120:123], v[40:43], a[180:183], v[120:123]\n"
    "v_mfma_f32_16x16x32_bf16 v[152:155], v[40:43], a[244:247], v[152:155]\n"
    "v_mfma_f32_16x16x32_bf16 v[60:63], v[44:47], a[52:55], v[60:63]\n"
    "ds_read_b64_tr_b16 v[36:37], v13 offset:57344\n"
    "v_mfma_f32_16x16x32_bf16 v[92:95], v[44:47], a[116:119], v[92:95]\n"
    "ds_read_b64_tr_b16 v[38:39], v13 offset:59392\n"
    "v_mfma_f32_16x16x32_bf16 v[124:127], v[44:47], a[180:183], v[124:127]\n"
    "v_mfma_f32_16x16x32_bf16 v[156:159], v[44:47], a[244:247], v[156:159]\n"
    "s_waitcnt lgkmcnt(8)\n"
    "v_mfma_f32_16x16x32_bf16 v[72:75], v[48:51], a[52:55], v[72:75]\n"
    "ds_read_b64_tr_b16 v[40:41], v12 offset:45056\n"
    "v_mfma_f32_16x16x32_bf16 v[104:107], v[48:51], a[116:119], v[104:107]\n"
    "ds_read_b64_tr_b16 v[42:43], v12 offset:47104\n"
    "v_mfma_f32_16x16x32_bf16 v[136:139], v[48:51], a[180:183], v[136:139]\n"
    "v_mfma_f32_16x16x32_bf16 v[168:171], v[48:51], a[244:247], v[168:171]\n"
    "v_mfma_f32_16x16x32_bf16 v[76:79], v[52:55], a[52:55], v[76:79]\n"
    "ds_read_b64_tr_b16 v[44:45], v13 offset:45056\n"
    "v_mfma_f32_16x16x32_bf16 v[108:111], v[52:55], a[116:119], v[108:111]\n"
    "ds_read_b64_tr_b16 v[46:47], v13 offset:47104\n"
    "v_mfma_f32_16x16x32_bf16 v[140:143], v[52:55], a[180:183], v[140:143]\n"
    "v_mfma_f32_16x16x32_bf16 v[172:175], v[52:55], a[244:247], v[172:175]\n"
    "s_waitcnt vmcnt(4)\n"
    "s_waitcnt lgkmcnt(8)\n"
    "v_mfma_f32_16x16x32_bf16 v[56:59], v[24:27], a[56:59], v[56:59]\n"
    "ds_read_b64_tr_b16 v[48:49], v12 offset:61440\n"
    "v_mfma_f32_16x16x32_bf16 v[88:91], v[24:27], a[120:123], v[88:91]\n"
    "ds_read_b64_tr_b16 v[50:51], v12 offset:63488\n"
    "v_mfma_f32_16x16x32_bf16 v[120:123], v[24:27], a[184:187], v[120:123]\n"
    "v_mfma_f32_16x16x32_bf16 v[152:155], v[24:27], a[248:251], v[152:155]\n"
    "v_mfma_f32_16x16x32_bf16 v[60:63], v[28:31], a[56:59], v[60:63]\n"
    "ds_read_b64_tr_b16 v[52:53], v13 offset:61440\n"
    "v_mfma_f32_16x16x32_bf16 v[92:95], v[28:31], a[120:123], v[92:95]\n"
    "ds_read_b64_tr_b16 v[54:55], v13 offset:63488\n"
    "v_mfma_f32_16x16x32_bf16 v[124:127], v[28:31], a[184:187], v[124:127]\n"
    "v_mfma_f32_16x16x32_bf16 v[156:159], v[28:31], a[248:251], v[156:159]\n"
    "s_waitcnt lgkmcnt(8)\n"
    "v_mfma_f32_16x16x32_bf16 v[72:75], v[32:35], a[56:59], v[72:75]\n"
    "v_mfma_f32_16x16x32_bf16 v[104:107], v[32:35], a[120:123], v[104:107]\n"
    "v_mfma_f32_16x16x32_bf16 v[136:139], v[32:35], a[184:187], v[136:139]\n"
    "v_mfma_f32_16x16x32_bf16 v[168:171], v[32:35], a[248:251], v[168:171]\n"
    "v_mfma_f32_16x16x32_bf16 v[76:79], v[36:39], a[56:59], v[76:79]\n"
    "v_mfma_f32_16x16x32_bf16 v[108:111], v[36:39], a[120:123], v[108:111]\n"
    "v_mfma_f32_16x16x32_bf16 v[140:143], v[36:39], a[184:187], v[140:143]\n"
    "v_mfma_f32_16x16x32_bf16 v[172:175], v[36:39], a[248:251], v[172:175]\n"
    "s_waitcnt vmcnt(0)\n"
    "s_waitcnt lgkmcnt(4)\n"
    "v_mfma_f32_16x16x32_bf16 v[56:59], v[40:43], a[60:63], v[56:59]\n"
    "ds_read_b64_tr_b16 v[24:25], v10 offset:0\n"
    "v_mfma_f32_16x16x32_bf16 v[88:91], v[40:43], a[124:127], v[88:91]\n"
    "ds_read_b64_tr_b16 v[26:27], v10 offset:2048\n"
    "v_mfma_f32_16x16x32_bf16 v[120:123], v[40:43], a[188:191], v[120:123]\n"
    "ds_read_b64_tr_b16 v[28:29], v11 offset:0\n"
    "v_mfma_f32_16x16x32_bf16 v[152:155], v[40:43], a[252:255], v[152:155]\n"
    "ds_read_b64_tr_b16 v[30:31], v11 offset:2048\n"
    "v_mfma_f32_16x16x32_bf16 v[60:63], v[44:47], a[60:63], v[60:63]\n"
    "ds_read_b64_tr_b16 v[32:33], v10 offset:16384\n"
    "v_mfma_f32_16x16x32_bf16 v[92:95], v[44:47], a[124:127], v[92:95]\n"
    "ds_read_b64_tr_b16 v[34:35], v10 offset:18432\n"
    "v_mfma_f32_16x16x32_bf16 v[124:127], v[44:47], a[188:191], v[124:127]\n"
    "ds_read_b64_tr_b16 v[36:37], v11 offset:16384\n"
    "v_mfma_f32_16x16x32_bf16 v[156:159], v[44:47], a[252:255], v[156:159]\n"
    "ds_read_b64_tr_b16 v[38:39], v11 offset:18432\n"
    "s_waitcnt lgkmcnt(8)\n"
    "v_mfma_f32_16x16x32_bf16 v[72:75], v[48:51], a[60:63], v[72:75]\n"
    "ds_read_b64_tr_b16 v[40:41], v10 offset:4096\n"
    "v_mfma_f32_16x16x32_bf16 v[104:107], v[48:51], a[124:127], v[104:107]\n"
    "ds_read_b64_tr_b16 v[42:43], v10 offset:6144\n"
    "v_mfma_f32_16x16x32_bf16 v[136:139], v[48:51], a[188:191], v[136:139]\n"
    "v_mfma_f32_16x16x32_bf16 v[168:171], v[48:51], a[252:255], v[168:171]\n"
    "v_mfma_f32_16x16x32_bf16 v[76:79], v[52:55], a[60:63], v[76:79]\n"
    "ds_read_b64_tr_b16 v[44:45], v11 offset:4096\n"
    "v_mfma_f32_16x16x32_bf16 v[108:111], v[52:55], a[124:127], v[108:111]\n"
    "ds_read_b64_tr_b16 v[46:47], v11 offset:6144\n"
    "v_mfma_f32_16x16x32_bf16 v[140:143], v[52:55], a[188:191], v[140:143]\n"
    "v_mfma_f32_16x16x32_bf16 v[172:175], v[52:55], a[252:255], v[172:175]\n"
    "s_waitcnt lgkmcnt(8)\n"
    "v_mfma_f32_16x16x32_bf16 v[64:67], v[24:27], a[0:3], 0\n"
    "ds_read_b64_tr_b16 v[48:49], v10 offset:20480\n"
    "v_mfma_f32_16x16x32_bf16 v[96:99], v[24:27], a[64:67], 0\n"
    "ds_read_b64_tr_b16 v[50:51], v10 offset:22528\n"
    "v_mfma_f32_16x16x32_bf16 v[128:131], v[24:27], a[128:131], 0\n"
    "v_mfma_f32_16x16x32_bf16 v[160:163], v[24:27], a[192:195], 0\n"
    "v_mfma_f32_16x16x32_bf16 v[68:71], v[28:31], a[0:3], 0\n"
    "ds_read_b64_tr_b16 v[52:53], v11 offset:20480\n"
    "v_mfma_f32_16x16x32_bf16 v[100:103], v[28:31], a[64:67], 0\n"
    "ds_read_b64_tr_b16 v[54:55], v11 offset:22528\n"
    "v_mfma_f32_16x16x32_bf16 v[132:135], v[28:31], a[128:131], 0\n"
    "v_mfma_f32_16x16x32_bf16 v[164:167], v[28:31], a[192:195], 0\n"
    "s_waitcnt lgkmcnt(8)\n"
    "v_mfma_f32_16x16x32_bf16 v[80:83], v[32:35], a[0:3], 0\n"
    "ds_read_b64_tr_b16 v[24:25], v10 offset:8192\n"
    "v_mfma_f32_16x16x32_bf16 v[112:115], v[32:35], a[64:67], 0\n"
    "ds_read_b64_tr_b16 v[26:27], v10 offset:10240\n"
    "v_mfma_f32_16x16x32_bf16 v[144:147], v[32:35], a[128:131], 0\n"
    "v_mul_f32 v56, v56, v72\n"
    "v_mfma_f32_16x16x32_bf16 v[176:179], v[32:35], a[192:195], 0\n"
    "v_mfma_f32_16x16x32_bf16 v[84:87], v[36:39], a[0:3], 0\n"
    "ds_read_b64_tr_b16 v[28:29], v11 offset:8192\n"
    "v_mfma_f32_16x16x32_bf16 v[116:119], v[36:39], a[64:67], 0\n"
    "ds_read_b64_tr_b16 v[30:31], v11 offset:10240\n"
    "v_mul_f32 v57, v57, v73\n"
    "v_mfma_f32_16x16x32_bf16 v[148:151], v[36:39], a[128:131], 0\n"
    "v_mfma_f32_16x16x32_bf16 v[180:183], v[36:39], a[192:195], 0\n"
    "s_waitcnt lgkmcnt(8)\n"
    "v_mfma_f32_16x16x32_bf16 v[64:67], v[40:43], a[4:7], v[64:67]\n"
    "ds_read_b64_tr_b16 v[32:33], v10 offset:24576\n"
    "v_mul_f32 v58, v58, v74\n"
    "v_mfma_f32_16x16x32_bf16 v[96:99], v[40:43], a[68:71], v[96:99]\n"
    "ds_read_b64_tr_b16 v[34:35], v10 offset:26624\n"
    "v_mfma_f32_16x16x32_bf16 v[128:131], v[40:43], a[132:135], v[128:131]\n"
    "v_mul_f32 v59, v59, v75\n"
    "v_mfma_f32_16x16x32_bf16 v[160:163], v[40:43], a[196:199], v[160:163]\n"
    "v_mfma_f32_16x16x32_bf16 v[68:71], v[44:47], a[4:7], v[68:71]\n"
    "ds_read_b64_tr_b16 v[36:37], v11 offset:24576\n"
    "v_mfma_f32_16x16x32_bf16 v[100:103], v[44:47], a[68:71], v[100:103]\n"
    "ds_read_b64_tr_b16 v[38:39], v11 offset:26624\n"
    "v_mul_f32 v60, v60, v76\n"
    "v_mfma_f32_16x16x32_bf16 v[132:135], v[44:47], a[132:135], v[132:135]\n"
    "v_mfma_f32_16x16x32_bf16 v[164:167], v[44:47], a[196:199], v[164:167]\n"
    "s_waitcnt lgkmcnt(8)\n"
    "v_mfma_f32_16x16x32_bf16 v[80:83], v[48:51], a[4:7], v[80:83]\n"
    "ds_read_b64_tr_b16 v[40:41], v10 offset:12288\n"
    "v_mul_f32 v61, v61, v77\n"
    "v_mfma_f32_16x16x32_bf16 v[112:115], v[48:51], a[68:71], v[112:115]\n"
    "ds_read_b64_tr_b16 v[42:43], v10 offset:14336\n"
    "v_mfma_f32_16x16x32_bf16 v[144:147], v[48:51], a[132:135], v[144:147]\n"
    "v_mfma_f32_16x16x32_bf16 v[176:179], v[48:51], a[196:199], v[176:179]\n"
    "v_mul_f32 v62, v62, v78\n"
    "v_mfma_f32_16x16x32_bf16 v[84:87], v[52:55], a[4:7], v[84:87]\n"
    "ds_read_b64_tr_b16 v[44:45], v11 offset:12288\n"
    "v_mfma_f32_16x16x32_bf16 v[116:119], v[52:55], a[68:71], v[116:119]\n"
    "ds_read_b64_tr_b16 v[46:47], v11 offset:14336\n"
    "v_mul_f32 v63, v63, v79\n"
    "v_mfma_f32_16x16x32_bf16 v[148:151], v[52:55], a[132:135], v[148:151]\n"
    "v_mfma_f32_16x16x32_bf16 v[180:183], v[52:55], a[196:199], v[180:183]\n"
    "s_waitcnt lgkmcnt(8)\n"
    "v_mfma_f32_16x16x32_bf16 v[64:67], v[24:27], a[8:11], v[64:67]\n"
    "ds_read_b64_tr_b16 v[48:49], v10 offset:28672\n"
    "v_cvt_pk_bf16_f32 v56, v56, v57\n"
    "v_mfma_f32_16x16x32_bf16 v[96:99], v[24:27], a[72:75], v[96:99]\n"
    "ds_read_b64_tr_b16 v[50:51], v10 offset:30720\n"
    "v_mfma_f32_16x16x32_bf16 v[128:131], v[24:27], a[136:139], v[128:131]\n"
    "v_mfma_f32_16x16x32_bf16 v[160:163], v[24:27], a[200:203], v[160:163]\n"
    "v_cvt_pk_bf16_f32 v57, v58, v59\n"
    "v_mfma_f32_16x16x32_bf16 v[68:71], v[28:31], a[8:11], v[68:71]\n"
    "ds_read_b64_tr_b16 v[52:53], v11 offset:28672\n"
    "v_mfma_f32_16x16x32_bf16 v[100:103], v[28:31], a[72:75], v[100:103]\n"
    "ds_read_b64_tr_b16 v[54:55], v11 offset:30720\n"
    "v_mfma_f32_16x16x32_bf16 v[132:135], v[28:31], a[136:139], v[132:135]\n"
    "v_cvt_pk_bf16_f32 v58, v60, v61\n"
    "v_mfma_f32_16x16x32_bf16 v[164:167], v[28:31], a[200:203], v[164:167]\n"
    "s_waitcnt lgkmcnt(8)\n"
    "v_mfma_f32_16x16x32_bf16 v[80:83], v[32:35], a[8:11], v[80:83]\n"
    "v_cvt_pk_bf16_f32 v59, v62, v63\n"
    "v_mfma_f32_16x16x32_bf16 v[112:115], v[32:35], a[72:75], v[112:115]\n"
    "v_mfma_f32_16x16x32_bf16 v[144:147], v[32:35], a[136:139], v[144:147]\n"
    "v_mfma_f32_16x16x32_bf16 v[176:179], v[32:35], a[200:203], v[176:179]\n"
    "s_mov_b32 s26, 0\n"
    "v_mfma_f32_16x16x32_bf16 v[84:87], v[36:39], a[8:11], v[84:87]\n"
    "v_mfma_f32_16x16x32_bf16 v[116:119], v[36:39], a[72:75], v[116:119]\n"
    "v_mfma_f32_16x16x32_bf16 v[148:151], v[36:39], a[136:139], v[148:151]\n"
    "buffer_store_dwordx4 v[56:59], v20, s[48:51], s26 offen offset:0 sc1\n"
    "v_mfma_f32_16x16x32_bf16 v[180:183], v[36:39], a[200:203], v[180:183]\n"
    "s_waitcnt lgkmcnt(4)\n"
    "v_mfma_f32_16x16x32_bf16 v[64:67], v[40:43], a[12:15], v[64:67]\n"
    "ds_read_b64_tr_b16 v[24:25], v10 offset:32768\n"
    "v_mfma_f32_16x16x32_bf16 v[96:99], v[40:43], a[76:79], v[96:99]\n"
    "ds_read_b64_tr_b16 v[26:27], v10 offset:34816\n"
    "v_mul_f32 v88, v88, v104\n"
    "v_mfma_f32_16x16x32_bf16 v[128:131], v[40:43], a[140:143], v[128:131]\n"
    "ds_read_b64_tr_b16 v[28:29], v11 offset:32768\n"
    "v_mfma_f32_16x16x32_bf16 v[160:163], v[40:43], a[204:207], v[160:163]\n"
    "ds_read_b64_tr_b16 v[30:31], v11 offset:34816\n"
    "v_mul_f32 v89, v89, v105\n"
    "v_mfma_f32_16x16x32_bf16 v[68:71], v[44:47], a[12:15], v[68:71]\n"
    "ds_read_b64_tr_b16 v[32:33], v10 offset:49152\n"
    "v_mfma_f32_16x16x32_bf16 v[100:103], v[44:47], a[76:79], v[100:103]\n"
    "ds_read_b64_tr_b16 v[34:35], v10 offset:51200\n"
    "v_mfma_f32_16x16x32_bf16 v[132:135], v[44:47], a[140:143], v[132:135]\n"
    "ds_read_b64_tr_b16 v[36:37], v11 offset:49152\n"
    "v_mul_f32 v90, v90, v106\n"
    "v_mfma_f32_16x16x32_bf16 v[164:167], v[44:47], a[204:207], v[164:167]\n"
    "ds_read_b64_tr_b16 v[38:39], v11 offset:51200\n"
    "s_waitcnt lgkmcnt(8)\n"
    "v_mfma_f32_16x16x32_bf16 v[80:83], v[48:51], a[12:15], v[80:83]\n"
    "ds_read_b64_tr_b16 v[40:41], v10 offset:36864\n"
    "v_mfma_f32_16x16x32_bf16 v[112:115], v[48:51], a[76:79], v[112:115]\n"
    "ds_read_b64_tr_b16 v[42:43], v10 offset:38912\n"
    "v_mul_f32 v91, v91, v107\n"
    "v_mfma_f32_16x16x32_bf16 v[144:147], v[48:51], a[140:143], v[144:147]\n"
    "v_mfma_f32_16x16x32_bf16 v[176:179], v[48:51], a[204:207], v[176:179]\n"
    "v_mfma_f32_16x16x32_bf16 v[84:87], v[52:55], a[12:15], v[84:87]\n"
    "ds_read_b64_tr_b16 v[44:45], v11 offset:36864\n"
    "v_mul_f32 v92, v92, v108\n"
    "v_mfma_f32_16x16x32_bf16 v[116:119], v[52:55], a[76:79], v[116:119]\n"
    "ds_read_b64_tr_b16 v[46:47], v11 offset:38912\n"
    "v_mfma_f32_16x16x32_bf16 v[148:151], v[52:55], a[140:143], v[148:151]\n"
    "v_mul_f32 v93, v93, v109\n"
    "v_mfma_f32_16x16x32_bf16 v[180:183], v[52:55], a[204:207], v[180:183]\n"
    "s_waitcnt lgkmcnt(8)\n"
    "v_mfma_f32_16x16x32_bf16 v[64:67], v[24:27], a[16:19], v[64:67]\n"
    "ds_read_b64_tr_b16 v[48:49], v10 offset:53248\n"
    "v_mfma_f32_16x16x32_bf16 v[96:99], v[24:27], a[80:83], v[96:99]\n"
    "ds_read_b64_tr_b16 v[50:51], v10 offset:55296\n"
    "v_mul_f32 v94, v94, v110\n"
    "v_mfma_f32_16x16x32_bf16 v[128:131], v[24:27], a[144:147], v[128:131]\n"
    "v_mfma_f32_16x16x32_bf16 v[160:163], v[24:27], a[208:211], v[160:163]\n"
    "v_mfma_f32_16x16x32_bf16 v[68:71], v[28:31], a[16:19], v[68:71]\n"
    "ds_read_b64_tr_b16 v[52:53], v11 offset:53248\n"
    "v_mul_f32 v95, v95, v111\n"
    "v_mfma_f32_16x16x32_bf16 v[100:103], v[28:31], a[80:83], v[100:103]\n"
    "ds_read_b64_tr_b16 v[54:55], v11 offset:55296\n"
    "v_mfma_f32_16x16x32_bf16 v[132:135], v[28:31], a[144:147], v[132:135]\n"
    "v_mfma_f32_16x16x32_bf16 v[164:167], v[28:31], a[208:211], v[164:167]\n"
    "v_cvt_pk_bf16_f32 v88, v88, v89\n"
    "s_waitcnt lgkmcnt(8)\n"
    "v_mfma_f32_16x16x32_bf16 v[80:83], v[32:35], a[16:19], v[80:83]\n"
    "ds_read_b64_tr_b16 v[24:25], v10 offset:40960\n"
    "v_mfma_f32_16x16x32_bf16 v[112:115], v[32:35], a[80:83], v[112:115]\n"
    "ds_read_b64_tr_b16 v[26:27], v10 offset:43008\n"
    "v_cvt_pk_bf16_f32 v89, v90, v91\n"
    "v_mfma_f32_16x16x32_bf16 v[144:147], v[32:35], a[144:147], v[144:147]\n"
    "v_mfma_f32_16x16x32_bf16 v[176:179], v[32:35], a[208:211], v[176:179]\n"
    "v_mfma_f32_16x16x32_bf16 v[84:87], v[36:39], a[16:19], v[84:87]\n"
    "ds_read_b64_tr_b16 v[28:29], v11 offset:40960\n"
    "v_cvt_pk_bf16_f32 v90, v92, v93\n"
    "v_mfma_f32_16x16x32_bf16 v[116:119], v[36:39], a[80:83], v[116:119]\n"
    "ds_read_b64_tr_b16 v[30:31], v11 offset:43008\n"
    "v_mfma_f32_16x16x32_bf16 v[148:151], v[36:39], a[144:147], v[148:151]\n"
    "v_mfma_f32_16x16x32_bf16 v[180:183], v[36:39], a[208:211], v[180:183]\n"
    "v_cvt_pk_bf16_f32 v91, v94, v95\n"
    "s_waitcnt lgkmcnt(8)\n"
    "v_mfma_f32_16x16x32_bf16 v[64:67], v[40:43], a[20:23], v[64:67]\n"
    "ds_read_b64_tr_b16 v[32:33], v10 offset:57344\n"
    "v_mfma_f32_16x16x32_bf16 v[96:99], v[40:43], a[84:87], v[96:99]\n"
    "ds_read_b64_tr_b16 v[34:35], v10 offset:59392\n"
    "v_mfma_f32_16x16x32_bf16 v[128:131], v[40:43], a[148:151], v[128:131]\n"
    "buffer_store_dwordx4 v[88:91], v20, s[48:51], s26 offen offset:1024 sc1\n"
    "v_mfma_f32_16x16x32_bf16 v[160:163], v[40:43], a[212:215], v[160:163]\n"
    "v_mfma_f32_16x16x32_bf16 v[68:71], v[44:47], a[20:23], v[68:71]\n"
    "ds_read_b64_tr_b16 v[36:37], v11 offset:57344\n"
    "v_mul_f32 v120, v120, v136\n"
    "v_mfma_f32_16x16x32_bf16 v[100:103], v[44:47], a[84:87], v[100:103]\n"
    "ds_read_b64_tr_b16 v[38:39], v11 offset:59392\n"
    "v_mfma_f32_16x16x32_bf16 v[132:135], v[44:47], a[148:151], v[132:135]\n"
    "v_mfma_f32_16x16x32_bf16 v[164:167], v[44:47], a[212:215], v[164:167]\n"
    "v_mul_f32 v121, v121, v137\n"
    "s_waitcnt lgkmcnt(8)\n"
    "v_mfma_f32_16x16x32_bf16 v[80:83], v[48:51], a[20:23], v[80:83]\n"
    "ds_read_b64_tr_b16 v[40:41], v10 offset:45056\n"
    "v_mfma_f32_16x16x32_bf16 v[112:115], v[48:51], a[84:87], v[112:115]\n"
    "ds_read_b64_tr_b16 v[42:43], v10 offset:47104\n"
    "v_mfma_f32_16x16x32_bf16 v[144:147], v[48:51], a[148:151], v[144:147]\n"
    "v_mul_f32 v122, v122, v138\n"
    "v_mfma_f32_16x16x32_bf16 v[176:179], v[48:51], a[212:215], v[176:179]\n"
    "v_mfma_f32_16x16x32_bf16 v[84:87], v[52:55], a[20:23], v[84:87]\n"
    "ds_read_b64_tr_b16 v[44:45], v11 offset:45056\n"
    "v_mfma_f32_16x16x32_bf16 v[116:119], v[52:55], a[84:87], v[116:119]\n"
    "ds_read_b64_tr_b16 v[46:47], v11 offset:47104\n"
    "v_mul_f32 v123, v123, v139\n"
    "v_mfma_f32_16x16x32_bf16 v[148:151], v[52:55], a[148:151], v[148:151]\n"
    "v_mfma_f32_16x16x32_bf16 v[180:183], v[52:55], a[212:215], v[180:183]\n"
    "v_mul_f32 v124, v124, v140\n"
    "s_waitcnt lgkmcnt(8)\n"
    "v_mfma_f32_16x16x32_bf16 v[64:67], v[24:27], a[24:27], v[64:67]\n"
    "ds_read_b64_tr_b16 v[48:49], v10 offset:61440\n"
    "v_mfma_f32_16x16x32_bf16 v[96:99], v[24:27], a[88:91], v[96:99]\n"
    "ds_read_b64_tr_b16 v[50:51], v10 offset:63488\n"
    "v_mfma_f32_16x16x32_bf16 v[128:131], v[24:27], a[152:155], v[128:131]\n"
    "v_mul_f32 v125, v125, v141\n"
    "v_mfma_f32_16x16x32_bf16 v[160:163], v[24:27], a[216:219], v[160:163]\n"
    "v_mfma_f32_16x16x32_bf16 v[68:71], v[28:31], a[24:27], v[68:71]\n"
    "ds_read_b64_tr_b16 v[52:53], v11 offset:61440\n"
    "v_mfma_f32_16x16x32_bf16 v[100:103], v[28:31], a[88:91], v[100:103]\n"
    "ds_read_b64_tr_b16 v[54:55], v11 offset:63488\n"
    "v_mul_f32 v126, v126, v142\n"
    "v_mfma_f32_16x16x32_bf16 v[132:135], v[28:31], a[152:155], v[132:135]\n"
    "v_mfma_f32_16x16x32_bf16 v[164:167], v[28:31], a[216:219], v[164:167]\n"
    "s_waitcnt lgkmcnt(8)\n"
    "v_mfma_f32_16x16x32_bf16 v[80:83], v[32:35], a[24:27], v[80:83]\n"
    "v_mul_f32 v127, v127, v143\n"
    "v_mfma_f32_16x16x32_bf16 v[112:115], v[32:35], a[88:91], v[112:115]\n"
    "v_mfma_f32_16x16x32_bf16 v[144:147], v[32:35], a[152:155], v[144:147]\n"
    "v_cvt_pk_bf16_f32 v120, v120, v121\n"
    "v_mfma_f32_16x16x32_bf16 v[176:179], v[32:35], a[216:219], v[176:179]\n"
    "v_mfma_f32_16x16x32_bf16 v[84:87], v[36:39], a[24:27], v[84:87]\n"
    "v_mfma_f32_16x16x32_bf16 v[116:119], v[36:39], a[88:91], v[116:119]\n"
    "v_cvt_pk_bf16_f32 v121, v122, v123\n"
    "v_mfma_f32_16x16x32_bf16 v[148:151], v[36:39], a[152:155], v[148:151]\n"
    "v_mfma_f32_16x16x32_bf16 v[180:183], v[36:39], a[216:219], v[180:183]\n"
    "s_waitcnt lgkmcnt(4)\n"
    "v_mfma_f32_16x16x32_bf16 v[64:67], v[40:43], a[28:31], v[64:67]\n"
    "ds_read_b64_tr_b16 v[24:25], v14 offset:0\n"
    "v_cvt_pk_bf16_f32 v122, v124, v125\n"
    "v_mfma_f32_16x16x32_bf16 v[96:99], v[40:43], a[92:95], v[96:99]\n"
    "ds_read_b64_tr_b16 v[26:27], v14 offset:2048\n"
    "v_mfma_f32_16x16x32_bf16 v[128:131], v[40:43], a[156:159], v[128:131]\n"
    "ds_read_b64_tr_b16 v[28:29], v15 offset:0\n"
    "v_mfma_f32_16x16x32_bf16 v[160:163], v[40:43], a[220:223], v[160:163]\n"
    "ds_read_b64_tr_b16 v[30:31], v15 offset:2048\n"
    "v_cvt_pk_bf16_f32 v123, v126, v127\n"
    "v_mfma_f32_16x16x32_bf16 v[68:71], v[44:47], a[28:31], v[68:71]\n"
    "ds_read_b64_tr_b16 v[32:33], v14 offset:16384\n"
    "v_mfma_f32_16x16x32_bf16 v[100:103], v[44:47], a[92:95], v[100:103]\n"
    "ds_read_b64_tr_b16 v[34:35], v14 offset:18432\n"
    "buffer_store_dwordx4 v[120:123], v20, s[48:51], s26 offen offset:2048 sc1\n"
    "v_mfma_f32_16x16x32_bf16 v[132:135], v[44:47], a[156:159], v[132:135]\n"
    "ds_read_b64_tr_b16 v[36:37], v15 offset:16384\n"
    "v_mfma_f32_16x16x32_bf16 v[164:167], v[44:47], a[220:223], v[164:167]\n"
    "ds_read_b64_tr_b16 v[38:39], v15 offset:18432\n"
    "s_waitcnt lgkmcnt(8)\n"
    "v_mfma_f32_16x16x32_bf16 v[80:83], v[48:51], a[28:31], v[80:83]\n"
    "ds_read_b64_tr_b16 v[40:41], v14 offset:4096\n"
    "v_mul_f32 v152, v152, v168\n"
    "v_mfma_f32_16x16x32_bf16 v[112:115], v[48:51], a[92:95], v[112:115]\n"
    "ds_read_b64_tr_b16 v[42:43], v14 offset:6144\n"
    "v_mfma_f32_16x16x32_bf16 v[144:147], v[48:51], a[156:159], v[144:147]\n"
    "v_mfma_f32_16x16x32_bf16 v[176:179], v[48:51], a[220:223], v[176:179]\n"
    "v_mul_f32 v153, v153, v169\n"
    "v_mfma_f32_16x16x32_bf16 v[84:87], v[52:55], a[28:31], v[84:87]\n"
    "ds_read_b64_tr_b16 v[44:45], v15 offset:4096\n"
    "v_mfma_f32_16x16x32_bf16 v[116:119], v[52:55], a[92:95], v[116:119]\n"
    "ds_read_b64_tr_b16 v[46:47], v15 offset:6144\n"
    "v_mfma_f32_16x16x32_bf16 v[148:151], v[52:55], a[156:159], v[148:151]\n"
    "v_mul_f32 v154, v154, v170\n"
    "v_mfma_f32_16x16x32_bf16 v[180:183], v[52:55], a[220:223], v[180:183]\n"
    "s_waitcnt lgkmcnt(8)\n"
    "v_mfma_f32_16x16x32_bf16 v[64:67], v[24:27], a[32:35], v[64:67]\n"
    "ds_read_b64_tr_b16 v[48:49], v14 offset:20480\n"
    "v_mul_f32 v155, v155, v171\n"
    "v_mfma_f32_16x16x32_bf16 v[96:99], v[24:27], a[96:99], v[96:99]\n"
    "ds_read_b64_tr_b16 v[50:51], v14 offset:22528\n"
    "v_mfma_f32_16x16x32_bf16 v[128:131], v[24:27], a[160:163], v[128:131]\n"
    "v_mfma_f32_16x16x32_bf16 v[160:163], v[24:27], a[224:227], v[160:163]\n"
    "v_mul_f32 v156, v156, v172\n"
    "v_mfma_f32_16x16x32_bf16 v[68:71], v[28:31], a[32:35], v[68:71]\n"
    "ds_read_b64_tr_b16 v[52:53], v15 offset:20480\n"
    "v_mfma_f32_16x16x32_bf16 v[100:103], v[28:31], a[96:99], v[100:103]\n"
    "ds_read_b64_tr_b16 v[54:55], v15 offset:22528\n"
    "v_mfma_f32_16x16x32_bf16 v[132:135], v[28:31], a[160:163], v[132:135]\n"
    "v_mul_f32 v157, v157, v173\n"
    "v_mfma_f32_16x16x32_bf16 v[164:167], v[28:31], a[224:227], v[164:167]\n"
    "s_waitcnt lgkmcnt(8)\n"
    "v_mfma_f32_16x16x32_bf16 v[80:83], v[32:35], a[32:35], v[80:83]\n"
    "ds_read_b64_tr_b16 v[24:25], v14 offset:8192\n"
    "v_mfma_f32_16x16x32_bf16 v[112:115], v[32:35], a[96:99], v[112:115]\n"
    "ds_read_b64_tr_b16 v[26:27], v14 offset:10240\n"
    "v_mul_f32 v158, v158, v174\n"
    "v_mfma_f32_16x16x32_bf16 v[144:147], v[32:35], a[160:163], v[144:147]\n"
    "v_mfma_f32_16x16x32_bf16 v[176:179], v[32:35], a[224:227], v[176:179]\n"
    "v_mul_f32 v159, v159, v175\n"
    "v_mfma_f32_16x16x32_bf16 v[84:87], v[36:39], a[32:35], v[84:87]\n"
    "ds_read_b64_tr_b16 v[28:29], v15 offset:8192\n"
    "v_mfma_f32_16x16x32_bf16 v[116:119], v[36:39], a[96:99], v[116:119]\n"
    "ds_read_b64_tr_b16 v[30:31], v15 offset:10240\n"
    "v_mfma_f32_16x16x32_bf16 v[148:151], v[36:39], a[160:163], v[148:151]\n"
    "v_cvt_pk_bf16_f32 v152, v152, v153\n"
    "v_mfma_f32_16x16x32_bf16 v[180:183], v[36:39], a[224:227], v[180:183]\n"
    "s_waitcnt lgkmcnt(8)\n"
    "v_mfma_f32_16x16x32_bf16 v[64:67], v[40:43], a[36:39], v[64:67]\n"
    "ds_read_b64_tr_b16 v[32:33], v14 offset:24576\n"
    "v_mfma_f32_16x16x32_bf16 v[96:99], v[40:43], a[100:103], v[96:99]\n"
    "ds_read_b64_tr_b16 v[34:35], v14 offset:26624\n"
    "v_cvt_pk_bf16_f32 v153, v154, v155\n"
    "v_mfma_f32_16x16x32_bf16 v[128:131], v[40:43], a[164:167], v[128:131]\n"
    "v_mfma_f32_16x16x32_bf16 v[160:163], v[40:43], a[228:231], v[160:163]\n"
    "v_mfma_f32_16x16x32_bf16 v[68:71], v[44:47], a[36:39], v[68:71]\n"
    "ds_read_b64_tr_b16 v[36:37], v15 offset:24576\n"
    "v_cvt_pk_bf16_f32 v154, v156, v157\n"
    "v_mfma_f32_16x16x32_bf16 v[100:103], v[44:47], a[100:103], v[100:103]\n"
    "ds_read_b64_tr_b16 v[38:39], v15 offset:26624\n"
    "v_mfma_f32_16x16x32_bf16 v[132:135], v[44:47], a[164:167], v[132:135]\n"
    "v_cvt_pk_bf16_f32 v155, v158, v159\n"
    "v_mfma_f32_16x16x32_bf16 v[164:167], v[44:47], a[228:231], v[164:167]\n"
    "s_waitcnt lgkmcnt(8)\n"
    "v_mfma_f32_16x16x32_bf16 v[80:83], v[48:51], a[36:39], v[80:83]\n"
    "ds_read_b64_tr_b16 v[40:41], v14 offset:12288\n"
    "v_mfma_f32_16x16x32_bf16 v[112:115], v[48:51], a[100:103], v[112:115]\n"
    "ds_read_b64_tr_b16 v[42:43], v14 offset:14336\n"
    "buffer_store_dwordx4 v[152:155], v20, s[48:51], s26 offen offset:3072 sc1\n"
    "v_mfma_f32_16x16x32_bf16 v[144:147], v[48:51], a[164:167], v[144:147]\n"
    "v_mfma_f32_16x16x32_bf16 v[176:179], v[48:51], a[228:231], v[176:179]\n"
    "v_mfma_f32_16x16x32_bf16 v[84:87], v[52:55], a[36:39], v[84:87]\n"
    "ds_read_b64_tr_b16 v[44:45], v15 offset:12288\n"
    "v_mfma_f32_16x16x32_bf16 v[116:119], v[52:55], a[100:103], v[116:119]\n"
    "ds_read_b64_tr_b16 v[46:47], v15 offset:14336\n"
    "v_mfma_f32_16x16x32_bf16 v[148:151], v[52:55], a[164:167], v[148:151]\n"
    "v_mfma_f32_16x16x32_bf16 v[180:183], v[52:55], a[228:231], v[180:183]\n"
    "s_waitcnt lgkmcnt(8)\n"
    "v_mfma_f32_16x16x32_bf16 v[64:67], v[24:27], a[40:43], v[64:67]\n"
    "ds_read_b64_tr_b16 v[48:49], v14 offset:28672\n"
    "v_mfma_f32_16x16x32_bf16 v[96:99], v[24:27], a[104:107], v[96:99]\n"
    "ds_read_b64_tr_b16 v[50:51], v14 offset:30720\n"
    "v_mfma_f32_16x16x32_bf16 v[128:131], v[24:27], a[168:171], v[128:131]\n"
    "v_mfma_f32_16x16x32_bf16 v[160:163], v[24:27], a[232:235], v[160:163]\n"
    "v_mfma_f32_16x16x32_bf16 v[68:71], v[28:31], a[40:43], v[68:71]\n"
    "ds_read_b64_tr_b16 v[52:53], v15 offset:28672\n"
    "v_mfma_f32_16x16x32_bf16 v[100:103], v[28:31], a[104:107], v[100:103]\n"
    "ds_read_b64_tr_b16 v[54:55], v15 offset:30720\n"
    "v_mfma_f32_16x16x32_bf16 v[132:135], v[28:31], a[168:171], v[132:135]\n"
    "v_mfma_f32_16x16x32_bf16 v[164:167], v[28:31], a[232:235], v[164:167]\n"
    "s_waitcnt lgkmcnt(8)\n"
    "v_mfma_f32_16x16x32_bf16 v[80:83], v[32:35], a[40:43], v[80:83]\n"
    "v_mfma_f32_16x16x32_bf16 v[112:115], v[32:35], a[104:107], v[112:115]\n"
    "v_mfma_f32_16x16x32_bf16 v[144:147], v[32:35], a[168:171], v[144:147]\n"
    "v_mfma_f32_16x16x32_bf16 v[176:179], v[32:35], a[232:235], v[176:179]\n"
    "v_mfma_f32_16x16x32_bf16 v[84:87], v[36:39], a[40:43], v[84:87]\n"
    "v_mfma_f32_16x16x32_bf16 v[116:119], v[36:39], a[104:107], v[116:119]\n"
    "v_mfma_f32_16x16x32_bf16 v[148:151], v[36:39], a[168:171], v[148:151]\n"
    "v_mfma_f32_16x16x32_bf16 v[180:183], v[36:39], a[232:235], v[180:183]\n"
    "s_waitcnt lgkmcnt(4)\n"
    "v_mfma_f32_16x16x32_bf16 v[64:67], v[40:43], a[44:47], v[64:67]\n"
    "ds_read_b64_tr_b16 v[24:25], v14 offset:32768\n"
    "v_mfma_f32_16x16x32_bf16 v[96:99], v[40:43], a[108:111], v[96:99]\n"
    "ds_read_b64_tr_b16 v[26:27], v14 offset:34816\n"
    "v_mfma_f32_16x16x32_bf16 v[128:131], v[40:43], a[172:175], v[128:131]\n"
    "ds_read_b64_tr_b16 v[28:29], v15 offset:32768\n"
    "v_mfma_f32_16x16x32_bf16 v[160:163], v[40:43], a[236:239], v[160:163]\n"
    "ds_read_b64_tr_b16 v[30:31], v15 offset:34816\n"
    "v_mfma_f32_16x16x32_bf16 v[68:71], v[44:47], a[44:47], v[68:71]\n"
    "ds_read_b64_tr_b16 v[32:33], v14 offset:49152\n"
    "v_mfma_f32_16x16x32_bf16 v[100:103], v[44:47], a[108:111], v[100:103]\n"
    "ds_read_b64_tr_b16 v[34:35], v14 offset:51200\n"
    "v_mfma_f32_16x16x32_bf16 v[132:135], v[44:47], a[172:175], v[132:135]\n"
    "ds_read_b64_tr_b16 v[36:37], v15 offset:49152\n"
    "v_mfma_f32_16x16x32_bf16 v[164:167], v[44:47], a[236:239], v[164:167]\n"
    "ds_read_b64_tr_b16 v[38:39], v15 offset:51200\n"
    "s_waitcnt lgkmcnt(8)\n"
    "v_mfma_f32_16x16x32_bf16 v[80:83], v[48:51], a[44:47], v[80:83]\n"
    "ds_read_b64_tr_b16 v[40:41], v14 offset:36864\n"
    "v_mfma_f32_16x16x32_bf16 v[112:115], v[48:51], a[108:111], v[112:115]\n"
    "ds_read_b64_tr_b16 v[42:43], v14 offset:38912\n"
    "v_mfma_f32_16x16x32_bf16 v[144:147], v[48:51], a[172:175], v[144:147]\n"
    "v_mfma_f32_16x16x32_bf16 v[176:179], v[48:51], a[236:239], v[176:179]\n"
    "v_mfma_f32_16x16x32_bf16 v[84:87], v[52:55], a[44:47], v[84:87]\n"
    "ds_read_b64_tr_b16 v[44:45], v15 offset:36864\n"
    "v_mfma_f32_16x16x32_bf16 v[116:119], v[52:55], a[108:111], v[116:119]\n"
    "ds_read_b64_tr_b16 v[46:47], v15 offset:38912\n"
    "v_mfma_f32_16x16x32_bf16 v[148:151], v[52:55], a[172:175], v[148:151]\n"
    "v_mfma_f32_16x16x32_bf16 v[180:183], v[52:55], a[236:239], v[180:183]\n"
    "s_waitcnt lgkmcnt(8)\n"
    "v_mfma_f32_16x16x32_bf16 v[64:67], v[24:27], a[48:51], v[64:67]\n"
    "ds_read_b64_tr_b16 v[48:49], v14 offset:53248\n"
    "v_mfma_f32_16x16x32_bf16 v[96:99], v[24:27], a[112:115], v[96:99]\n"
    "ds_read_b64_tr_b16 v[50:51], v14 offset:55296\n"
    "v_mfma_f32_16x16x32_bf16 v[128:131], v[24:27], a[176:179], v[128:131]\n"
    "v_mfma_f32_16x16x32_bf16 v[160:163], v[24:27], a[240:243], v[160:163]\n"
    "v_mfma_f32_16x16x32_bf16 v[68:71], v[28:31], a[48:51], v[68:71]\n"
    "ds_read_b64_tr_b16 v[52:53], v15 offset:53248\n"
    "v_mfma_f32_16x16x32_bf16 v[100:103], v[28:31], a[112:115], v[100:103]\n"
    "ds_read_b64_tr_b16 v[54:55], v15 offset:55296\n"
    "v_mfma_f32_16x16x32_bf16 v[132:135], v[28:31], a[176:179], v[132:135]\n"
    "v_mfma_f32_16x16x32_bf16 v[164:167], v[28:31], a[240:243], v[164:167]\n"
    "s_waitcnt lgkmcnt(8)\n"
    "v_mfma_f32_16x16x32_bf16 v[80:83], v[32:35], a[48:51], v[80:83]\n"
    "ds_read_b64_tr_b16 v[24:25], v14 offset:40960\n"
    "v_mfma_f32_16x16x32_bf16 v[112:115], v[32:35], a[112:115], v[112:115]\n"
    "ds_read_b64_tr_b16 v[26:27], v14 offset:43008\n"
    "v_mfma_f32_16x16x32_bf16 v[144:147], v[32:35], a[176:179], v[144:147]\n"
    "v_mfma_f32_16x16x32_bf16 v[176:179], v[32:35], a[240:243], v[176:179]\n"
    "v_mfma_f32_16x16x32_bf16 v[84:87], v[36:39], a[48:51], v[84:87]\n"
    "ds_read_b64_tr_b16 v[28:29], v15 offset:40960\n"
    "v_mfma_f32_16x16x32_bf16 v[116:119], v[36:39], a[112:115], v[116:119]\n"
    "ds_read_b64_tr_b16 v[30:31], v15 offset:43008\n"
    "v_mfma_f32_16x16x32_bf16 v[148:151], v[36:39], a[176:179], v[148:151]\n"
    "v_mfma_f32_16x16x32_bf16 v[180:183], v[36:39], a[240:243], v[180:183]\n"
    "s_waitcnt lgkmcnt(8)\n"
    "v_mfma_f32_16x16x32_bf16 v[64:67], v[40:43], a[52:55], v[64:67]\n"
    "ds_read_b64_tr_b16 v[32:33], v14 offset:57344\n"
    "v_mfma_f32_16x16x32_bf16 v[96:99], v[40:43], a[116:119], v[96:99]\n"
    "ds_read_b64_tr_b16 v[34:35], v14 offset:59392\n"
    "v_mfma_f32_16x16x32_bf16 v[128:131], v[40:43], a[180:183], v[128:131]\n"
    "v_mfma_f32_16x16x32_bf16 v[160:163], v[40:43], a[244:247], v[160:163]\n"
    "v_mfma_f32_16x16x32_bf16 v[68:71], v[44:47], a[52:55], v[68:71]\n"
    "ds_read_b64_tr_b16 v[36:37], v15 offset:57344\n"
    "v_mfma_f32_16x16x32_bf16 v[100:103], v[44:47], a[116:119], v[100:103]\n"
    "ds_read_b64_tr_b16 v[38:39], v15 offset:59392\n"
    "v_mfma_f32_16x16x32_bf16 v[132:135], v[44:47], a[180:183], v[132:135]\n"
    "v_mfma_f32_16x16x32_bf16 v[164:167], v[44:47], a[244:247], v[164:167]\n"
    "s_waitcnt lgkmcnt(8)\n"
    "v_mfma_f32_16x16x32_bf16 v[80:83], v[48:51], a[52:55], v[80:83]\n"
    "ds_read_b64_tr_b16 v[40:41], v14 offset:45056\n"
    "v_mfma_f32_16x16x32_bf16 v[112:115], v[48:51], a[116:119], v[112:115]\n"
    "ds_read_b64_tr_b16 v[42:43], v14 offset:47104\n"
    "v_mfma_f32_16x16x32_bf16 v[144:147], v[48:51], a[180:183], v[144:147]\n"
    "v_mfma_f32_16x16x32_bf16 v[176:179], v[48:51], a[244:247], v[176:179]\n"
    "v_mfma_f32_16x16x32_bf16 v[84:87], v[52:55], a[52:55], v[84:87]\n"
    "ds_read_b64_tr_b16 v[44:45], v15 offset:45056\n"
    "v_mfma_f32_16x16x32_bf16 v[116:119], v[52:55], a[116:119], v[116:119]\n"
    "ds_read_b64_tr_b16 v[46:47], v15 offset:47104\n"
    "v_mfma_f32_16x16x32_bf16 v[148:151], v[52:55], a[180:183], v[148:151]\n"
    "v_mfma_f32_16x16x32_bf16 v[180:183], v[52:55], a[244:247], v[180:183]\n"
    "s_waitcnt lgkmcnt(8)\n"
    "v_mfma_f32_16x16x32_bf16 v[64:67], v[24:27], a[56:59], v[64:67]\n"
    "ds_read_b64_tr_b16 v[48:49], v14 offset:61440\n"
    "v_mfma_f32_16x16x32_bf16 v[96:99], v[24:27], a[120:123], v[96:99]\n"
    "ds_read_b64_tr_b16 v[50:51], v14 offset:63488\n"
    "v_mfma_f32_16x16x32_bf16 v[128:131], v[24:27], a[184:187], v[128:131]\n"
    "v_mfma_f32_16x16x32_bf16 v[160:163], v[24:27], a[248:251], v[160:163]\n"
    "v_mfma_f32_16x16x32_bf16 v[68:71], v[28:31], a[56:59], v[68:71]\n"
    "ds_read_b64_tr_b16 v[52:53], v15 offset:61440\n"
    "v_mfma_f32_16x16x32_bf16 v[100:103], v[28:31], a[120:123], v[100:103]\n"
    "ds_read_b64_tr_b16 v[54:55], v15 offset:63488\n"
    "v_mfma_f32_16x16x32_bf16 v[132:135], v[28:31], a[184:187], v[132:135]\n"
    "v_mfma_f32_16x16x32_bf16 v[164:167], v[28:31], a[248:251], v[164:167]\n"
    "s_waitcnt lgkmcnt(8)\n"
    "v_mfma_f32_16x16x32_bf16 v[80:83], v[32:35], a[56:59], v[80:83]\n"
    "v_mfma_f32_16x16x32_bf16 v[112:115], v[32:35], a[120:123], v[112:115]\n"
    "v_mfma_f32_16x16x32_bf16 v[144:147], v[32:35], a[184:187], v[144:147]\n"
    "v_mfma_f32_16x16x32_bf16 v[176:179], v[32:35], a[248:251], v[176:179]\n"
    "v_mfma_f32_16x16x32_bf16 v[84:87], v[36:39], a[56:59], v[84:87]\n"
    "v_mfma_f32_16x16x32_bf16 v[116:119], v[36:39], a[120:123], v[116:119]\n"
    "v_mfma_f32_16x16x32_bf16 v[148:151], v[36:39], a[184:187], v[148:151]\n"
    "v_mfma_f32_16x16x32_bf16 v[180:183], v[36:39], a[248:251], v[180:183]\n"
    "s_waitcnt lgkmcnt(4)\n"
    "v_mfma_f32_16x16x32_bf16 v[64:67], v[40:43], a[60:63], v[64:67]\n"
    "v_mfma_f32_16x16x32_bf16 v[96:99], v[40:43], a[124:127], v[96:99]\n"
    "v_mfma_f32_16x16x32_bf16 v[128:131], v[40:43], a[188:191], v[128:131]\n"
    "v_mfma_f32_16x16x32_bf16 v[160:163], v[40:43], a[252:255], v[160:163]\n"
    "v_mfma_f32_16x16x32_bf16 v[68:71], v[44:47], a[60:63], v[68:71]\n"
    "v_mfma_f32_16x16x32_bf16 v[100:103], v[44:47], a[124:127], v[100:103]\n"
    "v_mfma_f32_16x16x32_bf16 v[132:135], v[44:47], a[188:191], v[132:135]\n"
    "v_mfma_f32_16x16x32_bf16 v[164:167], v[44:47], a[252:255], v[164:167]\n"
    "s_waitcnt lgkmcnt(0)\n"
    "v_mfma_f32_16x16x32_bf16 v[80:83], v[48:51], a[60:63], v[80:83]\n"
    "v_mfma_f32_16x16x32_bf16 v[112:115], v[48:51], a[124:127], v[112:115]\n"
    "v_mfma_f32_16x16x32_bf16 v[144:147], v[48:51], a[188:191], v[144:147]\n"
    "v_mfma_f32_16x16x32_bf16 v[176:179], v[48:51], a[252:255], v[176:179]\n"
    "v_mfma_f32_16x16x32_bf16 v[84:87], v[52:55], a[60:63], v[84:87]\n"
    "v_mfma_f32_16x16x32_bf16 v[116:119], v[52:55], a[124:127], v[116:119]\n"
    "v_mfma_f32_16x16x32_bf16 v[148:151], v[52:55], a[188:191], v[148:151]\n"
    "v_mfma_f32_16x16x32_bf16 v[180:183], v[52:55], a[252:255], v[180:183]\n"
    "s_barrier\n"
    "s_mov_b64 s[44:45], %9\n"
    "s_and_b32 s45, s45, 0xffff\n"
    "s_mov_b32 s46, 1834112\n"
    "s_mov_b32 s47, 0x20000\n"
    "s_mul_i32 s56, s20, 458752\n"
    "s_mul_i32 s57, s20, 24576\n"
    "v_lshrrev_b32 v17, 3, v16\n"
    "v_lshlrev_b32 v17, 10, v17\n"
    "v_lshrrev_b32 v18, 4, v16\n"
    "v_lshlrev_b32 v18, 1, v18\n"
    "v_and_b32 v19, 7, v16\n"
    "v_xor_b32 v18, v18, v19\n"
    "v_lshl_add_u32 v4, v18, 4, v17\n"
    "v_add_u32 v5, 7168, v4\n"
    "v_add_u32 v6, 14336, v4\n"
    "v_add_u32 v7, 21504, v4\n"
    "v_mul_f32 v64, v64, v80\n"
    "v_mul_f32 v65, v65, v81\n"
    "v_mul_f32 v66, v66, v82\n"
    "v_mul_f32 v67, v67, v83\n"
    "v_mul_f32 v68, v68, v84\n"
    "v_mul_f32 v69, v69, v85\n"
    "v_mul_f32 v70, v70, v86\n"
    "v_mul_f32 v71, v71, v87\n"
    "v_cvt_pk_bf16_f32 v64, v64, v65\n"
    "v_cvt_pk_bf16_f32 v65, v66, v67\n"
    "v_cvt_pk_bf16_f32 v66, v68, v69\n"
    "v_cvt_pk_bf16_f32 v67, v70, v71\n"
    "s_mov_b32 s26, 4096\n"
    "buffer_store_dwordx4 v[64:67], v20, s[48:51], s26 offen offset:0 sc1\n"
    "v_mul_f32 v96, v96, v112\n"
    "v_mul_f32 v97, v97, v113\n"
    "v_mul_f32 v98, v98, v114\n"
    "v_mul_f32 v99, v99, v115\n"
    "v_mul_f32 v100, v100, v116\n"
    "v_mul_f32 v101, v101, v117\n"
    "v_mul_f32 v102, v102, v118\n"
    "v_mul_f32 v103, v103, v119\n"
    "v_cvt_pk_bf16_f32 v96, v96, v97\n"
    "v_cvt_pk_bf16_f32 v97, v98, v99\n"
    "v_cvt_pk_bf16_f32 v98, v100, v101\n"
    "v_cvt_pk_bf16_f32 v99, v102, v103\n"
    "buffer_store_dwordx4 v[96:99], v20, s[48:51], s26 offen offset:1024 sc1\n"
    "v_mul_f32 v128, v128, v144\n"
    "v_mul_f32 v129, v129, v145\n"
    "v_mul_f32 v130, v130, v146\n"
    "v_mul_f32 v131, v131, v147\n"
    "v_mul_f32 v132, v132, v148\n"
    "v_mul_f32 v133, v133, v149\n"
    "v_mul_f32 v134, v134, v150\n"
    "v_mul_f32 v135, v135, v151\n"
    "v_cvt_pk_bf16_f32 v128, v128, v129\n"
    "v_cvt_pk_bf16_f32 v129, v130, v131\n"
    "v_cvt_pk_bf16_f32 v130, v132, v133\n"
    "v_cvt_pk_bf16_f32 v131, v134, v135\n"
    "buffer_store_dwordx4 v[128:131], v20, s[48:51], s26 offen offset:2048 sc1\n"
    "v_mul_f32 v160, v160, v176\n"
    "v_mul_f32 v161, v161, v177\n"
    "v_mul_f32 v162, v162, v178\n"
    "v_mul_f32 v163, v163, v179\n"
    "v_mul_f32 v164, v164, v180\n"
    "v_mul_f32 v165, v165, v181\n"
    "v_mul_f32 v166, v166, v182\n"
    "v_mul_f32 v167, v167, v183\n"
    "v_cvt_pk_bf16_f32 v160, v160, v161\n"
    "v_cvt_pk_bf16_f32 v161, v162, v163\n"
    "v_cvt_pk_bf16_f32 v162, v164, v165\n"
    "v_cvt_pk_bf16_f32 v163, v166, v167\n"
    "buffer_store_dwordx4 v[160:163], v20, s[48:51], s26 offen offset:3072 sc1\n"
    "s_add_u32 m0, s57, 0\n"
    "s_add_u32 s58, s56, 0\n"
    "buffer_load_dwordx4 v4, s[44:47], s58 offen offset:0 lds\n"
    "buffer_load_dwordx4 v5, s[44:47], s58 offen offset:1024 lds\n"
    "buffer_load_dwordx4 v6, s[44:47], s58 offen offset:2048 lds\n"
    "buffer_load_dwordx4 v7, s[44:47], s58 offen offset:3072 lds\n"
    "s_add_u32 m0, s57, 4096\n"
    "s_add_u32 s58, s56, 32768\n"
    "buffer_load_dwordx4 v4, s[44:47], s58 offen offset:0 lds\n"
    "buffer_load_dwordx4 v5, s[44:47], s58 offen offset:1024 lds\n"
    "buffer_load_dwordx4 v6, s[44:47], s58 offen offset:2048 lds\n"
    "buffer_load_dwordx4 v7, s[44:47], s58 offen offset:3072 lds\n"
    "s_add_u32 m0, s57, 8192\n"
    "s_add_u32 s58, s56, 65536\n"
    "buffer_load_dwordx4 v4, s[44:47], s58 offen offset:0 lds\n"
    "buffer_load_dwordx4 v5, s[44:47], s58 offen offset:1024 lds\n"
    "buffer_load_dwordx4 v6, s[44:47], s58 offen offset:2048 lds\n"
    "buffer_load_dwordx4 v7, s[44:47], s58 offen offset:3072 lds\n"
        :: "s"(ws + WS_A2 + (size_t)rt * 262144), "s"(ws + WS_HH + (size_t)rt * 16 * 57344 + ct * 8192), "s"(w), "s"(ws + WS_FLAG), "s"(arr), "s"(FL_A + rt * 128), "s"(ldsb),
           "s"((const char*)A->wg + ct * 128), "s"((const char*)A->wu + ct * 128), "s"((const char*)A->wd + K5_CT * 128), "s"(tag)
        : "v0","v1","v2","v3","v4","v5","v6","v7","v8","v9","v10","v11","v12","v13","v14","v15","v16","v17","v18","v19","v20","v21","v22","v23",
      "v24","v25","v26","v27","v28","v29","v30","v31","v32","v33","v34","v35","v36","v37","v38","v39","v40","v41","v42","v43","v44","v45","v46","v47",
      "v48","v49","v50","v51","v52","v53","v54","v55","v56","v57","v58","v59","v60","v61","v62","v63","v64","v65","v66","v67","v68","v69","v70","v71",
      "v72","v73","v74","v75","v76","v77","v78","v79","v80","v81","v82","v83","v84","v85","v86","v87","v88","v89","v90","v91","v92","v93","v94","v95",
      "v96","v97","v98","v99","v100","v101","v102","v103","v104","v105","v106","v107","v108","v109","v110","v111","v112","v113","v114","v115","v116","v117","v118","v119",
      "v120","v121","v122","v123","v124","v125","v126","v127","v128","v129","v130","v131","v132","v133","v134","v135","v136","v137","v138","v139","v140","v141","v142","v143",
      "v144","v145","v146","v147","v148","v149","v150","v151","v152","v153","v154","v155","v156","v157","v158","v159","v160","v161","v162","v163","v164","v165","v166","v167",
      "v168","v169","v170","v171","v172","v173","v174","v175","v176","v177","v178","v179","v180","v181","v182","v183","a0","a1","a2","a3","a4","a5","a6","a7",
      "a8","a9","a10","a11","a12","a13","a14","a15","a16","a17","a18","a19","a20","a21","a22","a23","a24","a25","a26","a27","a28","a29","a30","a31",
      "a32","a33","a34","a35","a36","a37","a38","a39","a40","a41","a42","a43","a44","a45","a46","a47","a48","a49","a50","a51","a52","a53","a54","a55",
      "a56","a57","a58","a59","a60","a61","a62","a63","a64","a65","a66","a67","a68","a69","a70","a71","a72","a73","a74","a75","a76","a77","a78","a79",
      "a80","a81","a82","a83","a84","a85","a86","a87","a88","a89","a90","a91","a92","a93","a94","a95","a96","a97","a98","a99","a100","a101","a102","a103",
      "a104","a105","a106","a107","a108","a109","a110","a111","a112","a113","a114","a115","a116","a117","a118","a119","a120","a121","a122","a123","a124","a125","a126","a127",
      "a128","a129","a130","a131","a132","a133","a134","a135","a136","a137","a138","a139","a140","a141","a142","a143","a144","a145","a146","a147","a148","a149","a150","a151",
      "a152","a153","a154","a155","a156","a157","a158","a159","a160","a161","a162","a163","a164","a165","a166","a167","a168","a169","a170","a171","a172","a173","a174","a175",
      "a176","a177","a178","a179","a180","a181","a182","a183","a184","a185","a186","a187","a188","a189","a190","a191","a192","a193","a194","a195","a196","a197","a198","a199",
      "a200","a201","a202","a203","a204","a205","a206","a207","a208","a209","a210","a211","a212","a213","a214","a215","a216","a217","a218","a219","a220","a221","a222","a223",
      "a224","a225","a226","a227","a228","a229","a230","a231","a232","a233","a234","a235","a236","a237","a238","a239","a240","a241","a242","a243","a244","a245","a246","a247",
      "a248","a249","a250","a251","a252","a253","a254","a255","s20","s21","s22","s23","s24","s25","s26","s27","s40","s41","s42","s43","s44","s45","s46","s47",
      "s48","s49","s50","s51","s52","s53","s54","s55","s56","s57","s58","s59","s60","s61","s62","s63","s64","s65","s66","s67","scc","vcc","memory");
      STAMP3(); STAMP(4);
    } else {
      asm volatile("s_waitcnt vmcnt(0)\n\ts_barrier" ::: "memory");
      const int t = w * 64 + lane_now();
      if (t == 0) flag_set(ws, arr, tag);
      const u32x4* p = (const u32x4*)A->wd + K5_PFBASE; unsigned acc = 0;
#pragma unroll 1
      for (int q0 = 0; q0 < K5_PFN; ++q0) { u32x4 v[14];
#pragma unroll
        for (int q = 0; q < 14; ++q) v[q] = p[(q0 * 14 + q) * K5_PFSTEP];
#pragma unroll
        for (int q = 0; q < 14; ++q) acc ^= v[q][0]; }
      asm volatile("" :: "v"(acc));
      asm volatile(
    "v_mbcnt_lo_u32_b32 v16, -1, 0\n"
    "v_mbcnt_hi_u32_b32 v16, -1, v16\n"
    "s_mov_b32 s20, %1\n"
    "s_barrier\n"
    "s_mov_b64 s[44:45], %0\n"
    "s_and_b32 s45, s45, 0xffff\n"
    "s_mov_b32 s46, 1834112\n"
    "s_mov_b32 s47, 0x20000\n"
    "s_mul_i32 s56, s20, 458752\n"
    "s_mul_i32 s57, s20, 24576\n"
    "v_lshrrev_b32 v17, 3, v16\n"
    "v_lshlrev_b32 v17, 10, v17\n"
    "v_lshrrev_b32 v18, 4, v16\n"
    "v_lshlrev_b32 v18, 1, v18\n"
    "v_and_b32 v19, 7, v16\n"
    "v_xor_b32 v18, v18, v19\n"
    "v_lshl_add_u32 v4, v18, 4, v17\n"
    "v_add_u32 v5, 7168, v4\n"
    "v_add_u32 v6, 14336, v4\n"
    "v_add_u32 v7, 21504, v4\n"
    "s_add_u32 m0, s57, 0\n"
    "s_add_u32 s58, s56, 0\n"
    "buffer_load_dwordx4 v4, s[44:47], s58 offen offset:0 lds\n"
    "buffer_load_dwordx4 v5, s[44:47], s58 offen offset:1024 lds\n"
    "buffer_load_dwordx4 v6, s[44:47], s58 offen offset:2048 lds\n"
    "buffer_load_dwordx4 v7, s[44:47], s58 offen offset:3072 lds\n"
    "s_add_u32 m0, s57, 4096\n"
    "s_add_u32 s58, s56, 32768\n"
    "buffer_load_dwordx4 v4, s[44:47], s58 offen offset:0 lds\n"
    "buffer_load_dwordx4 v5, s[44:47], s58 offen offset:1024 lds\n"
    "buffer_load_dwordx4 v6, s[44:47], s58 offen offset:2048 lds\n"
    "buffer_load_dwordx4 v7, s[44:47], s58 offen offset:3072 lds\n"
    "s_add_u32 m0, s57, 8192\n"
    "s_add_u32 s58, s56, 65536\n"
    "buffer_load_dwordx4 v4, s[44:47], s58 offen offset:0 lds\n"
    "buffer_load_dwordx4 v5, s[44:47], s58 offen offset:1024 lds\n"
    "buffer_load_dwordx4 v6, s[44:47], s58 offen offset:2048 lds\n"
    "buffer_load_dwordx4 v7, s[44:47], s58 offen offset:3072 lds\n"
        :: "s"((const char*)A->wd + K5_CT * 128), "s"(w), "s"(ldsb)
        : "v4","v5","v6","v7","v16","v17","v18","v19","v20","s20","s44","s45","s46","s47","s56","s57","s58","scc","vcc","memory");
    }
#undef DIAGK
  }
  { IDS; TailArgsP A = tail_args(); char* ws = A->ws;
    const int rt = K5_RT, ct = K5_CT, lane = lane_now(), c = lane & 15, g = lane >> 4, row = rt * 64 + w * 16 + c, col = ct * 64 + g * 4;
#define DIAGK 5
    STAMP(0);
    f32x4 gb[4]; u32x4 xr[2]; f32x2 ac[8];
    asm volatile(
    "s_mov_b64 s[40:41], %14\n"
    "s_and_b32 s41, s41, 0xffff\n"
    "s_mov_b32 s42, 229376\n"
    "s_mov_b32 s43, 0x20000\n"
    "s_mov_b64 s[44:45], %15\n"
    "s_and_b32 s45, s45, 0xffff\n"
    "s_mov_b32 s46, 1834112\n"
    "s_mov_b32 s47, 0x20000\n"
    "v_mbcnt_lo_u32_b32 v12, -1, 0\n"
    "v_mbcnt_hi_u32_b32 v12, -1, v12\n"
    "s_mov_b32 s48, %16\n"
    "s_mul_i32 s50, s48, 57344\n"
    "s_mul_i32 s51, s48, 458752\n"
    "s_mul_i32 s49, s48, 24576\n"
    "v_lshrrev_b32 v13, 3, v12\n"
    "v_lshlrev_b32 v13, 10, v13\n"
    "v_lshrrev_b32 v14, 4, v12\n"
    "v_lshlrev_b32 v14, 1, v14\n"
    "v_and_b32 v15, 7, v12\n"
    "v_xor_b32 v14, v14, v15\n"
    "v_lshl_add_u32 v4, v14, 4, v13\n"
    "v_add_u32 v5, 7168, v4\n"
    "v_add_u32 v6, 14336, v4\n"
    "v_add_u32 v7, 21504, v4\n"
    "v_lshlrev_b32 v0, 4, v12\n"
    "v_lshrrev_b32 v13, 2, v12\n"
    "v_lshlrev_b32 v13, 7, v13\n"
    "v_and_b32 v14, 3, v12\n"
    "v_lshl_add_u32 v13, v14, 3, v13\n"
    "v_add_u32 v13, s49, v13\n"
    "v_bfe_u32 v14, v12, 3, 2\n"
    "v_lshlrev_b32 v14, 5, v14\n"
    "v_xor_b32 v15, 0, v14\n"
    "v_add_u32 v8, v13, v15\n"
    "v_xor_b32 v15, 32, v14\n"
    "v_add_u32 v9, v13, v15\n"
    "v_xor_b32 v15, 64, v14\n"
    "v_add_u32 v10, v13, v15\n"
    "v_xor_b32 v15, 96, v14\n"
    "v_add_u32 v11, v13, v15\n"
    "s_mov_b64 s[54:55], %21\n"
    "s_cmp_lt_i32 %22, 0\n"
    "s_cselect_b32 s56, 1, s48\n"
    "s_waitcnt vmcnt(12)\n"
    "s_barrier\n"
    "s_cmp_lg_u32 s56, 0\n"
    "s_cbranch_scc1 L_ar_%=\n"
    "v_mov_b32 v2, %22\n"
    "v_mov_b32 v3, %24\n"
    "s_mov_b64 exec, 1\n"
    "global_atomic_swap v2, v3, s[54:55]\n"
    "s_mov_b64 exec, -1\n"
    "L_ar_%=:\n"
    "s_add_u32 m0, s49, 12288\n"
    "s_add_u32 s52, s51, 98304\n"
    "buffer_load_dwordx4 v4, s[44:47], s52 offen offset:0 lds\n"
    "buffer_load_dwordx4 v5, s[44:47], s52 offen offset:1024 lds\n"
    "buffer_load_dwordx4 v6, s[44:47], s52 offen offset:2048 lds\n"
    "buffer_load_dwordx4 v7, s[44:47], s52 offen offset:3072 lds\n"
    "s_add_u32 m0, s49, 16384\n"
    "s_add_u32 s52, s51, 131072\n"
    "buffer_load_dwordx4 v4, s[44:47], s52 offen offset:0 lds\n"
    "buffer_load_dwordx4 v5, s[44:47], s52 offen offset:1024 lds\n"
    "buffer_load_dwordx4 v6, s[44:47], s52 offen offset:2048 lds\n"
    "buffer_load_dwordx4 v7, s[44:47], s52 offen offset:3072 lds\n"
    "s_add_u32 m0, s49, 20480\n"
    "s_add_u32 s52, s51, 163840\n"
    "buffer_load_dwordx4 v4, s[44:47], s52 offen offset:0 lds\n"
    "buffer_load_dwordx4 v5, s[44:47], s52 offen offset:1024 lds\n"
    "buffer_load_dwordx4 v6, s[44:47], s52 offen offset:2048 lds\n"
    "buffer_load_dwordx4 v7, s[44:47], s52 offen offset:3072 lds\n"
    "s_waitcnt vmcnt(20)\n"
    "ds_read_b64_tr_b16 v[16:17], v8 offset:0\n"
    "ds_read_b64_tr_b16 v[18:19], v8 offset:2048\n"
    "ds_read_b64_tr_b16 v[20:21], v9 offset:0\n"
    "ds_read_b64_tr_b16 v[22:23], v9 offset:2048\n"
    "ds_read_b64_tr_b16 v[24:25], v10 offset:0\n"
    "ds_read_b64_tr_b16 v[26:27], v10 offset:2048\n"
    "ds_read_b64_tr_b16 v[28:29], v11 offset:0\n"
    "ds_read_b64_tr_b16 v[30:31], v11 offset:2048\n"
    "s_waitcnt vmcnt(16)\n"
    "ds_read_b64_tr_b16 v[32:33], v8 offset:4096\n"
    "ds_read_b64_tr_b16 v[34:35], v8 offset:6144\n"
    "ds_read_b64_tr_b16 v[36:37], v9 offset:4096\n"
    "ds_read_b64_tr_b16 v[38:39], v9 offset:6144\n"
    "ds_read_b64_tr_b16 v[40:41], v10 offset:4096\n"
    "ds_read_b64_tr_b16 v[42:43], v10 offset:6144\n"
    "ds_read_b64_tr_b16 v[44:45], v11 offset:4096\n"
    "ds_read_b64_tr_b16 v[46:47], v11 offset:6144\n"
    "s_waitcnt lgkmcnt(8)\n"
    "s_add_u32 m0, s49, 0\n"
    "s_add_u32 s52, s51, 196608\n"
    "buffer_load_dwordx4 v4, s[44:47], s52 offen offset:0 lds\n"
    "buffer_load_dwordx4 v5, s[44:47], s52 offen offset:1024 lds\n"
    "buffer_load_dwordx4 v6, s[44:47], s52 offen offset:2048 lds\n"
    "buffer_load_dwordx4 v7, s[44:47], s52 offen offset:3072 lds\n"
    "s_waitcnt vmcnt(16)\n"
    "ds_read_b64_tr_b16 v[48:49], v8 offset:8192\n"
    "ds_read_b64_tr_b16 v[50:51], v8 offset:10240\n"
    "ds_read_b64_tr_b16 v[52:53], v9 offset:8192\n"
    "ds_read_b64_tr_b16 v[54:55], v9 offset:10240\n"
    "ds_read_b64_tr_b16 v[56:57], v10 offset:8192\n"
    "ds_read_b64_tr_b16 v[58:59], v10 offset:10240\n"
    "ds_read_b64_tr_b16 v[60:61], v11 offset:8192\n"
    "ds_read_b64_tr_b16 v[62:63], v11 offset:10240\n"
    "s_waitcnt lgkmcnt(8)\n"
    "s_add_u32 m0, s49, 4096\n"
    "s_add_u32 s52, s51, 229376\n"
    "buffer_load_dwordx4 v4, s[44:47], s52 offen offset:0 lds\n"
    "buffer_load_dwordx4 v5, s[44:47], s52 offen offset:1024 lds\n"
    "buffer_load_dwordx4 v6, s[44:47], s52 offen offset:2048 lds\n"
    "buffer_load_dwordx4 v7, s[44:47], s52 offen offset:3072 lds\n"
    "s_waitcnt vmcnt(16)\n"
    "ds_read_b64_tr_b16 v[64:65], v8 offset:12288\n"
    "ds_read_b64_tr_b16 v[66:67], v8 offset:14336\n"
    "ds_read_b64_tr_b16 v[68:69], v9 offset:12288\n"
    "ds_read_b64_tr_b16 v[70:71], v9 offset:14336\n"
    "ds_read_b64_tr_b16 v[72:73], v10 offset:12288\n"
    "ds_read_b64_tr_b16 v[74:75], v10 offset:14336\n"
    "ds_read_b64_tr_b16 v[76:77], v11 offset:12288\n"
    "ds_read_b64_tr_b16 v[78:79], v11 offset:14336\n"
    "s_waitcnt lgkmcnt(8)\n"
    "s_add_u32 m0, s49, 8192\n"
    "s_add_u32 s52, s51, 262144\n"
    "buffer_load_dwordx4 v4, s[44:47], s52 offen offset:0 lds\n"
    "buffer_load_dwordx4 v5, s[44:47], s52 offen offset:1024 lds\n"
    "buffer_load_dwordx4 v6, s[44:47], s52 offen offset:2048 lds\n"
    "buffer_load_dwordx4 v7, s[44:47], s52 offen offset:3072 lds\n"
    "s_waitcnt vmcnt(16)\n"
    "ds_read_b64_tr_b16 v[80:81], v8 offset:16384\n"
    "ds_read_b64_tr_b16 v[82:83], v8 offset:18432\n"
    "ds_read_b64_tr_b16 v[84:85], v9 offset:16384\n"
    "ds_read_b64_tr_b16 v[86:87], v9 offset:18432\n"
    "ds_read_b64_tr_b16 v[88:89], v10 offset:16384\n"
    "ds_read_b64_tr_b16 v[90:91], v10 offset:18432\n"
    "ds_read_b64_tr_b16 v[92:93], v11 offset:16384\n"
    "ds_read_b64_tr_b16 v[94:95], v11 offset:18432\n"
    "s_waitcnt lgkmcnt(8)\n"
    "s_add_u32 m0, s49, 12288\n"
    "s_add_u32 s52, s51, 294912\n"
    "buffer_load_dwordx4 v4, s[44:47], s52 offen offset:0 lds\n"
    "buffer_load_dwordx4 v5, s[44:47], s52 offen offset:1024 lds\n"
    "buffer_load_dwordx4 v6, s[44:47], s52 offen offset:2048 lds\n"
    "buffer_load_dwordx4 v7, s[44:47], s52 offen offset:3072 lds\n"
    "s_waitcnt vmcnt(16)\n"
    "ds_read_b64_tr_b16 v[96:97], v8 offset:20480\n"
    "ds_read_b64_tr_b16 v[98:99], v8 offset:22528\n"
    "ds_read_b64_tr_b16 v[100:101], v9 offset:20480\n"
    "ds_read_b64_tr_b16 v[102:103], v9 offset:22528\n"
    "ds_read_b64_tr_b16 v[104:105], v10 offset:20480\n"
    "ds_read_b64_tr_b16 v[106:107], v10 offset:22528\n"
    "ds_read_b64_tr_b16 v[108:109], v11 offset:20480\n"
    "ds_read_b64_tr_b16 v[110:111], v11 offset:22528\n"
    "s_waitcnt lgkmcnt(8)\n"
    "s_add_u32 m0, s49, 16384\n"
    "s_add_u32 s52, s51, 327680\n"
    "buffer_load_dwordx4 v4, s[44:47], s52 offen offset:0 lds\n"
    "buffer_load_dwordx4 v5, s[44:47], s52 offen offset:1024 lds\n"
    "buffer_load_dwordx4 v6, s[44:47], s52 offen offset:2048 lds\n"
    "buffer_load_dwordx4 v7, s[44:47], s52 offen offset:3072 lds\n"
    "s_waitcnt vmcnt(16)\n"
    "ds_read_b64_tr_b16 v[112:113], v8 offset:0\n"
    "ds_read_b64_tr_b16 v[114:115], v8 offset:2048\n"
    "ds_read_b64_tr_b16 v[116:117], v9 offset:0\n"
    "ds_read_b64_tr_b16 v[118:119], v9 offset:2048\n"
    "ds_read_b64_tr_b16 v[120:121], v10 offset:0\n"
    "ds_read_b64_tr_b16 v[122:123], v10 offset:2048\n"
    "ds_read_b64_tr_b16 v[124:125], v11 offset:0\n"
    "ds_read_b64_tr_b16 v[126:127], v11 offset:2048\n"
    "s_waitcnt lgkmcnt(8)\n"
    "s_add_u32 m0, s49, 20480\n"
    "s_add_u32 s52, s51, 360448\n"
    "buffer_load_dwordx4 v4, s[44:47], s52 offen offset:0 lds\n"
    "buffer_load_dwordx4 v5, s[44:47], s52 offen offset:1024 lds\n"
    "buffer_load_dwordx4 v6, s[44:47], s52 offen offset:2048 lds\n"
    "buffer_load_dwordx4 v7, s[44:47], s52 offen offset:3072 lds\n"
    "s_waitcnt vmcnt(16)\n"
    "ds_read_b64_tr_b16 v[128:129], v8 offset:4096\n"
    "ds_read_b64_tr_b16 v[130:131], v8 offset:6144\n"
    "ds_read_b64_tr_b16 v[132:133], v9 offset:4096\n"
    "ds_read_b64_tr_b16 v[134:135], v9 offset:6144\n"
    "ds_read_b64_tr_b16 v[136:137], v10 offset:4096\n"
    "ds_read_b64_tr_b16 v[138:139], v10 offset:6144\n"
    "ds_read_b64_tr_b16 v[140:141], v11 offset:4096\n"
    "ds_read_b64_tr_b16 v[142:143], v11 offset:6144\n"
    "s_waitcnt lgkmcnt(8)\n"
    "s_add_u32 m0, s49, 0\n"
    "s_add_u32 s52, s51, 393216\n"
    "buffer_load_dwordx4 v4, s[44:47], s52 offen offset:0 lds\n"
    "buffer_load_dwordx4 v5, s[44:47], s52 offen offset:1024 lds\n"
    "buffer_load_dwordx4 v6, s[44:47], s52 offen offset:2048 lds\n"
    "buffer_load_dwordx4 v7, s[44:47], s52 offen offset:3072 lds\n"
    "s_waitcnt vmcnt(16)\n"
    "ds_read_b64_tr_b16 v[144:145], v8 offset:8192\n"
    "ds_read_b64_tr_b16 v[146:147], v8 offset:10240\n"
    "ds_read_b64_tr_b16 v[148:149], v9 offset:8192\n"
    "ds_read_b64_tr_b16 v[150:151], v9 offset:10240\n"
    "ds_read_b64_tr_b16 v[152:153], v10 offset:8192\n"
    "ds_read_b64_tr_b16 v[154:155], v10 offset:10240\n"
    "ds_read_b64_tr_b16 v[156:157], v11 offset:8192\n"
    "ds_read_b64_tr_b16 v[158:159], v11 offset:10240\n"
    "s_waitcnt lgkmcnt(8)\n"
    "s_add_u32 m0, s49, 4096\n"
    "s_add_u32 s52, s51, 425984\n"
    "buffer_load_dwordx4 v4, s[44:47], s52 offen offset:0 lds\n"
    "buffer_load_dwordx4 v5, s[44:47], s52 offen offset:1024 lds\n"
    "buffer_load_dwordx4 v6, s[44:47], s52 offen offset:2048 lds\n"
    "buffer_load_dwordx4 v7, s[44:47], s52 offen offset:3072 lds\n"
    "s_waitcnt vmcnt(16)\n"
    "ds_read_b64_tr_b16 a[176:177], v8 offset:12288\n"
    "ds_read_b64_tr_b16 a[178:179], v8 offset:14336\n"
    "ds_read_b64_tr_b16 a[180:181], v9 offset:12288\n"
    "ds_read_b64_tr_b16 a[182:183], v9 offset:14336\n"
    "ds_read_b64_tr_b16 a[184:185], v10 offset:12288\n"
    "ds_read_b64_tr_b16 a[186:187], v10 offset:14336\n"
    "ds_read_b64_tr_b16 a[188:189], v11 offset:12288\n"
    "ds_read_b64_tr_b16 a[190:191], v11 offset:14336\n"
    "s_waitcnt vmcnt(12)\n"
    "ds_read_b64_tr_b16 a[192:193], v8 offset:16384\n"
    "ds_read_b64_tr_b16 a[194:195], v8 offset:18432\n"
    "ds_read_b64_tr_b16 a[196:197], v9 offset:16384\n"
    "ds_read_b64_tr_b16 a[198:199], v9 offset:18432\n"
    "ds_read_b64_tr_b16 a[200:201], v10 offset:16384\n"
    "ds_read_b64_tr_b16 a[202:203], v10 offset:18432\n"
    "ds_read_b64_tr_b16 a[204:205], v11 offset:16384\n"
    "ds_read_b64_tr_b16 a[206:207], v11 offset:18432\n"
    "s_waitcnt vmcnt(8)\n"
    "ds_read_b64_tr_b16 a[208:209], v8 offset:20480\n"
    "ds_read_b64_tr_b16 a[210:211], v8 offset:22528\n"
    "ds_read_b64_tr_b16 a[212:213], v9 offset:20480\n"
    "ds_read_b64_tr_b16 a[214:215], v9 offset:22528\n"
    "ds_read_b64_tr_b16 a[216:217], v10 offset:20480\n"
    "ds_read_b64_tr_b16 a[218:219], v10 offset:22528\n"
    "ds_read_b64_tr_b16 a[220:221], v11 offset:20480\n"
    "ds_read_b64_tr_b16 a[222:223], v11 offset:22528\n"
    "s_waitcnt vmcnt(4)\n"
    "ds_read_b64_tr_b16 a[224:225], v8 offset:0\n"
    "ds_read_b64_tr_b16 a[226:227], v8 offset:2048\n"
    "ds_read_b64_tr_b16 a[228:229], v9 offset:0\n"
    "ds_read_b64_tr_b16 a[230:231], v9 offset:2048\n"
    "ds_read_b64_tr_b16 a[232:233], v10 offset:0\n"
    "ds_read_b64_tr_b16 a[234:235], v10 offset:2048\n"
    "ds_read_b64_tr_b16 a[236:237], v11 offset:0\n"
    "ds_read_b64_tr_b16 a[238:239], v11 offset:2048\n"
    "s_waitcnt vmcnt(0)\n"
    "ds_read_b64_tr_b16 a[240:241], v8 offset:4096\n"
    "ds_read_b64_tr_b16 a[242:243], v8 offset:6144\n"
    "ds_read_b64_tr_b16 a[244:245], v9 offset:4096\n"
    "ds_read_b64_tr_b16 a[246:247], v9 offset:6144\n"
    "ds_read_b64_tr_b16 a[248:249], v10 offset:4096\n"
    "ds_read_b64_tr_b16 a[250:251], v10 offset:6144\n"
    "ds_read_b64_tr_b16 a[252:253], v11 offset:4096\n"
    "ds_read_b64_tr_b16 a[254:255], v11 offset:6144\n"
    "global_load_dwordx4 %10, %20, %18 offset:0 sc1\n"
    "global_load_dwordx4 %11, %20, %18 offset:64 sc1\n"
    "global_load_dwordx4 %12, %20, %18 offset:128 sc1\n"
    "global_load_dwordx4 %13, %20, %18 offset:192 sc1\n"
    "s_cmp_lg_u32 s48, 0\n"
    "s_cbranch_scc1 L_po_%=_x\n"
    "v_min_u32 v2, 27, v12\n"
    "v_lshlrev_b32 v2, 2, v2\n"
    "v_add_u32 v2, %23, v2\n"
    "global_load_dword v3, v2, s[54:55] sc1\n"
    "s_waitcnt vmcnt(0)\n"
    "v_cmp_ne_u32 vcc, %24, v3\n"
    "s_mov_b32 s56, 0x400000\n"
    "s_nop 1\n"
    "s_cbranch_vccz L_po_%=_x\n"
    "L_po_%=:\n"
    "s_sleep 2\n"
    "global_load_dword v3, v2, s[54:55] sc1\n"
    "s_waitcnt vmcnt(0)\n"
    "v_cmp_ne_u32 vcc, %24, v3\n"
    "s_sub_u32 s56, s56, 1\n"
    "s_cbranch_scc1 L_po_%=_x\n"
    "s_nop 0\n"
    "s_cbranch_vccnz L_po_%=\n"
    "L_po_%=_x:\n"
    "s_waitcnt vmcnt(0)\n"
    "s_barrier\n"
    "s_add_u32 s53, s50, 0\n"
    "buffer_load_dwordx4 a[64:67], v0, s[40:43], s53 offen offset:0 sc1\n"
    "buffer_load_dwordx4 a[68:71], v0, s[40:43], s53 offen offset:1024 sc1\n"
    "buffer_load_dwordx4 a[72:75], v0, s[40:43], s53 offen offset:2048 sc1\n"
    "buffer_load_dwordx4 a[76:79], v0, s[40:43], s53 offen offset:3072 sc1\n"
    "s_add_u32 s53, s50, 4096\n"
    "buffer_load_dwordx4 a[80:83], v0, s[40:43], s53 offen offset:0 sc1\n"
    "buffer_load_dwordx4 a[84:87], v0, s[40:43], s53 offen offset:1024 sc1\n"
    "buffer_load_dwordx4 a[88:91], v0, s[40:43], s53 offen offset:2048 sc1\n"
    "buffer_load_dwordx4 a[92:95], v0, s[40:43], s53 offen offset:3072 sc1\n"
    "s_add_u32 s53, s50, 8192\n"
    "buffer_load_dwordx4 a[96:99], v0, s[40:43], s53 offen offset:0 sc1\n"
    "buffer_load_dwordx4 a[100:103], v0, s[40:43], s53 offen offset:1024 sc1\n"
    "buffer_load_dwordx4 a[104:107], v0, s[40:43], s53 offen offset:2048 sc1\n"
    "buffer_load_dwordx4 a[108:111], v0, s[40:43], s53 offen offset:3072 sc1\n"
    "s_add_u32 s53, s50, 12288\n"
    "buffer_load_dwordx4 a[112:115], v0, s[40:43], s53 offen offset:0 sc1\n"
    "buffer_load_dwordx4 a[116:119], v0, s[40:43], s53 offen offset:1024 sc1\n"
    "buffer_load_dwordx4 a[120:123], v0, s[40:43], s53 offen offset:2048 sc1\n"
    "buffer_load_dwordx4 a[124:127], v0, s[40:43], s53 offen offset:3072 sc1\n"
    "s_add_u32 s53, s50, 16384\n"
    "buffer_load_dwordx4 a[128:131], v0, s[40:43], s53 offen offset:0 sc1\n"
    "buffer_load_dwordx4 a[132:135], v0, s[40:43], s53 offen offset:1024 sc1\n"
    "buffer_load_dwordx4 a[136:139], v0, s[40:43], s53 offen offset:2048 sc1\n"
    "buffer_load_dwordx4 a[140:143], v0, s[40:43], s53 offen offset:3072 sc1\n"
    "s_add_u32 s53, s50, 20480\n"
    "buffer_load_dwordx4 a[144:147], v0, s[40:43], s53 offen offset:0 sc1\n"
    "buffer_load_dwordx4 a[148:151], v0, s[40:43], s53 offen offset:1024 sc1\n"
    "buffer_load_dwordx4 a[152:155], v0, s[40:43], s53 offen offset:2048 sc1\n"
    "buffer_load_dwordx4 a[156:159], v0, s[40:43], s53 offen offset:3072 sc1\n"
    "s_waitcnt lgkmcnt(0)\n"
    "s_waitcnt vmcnt(20)\n"
    "v_mfma_f32_16x16x32_bf16 a[0:3], v[16:19], a[64:67], 0\n"
    "s_add_u32 s53, s50, 24576\n"
    "v_mfma_f32_16x16x32_bf16 a[4:7], v[20:23], a[64:67], 0\n"
    "v_mfma_f32_16x16x32_bf16 a[8:11], v[24:27], a[64:67], 0\n"
    "v_mfma_f32_16x16x32_bf16 a[12:15], v[28:31], a[64:67], 0\n"
    "buffer_load_dwordx4 a[160:163], v0, s[40:43], s53 offen offset:0 sc1\n"
    "v_mfma_f32_16x16x32_bf16 a[16:19], v[16:19], a[68:71], 0\n"
    "v_mfma_f32_16x16x32_bf16 a[20:23], v[20:23], a[68:71], 0\n"
    "v_mfma_f32_16x16x32_bf16 a[24:27], v[24:27], a[68:71], 0\n"
    "buffer_load_dwordx4 a[164:167], v0, s[40:43], s53 offen offset:1024 sc1\n"
    "v_mfma_f32_16x16x32_bf16 a[28:31], v[28:31], a[68:71], 0\n"
    "v_mfma_f32_16x16x32_bf16 a[32:35], v[16:19], a[72:75], 0\n"
    "v_mfma_f32_16x16x32_bf16 a[36:39], v[20:23], a[72:75], 0\n"
    "buffer_load_dwordx4 a[168:171], v0, s[40:43], s53 offen offset:2048 sc1\n"
    "v_mfma_f32_16x16x32_bf16 a[40:43], v[24:27], a[72:75], 0\n"
    "v_mfma_f32_16x16x32_bf16 a[44:47], v[28:31], a[72:75], 0\n"
    "v_mfma_f32_16x16x32_bf16 a[48:51], v[16:19], a[76:79], 0\n"
    "buffer_load_dwordx4 a[172:175], v0, s[40:43], s53 offen offset:3072 sc1\n"
    "v_mfma_f32_16x16x32_bf16 a[52:55], v[20:23], a[76:79], 0\n"
    "v_mfma_f32_16x16x32_bf16 a[56:59], v[24:27], a[76:79], 0\n"
    "v_mfma_f32_16x16x32_bf16 a[60:63], v[28:31], a[76:79], 0\n"
    "s_waitcnt vmcnt(20)\n"
    "v_mfma_f32_16x16x32_bf16 a[0:3], v[32:35], a[80:83], a[0:3]\n"
    "s_add_u32 s53, s50, 28672\n"
    "v_mfma_f32_16x16x32_bf16 a[4:7], v[36:39], a[80:83], a[4:7]\n"
    "v_mfma_f32_16x16x32_bf16 a[8:11], v[40:43], a[80:83], a[8:11]\n"
    "v_mfma_f32_16x16x32_bf16 a[12:15], v[44:47], a[80:83], a[12:15]\n"
    "buffer_load_dwordx4 a[64:67], v0, s[40:43], s53 offen offset:0 sc1\n"
    "v_mfma_f32_16x16x32_bf16 a[16:19], v[32:35], a[84:87], a[16:19]\n"
    "v_mfma_f32_16x16x32_bf16 a[20:23], v[36:39], a[84:87], a[20:23]\n"
    "v_mfma_f32_16x16x32_bf16 a[24:27], v[40:43], a[84:87], a[24:27]\n"
    "buffer_load_dwordx4 a[68:71], v0, s[40:43], s53 offen offset:1024 sc1\n"
    "v_mfma_f32_16x16x32_bf16 a[28:31], v[44:47], a[84:87], a[28:31]\n"
    "v_mfma_f32_16x16x32_bf16 a[32:35], v[32:35], a[88:91], a[32:35]\n"
    "v_mfma_f32_16x16x32_bf16 a[36:39], v[36:39], a[88:91], a[36:39]\n"
    "buffer_load_dwordx4 a[72:75], v0, s[40:43], s53 offen offset:2048 sc1\n"
    "v_mfma_f32_16x16x32_bf16 a[40:43], v[40:43], a[88:91], a[40:43]\n"
    "v_mfma_f32_16x16x32_bf16 a[44:47], v[44:47], a[88:91], a[44:47]\n"
    "v_mfma_f32_16x16x32_bf16 a[48:51], v[32:35], a[92:95], a[48:51]\n"
    "buffer_load_dwordx4 a[76:79], v0, s[40:43], s53 offen offset:3072 sc1\n"
    "v_mfma_f32_16x16x32_bf16 a[52:55], v[36:39], a[92:95], a[52:55]\n"
    "v_mfma_f32_16x16x32_bf16 a[56:59], v[40:43], a[92:95], a[56:59]\n"
    "v_mfma_f32_16x16x32_bf16 a[60:63], v[44:47], a[92:95], a[60:63]\n"
    "s_waitcnt vmcnt(20)\n"
    "v_mfma_f32_16x16x32_bf16 a[0:3], v[48:51], a[96:99], a[0:3]\n"
    "s_add_u32 s53, s50, 32768\n"
    "v_mfma_f32_16x16x32_bf16 a[4:7], v[52:55], a[96:99], a[4:7]\n"
    "v_mfma_f32_16x16x32_bf16 a[8:11], v[56:59], a[96:99], a[8:11]\n"
    "v_mfma_f32_16x16x32_bf16 a[12:15], v[60:63], a[96:99], a[12:15]\n"
    "buffer_load_dwordx4 a[80:83], v0, s[40:43], s53 offen offset:0 sc1\n"
    "v_mfma_f32_16x16x32_bf16 a[16:19], v[48:51], a[100:103], a[16:19]\n"
    "v_mfma_f32_16x16x32_bf16 a[20:23], v[52:55], a[100:103], a[20:23]\n"
    "v_mfma_f32_16x16x32_bf16 a[24:27], v[56:59], a[100:103], a[24:27]\n"
    "buffer_load_dwordx4 a[84:87], v0, s[40:43], s53 offen offset:1024 sc1\n"
    "v_mfma_f32_16x16x32_bf16 a[28:31], v[60:63], a[100:103], a[28:31]\n"
    "v_mfma_f32_16x16x32_bf16 a[32:35], v[48:51], a[104:107], a[32:35]\n"
    "v_mfma_f32_16x16x32_bf16 a[36:39], v[52:55], a[104:107], a[36:39]\n"
    "buffer_load_dwordx4 a[88:91], v0, s[40:43], s53 offen offset:2048 sc1\n"
    "v_mfma_f32_16x16x32_bf16 a[40:43], v[56:59], a[104:107], a[40:43]\n"
    "v_mfma_f32_16x16x32_bf16 a[44:47], v[60:63], a[104:107], a[44:47]\n"
    "v_mfma_f32_16x16x32_bf16 a[48:51], v[48:51], a[108:111], a[48:51]\n"
    "buffer_load_dwordx4 a[92:95], v0, s[40:43], s53 offen offset:3072 sc1\n"
    "v_mfma_f32_16x16x32_bf16 a[52:55], v[52:55], a[108:111], a[52:55]\n"
    "v_mfma_f32_16x16x32_bf16 a[56:59], v[56:59], a[108:111], a[56:59]\n"
    "v_mfma_f32_16x16x32_bf16 a[60:63], v[60:63], a[108:111], a[60:63]\n"
    "s_waitcnt vmcnt(20)\n"
    "v_mfma_f32_16x16x32_bf16 a[0:3], v[64:67], a[112:115], a[0:3]\n"
    "s_add_u32 s53, s50, 36864\n"
    "v_mfma_f32_16x16x32_bf16 a[4:7], v[68:71], a[112:115], a[4:7]\n"
    "v_mfma_f32_16x16x32_bf16 a[8:11], v[72:75], a[112:115], a[8:11]\n"
    "v_mfma_f32_16x16x32_bf16 a[12:15], v[76:79], a[112:115], a[12:15]\n"
    "buffer_load_dwordx4 a[96:99], v0, s[40:43], s53 offen offset:0 sc1\n"
    "v_mfma_f32_16x16x32_bf16 a[16:19], v[64:67], a[116:119], a[16:19]\n"
    "v_mfma_f32_16x16x32_bf16 a[20:23], v[68:71], a[116:119], a[20:23]\n"
    "v_mfma_f32_16x16x32_bf16 a[24:27], v[72:75], a[116:119], a[24:27]\n"
    "buffer_load_dwordx4 a[100:103], v0, s[40:43], s53 offen offset:1024 sc1\n"
    "v_mfma_f32_16x16x32_bf16 a[28:31], v[76:79], a[116:119], a[28:31]\n"
    "v_mfma_f32_16x16x32_bf16 a[32:35], v[64:67], a[120:123], a[32:35]\n"
    "v_mfma_f32_16x16x32_bf16 a[36:39], v[68:71], a[120:123], a[36:39]\n"
    "buffer_load_dwordx4 a[104:107], v0, s[40:43], s53 offen offset:2048 sc1\n"
    "v_mfma_f32_16x16x32_bf16 a[40:43], v[72:75], a[120:123], a[40:43]\n"
    "v_mfma_f32_16x16x32_bf16 a[44:47], v[76:79], a[120:123], a[44:47]\n"
    "v_mfma_f32_16x16x32_bf16 a[48:51], v[64:67], a[124:127], a[48:51]\n"
    "buffer_load_dwordx4 a[108:111], v0, s[40:43], s53 offen offset:3072 sc1\n"
    "v_mfma_f32_16x16x32_bf16 a[52:55], v[68:71], a[124:127], a[52:55]\n"
    "v_mfma_f32_16x16x32_bf16 a[56:59], v[72:75], a[124:127], a[56:59]\n"
    "v_mfma_f32_16x16x32_bf16 a[60:63], v[76:79], a[124:127], a[60:63]\n"
    "s_waitcnt vmcnt(20)\n"
    "v_mfma_f32_16x16x32_bf16 a[0:3], v[80:83], a[128:131], a[0:3]\n"
    "s_add_u32 s53, s50, 40960\n"
    "v_mfma_f32_16x16x32_bf16 a[4:7], v[84:87], a[128:131], a[4:7]\n"
    "v_mfma_f32_16x16x32_bf16 a[8:11], v[88:91], a[128:131], a[8:11]\n"
    "v_mfma_f32_16x16x32_bf16 a[12:15], v[92:95], a[128:131], a[12:15]\n"
    "buffer_load_dwordx4 a[112:115], v0, s[40:43], s53 offen offset:0 sc1\n"
    "v_mfma_f32_16x16x32_bf16 a[16:19], v[80:83], a[132:135], a[16:19]\n"
    "v_mfma_f32_16x16x32_bf16 a[20:23], v[84:87], a[132:135], a[20:23]\n"
    "v_mfma_f32_16x16x32_bf16 a[24:27], v[88:91], a[132:135], a[24:27]\n"
    "buffer_load_dwordx4 a[116:119], v0, s[40:43], s53 offen offset:1024 sc1\n"
    "v_mfma_f32_16x16x32_bf16 a[28:31], v[92:95], a[132:135], a[28:31]\n"
    "v_mfma_f32_16x16x32_bf16 a[32:35], v[80:83], a[136:139], a[32:35]\n"
    "v_mfma_f32_16x16x32_bf16 a[36:39], v[84:87], a[136:139], a[36:39]\n"
    "buffer_load_dwordx4 a[120:123], v0, s[40:43], s53 offen offset:2048 sc1\n"
    "v_mfma_f32_16x16x32_bf16 a[40:43], v[88:91], a[136:139], a[40:43]\n"
    "v_mfma_f32_16x16x32_bf16 a[44:47], v[92:95], a[136:139], a[44:47]\n"
    "v_mfma_f32_16x16x32_bf16 a[48:51], v[80:83], a[140:143], a[48:51]\n"
    "buffer_load_dwordx4 a[124:127], v0, s[40:43], s53 offen offset:3072 sc1\n"
    "v_mfma_f32_16x16x32_bf16 a[52:55], v[84:87], a[140:143], a[52:55]\n"
    "v_mfma_f32_16x16x32_bf16 a[56:59], v[88:91], a[140:143], a[56:59]\n"
    "v_mfma_f32_16x16x32_bf16 a[60:63], v[92:95], a[140:143], a[60:63]\n"
    "s_waitcnt vmcnt(20)\n"
    "v_mfma_f32_16x16x32_bf16 a[0:3], v[96:99], a[144:147], a[0:3]\n"
    "s_add_u32 s53, s50, 45056\n"
    "v_mfma_f32_16x16x32_bf16 a[4:7], v[100:103], a[144:147], a[4:7]\n"
    "v_mfma_f32_16x16x32_bf16 a[8:11], v[104:107], a[144:147], a[8:11]\n"
    "v_mfma_f32_16x16x32_bf16 a[12:15], v[108:111], a[144:147], a[12:15]\n"
    "buffer_load_dwordx4 a[128:131], v0, s[40:43], s53 offen offset:0 sc1\n"
    "v_mfma_f32_16x16x32_bf16 a[16:19], v[96:99], a[148:151], a[16:19]\n"
    "v_mfma_f32_16x16x32_bf16 a[20:23], v[100:103], a[148:151], a[20:23]\n"
    "v_mfma_f32_16x16x32_bf16 a[24:27], v[104:107], a[148:151], a[24:27]\n"
    "buffer_load_dwordx4 a[132:135], v0, s[40:43], s53 offen offset:1024 sc1\n"
    "v_mfma_f32_16x16x32_bf16 a[28:31], v[108:111], a[148:151], a[28:31]\n"
    "v_mfma_f32_16x16x32_bf16 a[32:35], v[96:99], a[152:155], a[32:35]\n"
    "v_mfma_f32_16x16x32_bf16 a[36:39], v[100:103], a[152:155], a[36:39]\n"
    "buffer_load_dwordx4 a[136:139], v0, s[40:43], s53 offen offset:2048 sc1\n"
    "v_mfma_f32_16x16x32_bf16 a[40:43], v[104:107], a[152:155], a[40:43]\n"
    "v_mfma_f32_16x16x32_bf16 a[44:47], v[108:111], a[152:155], a[44:47]\n"
    "v_mfma_f32_16x16x32_bf16 a[48:51], v[96:99], a[156:159], a[48:51]\n"
    "buffer_load_dwordx4 a[140:143], v0, s[40:43], s53 offen offset:3072 sc1\n"
    "v_mfma_f32_16x16x32_bf16 a[52:55], v[100:103], a[156:159], a[52:55]\n"
    "v_mfma_f32_16x16x32_bf16 a[56:59], v[104:107], a[156:159], a[56:59]\n"
    "v_mfma_f32_16x16x32_bf16 a[60:63], v[108:111], a[156:159], a[60:63]\n"
    "s_waitcnt vmcnt(20)\n"
    "v_mfma_f32_16x16x32_bf16 a[0:3], v[112:115], a[160:163], a[0:3]\n"
    "s_add_u32 s53, s50, 49152\n"
    "v_mfma_f32_16x16x32_bf16 a[4:7], v[116:119], a[160:163], a[4:7]\n"
    "v_mfma_f32_16x16x32_bf16 a[8:11], v[120:123], a[160:163], a[8:11]\n"
    "v_mfma_f32_16x16x32_bf16 a[12:15], v[124:127], a[160:163], a[12:15]\n"
    "buffer_load_dwordx4 a[144:147], v0, s[40:43], s53 offen offset:0 sc1\n"
    "v_mfma_f32_16x16x32_bf16 a[16:19], v[112:115], a[164:167], a[16:19]\n"
    "v_mfma_f32_16x16x32_bf16 a[20:23], v[116:119], a[164:167], a[20:23]\n"
    "v_mfma_f32_16x16x32_bf16 a[24:27], v[120:123], a[164:167], a[24:27]\n"
    "buffer_load_dwordx4 a[148:151], v0, s[40:43], s53 offen offset:1024 sc1\n"
    "v_mfma_f32_16x16x32_bf16 a[28:31], v[124:127], a[164:167], a[28:31]\n"
    "v_mfma_f32_16x16x32_bf16 a[32:35], v[112:115], a[168:171], a[32:35]\n"
    "v_mfma_f32_16x16x32_bf16 a[36:39], v[116:119], a[168:171], a[36:39]\n"
    "buffer_load_dwordx4 a[152:155], v0, s[40:43], s53 offen offset:2048 sc1\n"
    "v_mfma_f32_16x16x32_bf16 a[40:43], v[120:123], a[168:171], a[40:43]\n"
    "v_mfma_f32_16x16x32_bf16 a[44:47], v[124:127], a[168:171], a[44:47]\n"
    "v_mfma_f32_16x16x32_bf16 a[48:51], v[112:115], a[172:175], a[48:51]\n"
    "buffer_load_dwordx4 a[156:159], v0, s[40:43], s53 offen offset:3072 sc1\n"
    "v_mfma_f32_16x16x32_bf16 a[52:55], v[116:119], a[172:175], a[52:55]\n"
    "v_mfma_f32_16x16x32_bf16 a[56:59], v[120:123], a[172:175], a[56:59]\n"
    "v_mfma_f32_16x16x32_bf16 a[60:63], v[124:127], a[172:175], a[60:63]\n"
    "s_waitcnt vmcnt(20)\n"
    "v_mfma_f32_16x16x32_bf16 a[0:3], v[128:131], a[64:67], a[0:3]\n"
    "s_add_u32 s53, s50, 53248\n"
    "v_mfma_f32_16x16x32_bf16 a[4:7], v[132:135], a[64:67], a[4:7]\n"
    "v_mfma_f32_16x16x32_bf16 a[8:11], v[136:139], a[64:67], a[8:11]\n"
    "v_mfma_f32_16x16x32_bf16 a[12:15], v[140:143], a[64:67], a[12:15]\n"
    "buffer_load_dwordx4 a[160:163], v0, s[40:43], s53 offen offset:0 sc1\n"
    "v_mfma_f32_16x16x32_bf16 a[16:19], v[128:131], a[68:71], a[16:19]\n"
    "v_mfma_f32_16x16x32_bf16 a[20:23], v[132:135], a[68:71], a[20:23]\n"
    "v_mfma_f32_16x16x32_bf16 a[24:27], v[136:139], a[68:71], a[24:27]\n"
    "buffer_load_dwordx4 a[164:167], v0, s[40:43], s53 offen offset:1024 sc1\n"
    "v_mfma_f32_16x16x32_bf16 a[28:31], v[140:143], a[68:71], a[28:31]\n"
    "v_mfma_f32_16x16x32_bf16 a[32:35], v[128:131], a[72:75], a[32:35]\n"
    "v_mfma_f32_16x16x32_bf16 a[36:39], v[132:135], a[72:75], a[36:39]\n"
    "buffer_load_dwordx4 a[168:171], v0, s[40:43], s53 offen offset:2048 sc1\n"
    "v_mfma_f32_16x16x32_bf16 a[40:43], v[136:139], a[72:75], a[40:43]\n"
    "v_mfma_f32_16x16x32_bf16 a[44:47], v[140:143], a[72:75], a[44:47]\n"
    "v_mfma_f32_16x16x32_bf16 a[48:51], v[128:131], a[76:79], a[48:51]\n"
    "buffer_load_dwordx4 a[172:175], v0, s[40:43], s53 offen offset:3072 sc1\n"
    "v_mfma_f32_16x16x32_bf16 a[52:55], v[132:135], a[76:79], a[52:55]\n"
    "v_mfma_f32_16x16x32_bf16 a[56:59], v[136:139], a[76:79], a[56:59]\n"
    "v_mfma_f32_16x16x32_bf16 a[60:63], v[140:143], a[76:79], a[60:63]\n"
    "s_waitcnt vmcnt(20)\n"
    "v_mfma_f32_16x16x32_bf16 a[0:3], v[144:147], a[80:83], a[0:3]\n"
    "global_load_dwordx4 %8, %19, %18 offset:0 sc1\n"
    "v_mfma_f32_16x16x32_bf16 a[4:7], v[148:151], a[80:83], a[4:7]\n"
    "v_mfma_f32_16x16x32_bf16 a[8:11], v[152:155], a[80:83], a[8:11]\n"
    "v_mfma_f32_16x16x32_bf16 a[12:15], v[156:159], a[80:83], a[12:15]\n"
    "global_load_dwordx4 %9, %19, %18 offset:1024 sc1\n"
    "v_mfma_f32_16x16x32_bf16 a[16:19], v[144:147], a[84:87], a[16:19]\n"
    "v_mfma_f32_16x16x32_bf16 a[20:23], v[148:151], a[84:87], a[20:23]\n"
    "v_mfma_f32_16x16x32_bf16 a[24:27], v[152:155], a[84:87], a[24:27]\n"
    "v_mfma_f32_16x16x32_bf16 a[28:31], v[156:159], a[84:87], a[28:31]\n"
    "v_mfma_f32_16x16x32_bf16 a[32:35], v[144:147], a[88:91], a[32:35]\n"
    "v_mfma_f32_16x16x32_bf16 a[36:39], v[148:151], a[88:91], a[36:39]\n"
    "v_mfma_f32_16x16x32_bf16 a[40:43], v[152:155], a[88:91], a[40:43]\n"
    "v_mfma_f32_16x16x32_bf16 a[44:47], v[156:159], a[88:91], a[44:47]\n"
    "v_mfma_f32_16x16x32_bf16 a[48:51], v[144:147], a[92:95], a[48:51]\n"
    "v_mfma_f32_16x16x32_bf16 a[52:55], v[148:151], a[92:95], a[52:55]\n"
    "v_mfma_f32_16x16x32_bf16 a[56:59], v[152:155], a[92:95], a[56:59]\n"
    "v_mfma_f32_16x16x32_bf16 a[60:63], v[156:159], a[92:95], a[60:63]\n"
    "s_waitcnt vmcnt(18)\n"
    "v_mfma_f32_16x16x32_bf16 a[0:3], a[176:179], a[96:99], a[0:3]\n"
    "v_mfma_f32_16x16x32_bf16 a[4:7], a[180:183], a[96:99], a[4:7]\n"
    "v_mfma_f32_16x16x32_bf16 a[8:11], a[184:187], a[96:99], a[8:11]\n"
    "v_mfma_f32_16x16x32_bf16 a[12:15], a[188:191], a[96:99], a[12:15]\n"
    "v_mfma_f32_16x16x32_bf16 a[16:19], a[176:179], a[100:103], a[16:19]\n"
    "v_mfma_f32_16x16x32_bf16 a[20:23], a[180:183], a[100:103], a[20:23]\n"
    "v_mfma_f32_16x16x32_bf16 a[24:27], a[184:187], a[100:103], a[24:27]\n"
    "v_mfma_f32_16x16x32_bf16 a[28:31], a[188:191], a[100:103], a[28:31]\n"
    "v_mfma_f32_16x16x32_bf16 a[32:35], a[176:179], a[104:107], a[32:35]\n"
    "v_mfma_f32_16x16x32_bf16 a[36:39], a[180:183], a[104:107], a[36:39]\n"
    "v_mfma_f32_16x16x32_bf16 a[40:43], a[184:187], a[104:107], a[40:43]\n"
    "v_mfma_f32_16x16x32_bf16 a[44:47], a[188:191], a[104:107], a[44:47]\n"
    "v_mfma_f32_16x16x32_bf16 a[48:51], a[176:179], a[108:111], a[48:51]\n"
    "v_mfma_f32_16x16x32_bf16 a[52:55], a[180:183], a[108:111], a[52:55]\n"
    "v_mfma_f32_16x16x32_bf16 a[56:59], a[184:187], a[108:111], a[56:59]\n"
    "v_mfma_f32_16x16x32_bf16 a[60:63], a[188:191], a[108:111], a[60:63]\n"
    "s_waitcnt vmcnt(14)\n"
    "v_mfma_f32_16x16x32_bf16 a[0:3], a[192:195], a[112:115], a[0:3]\n"
    "v_mfma_f32_16x16x32_bf16 a[4:7], a[196:199], a[112:115], a[4:7]\n"
    "v_mfma_f32_16x16x32_bf16 a[8:11], a[200:203], a[112:115], a[8:11]\n"
    "v_mfma_f32_16x16x32_bf16 a[12:15], a[204:207], a[112:115], a[12:15]\n"
    "v_mfma_f32_16x16x32_bf16 a[16:19], a[192:195], a[116:119], a[16:19]\n"
    "v_mfma_f32_16x16x32_bf16 a[20:23], a[196:199], a[116:119], a[20:23]\n"
    "v_mfma_f32_16x16x32_bf16 a[24:27], a[200:203], a[116:119], a[24:27]\n"
    "v_mfma_f32_16x16x32_bf16 a[28:31], a[204:207], a[116:119], a[28:31]\n"
    "v_mfma_f32_16x16x32_bf16 a[32:35], a[192:195], a[120:123], a[32:35]\n"
    "v_mfma_f32_16x16x32_bf16 a[36:39], a[196:199], a[120:123], a[36:39]\n"
    "v_mfma_f32_16x16x32_bf16 a[40:43], a[200:203], a[120:123], a[40:43]\n"
    "v_mfma_f32_16x16x32_bf16 a[44:47], a[204:207], a[120:123], a[44:47]\n"
    "v_mfma_f32_16x16x32_bf16 a[48:51], a[192:195], a[124:127], a[48:51]\n"
    "v_mfma_f32_16x16x32_bf16 a[52:55], a[196:199], a[124:127], a[52:55]\n"
    "v_mfma_f32_16x16x32_bf16 a[56:59], a[200:203], a[124:127], a[56:59]\n"
    "v_mfma_f32_16x16x32_bf16 a[60:63], a[204:207], a[124:127], a[60:63]\n"
    "s_waitcnt vmcnt(10)\n"
    "v_mfma_f32_16x16x32_bf16 a[0:3], a[208:211], a[128:131], a[0:3]\n"
    "v_mfma_f32_16x16x32_bf16 a[4:7], a[212:215], a[128:131], a[4:7]\n"
    "v_mfma_f32_16x16x32_bf16 a[8:11], a[216:219], a[128:131], a[8:11]\n"
    "v_mfma_f32_16x16x32_bf16 a[12:15], a[220:223], a[128:131], a[12:15]\n"
    "v_mfma_f32_16x16x32_bf16 a[16:19], a[208:211], a[132:135], a[16:19]\n"
    "v_mfma_f32_16x16x32_bf16 a[20:23], a[212:215], a[132:135], a[20:23]\n"
    "v_mfma_f32_16x16x32_bf16 a[24:27], a[216:219], a[132:135], a[24:27]\n"
    "v_mfma_f32_16x16x32_bf16 a[28:31], a[220:223], a[132:135], a[28:31]\n"
    "v_mfma_f32_16x16x32_bf16 a[32:35], a[208:211], a[136:139], a[32:35]\n"
    "v_mfma_f32_16x16x32_bf16 a[36:39], a[212:215], a[136:139], a[36:39]\n"
    "v_mfma_f32_16x16x32_bf16 a[40:43], a[216:219], a[136:139], a[40:43]\n"
    "v_mfma_f32_16x16x32_bf16 a[44:47], a[220:223], a[136:139], a[44:47]\n"
    "v_mfma_f32_16x16x32_bf16 a[48:51], a[208:211], a[140:143], a[48:51]\n"
    "v_mfma_f32_16x16x32_bf16 a[52:55], a[212:215], a[140:143], a[52:55]\n"
    "v_mfma_f32_16x16x32_bf16 a[56:59], a[216:219], a[140:143], a[56:59]\n"
    "v_mfma_f32_16x16x32_bf16 a[60:63], a[220:223], a[140:143], a[60:63]\n"
    "s_waitcnt vmcnt(6)\n"
    "v_mfma_f32_16x16x32_bf16 a[0:3], a[224:227], a[144:147], a[0:3]\n"
    "v_mfma_f32_16x16x32_bf16 a[4:7], a[228:231], a[144:147], a[4:7]\n"
    "v_mfma_f32_16x16x32_bf16 a[8:11], a[232:235], a[144:147], a[8:11]\n"
    "v_mfma_f32_16x16x32_bf16 a[12:15], a[236:239], a[144:147], a[12:15]\n"
    "v_mfma_f32_16x16x32_bf16 a[16:19], a[224:227], a[148:151], a[16:19]\n"
    "v_mfma_f32_16x16x32_bf16 a[20:23], a[228:231], a[148:151], a[20:23]\n"
    "v_mfma_f32_16x16x32_bf16 a[24:27], a[232:235], a[148:151], a[24:27]\n"
    "v_mfma_f32_16x16x32_bf16 a[28:31], a[236:239], a[148:151], a[28:31]\n"
    "v_mfma_f32_16x16x32_bf16 a[32:35], a[224:227], a[152:155], a[32:35]\n"
    "v_mfma_f32_16x16x32_bf16 a[36:39], a[228:231], a[152:155], a[36:39]\n"
    "v_mfma_f32_16x16x32_bf16 a[40:43], a[232:235], a[152:155], a[40:43]\n"
    "v_mfma_f32_16x16x32_bf16 a[44:47], a[236:239], a[152:155], a[44:47]\n"
    "v_mfma_f32_16x16x32_bf16 a[48:51], a[224:227], a[156:159], a[48:51]\n"
    "v_mfma_f32_16x16x32_bf16 a[52:55], a[228:231], a[156:159], a[52:55]\n"
    "v_mfma_f32_16x16x32_bf16 a[56:59], a[232:235], a[156:159], a[56:59]\n"
    "v_mfma_f32_16x16x32_bf16 a[60:63], a[236:239], a[156:159], a[60:63]\n"
    "s_waitcnt vmcnt(2)\n"
    "v_mfma_f32_16x16x32_bf16 a[0:3], a[240:243], a[160:163], a[0:3]\n"
    "v_mfma_f32_16x16x32_bf16 a[4:7], a[244:247], a[160:163], a[4:7]\n"
    "v_mfma_f32_16x16x32_bf16 a[8:11], a[248:251], a[160:163], a[8:11]\n"
    "v_mfma_f32_16x16x32_bf16 a[12:15], a[252:255], a[160:163], a[12:15]\n"
    "v_mfma_f32_16x16x32_bf16 a[16:19], a[240:243], a[164:167], a[16:19]\n"
    "v_mfma_f32_16x16x32_bf16 a[20:23], a[244:247], a[164:167], a[20:23]\n"
    "v_mfma_f32_16x16x32_bf16 a[24:27], a[248:251], a[164:167], a[24:27]\n"
    "v_mfma_f32_16x16x32_bf16 a[28:31], a[252:255], a[164:167], a[28:31]\n"
    "v_mfma_f32_16x16x32_bf16 a[32:35], a[240:243], a[168:171], a[32:35]\n"
    "v_mfma_f32_16x16x32_bf16 a[36:39], a[244:247], a[168:171], a[36:39]\n"
    "v_mfma_f32_16x16x32_bf16 a[40:43], a[248:251], a[168:171], a[40:43]\n"
    "v_mfma_f32_16x16x32_bf16 a[44:47], a[252:255], a[168:171], a[44:47]\n"
    "v_mfma_f32_16x16x32_bf16 a[48:51], a[240:243], a[172:175], a[48:51]\n"
    "v_mfma_f32_16x16x32_bf16 a[52:55], a[244:247], a[172:175], a[52:55]\n"
    "v_mfma_f32_16x16x32_bf16 a[56:59], a[248:251], a[172:175], a[56:59]\n"
    "v_mfma_f32_16x16x32_bf16 a[60:63], a[252:255], a[172:175], a[60:63]\n"
    "s_lshl_b32 s52, s48, 12\n"
    "v_add_u32 v13, s52, v0\n"
    "s_lshl_b32 s52, s48, 14\n"
    "v_add_u32 v14, s52, v0\n"
    "v_add_u32 v13, 0x18000, v13\n"
    "v_add_u32 v14, 0x18000, v14\n"
    "s_nop 0\n"
    "ds_write_b128 v13, a[0:3] offset:0\n"
    "ds_write_b128 v13, a[4:7] offset:1024\n"
    "ds_write_b128 v13, a[8:11] offset:2048\n"
    "ds_write_b128 v13, a[12:15] offset:3072\n"
    "ds_write_b128 v13, a[16:19] offset:16384\n"
    "ds_write_b128 v13, a[20:23] offset:17408\n"
    "ds_write_b128 v13, a[24:27] offset:18432\n"
    "ds_write_b128 v13, a[28:31] offset:19456\n"
    "ds_write_b128 v13, a[32:35] offset:32768\n"
    "ds_write_b128 v13, a[36:39] offset:33792\n"
    "ds_write_b128 v13, a[40:43] offset:34816\n"
    "ds_write_b128 v13, a[44:47] offset:35840\n"
    "ds_write_b128 v13, a[48:51] offset:49152\n"
    "ds_write_b128 v13, a[52:55] offset:50176\n"
    "ds_write_b128 v13, a[56:59] offset:51200\n"
    "ds_write_b128 v13, a[60:63] offset:52224\n"
    "s_waitcnt lgkmcnt(0)\n"
    "s_barrier\n"
    "ds_read_b128 v[16:19], v14 offset:0\n"
    "ds_read_b128 v[20:23], v14 offset:1024\n"
    "ds_read_b128 v[24:27], v14 offset:2048\n"
    "ds_read_b128 v[28:31], v14 offset:3072\n"
    "ds_read_b128 v[32:35], v14 offset:4096\n"
    "ds_read_b128 v[36:39], v14 offset:5120\n"
    "ds_read_b128 v[40:43], v14 offset:6144\n"
    "ds_read_b128 v[44:47], v14 offset:7168\n"
    "ds_read_b128 v[48:51], v14 offset:8192\n"
    "ds_read_b128 v[52:55], v14 offset:9216\n"
    "ds_read_b128 v[56:59], v14 offset:10240\n"
    "ds_read_b128 v[60:63], v14 offset:11264\n"
    "ds_read_b128 v[64:67], v14 offset:12288\n"
    "ds_read_b128 v[68:71], v14 offset:13312\n"
    "ds_read_b128 v[72:75], v14 offset:14336\n"
    "ds_read_b128 v[76:79], v14 offset:15360\n"
    "s_waitcnt lgkmcnt(8)\n"
    "v_pk_add_f32 v[16:17], v[16:17], v[32:33]\n"
    "v_pk_add_f32 v[18:19], v[18:19], v[34:35]\n"
    "v_pk_add_f32 v[20:21], v[20:21], v[36:37]\n"
    "v_pk_add_f32 v[22:23], v[22:23], v[38:39]\n"
    "v_pk_add_f32 v[24:25], v[24:25], v[40:41]\n"
    "v_pk_add_f32 v[26:27], v[26:27], v[42:43]\n"
    "v_pk_add_f32 v[28:29], v[28:29], v[44:45]\n"
    "v_pk_add_f32 v[30:31], v[30:31], v[46:47]\n"
    "s_waitcnt vmcnt(0) lgkmcnt(0)\n"
    "v_pk_add_f32 v[48:49], v[48:49], v[64:65]\n"
    "v_pk_add_f32 v[50:51], v[50:51], v[66:67]\n"
    "v_pk_add_f32 v[52:53], v[52:53], v[68:69]\n"
    "v_pk_add_f32 v[54:55], v[54:55], v[70:71]\n"
    "v_pk_add_f32 v[56:57], v[56:57], v[72:73]\n"
    "v_pk_add_f32 v[58:59], v[58:59], v[74:75]\n"
    "v_pk_add_f32 v[60:61], v[60:61], v[76:77]\n"
    "v_pk_add_f32 v[62:63], v[62:63], v[78:79]\n"
    "v_pk_add_f32 %0, v[16:17], v[48:49]\n"
    "v_pk_add_f32 %1, v[18:19], v[50:51]\n"
    "v_pk_add_f32 %2, v[20:21], v[52:53]\n"
    "v_pk_add_f32 %3, v[22:23], v[54:55]\n"
    "v_pk_add_f32 %4, v[24:25], v[56:57]\n"
    "v_pk_add_f32 %5, v[26:27], v[58:59]\n"
    "v_pk_add_f32 %6, v[28:29], v[60:61]\n"
    "v_pk_add_f32 %7, v[30:31], v[62:63]\n"
    "code_k5_end:\n"
      : ACC16_OUT, "=&v"(xr[0]), "=&v"(xr[1]), "=&v"(gb[0]), "=&v"(gb[1]), "=&v"(gb[2]), "=&v"(gb[3])
      : "s"(ws + WS_HH + (size_t)rt * 4 * 57344), "s"((const char*)A->wd + ct * 128), "s"(w), "s"(ldsb),
        "s"(ws), "v"((unsigned)WS_X1 + (((rt * 4 + w) * 16 + ct * 2) * 64 + lane) * 16), "v"((unsigned)WS_P3 + (1536 + col) * 4),
        "s"(ws + WS_FLAG), "s"(j < 28 ? FL_G + K4_RT * 128 + K4_CT * 4 : -1), "s"(FL_G + (rt >> 2) * 128), "s"(tag)
      : "v0","v1","v2","v3","v4","v5","v6","v7","v8","v9","v10","v11","v12","v13","v14","v15","v16","v17","v18","v19","v20","v21","v22","v23",
      "v24","v25","v26","v27","v28","v29","v30","v31","v32","v33","v34","v35","v36","v37","v38","v39","v40","v41","v42","v43","v44","v45","v46","v47",
      "v48","v49","v50","v51","v52","v53","v54","v55","v56","v57","v58","v59","v60","v61","v62","v63","v64","v65","v66","v67","v68","v69","v70","v71",
      "v72","v73","v74","v75","v76","v77","v78","v79","v80","v81","v82","v83","v84","v85","v86","v87","v88","v89","v90","v91","v92","v93","v94","v95",
      "v96","v97","v98","v99","v100","v101","v102","v103","v104","v105","v106","v107","v108","v109","v110","v111","v112","v113","v114","v115","v116","v117","v118","v119",
      "v120","v121","v122","v123","v124","v125","v126","v127","v128","v129","v130","v131","v132","v133","v134","v135","v136","v137","v138","v139","v140","v141","v142","v143",
      "v144","v145","v146","v147","v148","v149","v150","v151","v152","v153","v154","v155","v156","v157","v158","v159","a0","a1","a2","a3","a4","a5","a6","a7",
      "a8","a9","a10","a11","a12","a13","a14","a15","a16","a17","a18","a19","a20","a21","a22","a23","a24","a25","a26","a27","a28","a29","a30","a31",
      "a32","a33","a34","a35","a36","a37","a38","a39","a40","a41","a42","a43","a44","a45","a46","a47","a48","a49","a50","a51","a52","a53","a54","a55",
      "a56","a57","a58","a59","a60","a61","a62","a63","a64","a65","a66","a67","a68","a69","a70","a71","a72","a73","a74","a75","a76","a77","a78","a79",
      "a80","a81","a82","a83","a84","a85","a86","a87","a88","a89","a90","a91","a92","a93","a94","a95","a96","a97","a98","a99","a100","a101","a102","a103",
      "a104","a105","a106","a107","a108","a109","a110","a111","a112","a113","a114","a115","a116","a117","a118","a119","a120","a121","a122","a123","a124","a125","a126","a127",
      "a128","a129","a130","a131","a132","a133","a134","a135","a136","a137","a138","a139","a140","a141","a142","a143","a144","a145","a146","a147","a148","a149","a150","a151",
      "a152","a153","a154","a155","a156","a157","a158","a159","a160","a161","a162","a163","a164","a165","a166","a167","a168","a169","a170","a171","a172","a173","a174","a175",
      "a176","a177","a178","a179","a180","a181","a182","a183","a184","a185","a186","a187","a188","a189","a190","a191","a192","a193","a194","a195","a196","a197","a198","a199",
      "a200","a201","a202","a203","a204","a205","a206","a207","a208","a209","a210","a211","a212","a213","a214","a215","a216","a217","a218","a219","a220","a221","a222","a223",
      "a224","a225","a226","a227","a228","a229","a230","a231","a232","a233","a234","a235","a236","a237","a238","a239","a240","a241","a242","a243","a244","a245","a246","a247",
      "a248","a249","a250","a251","a252","a253","a254","a255","s40","s41","s42","s43","s44","s45","s46","s47","s48","s49","s50","s51","s52","s53","s54","s55",
      "s56","s57","s58","s59","s60","s61","s62","s63","s64","s65","scc","vcc","memory");
    STAMP3(); STAMP(4);
    u16* out = A->out;
#pragma unroll
    for (int nb = 0; nb < 4; ++nb) { u32x2 o;
#pragma unroll
      for (int e = 0; e < 2; ++e) { const unsigned xw = xr[nb >> 1][(nb & 1) * 2 + e];
        o[e] = pk2(bflo(xw) + rbf(gb[nb][2 * e] * rbf(AC(nb * 4 + 2 * e))), bfhi(xw) + rbf(gb[nb][2 * e + 1] * rbf(AC(nb * 4 + 2 * e + 1)))); }
      *(u32x2*)(out + (size_t)row * 512 + col + nb * 16) = o; }
    if (b == 0 && w == 0 && lane == 0) (void)__hip_atomic_fetch_add((unsigned*)(ws + WS_EPOCH), 1u, __ATOMIC_RELAXED, __HIP_MEMORY_SCOPE_AGENT);
    STAMP(5);
#undef DIAGK
  }
}
extern "C" void kernel_launch(void* const* d_in, const int* in_sizes, int n_in, void* d_out, int out_size, void* d_ws, size_t ws_size, hipStream_t stream) {
  (void)in_sizes; (void)n_in; (void)out_size; (void)ws_size;
  k_block<<<256, 256, 0, stream>>>((const u16*)d_in[3], (const u16*)d_in[4], (const u16*)d_in[0], (const u16*)d_in[2], (const u16*)d_in[5], (const u16*)d_in[7], (const u16*)d_in[8],
                                   (const u16*)d_in[9], (char*)d_ws, (u16*)d_out, (const u16*)d_in[1], (const u16*)d_in[10], (const u16*)d_in[11]);
}
```
